```python
import torch

def _softmax(x, axis=-1):
    return torch.softmax(x, dim=axis)
def _gelu(x):
    return torch.nn.functional.gelu(x, approximate="tanh")
def _silu(x):
    return torch.nn.functional.silu(x)
def _sigmoid(x):
    return torch.sigmoid(x)
def _layernorm(x, axis=-1, eps=1e-5):
    m = x.mean(dim=axis, keepdim=True)
    v = x.var(dim=axis, keepdim=True, unbiased=False)
    return (x - m) / torch.sqrt(v + eps)
def _scatter_add(tgt, idx, src, axis=0):
    return tgt.clone().index_add_(axis, idx, src)
def _segment_sum(src, seg, n):
    out = torch.zeros((n,) + src.shape[1:], dtype=src.dtype)
    return out.index_add_(0, seg, src)
def _pad(x, pw):
    flat = tuple(v for pair in reversed(pw) for v in pair)
    return torch.nn.functional.pad(x, flat)

kernel_name = 'standalone__mha_block_cached__near_ridge'

def setup_inputs():

    _w = (lambda a: (lambda t: t.to(torch.bfloat16) if t.is_floating_point() else t)(torch.as_tensor(a)))
    B, Sq, D = 16, 144, 1024
    H, Sk, Dh = 32, 512, 128
    return {
        "x": _w((lambda *s: torch.randn(*s, dtype=torch.bfloat16) * 0.8)(B, Sq, D)),
        "k_cache": _w((lambda *s: torch.randn(*s, dtype=torch.bfloat16) * 0.8)(B, H, Sk, Dh)),
        "v_cache": _w((lambda *s: torch.randn(*s, dtype=torch.bfloat16) * 0.05)(B, H, Sk, Dh)),
        "wq": _w((lambda *s: torch.randn(*s, dtype=torch.bfloat16) * 0.8)(D, H * Dh) * (D ** -0.5)),
        "wo": _w((lambda *s: torch.randn(*s, dtype=torch.bfloat16) * 0.05)(H * Dh, D) * ((H * Dh) ** -0.5)),
    }

def reference(x, k_cache, v_cache, wq, wo):
    B, Sq, D = x.shape
    H, Dh = 32, 128
    q = (lambda x, *d: x.permute(*d))((x @ wq).reshape(B, Sq, H, Dh), 0, 2, 1, 3)
    scores = torch.einsum("bhqd,bhkd->bhqk", q, k_cache) * (Dh ** -0.5)
    p = _softmax(scores, axis=-1)
    o = torch.einsum("bhqk,bhkd->bhqd", p, v_cache)
    return (lambda x, *d: x.permute(*d))(o, 0, 2, 1, 3).reshape(B, Sq, H * Dh) @ wo
```

```cpp
#include <hip/hip_runtime.h>
#include <hip/hip_bf16.h>
typedef __attribute__((ext_vector_type(8))) __bf16 bf16x8;
typedef __attribute__((ext_vector_type(4))) float f32x4;
typedef unsigned short u16;
constexpr int B=16,SQ=144,D=1024,H=32,SK=512,DH=128;
__device__ inline float bf2f(u16 v){return __uint_as_float(((unsigned)v)<<16);}
__device__ inline u16 f2bf(float f){ __bf16 b=(__bf16)f; return __builtin_bit_cast(u16,b);}

__global__ __launch_bounds__(256) void gemm_naive(const u16* A,const u16* Bm,u16* C,int M,int N,int K){
  __shared__ u16 As[48][40];
  __shared__ u16 Bs[64][40];
  int bm=blockIdx.y*48,bn=blockIdx.x*64;
  int t=threadIdx.x,l=t&63,w=t>>6;
  f32x4 acc[3]={};
  for(int k0=0;k0<K;k0+=32){
    for(int i=t;i<48*32;i+=256){int r=i>>5,c=i&31;As[r][c]=A[(size_t)(bm+r)*K+k0+c];}
    for(int i=t;i<32*64;i+=256){int r=i>>6,c=i&63;Bs[c][r]=Bm[(size_t)(k0+r)*N+bn+c];}
    __syncthreads();
    bf16x8 b=*(const bf16x8*)&Bs[w*16+(l&15)][(l>>4)*8];
    for(int m=0;m<3;m++){
      bf16x8 a=*(const bf16x8*)&As[m*16+(l&15)][(l>>4)*8];
      acc[m]=__builtin_amdgcn_mfma_f32_16x16x32_bf16(a,b,acc[m],0,0,0);
    }
    __syncthreads();
  }
  for(int m=0;m<3;m++)for(int j=0;j<4;j++) C[(size_t)(bm+m*16+(l>>4)*4+j)*N+bn+w*16+(l&15)]=f2bf(acc[m][j]);
}
__global__ __launch_bounds__(256) void attn_naive(const u16* Q,const u16* Kc,const u16* Vc,u16* O){
  __shared__ float S[16][SK+4];
  __shared__ u16 P[16][SK+8];
  __shared__ u16 Vt[128][40];
  __shared__ float linv[16];
  int rb=blockIdx.x,h=blockIdx.y,b=blockIdx.z;
  int t=threadIdx.x,l=t&63,w=t>>6;
  const u16* q=Q+((size_t)(b*SQ+rb*16))*(H*DH)+h*DH;
  const u16* kp=Kc+((size_t)(b*H+h))*SK*DH;
  const u16* vp=Vc+((size_t)(b*H+h))*SK*DH;
  bf16x8 qa[4];
  for(int k=0;k<4;k++) qa[k]=*(const bf16x8*)&q[(size_t)(l&15)*(H*DH)+k*32+(l>>4)*8];
  for(int n=w;n<SK/16;n+=4){
    f32x4 acc={};
    for(int k=0;k<4;k++){
      bf16x8 kb=*(const bf16x8*)&kp[(size_t)(n*16+(l&15))*DH+k*32+(l>>4)*8];
      acc=__builtin_amdgcn_mfma_f32_16x16x32_bf16(qa[k],kb,acc,0,0,0);
    }
    for(int j=0;j<4;j++) S[(l>>4)*4+j][n*16+(l&15)]=acc[j]*0.08838834764831845f;
  }
  __syncthreads();
  for(int r=w*4;r<w*4+4;r++){
    float m=-1e30f;
    for(int c=l;c<SK;c+=64) m=fmaxf(m,S[r][c]);
    for(int o=32;o>0;o>>=1) m=fmaxf(m,__shfl_xor(m,o));
    float s=0;
    for(int c=l;c<SK;c+=64){float e=__expf(S[r][c]-m);s+=e;P[r][c]=f2bf(e);}
    for(int o=32;o>0;o>>=1) s+=__shfl_xor(s,o);
    if(l==0) linv[r]=1.f/s;
  }
  __syncthreads();
  f32x4 acc[2]={};
  for(int k0=0;k0<SK;k0+=32){
    for(int i=t;i<32*128;i+=256){int r=i>>7,c=i&127;Vt[c][r]=vp[(size_t)(k0+r)*DH+c];}
    __syncthreads();
    bf16x8 a=*(const bf16x8*)&P[l&15][k0+(l>>4)*8];
    for(int n=0;n<2;n++){
      bf16x8 bb=*(const bf16x8*)&Vt[w*32+n*16+(l&15)][(l>>4)*8];
      acc[n]=__builtin_amdgcn_mfma_f32_16x16x32_bf16(a,bb,acc[n],0,0,0);
    }
    __syncthreads();
  }
  for(int n=0;n<2;n++)for(int j=0;j<4;j++){int r=(l>>4)*4+j;
    O[((size_t)(b*SQ+rb*16+r))*(H*DH)+h*DH+w*32+n*16+(l&15)]=f2bf(acc[n][j]*linv[r]);}
}
extern "C" void kernel_launch(void* const* d_in, const int* in_sizes, int n_in, void* d_out, int out_size, void* d_ws, size_t ws_size, hipStream_t stream){
  const u16* x=(const u16*)d_in[0]; const u16* kc=(const u16*)d_in[1]; const u16* vc=(const u16*)d_in[2];
  const u16* wq=(const u16*)d_in[3]; const u16* wo=(const u16*)d_in[4];
  u16* q=(u16*)d_ws; u16* o=q+(size_t)B*SQ*H*DH;
  gemm_naive<<<dim3(4096/64,2304/48),256,0,stream>>>(x,wq,q,2304,4096,1024);
  attn_naive<<<dim3(9,H,B),256,0,stream>>>(q,kc,vc,o);
  gemm_naive<<<dim3(1024/64,2304/48),256,0,stream>>>(o,wo,(u16*)d_out,2304,1024,4096);
}
```

```cpp
#include <hip/hip_runtime.h>
#include <hip/hip_bf16.h>
typedef __attribute__((ext_vector_type(8))) __bf16 bf16x8;
typedef __attribute__((ext_vector_type(4))) float f32x4;
typedef unsigned short u16;
constexpr int B=16,SQ=144,D=1024,H=32,SK=512,DH=128;
__device__ inline float bf2f(u16 v){return __uint_as_float(((unsigned)v)<<16);}
__device__ inline u16 f2bf(float f){ __bf16 b=(__bf16)f; return __builtin_bit_cast(u16,b);}

__global__ __launch_bounds__(256) void gemm_naive(const u16* A,const u16* Bm,u16* C,int M,int N,int K){
  __shared__ u16 As[48][40];
  __shared__ u16 Bs[64][40];
  int bm=blockIdx.y*48,bn=blockIdx.x*64;
  int t=threadIdx.x,l=t&63,w=t>>6;
  f32x4 acc[3]={};
  for(int k0=0;k0<K;k0+=32){
    for(int i=t;i<48*32;i+=256){int r=i>>5,c=i&31;As[r][c]=A[(size_t)(bm+r)*K+k0+c];}
    for(int i=t;i<32*64;i+=256){int r=i>>6,c=i&63;Bs[c][r]=Bm[(size_t)(k0+r)*N+bn+c];}
    __syncthreads();
    bf16x8 b=*(const bf16x8*)&Bs[w*16+(l&15)][(l>>4)*8];
    for(int m=0;m<3;m++){
      bf16x8 a=*(const bf16x8*)&As[m*16+(l&15)][(l>>4)*8];
      acc[m]=__builtin_amdgcn_mfma_f32_16x16x32_bf16(a,b,acc[m],0,0,0);
    }
    __syncthreads();
  }
  for(int m=0;m<3;m++)for(int j=0;j<4;j++) C[(size_t)(bm+m*16+(l>>4)*4+j)*N+bn+w*16+(l&15)]=f2bf(acc[m][j]);
}
__global__ __launch_bounds__(256) void attn_naive(const u16* Q,const u16* Kc,const u16* Vc,u16* O){
  __shared__ float S[16][SK+4];
  __shared__ u16 P[16][SK+8];
  __shared__ u16 Vt[128][40];
  __shared__ float linv[16];
  int rb=blockIdx.x,h=blockIdx.y,b=blockIdx.z;
  int t=threadIdx.x,l=t&63,w=t>>6;
  const u16* q=Q+((size_t)(b*SQ+rb*16))*(H*DH)+h*DH;
  const u16* kp=Kc+((size_t)(b*H+h))*SK*DH;
  const u16* vp=Vc+((size_t)(b*H+h))*SK*DH;
  bf16x8 qa[4];
  for(int k=0;k<4;k++) qa[k]=*(const bf16x8*)&q[(size_t)(l&15)*(H*DH)+k*32+(l>>4)*8];
  for(int n=w;n<SK/16;n+=4){
    f32x4 acc={};
    for(int k=0;k<4;k++){
      bf16x8 kb=*(const bf16x8*)&kp[(size_t)(n*16+(l&15))*DH+k*32+(l>>4)*8];
      acc=__builtin_amdgcn_mfma_f32_16x16x32_bf16(qa[k],kb,acc,0,0,0);
    }
    for(int j=0;j<4;j++) S[(l>>4)*4+j][n*16+(l&15)]=acc[j]*0.08838834764831845f;
  }
  __syncthreads();
  for(int r=w*4;r<w*4+4;r++){
    float m=-1e30f;
    for(int c=l;c<SK;c+=64) m=fmaxf(m,S[r][c]);
    for(int o=32;o>0;o>>=1) m=fmaxf(m,__shfl_xor(m,o));
    float s=0;
    for(int c=l;c<SK;c+=64){float e=__expf(S[r][c]-m);s+=e;P[r][c]=f2bf(e);}
    for(int o=32;o>0;o>>=1) s+=__shfl_xor(s,o);
    if(l==0) linv[r]=1.f/s;
  }
  __syncthreads();
  f32x4 acc[2]={};
  for(int k0=0;k0<SK;k0+=32){
    for(int i=t;i<32*128;i+=256){int r=i>>7,c=i&127;Vt[c][r]=vp[(size_t)(k0+r)*DH+c];}
    __syncthreads();
    bf16x8 a=*(const bf16x8*)&P[l&15][k0+(l>>4)*8];
    for(int n=0;n<2;n++){
      bf16x8 bb=*(const bf16x8*)&Vt[w*32+n*16+(l&15)][(l>>4)*8];
      acc[n]=__builtin_amdgcn_mfma_f32_16x16x32_bf16(a,bb,acc[n],0,0,0);
    }
    __syncthreads();
  }
  for(int n=0;n<2;n++)for(int j=0;j<4;j++){int r=(l>>4)*4+j;
    O[((size_t)(b*SQ+rb*16+r))*(H*DH)+h*DH+w*32+n*16+(l&15)]=f2bf(acc[n][j]*linv[r]);}
}

__global__ void probe(unsigned* st){ st[threadIdx.x]=0; }

__global__ void spin(const unsigned* st){
  unsigned s=0; for(int i=0;i<256;i++) s+=st[i];
  unsigned long long t0=__builtin_amdgcn_s_memrealtime();
  unsigned long long d=(unsigned long long)s*100/256; if(d>2000000) d=2000000;
  while(__builtin_amdgcn_s_memrealtime()-t0<d) __builtin_amdgcn_s_sleep(8);
}
extern "C" void kernel_launch(void* const* d_in, const int* in_sizes, int n_in, void* d_out, int out_size, void* d_ws, size_t ws_size, hipStream_t stream){
  const u16* x=(const u16*)d_in[0]; const u16* kc=(const u16*)d_in[1]; const u16* vc=(const u16*)d_in[2];
  const u16* wq=(const u16*)d_in[3]; const u16* wo=(const u16*)d_in[4];
  u16* q=(u16*)d_ws; u16* o=q+(size_t)B*SQ*H*DH;
  unsigned* st=(unsigned*)((char*)d_ws+(100<<20));
  probe<<<1,256,0,stream>>>(st);
  gemm_naive<<<dim3(4096/64,2304/48),256,0,stream>>>(x,wq,q,2304,4096,1024);
  attn_naive<<<dim3(9,H,B),256,0,stream>>>(q,kc,vc,o);
  gemm_naive<<<dim3(1024/64,2304/48),256,0,stream>>>(o,wo,(u16*)d_out,2304,1024,4096);
  spin<<<1,1,0,stream>>>(st);
}
```

```cpp
#include <hip/hip_runtime.h>
#include <hip/hip_bf16.h>
__global__ __launch_bounds__(256, 1) void mha_fused(const void* x, const void* k, const void* v, const void* wq, const void* wo, void* out, void* ws) {
  unsigned tid = threadIdx.x;
  asm volatile(
"s_mov_b64 s[8:9], %1\n"
"s_mov_b32 s10, %2\n"
"s_load_dwordx8 s[12:19], s[8:9], 0x0\n"
"s_load_dwordx4 s[20:23], s[8:9], 0x20\n"
"s_load_dwordx2 s[24:25], s[8:9], 0x30\n"
"s_getpc_b64 s[98:99]\n"
".Lmha_CODE0:\n"
"v_lshrrev_b32 v93, 3, v0\n"
"v_lshlrev_b32 v93, 7, v93\n"
"global_load_dword v94, v93, s[98:99] offset:256\n"
"s_and_b32 s70, s10, 6\n"
"s_and_b32 s68, s10, 1\n"
"s_lshl_b32 s70, s70, 1\n"
"s_lshl_b32 s68, s68, 3\n"
"s_lshl_b32 s72, s70, 1\n"
"s_lshr_b32 s27, s10, 6\n"
"s_add_u32 s72, s72, s68\n"
"s_or_b32 s27, s27, s70\n"
"s_add_u32 s74, s72, 0\n"
"v_lshrrev_b32 v95, 6, v0\n"
"v_and_b32 v83, 3, v0\n"
"v_readfirstlane_b32 s11, v95\n"
"s_lshl_b32 s71, s27, 9\n"
"v_lshlrev_b32 v96, 4, v83\n"
"v_lshrrev_b32 v88, 5, v0\n"
"s_and_b32 s74, s74, 15\n"
"v_bfe_u32 v81, v0, 2, 3\n"
"v_lshl_add_u32 v88, v88, 6, v96\n"
"s_mul_i32 s30, s11, 0xa000\n"
"s_sub_u32 s42, 0x800000, s71\n"
"s_mov_b32 s43, 0x20000\n"
"v_lshl_add_u32 v37, v81, 13, v88\n"
"s_lshl_b32 s74, s74, 19\n"
"v_xor_b32 v89, 32, v37\n"
"s_bfe_u32 s26, s10, 0x30003\n"
"v_add_u32 v38, 0xfc00, v89\n"
"v_and_b32 v80, 63, v0\n"
"s_or_b32 s26, s26, s68\n"
"v_add_u32 v39, 0x1f800, v37\n"
"s_add_u32 s73, s72, 0\n"
"v_and_b32 v84, 15, v80\n"
"v_lshrrev_b32 v90, 3, v84\n"
"s_mul_i32 s69, s26, 0x48000\n"
"v_add_u32 v40, 0x2f400, v89\n"
"v_and_b32 v87, 0x7f, v88\n"
"s_and_b32 s73, s73, 15\n"
"v_lshrrev_b32 v85, 4, v80\n"
"v_lshlrev_b32 v91, 1, v90\n"
"v_lshl_add_u32 v97, v81, 11, v87\n"
"v_xor_b32 v91, v91, v85\n"
"s_mov_b32 s38, 0x48000\n"
"s_mov_b32 s39, 0x20000\n"
"v_lshl_add_u32 v32, v95, 16, v97\n"
"s_lshl_b32 s73, s73, 7\n"
"v_lshlrev_b32 v91, 4, v91\n"
"v_and_b32 v92, 7, v84\n"
"v_xor_b32 v97, 32, v32\n"
"v_lshl_add_u32 v91, v92, 6, v91\n"
"v_add_u32 v33, 0x3c00, v97\n"
"v_bfe_u32 v86, v80, 2, 2\n"
"v_and_b32 v92, 0xff, v91\n"
"v_lshl_add_u32 v21, v86, 10, v92\n"
"v_add_u32 v34, 0x7800, v32\n"
"v_bfe_u32 v88, v0, 6, 1\n"
"v_mul_u32_u24 v92, 0xa000, v90\n"
"v_mul_u32_u24 v88, 0x4020, v88\n"
"v_and_b32 v89, 0x80, v0\n"
"v_lshl_add_u32 v27, v90, 10, v91\n"
"v_add_u32 v92, v92, v91\n"
"s_bfe_u32 s31, s10, 0x20006\n"
"v_add_u32 v35, 0xb400, v97\n"
"v_xor_b32 v88, v88, v87\n"
"v_lshl_add_u32 v89, v81, 11, v89\n"
"v_lshlrev_b32 v91, 3, v83\n"
"s_lshl_b32 s5, s10, 9\n"
"s_waitcnt lgkmcnt(0)\n"
"s_add_u32 s40, s18, s71\n"
"s_addc_u32 s41, s19, 0\n"
"s_add_u32 m0, s30, 0x4000\n"
"s_nop 0\n"
"buffer_load_dwordx4 v37, s[40:43], s74 offen offset:0 lds\n"
"buffer_load_dwordx4 v38, s[40:43], s74 offen offset:1024 lds\n"
"buffer_load_dwordx4 v39, s[40:43], s74 offen offset:2048 lds\n"
"s_add_u32 s36, s12, s69\n"
"s_addc_u32 s37, s13, 0\n"
"buffer_load_dwordx4 v40, s[40:43], s74 offen offset:3072 lds\n"
"s_add_u32 m0, s30, 0x2000\n"
"s_nop 0\n"
"buffer_load_dwordx4 v32, s[36:39], s73 offen offset:0 lds\n"
"buffer_load_dwordx4 v33, s[36:39], s73 offen offset:1024 lds\n"
"buffer_load_dwordx4 v34, s[36:39], s73 offen offset:2048 lds\n"
"s_bfe_u32 s28, s10, 0x10002\n"
"s_and_b32 s29, s27, 7\n"
"v_add_u32 v88, v88, v89\n"
"v_and_b32 v90, 1, v85\n"
"v_lshl_add_u32 v91, v86, 6, v91\n"
"v_lshrrev_b32 v98, 6, v0\n"
"v_bfe_u32 v82, v0, 5, 1\n"
"v_min_u32 v25, 1, v0\n"
"s_lshl_b32 s31, s31, 11\n"
"v_add_u32 v36, 0x40000, v88\n"
"buffer_load_dwordx4 v35, s[36:39], s73 offen offset:3072 lds\n"
"v_add_u32 v44, 0x9000, v92\n"
"v_lshl_add_u32 v91, v90, 5, v91\n"
"v_lshl_add_u32 v98, v98, 7, s5\n"
"v_lshrrev_b32 v0, 4, v0\n"
"s_lshl_b32 s70, s26, 4\n"
"s_add_u32 s70, s70, s27\n"
"s_lshl_b32 s70, s70, 18\n"
"s_add_u32 s44, s14, s70\n"
"s_addc_u32 s45, s15, 0\n"
"s_mov_b32 s46, 0x40000\n"
"s_mov_b32 s47, 0x20000\n"
"s_add_u32 s48, s16, s70\n"
"s_addc_u32 s49, s17, 0\n"
"s_mov_b32 s50, 0x40000\n"
"s_mov_b32 s51, 0x20000\n"
"s_lshl_b32 s70, s29, 8\n"
"s_sub_u32 s71, 0x800000, s70\n"
"s_add_u32 s52, s20, s70\n"
"s_addc_u32 s53, s21, 0\n"
"s_mov_b32 s54, s71\n"
"s_mov_b32 s55, 0x20000\n"
"s_mul_i32 s70, s26, 0x120000\n"
"s_add_u32 s56, s24, s70\n"
"s_addc_u32 s57, s25, 0\n"
"s_mov_b32 s58, 0x5120080\n"
"s_mov_b32 s59, 0x20000\n"
"s_mul_i32 s70, s26, 0x48000\n"
"s_add_u32 s60, s22, s70\n"
"s_addc_u32 s61, s23, 0\n"
"s_mov_b32 s62, 0x48000\n"
"s_mov_b32 s63, 0x20000\n"
"s_add_u32 s64, s24, 0x1200000\n"
"s_addc_u32 s65, s25, 0\n"
"s_mov_b32 s66, 0x5900080\n"
"s_mov_b32 s67, 0x20000\n"
"s_lshl_b32 s68, s11, 21\n"
"s_lshl_b32 s69, s29, 1\n"
"s_add_u32 s69, s69, s28\n"
"s_lshl_b32 s69, s69, 7\n"
"s_buffer_load_dword s83, s[60:63], s69\n"
"s_sub_u32 s69, 0x180, s56\n"
"s_and_b32 s69, s69, 0x380\n"
"s_lshr_b32 s70, s11, 1\n"
"s_mul_i32 s70, s70, 0x5000000\n"
"s_add_u32 s69, s69, s70\n"
"s_and_b32 s70, s11, 1\n"
"s_mul_i32 s71, s70, 0x11fc00\n"
"s_add_u32 s71, s71, s69\n"
"s_mul_i32 s70, s70, 0x8fc00\n"
"s_add_u32 s69, s69, s70\n"
"s_mul_i32 s70, s26, 0x90000\n"
"s_add_u32 s69, s69, s70\n"
"s_buffer_load_dword s79, s[44:47], 0\n"
"s_buffer_load_dword s80, s[48:51], 0\n"
"s_buffer_load_dword s81, s[40:43], s68\n"
"s_buffer_load_dword s82, s[52:55], s68\n"
"s_buffer_load_dword s85, s[56:59], s71\n"
"s_buffer_load_dword s97, s[64:67], s69\n"
"v_lshl_add_u32 v92, v85, 10, v91\n"
"v_min_u32 v98, .Lmha_END-.Lmha_CODE0-4, v98\n"
"v_lshl_add_u32 v0, v0, 7, s31\n"
"s_add_u32 m0, s30, 0x9000\n"
"s_nop 0\n"
"buffer_load_dwordx4 v36, s[36:39], s73 offen offset:0 lds\n"
"v_add_u32 v45, s30, v92\n"
"v_add_u32 v41, 0xa000, v27\n"
"v_add_u32 v42, 0x14000, v27\n"
"v_add_u32 v43, 0x1e000, v27\n"
"v_add_u32 v30, 0x14000, v44\n"
"v_xor_b32 v46, 32, v45\n"
"global_load_dword v98, v98, s[98:99] offset:0\n"
"v_lshlrev_b32 v25, 31, v25\n"
"global_load_dword v99, v0, s[98:99] offset:4092\n"
"s_add_u32 s75, s74, 0x40000\n"
"s_add_u32 m0, s30, 0x5000\n"
"s_nop 0\n"
"buffer_load_dwordx4 v37, s[40:43], s75 offen offset:0 lds\n"
"buffer_load_dwordx4 v38, s[40:43], s75 offen offset:1024 lds\n"
"s_add_u32 s90, s72, 1\n"
"buffer_load_dwordx4 v39, s[40:43], s75 offen offset:2048 lds\n"
"s_and_b32 s90, s90, 15\n"
"buffer_load_dwordx4 v40, s[40:43], s75 offen offset:3072 lds\n"
"s_lshl_b32 s90, s90, 19\n"
"s_add_u32 m0, s30, 0x6000\n"
"s_nop 0\n"
"buffer_load_dwordx4 v37, s[40:43], s90 offen offset:0 lds\n"
"buffer_load_dwordx4 v38, s[40:43], s90 offen offset:1024 lds\n"
"s_add_u32 s88, s72, 1\n"
"buffer_load_dwordx4 v39, s[40:43], s90 offen offset:2048 lds\n"
"s_and_b32 s88, s88, 15\n"
"buffer_load_dwordx4 v40, s[40:43], s90 offen offset:3072 lds\n"
"s_lshl_b32 s88, s88, 7\n"
"s_add_u32 m0, s30, 0x8000\n"
"s_nop 0\n"
"buffer_load_dwordx4 v32, s[36:39], s88 offen offset:0 lds\n"
"buffer_load_dwordx4 v33, s[36:39], s88 offen offset:1024 lds\n"
"buffer_load_dwordx4 v34, s[36:39], s88 offen offset:2048 lds\n"
"buffer_load_dwordx4 v35, s[36:39], s88 offen offset:3072 lds\n"
"s_waitcnt vmcnt(14)\n"
"s_barrier\n"
"ds_read_b64_tr_b16 a[0:1], v45 offset:16384\n"
"ds_read_b64_tr_b16 a[2:3], v45 offset:16640\n"
"ds_read_b128 v[48:51], v27 offset:8192\n"
"ds_read_b64_tr_b16 a[4:5], v46 offset:16384\n"
"ds_read_b64_tr_b16 a[6:7], v46 offset:16640\n"
"ds_read_b64_tr_b16 a[8:9], v45 offset:16896\n"
"ds_read_b64_tr_b16 a[10:11], v45 offset:17152\n"
"ds_read_b64_tr_b16 a[12:13], v46 offset:16896\n"
"ds_read_b64_tr_b16 a[14:15], v46 offset:17152\n"
"ds_read_b128 v[52:55], v27 offset:10240\n"
"ds_read_b128 v[56:59], v41 offset:8192\n"
"ds_read_b128 v[60:63], v41 offset:10240\n"
"ds_read_b128 v[64:67], v42 offset:8192\n"
"ds_read_b128 v[68:71], v42 offset:10240\n"
"ds_read_b128 v[72:75], v43 offset:8192\n"
"ds_read_b128 v[76:79], v43 offset:10240\n"
"s_waitcnt lgkmcnt(0)\n"
"v_mfma_f32_16x16x32_bf16 v[112:115], a[0:3], v[48:51], 0\n"
"s_add_u32 s76, s90, 0x40000\n"
"s_add_u32 m0, s30, 0x7000\n"
"s_nop 0\n"
"buffer_load_dwordx4 v37, s[40:43], s76 offen offset:0 lds\n"
"v_mfma_f32_16x16x32_bf16 v[116:119], a[4:7], v[48:51], 0\n"
"s_add_u32 s91, s72, 2\n"
"v_mov_b32 v91, v92\n"
"v_lshlrev_b32 v22, 4, v80\n"
"v_mfma_f32_16x16x32_bf16 v[120:123], a[8:11], v[48:51], 0\n"
"s_and_b32 s91, s91, 15\n"
"v_lshlrev_b32 v92, 3, v90\n"
"v_lshl_add_u32 v92, v84, 4, v92\n"
"v_mfma_f32_16x16x32_bf16 v[124:127], a[12:15], v[48:51], 0\n"
"s_lshl_b32 s91, s91, 19\n"
"buffer_load_dwordx4 v38, s[40:43], s76 offen offset:1024 lds\n"
"ds_read_b128 v[48:51], v44 offset:0\n"
"v_mfma_f32_16x16x32_bf16 v[128:131], a[0:3], v[52:55], 0\n"
"s_xor_b32 s70, s11, 0\n"
"v_lshl_add_u32 v92, v82, 8, v92\n"
"v_add_u32 v47, s30, v92\n"
"v_mfma_f32_16x16x32_bf16 v[132:135], a[4:7], v[52:55], 0\n"
"s_mul_i32 s71, s70, 0xa000\n"
"v_lshlrev_b32 v92, 4, v83\n"
"v_lshl_add_u32 v92, v85, 6, v92\n"
"v_mfma_f32_16x16x32_bf16 v[136:139], a[8:11], v[52:55], 0\n"
"buffer_load_dwordx4 v39, s[40:43], s76 offen offset:2048 lds\n"
"v_add_u32 v5, s71, v91\n"
"v_mfma_f32_16x16x32_bf16 v[140:143], a[12:15], v[52:55], 0\n"
"s_mul_i32 s79, s70, 0x2400\n"
"ds_read_b128 v[52:55], v27 offset:8704\n"
"v_add_u32 v1, s71, v21\n"
"v_mfma_f32_16x16x32_bf16 v[144:147], a[0:3], v[56:59], 0\n"
"s_xor_b32 s70, s11, 1\n"
"v_add_u32 v13, s71, v22\n"
"v_lshl_add_u32 v92, v86, 8, v92\n"
"v_mfma_f32_16x16x32_bf16 v[148:151], a[4:7], v[56:59], 0\n"
"buffer_load_dwordx4 v40, s[40:43], s76 offen offset:3072 lds\n"
"v_lshrrev_b32 v90, 2, v81\n"
"v_mfma_f32_16x16x32_bf16 v[152:155], a[8:11], v[56:59], 0\n"
"s_mul_i32 s71, s70, 0xa000\n"
"v_lshlrev_b32 v90, 5, v90\n"
"v_xor_b32 v6, 32, v5\n"
"v_mfma_f32_16x16x32_bf16 v[156:159], a[12:15], v[56:59], 0\n"
"s_mul_i32 s80, s70, 0x2400\n"
"ds_read_b128 v[56:59], v27 offset:10752\n"
"v_add_u32 v7, s71, v91\n"
"v_mfma_f32_16x16x32_bf16 v[160:163], a[0:3], v[60:63], 0\n"
"s_xor_b32 s70, s11, 2\n"
"v_add_u32 v2, s71, v21\n"
"v_add_u32 v14, s71, v22\n"
"v_mfma_f32_16x16x32_bf16 v[164:167], a[4:7], v[60:63], 0\n"
"s_add_u32 m0, s30, 0x4000\n"
"s_nop 0\n"
"buffer_load_dwordx4 v37, s[40:43], s91 offen offset:0 lds\n"
"v_xor_b32 v8, 32, v7\n"
"v_mfma_f32_16x16x32_bf16 v[168:171], a[8:11], v[60:63], 0\n"
"s_waitcnt vmcnt(13)\n"
"ds_read_b64_tr_b16 a[16:17], v45 offset:20480\n"
"ds_read_b64_tr_b16 a[18:19], v45 offset:20736\n"
"v_mfma_f32_16x16x32_bf16 v[172:175], a[12:15], v[60:63], 0\n"
"s_mul_i32 s71, s70, 0xa000\n"
"ds_read_b64_tr_b16 a[20:21], v46 offset:20480\n"
"ds_read_b64_tr_b16 a[22:23], v46 offset:20736\n"
"v_mfma_f32_16x16x32_bf16 v[176:179], a[0:3], v[64:67], 0\n"
"s_mul_i32 s81, s70, 0x2400\n"
"ds_read_b64_tr_b16 a[24:25], v45 offset:20992\n"
"ds_read_b64_tr_b16 a[26:27], v45 offset:21248\n"
"v_mfma_f32_16x16x32_bf16 v[180:183], a[4:7], v[64:67], 0\n"
"s_xor_b32 s70, s11, 3\n"
"ds_read_b64_tr_b16 a[28:29], v46 offset:20992\n"
"ds_read_b64_tr_b16 a[30:31], v46 offset:21248\n"
"v_mfma_f32_16x16x32_bf16 v[184:187], a[8:11], v[64:67], 0\n"
"s_mul_i32 s82, s70, 0x2400\n"
"ds_read_b128 v[60:63], v41 offset:8704\n"
"v_add_u32 v9, s71, v91\n"
"v_mfma_f32_16x16x32_bf16 v[188:191], a[12:15], v[64:67], 0\n"
"s_lshl_b32 s68, s10, 2\n"
"buffer_load_dwordx4 v38, s[40:43], s91 offen offset:1024 lds\n"
"ds_read_b128 v[64:67], v41 offset:10752\n"
"v_mfma_f32_16x16x32_bf16 v[192:195], a[0:3], v[68:71], 0\n"
"s_add_u32 s68, s68, s11\n"
"v_add_u32 v3, s71, v21\n"
"v_add_u32 v15, s71, v22\n"
"v_mfma_f32_16x16x32_bf16 v[196:199], a[4:7], v[68:71], 0\n"
"s_mul_i32 s71, s70, 0xa000\n"
"s_lshr_b32 s69, s68, 7\n"
"v_xor_b32 v10, 32, v9\n"
"v_mfma_f32_16x16x32_bf16 v[200:203], a[8:11], v[68:71], 0\n"
"s_lshl_b32 s70, s11, 12\n"
"v_add_u32 v11, s71, v91\n"
"v_add_u32 v4, s71, v21\n"
"v_mfma_f32_16x16x32_bf16 v[204:207], a[12:15], v[68:71], 0\n"
"s_and_b32 s68, s68, 0x7f\n"
"ds_read_b128 v[68:71], v42 offset:8704\n"
"v_add_u32 v16, s71, v22\n"
"v_mfma_f32_16x16x32_bf16 v[208:211], a[0:3], v[72:75], 0\n"
"buffer_load_dwordx4 v39, s[40:43], s91 offen offset:2048 lds\n"
"v_add_u32 v17, s70, v92\n"
"v_mfma_f32_16x16x32_bf16 v[212:215], a[4:7], v[72:75], 0\n"
"s_lshl_b32 s71, s11, 6\n"
"v_xor_b32 v92, v90, v87\n"
"v_xor_b32 v18, 32, v17\n"
"v_mfma_f32_16x16x32_bf16 v[216:219], a[8:11], v[72:75], 0\n"
"s_mul_i32 s70, s27, 0x12000\n"
"v_xor_b32 v19, s71, v17\n"
"v_xor_b32 v20, 32, v19\n"
"v_mfma_f32_16x16x32_bf16 v[220:223], a[12:15], v[72:75], 0\n"
"s_lshl_b32 s71, s11, 5\n"
"ds_read_b128 v[72:75], v42 offset:10752\n"
"v_xor_b32 v12, 32, v11\n"
"v_mfma_f32_16x16x32_bf16 v[224:227], a[0:3], v[76:79], 0\n"
"s_add_u32 s83, s79, s70\n"
"v_xor_b32 v92, s71, v92\n"
"v_lshl_add_u32 v23, v81, 11, v92\n"
"v_mfma_f32_16x16x32_bf16 v[228:231], a[4:7], v[76:79], 0\n"
"s_lshl_b32 s71, s11, 11\n"
"s_lshl_b32 s68, s68, 11\n"
"v_add_u32 v24, 0x3c00, v23\n"
"v_mfma_f32_16x16x32_bf16 v[232:235], a[8:11], v[76:79], 0\n"
"s_add_u32 s70, s70, s71\n"
"s_mov_b32 s85, 0x3e0293ee\n"
"buffer_load_dwordx4 v40, s[40:43], s91 offen offset:3072 lds\n"
"v_mfma_f32_16x16x32_bf16 v[236:239], a[12:15], v[76:79], 0\n"
"s_waitcnt lgkmcnt(2)\n"
"s_add_u32 s79, s79, s70\n"
"ds_read_b128 v[76:79], v43 offset:8704\n"
"v_mfma_f32_16x16x32_bf16 v[240:243], a[0:3], v[48:51], 0\n"
"s_add_u32 s80, s80, s70\n"
"s_add_u32 s81, s81, s70\n"
"s_add_u32 s82, s82, s70\n"
"v_mfma_f32_16x16x32_bf16 v[244:247], a[4:7], v[48:51], 0\n"
"s_and_b32 s70, s69, 4\n"
"s_lshl_b32 s94, s26, 13\n"
"s_mov_b32 s97, 0x5f800000\n"
"v_mfma_f32_16x16x32_bf16 v[248:251], a[8:11], v[48:51], 0\n"
"s_waitcnt vmcnt(12)\n"
"s_add_u32 s69, s69, s70\n"
"ds_read_b64_tr_b16 a[32:33], v45 offset:24576\n"
"v_mfma_f32_16x16x32_bf16 v[252:255], a[12:15], v[48:51], 0\n"
"s_lshl_b32 s69, s69, 19\n"
"s_add_u32 s77, s91, 0x40000\n"
"s_add_u32 m0, s30, 0x5000\n"
"s_nop 0\n"
"buffer_load_dwordx4 v37, s[40:43], s77 offen offset:0 lds\n"
"ds_read_b128 v[48:51], v43 offset:10752\n"
"v_mfma_f32_16x16x32_bf16 v[112:115], a[16:19], v[52:55], v[112:115]\n"
"s_add_u32 s68, s68, s69\n"
"s_lshl_b32 s70, s28, 3\n"
"ds_read_b64_tr_b16 a[34:35], v45 offset:24832\n"
"v_mfma_f32_16x16x32_bf16 v[116:119], a[20:23], v[52:55], v[116:119]\n"
"s_lshl_b32 s69, s29, 8\n"
"s_add_u32 s94, s94, 0x900000\n"
"ds_read_b64_tr_b16 a[36:37], v46 offset:24576\n"
"v_mfma_f32_16x16x32_bf16 v[120:123], a[24:27], v[52:55], v[120:123]\n"
"s_max_u32 s68, s68, s69\n"
"s_add_u32 s83, s83, 0x2000\n"
"ds_read_b64_tr_b16 a[38:39], v46 offset:24832\n"
"v_mfma_f32_16x16x32_bf16 v[124:127], a[28:31], v[52:55], v[124:127]\n"
"s_sub_u32 s31, s68, s69\n"
"s_mov_b32 s95, 0\n"
"ds_read_b128 v[52:55], v44 offset:512\n"
"v_mfma_f32_16x16x32_bf16 v[128:131], a[16:19], v[56:59], v[128:131]\n"
"s_lshr_b32 s68, s10, 3\n"
"buffer_load_dwordx4 v38, s[40:43], s77 offen offset:1024 lds\n"
"ds_read_b64_tr_b16 a[40:41], v45 offset:25088\n"
"v_mfma_f32_16x16x32_bf16 v[132:135], a[20:23], v[56:59], v[132:135]\n"
"s_lshl_b32 s68, s68, 2\n"
"ds_read_b64_tr_b16 a[42:43], v45 offset:25344\n"
"v_mfma_f32_16x16x32_bf16 v[136:139], a[24:27], v[56:59], v[136:139]\n"
"s_add_u32 s68, s68, s11\n"
"ds_read_b64_tr_b16 a[44:45], v46 offset:25088\n"
"v_mfma_f32_16x16x32_bf16 v[140:143], a[28:31], v[56:59], v[140:143]\n"
"s_lshr_b32 s69, s68, 4\n"
"ds_read_b64_tr_b16 a[46:47], v46 offset:25344\n"
"v_mfma_f32_16x16x32_bf16 v[144:147], a[16:19], v[60:63], v[144:147]\n"
"s_add_u32 s69, s69, s70\n"
"s_and_b32 s68, s68, 15\n"
"v_mfma_f32_16x16x32_bf16 v[148:151], a[20:23], v[60:63], v[148:151]\n"
"s_add_u32 s89, s72, 2\n"
"s_lshl_b32 s70, s11, 12\n"
"s_lshl_b32 s69, s69, 19\n"
"v_mfma_f32_16x16x32_bf16 v[152:155], a[24:27], v[60:63], v[152:155]\n"
"s_and_b32 s89, s89, 15\n"
"s_add_u32 s70, s70, 0x4000\n"
"buffer_load_dwordx4 v39, s[40:43], s77 offen offset:2048 lds\n"
"v_mfma_f32_16x16x32_bf16 v[156:159], a[28:31], v[60:63], v[156:159]\n"
"s_lshl_b32 s89, s89, 7\n"
"v_add_u32 v28, s70, v22\n"
"v_add_u32 v29, 0x14000, v28\n"
"v_mfma_f32_16x16x32_bf16 v[160:163], a[16:19], v[64:67], v[160:163]\n"
"s_lshl_b32 s68, s68, 14\n"
"v_mfma_f32_16x16x32_bf16 v[164:167], a[20:23], v[64:67], v[164:167]\n"
"s_add_u32 s68, s68, s69\n"
"v_mfma_f32_16x16x32_bf16 v[168:171], a[24:27], v[64:67], v[168:171]\n"
"s_sub_u32 s69, 0x180, s20\n"
"v_mfma_f32_16x16x32_bf16 v[172:175], a[28:31], v[64:67], v[172:175]\n"
"s_waitcnt lgkmcnt(4)\n"
"s_memrealtime s[14:15]\n"
"buffer_load_dwordx4 v40, s[40:43], s77 offen offset:3072 lds\n"
"v_mfma_f32_16x16x32_bf16 v[176:179], a[16:19], v[68:71], v[176:179]\n"
"s_memtime s[86:87]\n"
"s_and_b32 s69, s69, 0x380\n"
"v_mfma_f32_16x16x32_bf16 v[180:183], a[20:23], v[68:71], v[180:183]\n"
"s_add_u32 s68, s68, s69\n"
"v_mfma_f32_16x16x32_bf16 v[184:187], a[24:27], v[68:71], v[184:187]\n"
"s_and_b32 s69, s29, 3\n"
"v_mfma_f32_16x16x32_bf16 v[188:191], a[28:31], v[68:71], v[188:191]\n"
"s_lshl_b32 s69, s69, 8\n"
"v_mfma_f32_16x16x32_bf16 v[192:195], a[16:19], v[72:75], v[192:195]\n"
"s_max_u32 s68, s68, s69\n"
"v_mfma_f32_16x16x32_bf16 v[196:199], a[20:23], v[72:75], v[196:199]\n"
"s_waitcnt vmcnt(12)\n"
"s_barrier\n"
"s_add_u32 m0, s30, 0x3000\n"
"s_nop 0\n"
"buffer_load_dwordx4 v32, s[36:39], s89 offen offset:0 lds\n"
"v_mfma_f32_16x16x32_bf16 v[200:203], a[24:27], v[72:75], v[200:203]\n"
"s_sub_u32 s3, s68, s69\n"
"ds_read_b128 v[56:59], v27 offset:32768\n"
"ds_read_b128 v[60:63], v27 offset:33280\n"
"v_mfma_f32_16x16x32_bf16 v[204:207], a[28:31], v[72:75], v[204:207]\n"
"s_waitcnt vmcnt(9)\n"
"ds_read_b64_tr_b16 a[48:49], v45 offset:28672\n"
"ds_read_b64_tr_b16 a[50:51], v45 offset:28928\n"
"v_mfma_f32_16x16x32_bf16 v[208:211], a[16:19], v[76:79], v[208:211]\n"
"s_waitcnt lgkmcnt(0)\n"
"s_mul_i32 s14, s14, 0xa4\n"
"ds_read_b64_tr_b16 a[52:53], v46 offset:28672\n"
"ds_read_b64_tr_b16 a[54:55], v46 offset:28928\n"
"v_mfma_f32_16x16x32_bf16 v[212:215], a[20:23], v[76:79], v[212:215]\n"
"s_mul_i32 s86, s86, 10\n"
"ds_read_b64_tr_b16 a[56:57], v45 offset:29184\n"
"ds_read_b64_tr_b16 a[58:59], v45 offset:29440\n"
"v_mfma_f32_16x16x32_bf16 v[216:219], a[24:27], v[76:79], v[216:219]\n"
"s_sub_u32 s92, s86, s14\n"
"buffer_load_dwordx4 v33, s[36:39], s89 offen offset:1024 lds\n"
"ds_read_b64_tr_b16 a[60:61], v46 offset:29184\n"
"v_mfma_f32_16x16x32_bf16 v[220:223], a[28:31], v[76:79], v[220:223]\n"
"s_mov_b32 s70, s27\n"
"ds_read_b64_tr_b16 a[62:63], v46 offset:29440\n"
"ds_read_b128 v[64:67], v27 offset:34816\n"
"v_mfma_f32_16x16x32_bf16 v[224:227], a[16:19], v[48:51], v[224:227]\n"
"s_lshl_b32 s70, s70, 3\n"
"s_and_b32 s71, s27, 8\n"
"ds_read_b128 v[68:71], v27 offset:35328\n"
"v_mfma_f32_16x16x32_bf16 v[228:231], a[20:23], v[48:51], v[228:231]\n"
"s_add_u32 s70, s70, s11\n"
"s_lshl_b32 s68, s28, 3\n"
"ds_read_b128 v[72:75], v41 offset:32768\n"
"v_mfma_f32_16x16x32_bf16 v[232:235], a[24:27], v[48:51], v[232:235]\n"
"s_mul_i32 s12, s70, 0x2400\n"
"s_lshl_b32 s13, s70, 16\n"
"ds_read_b128 v[76:79], v41 offset:33280\n"
"v_mfma_f32_16x16x32_bf16 v[236:239], a[28:31], v[48:51], v[236:239]\n"
"s_and_b32 s70, s27, 7\n"
"buffer_load_dwordx4 v34, s[36:39], s89 offen offset:2048 lds\n"
"ds_read_b128 v[48:51], v41 offset:34816\n"
"v_mfma_f32_16x16x32_bf16 v[240:243], a[16:19], v[52:55], v[240:243]\n"
"s_sub_u32 s70, s70, 1\n"
"s_add_u32 s68, s68, s11\n"
"s_and_b32 s69, s10, 0\n"
"v_mfma_f32_16x16x32_bf16 v[244:247], a[20:23], v[52:55], v[244:247]\n"
"s_lshr_b32 s70, s70, 31\n"
"s_lshl_b32 s68, s68, 3\n"
"v_mfma_f32_16x16x32_bf16 v[248:251], a[24:27], v[52:55], v[248:251]\n"
"s_add_u32 s70, s70, s71\n"
"s_lshl_b32 s68, s28, 7\n"
"v_mfma_f32_16x16x32_bf16 v[252:255], a[28:31], v[52:55], v[252:255]\n"
"s_add_u32 s70, s70, 0\n"
"ds_read_b128 v[52:55], v41 offset:35328\n"
"v_add_u32 v23, s68, v23\n"
"v_mfma_f32_16x16x32_bf16 v[112:115], a[32:35], v[56:59], v[112:115]\n"
"s_add_u32 s74, s72, 3\n"
"s_lshl_b32 s70, s70, 3\n"
"v_add_u32 v24, s68, v24\n"
"v_mfma_f32_16x16x32_bf16 v[112:115], a[48:51], v[60:63], v[112:115]\n"
"s_and_b32 s74, s74, 15\n"
"s_add_u32 s70, s70, s11\n"
"buffer_load_dwordx4 v35, s[36:39], s89 offen offset:3072 lds\n"
"v_mfma_f32_16x16x32_bf16 v[116:119], a[36:39], v[56:59], v[116:119]\n"
"s_waitcnt lgkmcnt(5)\n"
"s_lshl_b32 s74, s74, 19\n"
"s_and_b32 s71, s27, 8\n"
"v_mfma_f32_16x16x32_bf16 v[116:119], a[52:55], v[60:63], v[116:119]\n"
"s_lshl_b32 s68, s26, 4\n"
"v_mfma_f32_16x16x32_bf16 v[120:123], a[40:43], v[56:59], v[120:123]\n"
"s_add_u32 s68, s68, s27\n"
"v_mfma_f32_16x16x32_bf16 v[120:123], a[56:59], v[60:63], v[120:123]\n"
"s_lshl_b32 s68, s68, 2\n"
"s_add_u32 m0, s30, 0x9400\n"
"v_mfma_f32_16x16x32_bf16 v[124:127], a[44:47], v[56:59], v[124:127]\n"
"s_add_u32 s68, s68, s11\n"
"buffer_load_dwordx4 v36, s[36:39], s89 offen offset:0 lds\n"
"ds_read_b128 v[56:59], v42 offset:32768\n"
"v_mfma_f32_16x16x32_bf16 v[124:127], a[60:63], v[60:63], v[124:127]\n"
"ds_read_b128 v[60:63], v42 offset:33280\n"
"v_mfma_f32_16x16x32_bf16 v[128:131], a[32:35], v[64:67], v[128:131]\n"
"s_waitcnt vmcnt(9) lgkmcnt(3)\n"
"ds_read_b64_tr_b16 a[0:1], v45 offset:16384\n"
"v_mfma_f32_16x16x32_bf16 v[128:131], a[48:51], v[68:71], v[128:131]\n"
"ds_read_b64_tr_b16 a[2:3], v45 offset:16640\n"
"v_mfma_f32_16x16x32_bf16 v[132:135], a[36:39], v[64:67], v[132:135]\n"
"ds_read_b64_tr_b16 a[4:5], v46 offset:16384\n"
"v_mfma_f32_16x16x32_bf16 v[132:135], a[52:55], v[68:71], v[132:135]\n"
"s_add_u32 m0, s30, 0x6000\n"
"ds_read_b64_tr_b16 a[6:7], v46 offset:16640\n"
"v_mfma_f32_16x16x32_bf16 v[136:139], a[40:43], v[64:67], v[136:139]\n"
"buffer_load_dwordx4 v37, s[40:43], s74 offen offset:0 lds\n"
"ds_read_b64_tr_b16 a[8:9], v45 offset:16896\n"
"v_mfma_f32_16x16x32_bf16 v[136:139], a[56:59], v[68:71], v[136:139]\n"
"ds_read_b64_tr_b16 a[10:11], v45 offset:17152\n"
"v_mfma_f32_16x16x32_bf16 v[140:143], a[44:47], v[64:67], v[140:143]\n"
"ds_read_b128 v[64:67], v42 offset:34816\n"
"v_mfma_f32_16x16x32_bf16 v[140:143], a[60:63], v[68:71], v[140:143]\n"
"ds_read_b128 v[68:71], v42 offset:35328\n"
"v_mfma_f32_16x16x32_bf16 v[144:147], a[32:35], v[72:75], v[144:147]\n"
"ds_read_b64_tr_b16 a[12:13], v46 offset:16896\n"
"v_mfma_f32_16x16x32_bf16 v[144:147], a[48:51], v[76:79], v[144:147]\n"
"buffer_load_dwordx4 v38, s[40:43], s74 offen offset:1024 lds\n"
"ds_read_b64_tr_b16 a[14:15], v46 offset:17152\n"
"v_mfma_f32_16x16x32_bf16 v[148:151], a[36:39], v[72:75], v[148:151]\n"
"v_mfma_f32_16x16x32_bf16 v[148:151], a[52:55], v[76:79], v[148:151]\n"
"v_mfma_f32_16x16x32_bf16 v[152:155], a[40:43], v[72:75], v[152:155]\n"
"v_mfma_f32_16x16x32_bf16 v[152:155], a[56:59], v[76:79], v[152:155]\n"
"v_mfma_f32_16x16x32_bf16 v[156:159], a[44:47], v[72:75], v[156:159]\n"
"ds_read_b128 v[72:75], v43 offset:32768\n"
"v_mfma_f32_16x16x32_bf16 v[156:159], a[60:63], v[76:79], v[156:159]\n"
"buffer_load_dwordx4 v39, s[40:43], s74 offen offset:2048 lds\n"
"ds_read_b128 v[76:79], v43 offset:33280\n"
"v_mfma_f32_16x16x32_bf16 v[160:163], a[32:35], v[48:51], v[160:163]\n"
"s_waitcnt lgkmcnt(6)\n"
"v_mfma_f32_16x16x32_bf16 v[160:163], a[48:51], v[52:55], v[160:163]\n"
"v_mfma_f32_16x16x32_bf16 v[164:167], a[36:39], v[48:51], v[164:167]\n"
"v_mfma_f32_16x16x32_bf16 v[164:167], a[52:55], v[52:55], v[164:167]\n"
"v_mfma_f32_16x16x32_bf16 v[168:171], a[40:43], v[48:51], v[168:171]\n"
"buffer_load_dwordx4 v40, s[40:43], s74 offen offset:3072 lds\n"
"v_mfma_f32_16x16x32_bf16 v[168:171], a[56:59], v[52:55], v[168:171]\n"
"s_waitcnt vmcnt(9)\n"
"ds_read_b64_tr_b16 a[16:17], v45 offset:20480\n"
"v_mfma_f32_16x16x32_bf16 v[172:175], a[44:47], v[48:51], v[172:175]\n"
"ds_read_b128 v[48:51], v43 offset:34816\n"
"v_mfma_f32_16x16x32_bf16 v[172:175], a[60:63], v[52:55], v[172:175]\n"
"ds_read_b128 v[52:55], v43 offset:35328\n"
"v_mfma_f32_16x16x32_bf16 v[176:179], a[32:35], v[56:59], v[176:179]\n"
"s_add_u32 s78, s74, 0x40000\n"
"s_add_u32 m0, s30, 0x7000\n"
"ds_read_b64_tr_b16 a[18:19], v45 offset:20736\n"
"v_mfma_f32_16x16x32_bf16 v[176:179], a[48:51], v[60:63], v[176:179]\n"
"buffer_load_dwordx4 v37, s[40:43], s78 offen offset:0 lds\n"
"ds_read_b64_tr_b16 a[20:21], v46 offset:20480\n"
"v_mfma_f32_16x16x32_bf16 v[180:183], a[36:39], v[56:59], v[180:183]\n"
"ds_read_b64_tr_b16 a[22:23], v46 offset:20736\n"
"v_mfma_f32_16x16x32_bf16 v[180:183], a[52:55], v[60:63], v[180:183]\n"
"ds_read_b64_tr_b16 a[24:25], v45 offset:20992\n"
"v_mfma_f32_16x16x32_bf16 v[184:187], a[40:43], v[56:59], v[184:187]\n"
"ds_read_b64_tr_b16 a[26:27], v45 offset:21248\n"
"v_mfma_f32_16x16x32_bf16 v[184:187], a[56:59], v[60:63], v[184:187]\n"
"s_getpc_b64 s[98:99]\n"
"7:\n"
"s_add_u32 s98, s98, .Lmha_VAR1-7b\n"
"s_addc_u32 s99, s99, (.Lmha_VAR1-7b)>>32\n"
"ds_read_b64_tr_b16 a[28:29], v46 offset:20992\n"
"v_mfma_f32_16x16x32_bf16 v[188:191], a[44:47], v[56:59], v[188:191]\n"
"ds_read_b128 v[56:59], v30 offset:0\n"
"v_mfma_f32_16x16x32_bf16 v[188:191], a[60:63], v[60:63], v[188:191]\n"
"s_waitcnt lgkmcnt(10)\n"
"buffer_load_dwordx4 v38, s[40:43], s78 offen offset:1024 lds\n"
"ds_read_b128 v[60:63], v30 offset:512\n"
"v_mfma_f32_16x16x32_bf16 v[192:195], a[32:35], v[64:67], v[192:195]\n"
"global_load_dword v26, v0, s[98:99] offset:0\n"
"ds_read_b64_tr_b16 a[30:31], v46 offset:21248\n"
"v_mfma_f32_16x16x32_bf16 v[192:195], a[48:51], v[68:71], v[192:195]\n"
"v_mfma_f32_16x16x32_bf16 v[196:199], a[36:39], v[64:67], v[196:199]\n"
"v_mfma_f32_16x16x32_bf16 v[196:199], a[52:55], v[68:71], v[196:199]\n"
"s_add_u32 s73, s72, 3\n"
"v_mfma_f32_16x16x32_bf16 v[200:203], a[40:43], v[64:67], v[200:203]\n"
"s_and_b32 s73, s73, 15\n"
"buffer_load_dwordx4 v39, s[40:43], s78 offen offset:2048 lds\n"
"v_mfma_f32_16x16x32_bf16 v[200:203], a[56:59], v[68:71], v[200:203]\n"
"s_lshl_b32 s73, s73, 7\n"
"v_mfma_f32_16x16x32_bf16 v[204:207], a[44:47], v[64:67], v[204:207]\n"
"v_mfma_f32_16x16x32_bf16 v[204:207], a[60:63], v[68:71], v[204:207]\n"
"v_mfma_f32_16x16x32_bf16 v[208:211], a[32:35], v[72:75], v[208:211]\n"
"v_mfma_f32_16x16x32_bf16 v[208:211], a[48:51], v[76:79], v[208:211]\n"
"s_getpc_b64 s[98:99]\n"
"7:\n"
"s_add_u32 s98, s98, .Lmha_VAR2-7b\n"
"s_addc_u32 s99, s99, (.Lmha_VAR2-7b)>>32\n"
"v_mfma_f32_16x16x32_bf16 v[212:215], a[36:39], v[72:75], v[212:215]\n"
"buffer_load_dwordx4 v40, s[40:43], s78 offen offset:3072 lds\n"
"v_mfma_f32_16x16x32_bf16 v[212:215], a[52:55], v[76:79], v[212:215]\n"
"v_mfma_f32_16x16x32_bf16 v[216:219], a[40:43], v[72:75], v[216:219]\n"
"global_load_dword v26, v0, s[98:99] offset:0\n"
"v_mfma_f32_16x16x32_bf16 v[216:219], a[56:59], v[76:79], v[216:219]\n"
"v_mfma_f32_16x16x32_bf16 v[220:223], a[44:47], v[72:75], v[220:223]\n"
"s_add_u32 m0, s30, 0x2000\n"
"v_mfma_f32_16x16x32_bf16 v[220:223], a[60:63], v[76:79], v[220:223]\n"
"s_waitcnt lgkmcnt(0)\n"
"buffer_load_dwordx4 v32, s[36:39], s73 offen offset:0 lds\n"
"v_mfma_f32_16x16x32_bf16 v[224:227], a[32:35], v[48:51], v[224:227]\n"
"v_mfma_f32_16x16x32_bf16 v[224:227], a[48:51], v[52:55], v[224:227]\n"
"s_waitcnt vmcnt(11)\n"
"s_barrier\n"
"v_mfma_f32_16x16x32_bf16 v[228:231], a[36:39], v[48:51], v[228:231]\n"
"ds_read_b128 v[64:67], v27 offset:12288\n"
"ds_read_b128 v[68:71], v27 offset:12800\n"
"v_mfma_f32_16x16x32_bf16 v[228:231], a[52:55], v[52:55], v[228:231]\n"
"ds_read_b128 v[72:75], v27 offset:14336\n"
"ds_read_b128 v[76:79], v27 offset:14848\n"
"v_mfma_f32_16x16x32_bf16 v[232:235], a[40:43], v[48:51], v[232:235]\n"
"buffer_load_dwordx4 v33, s[36:39], s73 offen offset:1024 lds\n"
"v_mfma_f32_16x16x32_bf16 v[232:235], a[56:59], v[52:55], v[232:235]\n"
"s_getpc_b64 s[98:99]\n"
"7:\n"
"s_add_u32 s98, s98, .Lmha_VAR3-7b\n"
"s_addc_u32 s99, s99, (.Lmha_VAR3-7b)>>32\n"
"v_mfma_f32_16x16x32_bf16 v[236:239], a[44:47], v[48:51], v[236:239]\n"
"ds_read_b128 v[48:51], v41 offset:12288\n"
"v_mfma_f32_16x16x32_bf16 v[236:239], a[60:63], v[52:55], v[236:239]\n"
"ds_read_b128 v[52:55], v41 offset:12800\n"
"v_mfma_f32_16x16x32_bf16 v[240:243], a[32:35], v[56:59], v[240:243]\n"
"global_load_dword v26, v0, s[98:99] offset:0\n"
"v_mfma_f32_16x16x32_bf16 v[240:243], a[48:51], v[60:63], v[240:243]\n"
"s_add_u32 s90, s72, 4\n"
"v_mfma_f32_16x16x32_bf16 v[244:247], a[36:39], v[56:59], v[244:247]\n"
"s_and_b32 s90, s90, 15\n"
"buffer_load_dwordx4 v34, s[36:39], s73 offen offset:2048 lds\n"
"v_mfma_f32_16x16x32_bf16 v[244:247], a[52:55], v[60:63], v[244:247]\n"
"s_lshl_b32 s90, s90, 19\n"
"v_mfma_f32_16x16x32_bf16 v[248:251], a[40:43], v[56:59], v[248:251]\n"
"v_mfma_f32_16x16x32_bf16 v[248:251], a[56:59], v[60:63], v[248:251]\n"
"v_mfma_f32_16x16x32_bf16 v[252:255], a[44:47], v[56:59], v[252:255]\n"
"ds_read_b128 v[56:59], v41 offset:14336\n"
"v_mfma_f32_16x16x32_bf16 v[252:255], a[60:63], v[60:63], v[252:255]\n"
"s_waitcnt lgkmcnt(3)\n"
"buffer_load_dwordx4 v35, s[36:39], s73 offen offset:3072 lds\n"
"ds_read_b128 v[60:63], v41 offset:14848\n"
"v_mfma_f32_16x16x32_bf16 v[112:115], a[0:3], v[64:67], v[112:115]\n"
"v_mfma_f32_16x16x32_bf16 v[112:115], a[16:19], v[68:71], v[112:115]\n"
"v_mfma_f32_16x16x32_bf16 v[116:119], a[4:7], v[64:67], v[116:119]\n"
"v_mfma_f32_16x16x32_bf16 v[116:119], a[20:23], v[68:71], v[116:119]\n"
"v_mfma_f32_16x16x32_bf16 v[120:123], a[8:11], v[64:67], v[120:123]\n"
"s_add_u32 m0, s30, 0x4000\n"
"v_mfma_f32_16x16x32_bf16 v[120:123], a[24:27], v[68:71], v[120:123]\n"
"s_waitcnt vmcnt(11)\n"
"buffer_load_dwordx4 v37, s[40:43], s90 offen offset:0 lds\n"
"ds_read_b64_tr_b16 a[32:33], v45 offset:24576\n"
"v_mfma_f32_16x16x32_bf16 v[124:127], a[12:15], v[64:67], v[124:127]\n"
"ds_read_b128 v[64:67], v42 offset:12288\n"
"v_mfma_f32_16x16x32_bf16 v[124:127], a[28:31], v[68:71], v[124:127]\n"
"ds_read_b128 v[68:71], v42 offset:12800\n"
"v_mfma_f32_16x16x32_bf16 v[128:131], a[0:3], v[72:75], v[128:131]\n"
"ds_read_b64_tr_b16 a[34:35], v45 offset:24832\n"
"v_mfma_f32_16x16x32_bf16 v[128:131], a[16:19], v[76:79], v[128:131]\n"
"ds_read_b64_tr_b16 a[36:37], v46 offset:24576\n"
"v_mfma_f32_16x16x32_bf16 v[132:135], a[4:7], v[72:75], v[132:135]\n"
"buffer_load_dwordx4 v38, s[40:43], s90 offen offset:1024 lds\n"
"ds_read_b64_tr_b16 a[38:39], v46 offset:24832\n"
"v_mfma_f32_16x16x32_bf16 v[132:135], a[20:23], v[76:79], v[132:135]\n"
"ds_read_b64_tr_b16 a[40:41], v45 offset:25088\n"
"v_mfma_f32_16x16x32_bf16 v[136:139], a[8:11], v[72:75], v[136:139]\n"
"ds_read_b64_tr_b16 a[42:43], v45 offset:25344\n"
"v_mfma_f32_16x16x32_bf16 v[136:139], a[24:27], v[76:79], v[136:139]\n"
"ds_read_b64_tr_b16 a[44:45], v46 offset:25088\n"
"v_mfma_f32_16x16x32_bf16 v[140:143], a[12:15], v[72:75], v[140:143]\n"
"ds_read_b128 v[72:75], v42 offset:14336\n"
"v_mfma_f32_16x16x32_bf16 v[140:143], a[28:31], v[76:79], v[140:143]\n"
"s_waitcnt lgkmcnt(10)\n"
"buffer_load_dwordx4 v39, s[40:43], s90 offen offset:2048 lds\n"
"ds_read_b128 v[76:79], v42 offset:14848\n"
"v_mfma_f32_16x16x32_bf16 v[144:147], a[0:3], v[48:51], v[144:147]\n"
"ds_read_b64_tr_b16 a[46:47], v46 offset:25344\n"
"v_mfma_f32_16x16x32_bf16 v[144:147], a[16:19], v[52:55], v[144:147]\n"
"v_mfma_f32_16x16x32_bf16 v[148:151], a[4:7], v[48:51], v[148:151]\n"
"v_mfma_f32_16x16x32_bf16 v[148:151], a[20:23], v[52:55], v[148:151]\n"
"v_mfma_f32_16x16x32_bf16 v[152:155], a[8:11], v[48:51], v[152:155]\n"
"v_mfma_f32_16x16x32_bf16 v[152:155], a[24:27], v[52:55], v[152:155]\n"
"buffer_load_dwordx4 v40, s[40:43], s90 offen offset:3072 lds\n"
"v_mfma_f32_16x16x32_bf16 v[156:159], a[12:15], v[48:51], v[156:159]\n"
"ds_read_b128 v[48:51], v43 offset:12288\n"
"v_mfma_f32_16x16x32_bf16 v[156:159], a[28:31], v[52:55], v[156:159]\n"
"ds_read_b128 v[52:55], v43 offset:12800\n"
"v_mfma_f32_16x16x32_bf16 v[160:163], a[0:3], v[56:59], v[160:163]\n"
"v_mfma_f32_16x16x32_bf16 v[160:163], a[16:19], v[60:63], v[160:163]\n"
"s_add_u32 s84, s90, 0x40000\n"
"s_add_u32 m0, s30, 0x5000\n"
"v_mfma_f32_16x16x32_bf16 v[164:167], a[4:7], v[56:59], v[164:167]\n"
"buffer_load_dwordx4 v37, s[40:43], s84 offen offset:0 lds\n"
"v_mfma_f32_16x16x32_bf16 v[164:167], a[20:23], v[60:63], v[164:167]\n"
"v_mfma_f32_16x16x32_bf16 v[168:171], a[8:11], v[56:59], v[168:171]\n"
"s_waitcnt vmcnt(11)\n"
"ds_read_b64_tr_b16 a[48:49], v45 offset:28672\n"
"v_mfma_f32_16x16x32_bf16 v[168:171], a[24:27], v[60:63], v[168:171]\n"
"ds_read_b64_tr_b16 a[50:51], v45 offset:28928\n"
"v_mfma_f32_16x16x32_bf16 v[172:175], a[12:15], v[56:59], v[172:175]\n"
"ds_read_b128 v[56:59], v43 offset:14336\n"
"v_mfma_f32_16x16x32_bf16 v[172:175], a[28:31], v[60:63], v[172:175]\n"
"s_waitcnt lgkmcnt(5)\n"
"ds_read_b128 v[60:63], v43 offset:14848\n"
"v_mfma_f32_16x16x32_bf16 v[176:179], a[0:3], v[64:67], v[176:179]\n"
"buffer_load_dwordx4 v38, s[40:43], s84 offen offset:1024 lds\n"
"ds_read_b64_tr_b16 a[52:53], v46 offset:28672\n"
"v_mfma_f32_16x16x32_bf16 v[176:179], a[16:19], v[68:71], v[176:179]\n"
"ds_read_b64_tr_b16 a[54:55], v46 offset:28928\n"
"v_mfma_f32_16x16x32_bf16 v[180:183], a[4:7], v[64:67], v[180:183]\n"
"ds_read_b64_tr_b16 a[56:57], v45 offset:29184\n"
"v_mfma_f32_16x16x32_bf16 v[180:183], a[20:23], v[68:71], v[180:183]\n"
"ds_read_b64_tr_b16 a[58:59], v45 offset:29440\n"
"v_mfma_f32_16x16x32_bf16 v[184:187], a[8:11], v[64:67], v[184:187]\n"
"s_add_u32 s88, s72, 4\n"
"ds_read_b64_tr_b16 a[60:61], v46 offset:29184\n"
"v_mfma_f32_16x16x32_bf16 v[184:187], a[24:27], v[68:71], v[184:187]\n"
"s_and_b32 s88, s88, 15\n"
"buffer_load_dwordx4 v39, s[40:43], s84 offen offset:2048 lds\n"
"ds_read_b64_tr_b16 a[62:63], v46 offset:29440\n"
"v_mfma_f32_16x16x32_bf16 v[188:191], a[12:15], v[64:67], v[188:191]\n"
"s_lshl_b32 s88, s88, 7\n"
"ds_read_b128 v[64:67], v44 offset:1024\n"
"v_mfma_f32_16x16x32_bf16 v[188:191], a[28:31], v[68:71], v[188:191]\n"
"ds_read_b128 v[68:71], v44 offset:1536\n"
"v_mfma_f32_16x16x32_bf16 v[192:195], a[0:3], v[72:75], v[192:195]\n"
"v_mfma_f32_16x16x32_bf16 v[192:195], a[16:19], v[76:79], v[192:195]\n"
"v_mfma_f32_16x16x32_bf16 v[196:199], a[4:7], v[72:75], v[196:199]\n"
"buffer_load_dwordx4 v40, s[40:43], s84 offen offset:3072 lds\n"
"v_mfma_f32_16x16x32_bf16 v[196:199], a[20:23], v[76:79], v[196:199]\n"
"v_mfma_f32_16x16x32_bf16 v[200:203], a[8:11], v[72:75], v[200:203]\n"
"v_mfma_f32_16x16x32_bf16 v[200:203], a[24:27], v[76:79], v[200:203]\n"
"v_mfma_f32_16x16x32_bf16 v[204:207], a[12:15], v[72:75], v[204:207]\n"
"v_mfma_f32_16x16x32_bf16 v[204:207], a[28:31], v[76:79], v[204:207]\n"
"s_waitcnt lgkmcnt(3)\n"
"s_add_u32 m0, s30, 0x8000\n"
"v_mfma_f32_16x16x32_bf16 v[208:211], a[0:3], v[48:51], v[208:211]\n"
"buffer_load_dwordx4 v32, s[36:39], s88 offen offset:0 lds\n"
"v_mfma_f32_16x16x32_bf16 v[208:211], a[16:19], v[52:55], v[208:211]\n"
"v_mfma_f32_16x16x32_bf16 v[212:215], a[4:7], v[48:51], v[212:215]\n"
"v_mfma_f32_16x16x32_bf16 v[212:215], a[20:23], v[52:55], v[212:215]\n"
"s_memrealtime s[14:15]\n"
"v_mfma_f32_16x16x32_bf16 v[216:219], a[8:11], v[48:51], v[216:219]\n"
"s_memtime s[86:87]\n"
"v_mfma_f32_16x16x32_bf16 v[216:219], a[24:27], v[52:55], v[216:219]\n"
"buffer_load_dwordx4 v33, s[36:39], s88 offen offset:1024 lds\n"
"v_mfma_f32_16x16x32_bf16 v[220:223], a[12:15], v[48:51], v[220:223]\n"
"v_mfma_f32_16x16x32_bf16 v[220:223], a[28:31], v[52:55], v[220:223]\n"
"v_mfma_f32_16x16x32_bf16 v[224:227], a[0:3], v[56:59], v[224:227]\n"
"s_waitcnt lgkmcnt(0)\n"
"s_lshl_b32 s15, s70, 16\n"
"v_mfma_f32_16x16x32_bf16 v[224:227], a[16:19], v[60:63], v[224:227]\n"
"s_waitcnt vmcnt(10)\n"
"s_barrier\n"
"v_mfma_f32_16x16x32_bf16 v[228:231], a[4:7], v[56:59], v[228:231]\n"
"s_add_u32 s91, s72, 5\n"
"ds_read_b128 v[72:75], v27 offset:8192\n"
"ds_read_b128 v[76:79], v27 offset:8704\n"
"v_mfma_f32_16x16x32_bf16 v[228:231], a[20:23], v[60:63], v[228:231]\n"
"s_and_b32 s91, s91, 15\n"
"buffer_load_dwordx4 v34, s[36:39], s88 offen offset:2048 lds\n"
"ds_read_b128 v[48:51], v27 offset:10240\n"
"v_mfma_f32_16x16x32_bf16 v[232:235], a[8:11], v[56:59], v[232:235]\n"
"s_lshl_b32 s91, s91, 19\n"
"s_mul_i32 s14, s14, 0xa4\n"
"ds_read_b128 v[52:55], v27 offset:10752\n"
"v_mfma_f32_16x16x32_bf16 v[232:235], a[24:27], v[60:63], v[232:235]\n"
"s_mul_i32 s86, s86, 10\n"
"v_mfma_f32_16x16x32_bf16 v[236:239], a[12:15], v[56:59], v[236:239]\n"
"s_sub_u32 s86, s86, s14\n"
"buffer_load_dwordx4 v35, s[36:39], s88 offen offset:3072 lds\n"
"ds_read_b128 v[56:59], v41 offset:8192\n"
"v_mfma_f32_16x16x32_bf16 v[236:239], a[28:31], v[60:63], v[236:239]\n"
"s_sub_u32 s86, s86, s92\n"
"s_mul_i32 s14, s70, 0x2400\n"
"ds_read_b128 v[60:63], v41 offset:8704\n"
"v_mfma_f32_16x16x32_bf16 v[240:243], a[0:3], v[64:67], v[240:243]\n"
"s_ashr_i32 s86, s86, 31\n"
"s_and_b32 s70, s27, 7\n"
"v_mfma_f32_16x16x32_bf16 v[240:243], a[16:19], v[68:71], v[240:243]\n"
"s_mul_i32 s92, s86, 14\n"
"s_sub_u32 s70, s70, 2\n"
"s_add_u32 m0, s30, 0x9000\n"
"v_mfma_f32_16x16x32_bf16 v[244:247], a[4:7], v[64:67], v[244:247]\n"
"s_lshr_b32 s70, s70, 31\n"
"buffer_load_dwordx4 v36, s[36:39], s88 offen offset:0 lds\n"
"v_mfma_f32_16x16x32_bf16 v[244:247], a[20:23], v[68:71], v[244:247]\n"
"s_add_u32 s70, s70, s71\n"
"v_mfma_f32_16x16x32_bf16 v[248:251], a[8:11], v[64:67], v[248:251]\n"
"s_add_u32 s70, s70, 1\n"
"s_and_b32 s71, s27, 8\n"
"v_mfma_f32_16x16x32_bf16 v[248:251], a[24:27], v[68:71], v[248:251]\n"
"s_lshl_b32 s70, s70, 3\n"
"s_add_u32 m0, s30, 0x6000\n"
"v_mfma_f32_16x16x32_bf16 v[252:255], a[12:15], v[64:67], v[252:255]\n"
"s_add_u32 s70, s70, s11\n"
"buffer_load_dwordx4 v37, s[40:43], s91 offen offset:0 lds\n"
"ds_read_b128 v[64:67], v41 offset:10240\n"
"v_mfma_f32_16x16x32_bf16 v[252:255], a[28:31], v[68:71], v[252:255]\n"
"s_waitcnt lgkmcnt(3)\n"
"s_mul_i32 s16, s70, 0x2400\n"
"ds_read_b128 v[68:71], v41 offset:10752\n"
"v_mfma_f32_16x16x32_bf16 v[112:115], a[32:35], v[72:75], v[112:115]\n"
"s_waitcnt vmcnt(10)\n"
"s_lshl_b32 s17, s70, 16\n"
"ds_read_b64_tr_b16 a[0:1], v45 offset:16384\n"
"v_mfma_f32_16x16x32_bf16 v[112:115], a[48:51], v[76:79], v[112:115]\n"
"s_and_b32 s70, s27, 7\n"
"ds_read_b64_tr_b16 a[2:3], v45 offset:16640\n"
"v_mfma_f32_16x16x32_bf16 v[116:119], a[36:39], v[72:75], v[116:119]\n"
"s_sub_u32 s70, s70, 3\n"
"buffer_load_dwordx4 v38, s[40:43], s91 offen offset:1024 lds\n"
"ds_read_b64_tr_b16 a[4:5], v46 offset:16384\n"
"v_mfma_f32_16x16x32_bf16 v[116:119], a[52:55], v[76:79], v[116:119]\n"
"s_lshr_b32 s70, s70, 31\n"
"ds_read_b64_tr_b16 a[6:7], v46 offset:16640\n"
"v_mfma_f32_16x16x32_bf16 v[120:123], a[40:43], v[72:75], v[120:123]\n"
"s_add_u32 s70, s70, s71\n"
"ds_read_b64_tr_b16 a[8:9], v45 offset:16896\n"
"v_mfma_f32_16x16x32_bf16 v[120:123], a[56:59], v[76:79], v[120:123]\n"
"s_add_u32 s70, s70, 2\n"
"s_and_b32 s71, s27, 8\n"
"ds_read_b64_tr_b16 a[10:11], v45 offset:17152\n"
"v_mfma_f32_16x16x32_bf16 v[124:127], a[44:47], v[72:75], v[124:127]\n"
"s_lshl_b32 s70, s70, 3\n"
"buffer_load_dwordx4 v39, s[40:43], s91 offen offset:2048 lds\n"
"ds_read_b128 v[72:75], v42 offset:8192\n"
"v_mfma_f32_16x16x32_bf16 v[124:127], a[60:63], v[76:79], v[124:127]\n"
"s_add_u32 s70, s70, s11\n"
"ds_read_b128 v[76:79], v42 offset:8704\n"
"v_mfma_f32_16x16x32_bf16 v[128:131], a[32:35], v[48:51], v[128:131]\n"
"s_mul_i32 s18, s70, 0x2400\n"
"s_lshl_b32 s19, s70, 16\n"
"ds_read_b64_tr_b16 a[12:13], v46 offset:16896\n"
"v_mfma_f32_16x16x32_bf16 v[128:131], a[48:51], v[52:55], v[128:131]\n"
"s_and_b32 s70, s27, 7\n"
"ds_read_b64_tr_b16 a[14:15], v46 offset:17152\n"
"v_mfma_f32_16x16x32_bf16 v[132:135], a[36:39], v[48:51], v[132:135]\n"
"s_sub_u32 s70, s70, 4\n"
"buffer_load_dwordx4 v40, s[40:43], s91 offen offset:3072 lds\n"
"v_mfma_f32_16x16x32_bf16 v[132:135], a[52:55], v[52:55], v[132:135]\n"
"s_lshr_b32 s70, s70, 31\n"
"v_mfma_f32_16x16x32_bf16 v[136:139], a[40:43], v[48:51], v[136:139]\n"
"s_add_u32 s70, s70, s71\n"
"s_add_u32 m0, s30, 0x7000\n"
"v_mfma_f32_16x16x32_bf16 v[136:139], a[56:59], v[52:55], v[136:139]\n"
"s_add_u32 s70, s70, 3\n"
"s_and_b32 s71, s27, 8\n"
"s_add_u32 s96, s91, 0x40000\n"
"v_mfma_f32_16x16x32_bf16 v[140:143], a[44:47], v[48:51], v[140:143]\n"
"s_lshl_b32 s70, s70, 3\n"
"buffer_load_dwordx4 v37, s[40:43], s96 offen offset:0 lds\n"
"ds_read_b128 v[48:51], v42 offset:10240\n"
"v_mfma_f32_16x16x32_bf16 v[140:143], a[60:63], v[52:55], v[140:143]\n"
"s_waitcnt lgkmcnt(6)\n"
"s_add_u32 s70, s70, s11\n"
"ds_read_b128 v[52:55], v42 offset:10752\n"
"v_mfma_f32_16x16x32_bf16 v[144:147], a[32:35], v[56:59], v[144:147]\n"
"s_mul_i32 s20, s70, 0x2400\n"
"s_lshl_b32 s21, s70, 16\n"
"v_mfma_f32_16x16x32_bf16 v[144:147], a[48:51], v[60:63], v[144:147]\n"
"s_and_b32 s70, s27, 7\n"
"v_mfma_f32_16x16x32_bf16 v[148:151], a[36:39], v[56:59], v[148:151]\n"
"s_add_u32 s89, s72, 5\n"
"s_sub_u32 s70, s70, 5\n"
"buffer_load_dwordx4 v38, s[40:43], s96 offen offset:1024 lds\n"
"v_mfma_f32_16x16x32_bf16 v[148:151], a[52:55], v[60:63], v[148:151]\n"
"s_and_b32 s89, s89, 15\n"
"s_lshr_b32 s70, s70, 31\n"
"v_mfma_f32_16x16x32_bf16 v[152:155], a[40:43], v[56:59], v[152:155]\n"
"s_waitcnt vmcnt(11)\n"
"s_lshl_b32 s89, s89, 7\n"
"ds_read_b64_tr_b16 a[16:17], v45 offset:20480\n"
"v_mfma_f32_16x16x32_bf16 v[152:155], a[56:59], v[60:63], v[152:155]\n"
"s_add_u32 s70, s70, s71\n"
"ds_read_b64_tr_b16 a[18:19], v45 offset:20736\n"
"v_mfma_f32_16x16x32_bf16 v[156:159], a[44:47], v[56:59], v[156:159]\n"
"s_add_u32 s70, s70, 4\n"
"buffer_load_dwordx4 v39, s[40:43], s96 offen offset:2048 lds\n"
"ds_read_b128 v[56:59], v43 offset:8192\n"
"v_mfma_f32_16x16x32_bf16 v[156:159], a[60:63], v[60:63], v[156:159]\n"
"s_lshl_b32 s70, s70, 3\n"
"s_and_b32 s71, s27, 8\n"
"ds_read_b128 v[60:63], v43 offset:8704\n"
"v_mfma_f32_16x16x32_bf16 v[160:163], a[32:35], v[64:67], v[160:163]\n"
"s_add_u32 s70, s70, s11\n"
"ds_read_b64_tr_b16 a[20:21], v46 offset:20480\n"
"v_mfma_f32_16x16x32_bf16 v[160:163], a[48:51], v[68:71], v[160:163]\n"
"s_mul_i32 s22, s70, 0x2400\n"
"s_lshl_b32 s23, s70, 16\n"
"ds_read_b64_tr_b16 a[22:23], v46 offset:20736\n"
"v_mfma_f32_16x16x32_bf16 v[164:167], a[36:39], v[64:67], v[164:167]\n"
"s_and_b32 s70, s27, 7\n"
"buffer_load_dwordx4 v40, s[40:43], s96 offen offset:3072 lds\n"
"ds_read_b64_tr_b16 a[24:25], v45 offset:20992\n"
"v_mfma_f32_16x16x32_bf16 v[164:167], a[52:55], v[68:71], v[164:167]\n"
"s_sub_u32 s70, s70, 6\n"
"ds_read_b64_tr_b16 a[26:27], v45 offset:21248\n"
"v_mfma_f32_16x16x32_bf16 v[168:171], a[40:43], v[64:67], v[168:171]\n"
"s_lshr_b32 s70, s70, 31\n"
"ds_read_b64_tr_b16 a[28:29], v46 offset:20992\n"
"v_mfma_f32_16x16x32_bf16 v[168:171], a[56:59], v[68:71], v[168:171]\n"
"s_add_u32 s70, s70, s71\n"
"s_add_u32 m0, s30, 0x3000\n"
"ds_read_b64_tr_b16 a[30:31], v46 offset:21248\n"
"v_mfma_f32_16x16x32_bf16 v[172:175], a[44:47], v[64:67], v[172:175]\n"
"s_add_u32 s70, s70, 5\n"
"buffer_load_dwordx4 v32, s[36:39], s89 offen offset:0 lds\n"
"ds_read_b128 v[64:67], v43 offset:10240\n"
"v_mfma_f32_16x16x32_bf16 v[172:175], a[60:63], v[68:71], v[172:175]\n"
"s_waitcnt lgkmcnt(10)\n"
"s_lshl_b32 s70, s70, 3\n"
"ds_read_b128 v[68:71], v43 offset:10752\n"
"v_mfma_f32_16x16x32_bf16 v[176:179], a[32:35], v[72:75], v[176:179]\n"
"s_add_u32 s70, s70, s11\n"
"s_and_b32 s71, s27, 8\n"
"v_mfma_f32_16x16x32_bf16 v[176:179], a[48:51], v[76:79], v[176:179]\n"
"v_mfma_f32_16x16x32_bf16 v[180:183], a[36:39], v[72:75], v[180:183]\n"
"s_add_u32 s74, s72, 6\n"
"buffer_load_dwordx4 v33, s[36:39], s89 offen offset:1024 lds\n"
"v_mfma_f32_16x16x32_bf16 v[180:183], a[52:55], v[76:79], v[180:183]\n"
"s_and_b32 s74, s74, 15\n"
"v_mfma_f32_16x16x32_bf16 v[184:187], a[40:43], v[72:75], v[184:187]\n"
"s_lshl_b32 s74, s74, 19\n"
"v_mfma_f32_16x16x32_bf16 v[184:187], a[56:59], v[76:79], v[184:187]\n"
"v_mfma_f32_16x16x32_bf16 v[188:191], a[44:47], v[72:75], v[188:191]\n"
"buffer_load_dwordx4 v34, s[36:39], s89 offen offset:2048 lds\n"
"ds_read_b128 v[72:75], v30 offset:1024\n"
"v_mfma_f32_16x16x32_bf16 v[188:191], a[60:63], v[76:79], v[188:191]\n"
"ds_read_b128 v[76:79], v30 offset:1536\n"
"v_mfma_f32_16x16x32_bf16 v[192:195], a[32:35], v[48:51], v[192:195]\n"
"v_mfma_f32_16x16x32_bf16 v[192:195], a[48:51], v[52:55], v[192:195]\n"
"v_mfma_f32_16x16x32_bf16 v[196:199], a[36:39], v[48:51], v[196:199]\n"
"buffer_load_dwordx4 v35, s[36:39], s89 offen offset:3072 lds\n"
"v_mfma_f32_16x16x32_bf16 v[196:199], a[52:55], v[52:55], v[196:199]\n"
"v_mfma_f32_16x16x32_bf16 v[200:203], a[40:43], v[48:51], v[200:203]\n"
"v_mfma_f32_16x16x32_bf16 v[200:203], a[56:59], v[52:55], v[200:203]\n"
"s_add_u32 m0, s30, 0x4000\n"
"v_mfma_f32_16x16x32_bf16 v[204:207], a[44:47], v[48:51], v[204:207]\n"
"buffer_load_dwordx4 v37, s[40:43], s74 offen offset:0 lds\n"
"v_mfma_f32_16x16x32_bf16 v[204:207], a[60:63], v[52:55], v[204:207]\n"
"s_waitcnt lgkmcnt(2)\n"
"v_mfma_f32_16x16x32_bf16 v[208:211], a[32:35], v[56:59], v[208:211]\n"
"v_mfma_f32_16x16x32_bf16 v[208:211], a[48:51], v[60:63], v[208:211]\n"
"v_mfma_f32_16x16x32_bf16 v[212:215], a[36:39], v[56:59], v[212:215]\n"
"buffer_load_dwordx4 v38, s[40:43], s74 offen offset:1024 lds\n"
"v_mfma_f32_16x16x32_bf16 v[212:215], a[52:55], v[60:63], v[212:215]\n"
"v_mfma_f32_16x16x32_bf16 v[216:219], a[40:43], v[56:59], v[216:219]\n"
"v_mfma_f32_16x16x32_bf16 v[216:219], a[56:59], v[60:63], v[216:219]\n"
"v_mfma_f32_16x16x32_bf16 v[220:223], a[44:47], v[56:59], v[220:223]\n"
"buffer_load_dwordx4 v39, s[40:43], s74 offen offset:2048 lds\n"
"v_mfma_f32_16x16x32_bf16 v[220:223], a[60:63], v[60:63], v[220:223]\n"
"v_mfma_f32_16x16x32_bf16 v[224:227], a[32:35], v[64:67], v[224:227]\n"
"v_mfma_f32_16x16x32_bf16 v[224:227], a[48:51], v[68:71], v[224:227]\n"
"s_waitcnt vmcnt(15) lgkmcnt(0)\n"
"s_barrier\n"
"v_mfma_f32_16x16x32_bf16 v[228:231], a[36:39], v[64:67], v[228:231]\n"
"buffer_load_dwordx4 v40, s[40:43], s74 offen offset:3072 lds\n"
"ds_read_b128 v[48:51], v27 offset:32768\n"
"v_mfma_f32_16x16x32_bf16 v[228:231], a[52:55], v[68:71], v[228:231]\n"
"ds_read_b128 v[52:55], v27 offset:33280\n"
"ds_read_b128 v[56:59], v27 offset:34816\n"
"v_mfma_f32_16x16x32_bf16 v[232:235], a[40:43], v[64:67], v[232:235]\n"
"ds_read_b128 v[60:63], v27 offset:35328\n"
"v_mfma_f32_16x16x32_bf16 v[232:235], a[56:59], v[68:71], v[232:235]\n"
"s_add_u32 s75, s74, 0x40000\n"
"s_add_u32 m0, s30, 0x5000\n"
"v_mfma_f32_16x16x32_bf16 v[236:239], a[44:47], v[64:67], v[236:239]\n"
"buffer_load_dwordx4 v37, s[40:43], s75 offen offset:0 lds\n"
"ds_read_b128 v[64:67], v41 offset:32768\n"
"v_mfma_f32_16x16x32_bf16 v[236:239], a[60:63], v[68:71], v[236:239]\n"
"s_add_u32 s4, s26, s27\n"
"s_and_b32 s4, s4, 3\n"
"s_cmp_eq_u32 s4, 1\n"
"s_cbranch_scc1 .Lmha_VAR1\n"
"s_cmp_eq_u32 s4, 2\n"
"s_cbranch_scc1 .Lmha_VAR2\n"
"s_cmp_eq_u32 s4, 3\n"
"s_cbranch_scc1 .Lmha_VAR3\n"
"s_add_u32 m0, s30, 0\n"
"ds_read_b128 v[68:71], v41 offset:33280\n"
"v_mfma_f32_16x16x32_bf16 v[240:243], a[32:35], v[72:75], v[240:243]\n"
"s_waitcnt vmcnt(13)\n"
"buffer_load_dwordx4 v17, s[44:47], 0 offen offset:0 nt lds\n"
"ds_read_b64_tr_b16 a[32:33], v45 offset:24576\n"
"v_mfma_f32_16x16x32_bf16 v[240:243], a[48:51], v[76:79], v[240:243]\n"
"ds_read_b64_tr_b16 a[34:35], v45 offset:24832\n"
"v_mfma_f32_16x16x32_bf16 v[244:247], a[36:39], v[72:75], v[244:247]\n"
"ds_read_b64_tr_b16 a[36:37], v46 offset:24576\n"
"v_mfma_f32_16x16x32_bf16 v[244:247], a[52:55], v[76:79], v[244:247]\n"
"ds_read_b64_tr_b16 a[38:39], v46 offset:24832\n"
"v_mfma_f32_16x16x32_bf16 v[248:251], a[40:43], v[72:75], v[248:251]\n"
"buffer_load_dwordx4 v17, s[44:47], 0 offen offset:1024 nt lds\n"
"ds_read_b64_tr_b16 a[40:41], v45 offset:25088\n"
"v_mfma_f32_16x16x32_bf16 v[248:251], a[56:59], v[76:79], v[248:251]\n"
"ds_read_b64_tr_b16 a[42:43], v45 offset:25344\n"
"v_mfma_f32_16x16x32_bf16 v[252:255], a[44:47], v[72:75], v[252:255]\n"
"ds_read_b128 v[72:75], v41 offset:34816\n"
"v_mfma_f32_16x16x32_bf16 v[252:255], a[60:63], v[76:79], v[252:255]\n"
"s_waitcnt lgkmcnt(9)\n"
"ds_read_b128 v[76:79], v41 offset:35328\n"
"v_mfma_f32_16x16x32_bf16 v[112:115], a[0:3], v[48:51], v[112:115]\n"
"buffer_load_dwordx4 v18, s[44:47], 0 offen offset:2048 nt lds\n"
"ds_read_b64_tr_b16 a[44:45], v46 offset:25088\n"
"v_mfma_f32_16x16x32_bf16 v[112:115], a[16:19], v[52:55], v[112:115]\n"
"ds_read_b64_tr_b16 a[46:47], v46 offset:25344\n"
"v_mfma_f32_16x16x32_bf16 v[116:119], a[4:7], v[48:51], v[116:119]\n"
"v_mfma_f32_16x16x32_bf16 v[116:119], a[20:23], v[52:55], v[116:119]\n"
"v_mfma_f32_16x16x32_bf16 v[120:123], a[8:11], v[48:51], v[120:123]\n"
"s_waitcnt vmcnt(12)\n"
"buffer_load_dwordx4 v18, s[44:47], 0 offen offset:3072 nt lds\n"
"ds_read_b64_tr_b16 a[48:49], v45 offset:28672\n"
"v_mfma_f32_16x16x32_bf16 v[120:123], a[24:27], v[52:55], v[120:123]\n"
"ds_read_b64_tr_b16 a[50:51], v45 offset:28928\n"
"v_mfma_f32_16x16x32_bf16 v[124:127], a[12:15], v[48:51], v[124:127]\n"
"ds_read_b128 v[48:51], v42 offset:32768\n"
"v_mfma_f32_16x16x32_bf16 v[124:127], a[28:31], v[52:55], v[124:127]\n"
"s_mov_b32 s76, 0x4000\n"
"s_add_u32 m0, s30, 0x1000\n"
"ds_read_b128 v[52:55], v42 offset:33280\n"
"v_mfma_f32_16x16x32_bf16 v[128:131], a[0:3], v[56:59], v[128:131]\n"
"buffer_load_dwordx4 v17, s[44:47], s76 offen offset:0 nt lds\n"
"ds_read_b64_tr_b16 a[52:53], v46 offset:28672\n"
"v_mfma_f32_16x16x32_bf16 v[128:131], a[16:19], v[60:63], v[128:131]\n"
"ds_read_b64_tr_b16 a[54:55], v46 offset:28928\n"
"v_mfma_f32_16x16x32_bf16 v[132:135], a[4:7], v[56:59], v[132:135]\n"
"ds_read_b64_tr_b16 a[56:57], v45 offset:29184\n"
"v_mfma_f32_16x16x32_bf16 v[132:135], a[20:23], v[60:63], v[132:135]\n"
"ds_read_b64_tr_b16 a[58:59], v45 offset:29440\n"
"v_mfma_f32_16x16x32_bf16 v[136:139], a[8:11], v[56:59], v[136:139]\n"
"buffer_load_dwordx4 v17, s[44:47], s76 offen offset:1024 nt lds\n"
"ds_read_b64_tr_b16 a[60:61], v46 offset:29184\n"
"v_mfma_f32_16x16x32_bf16 v[136:139], a[24:27], v[60:63], v[136:139]\n"
"ds_read_b64_tr_b16 a[62:63], v46 offset:29440\n"
"v_mfma_f32_16x16x32_bf16 v[140:143], a[12:15], v[56:59], v[140:143]\n"
"ds_read_b128 v[56:59], v42 offset:34816\n"
"v_mfma_f32_16x16x32_bf16 v[140:143], a[28:31], v[60:63], v[140:143]\n"
"s_waitcnt lgkmcnt(11)\n"
"ds_read_b128 v[60:63], v42 offset:35328\n"
"v_mfma_f32_16x16x32_bf16 v[144:147], a[0:3], v[64:67], v[144:147]\n"
"buffer_load_dwordx4 v18, s[44:47], s76 offen offset:2048 nt lds\n"
"v_mfma_f32_16x16x32_bf16 v[144:147], a[16:19], v[68:71], v[144:147]\n"
"v_mfma_f32_16x16x32_bf16 v[148:151], a[4:7], v[64:67], v[148:151]\n"
"v_mfma_f32_16x16x32_bf16 v[148:151], a[20:23], v[68:71], v[148:151]\n"
"v_mfma_f32_16x16x32_bf16 v[152:155], a[8:11], v[64:67], v[152:155]\n"
"buffer_load_dwordx4 v18, s[44:47], s76 offen offset:3072 nt lds\n"
"v_mfma_f32_16x16x32_bf16 v[152:155], a[24:27], v[68:71], v[152:155]\n"
"v_mfma_f32_16x16x32_bf16 v[156:159], a[12:15], v[64:67], v[156:159]\n"
"ds_read_b128 v[64:67], v43 offset:32768\n"
"v_mfma_f32_16x16x32_bf16 v[156:159], a[28:31], v[68:71], v[156:159]\n"
"ds_read_b128 v[68:71], v43 offset:33280\n"
"v_mfma_f32_16x16x32_bf16 v[160:163], a[0:3], v[72:75], v[160:163]\n"
"v_mfma_f32_16x16x32_bf16 v[160:163], a[16:19], v[76:79], v[160:163]\n"
"v_mfma_f32_16x16x32_bf16 v[164:167], a[4:7], v[72:75], v[164:167]\n"
"v_mfma_f32_16x16x32_bf16 v[164:167], a[20:23], v[76:79], v[164:167]\n"
"v_mfma_f32_16x16x32_bf16 v[168:171], a[8:11], v[72:75], v[168:171]\n"
"v_mfma_f32_16x16x32_bf16 v[168:171], a[24:27], v[76:79], v[168:171]\n"
"v_mfma_f32_16x16x32_bf16 v[172:175], a[12:15], v[72:75], v[172:175]\n"
"ds_read_b128 v[72:75], v43 offset:34816\n"
"v_mfma_f32_16x16x32_bf16 v[172:175], a[28:31], v[76:79], v[172:175]\n"
"s_waitcnt lgkmcnt(3)\n"
"ds_read_b128 v[76:79], v43 offset:35328\n"
"v_mfma_f32_16x16x32_bf16 v[176:179], a[0:3], v[48:51], v[176:179]\n"
"v_mfma_f32_16x16x32_bf16 v[176:179], a[16:19], v[52:55], v[176:179]\n"
"v_mfma_f32_16x16x32_bf16 v[180:183], a[4:7], v[48:51], v[180:183]\n"
"v_mfma_f32_16x16x32_bf16 v[180:183], a[20:23], v[52:55], v[180:183]\n"
"v_mfma_f32_16x16x32_bf16 v[184:187], a[8:11], v[48:51], v[184:187]\n"
"v_mfma_f32_16x16x32_bf16 v[184:187], a[24:27], v[52:55], v[184:187]\n"
"v_mfma_f32_16x16x32_bf16 v[188:191], a[12:15], v[48:51], v[188:191]\n"
"ds_read_b128 v[48:51], v44 offset:0\n"
"v_mfma_f32_16x16x32_bf16 v[188:191], a[28:31], v[52:55], v[188:191]\n"
"ds_read_b128 v[52:55], v44 offset:512\n"
"v_mfma_f32_16x16x32_bf16 v[192:195], a[0:3], v[56:59], v[192:195]\n"
"v_mfma_f32_16x16x32_bf16 v[192:195], a[16:19], v[60:63], v[192:195]\n"
"v_mfma_f32_16x16x32_bf16 v[196:199], a[4:7], v[56:59], v[196:199]\n"
"v_mfma_f32_16x16x32_bf16 v[196:199], a[20:23], v[60:63], v[196:199]\n"
"s_add_u32 m0, s30, 0x5000\n"
"v_mfma_f32_16x16x32_bf16 v[200:203], a[8:11], v[56:59], v[200:203]\n"
"s_add_u32 s73, s72, 6\n"
"buffer_load_dwordx4 v38, s[40:43], s75 offen offset:1024 lds\n"
"v_mfma_f32_16x16x32_bf16 v[200:203], a[24:27], v[60:63], v[200:203]\n"
"s_and_b32 s73, s73, 15\n"
"v_mfma_f32_16x16x32_bf16 v[204:207], a[12:15], v[56:59], v[204:207]\n"
"s_lshl_b32 s73, s73, 7\n"
"v_mfma_f32_16x16x32_bf16 v[204:207], a[28:31], v[60:63], v[204:207]\n"
"s_waitcnt lgkmcnt(2)\n"
"v_mfma_f32_16x16x32_bf16 v[208:211], a[0:3], v[64:67], v[208:211]\n"
"buffer_load_dwordx4 v39, s[40:43], s75 offen offset:2048 lds\n"
"v_mfma_f32_16x16x32_bf16 v[208:211], a[16:19], v[68:71], v[208:211]\n"
"v_mfma_f32_16x16x32_bf16 v[212:215], a[4:7], v[64:67], v[212:215]\n"
"v_mfma_f32_16x16x32_bf16 v[212:215], a[20:23], v[68:71], v[212:215]\n"
"v_mfma_f32_16x16x32_bf16 v[216:219], a[8:11], v[64:67], v[216:219]\n"
"buffer_load_dwordx4 v40, s[40:43], s75 offen offset:3072 lds\n"
"v_mfma_f32_16x16x32_bf16 v[216:219], a[24:27], v[68:71], v[216:219]\n"
"v_mfma_f32_16x16x32_bf16 v[220:223], a[12:15], v[64:67], v[220:223]\n"
"v_mfma_f32_16x16x32_bf16 v[220:223], a[28:31], v[68:71], v[220:223]\n"
"s_add_u32 m0, s30, 0x2000\n"
"v_mfma_f32_16x16x32_bf16 v[224:227], a[0:3], v[72:75], v[224:227]\n"
"buffer_load_dwordx4 v32, s[36:39], s73 offen offset:0 lds\n"
"v_mfma_f32_16x16x32_bf16 v[224:227], a[16:19], v[76:79], v[224:227]\n"
"s_waitcnt vmcnt(17) lgkmcnt(0)\n"
"s_barrier\n"
"v_mfma_f32_16x16x32_bf16 v[228:231], a[4:7], v[72:75], v[228:231]\n"
"ds_read_b128 v[56:59], v27 offset:12288\n"
"ds_read_b128 v[60:63], v27 offset:12800\n"
"v_mfma_f32_16x16x32_bf16 v[228:231], a[20:23], v[76:79], v[228:231]\n"
"ds_read_b128 v[64:67], v27 offset:14336\n"
"ds_read_b128 v[68:71], v27 offset:14848\n"
"v_mfma_f32_16x16x32_bf16 v[232:235], a[8:11], v[72:75], v[232:235]\n"
"buffer_load_dwordx4 v33, s[36:39], s73 offen offset:1024 lds\n"
"v_mfma_f32_16x16x32_bf16 v[232:235], a[24:27], v[76:79], v[232:235]\n"
"v_mfma_f32_16x16x32_bf16 v[236:239], a[12:15], v[72:75], v[236:239]\n"
"ds_read_b128 v[72:75], v41 offset:12288\n"
"v_mfma_f32_16x16x32_bf16 v[236:239], a[28:31], v[76:79], v[236:239]\n"
"ds_read_b128 v[76:79], v41 offset:12800\n"
"v_mfma_f32_16x16x32_bf16 v[240:243], a[0:3], v[48:51], v[240:243]\n"
"s_waitcnt vmcnt(14)\n"
"buffer_load_dwordx4 v34, s[36:39], s73 offen offset:2048 lds\n"
"ds_read_b64_tr_b16 a[0:1], v45 offset:16384\n"
"v_mfma_f32_16x16x32_bf16 v[240:243], a[16:19], v[52:55], v[240:243]\n"
"s_add_u32 s90, s72, 7\n"
"ds_read_b64_tr_b16 a[2:3], v45 offset:16640\n"
"v_mfma_f32_16x16x32_bf16 v[244:247], a[4:7], v[48:51], v[244:247]\n"
"s_and_b32 s90, s90, 15\n"
"ds_read_b64_tr_b16 a[4:5], v46 offset:16384\n"
"v_mfma_f32_16x16x32_bf16 v[244:247], a[20:23], v[52:55], v[244:247]\n"
"s_lshl_b32 s90, s90, 19\n"
"ds_read_b64_tr_b16 a[6:7], v46 offset:16640\n"
"v_mfma_f32_16x16x32_bf16 v[248:251], a[8:11], v[48:51], v[248:251]\n"
"buffer_load_dwordx4 v35, s[36:39], s73 offen offset:3072 lds\n"
"ds_read_b64_tr_b16 a[8:9], v45 offset:16896\n"
"v_mfma_f32_16x16x32_bf16 v[248:251], a[24:27], v[52:55], v[248:251]\n"
"ds_read_b64_tr_b16 a[10:11], v45 offset:17152\n"
"v_mfma_f32_16x16x32_bf16 v[252:255], a[12:15], v[48:51], v[252:255]\n"
"ds_read_b128 v[48:51], v41 offset:14336\n"
"v_mfma_f32_16x16x32_bf16 v[252:255], a[28:31], v[52:55], v[252:255]\n"
"s_waitcnt lgkmcnt(9)\n"
"s_add_u32 m0, s30, 0x9400\n"
"ds_read_b128 v[52:55], v41 offset:14848\n"
"v_mfma_f32_16x16x32_bf16 v[112:115], a[32:35], v[56:59], v[112:115]\n"
"buffer_load_dwordx4 v36, s[36:39], s73 offen offset:0 lds\n"
"ds_read_b64_tr_b16 a[12:13], v46 offset:16896\n"
"v_mfma_f32_16x16x32_bf16 v[112:115], a[48:51], v[60:63], v[112:115]\n"
"ds_read_b64_tr_b16 a[14:15], v46 offset:17152\n"
"v_mfma_f32_16x16x32_bf16 v[116:119], a[36:39], v[56:59], v[116:119]\n"
"v_mfma_f32_16x16x32_bf16 v[116:119], a[52:55], v[60:63], v[116:119]\n"
"s_add_u32 m0, s30, 0x6000\n"
"v_mfma_f32_16x16x32_bf16 v[120:123], a[40:43], v[56:59], v[120:123]\n"
"buffer_load_dwordx4 v37, s[40:43], s90 offen offset:0 lds\n"
"v_mfma_f32_16x16x32_bf16 v[120:123], a[56:59], v[60:63], v[120:123]\n"
"v_mfma_f32_16x16x32_bf16 v[124:127], a[44:47], v[56:59], v[124:127]\n"
"ds_read_b128 v[56:59], v42 offset:12288\n"
"v_mfma_f32_16x16x32_bf16 v[124:127], a[60:63], v[60:63], v[124:127]\n"
"ds_read_b128 v[60:63], v42 offset:12800\n"
"v_mfma_f32_16x16x32_bf16 v[128:131], a[32:35], v[64:67], v[128:131]\n"
"buffer_load_dwordx4 v38, s[40:43], s90 offen offset:1024 lds\n"
"v_mfma_f32_16x16x32_bf16 v[128:131], a[48:51], v[68:71], v[128:131]\n"
"v_mfma_f32_16x16x32_bf16 v[132:135], a[36:39], v[64:67], v[132:135]\n"
"v_mfma_f32_16x16x32_bf16 v[132:135], a[52:55], v[68:71], v[132:135]\n"
"v_mfma_f32_16x16x32_bf16 v[136:139], a[40:43], v[64:67], v[136:139]\n"
"buffer_load_dwordx4 v39, s[40:43], s90 offen offset:2048 lds\n"
"v_mfma_f32_16x16x32_bf16 v[136:139], a[56:59], v[68:71], v[136:139]\n"
"v_mfma_f32_16x16x32_bf16 v[140:143], a[44:47], v[64:67], v[140:143]\n"
"ds_read_b128 v[64:67], v42 offset:14336\n"
"v_mfma_f32_16x16x32_bf16 v[140:143], a[60:63], v[68:71], v[140:143]\n"
"s_waitcnt lgkmcnt(3)\n"
"ds_read_b128 v[68:71], v42 offset:14848\n"
"v_mfma_f32_16x16x32_bf16 v[144:147], a[32:35], v[72:75], v[144:147]\n"
"buffer_load_dwordx4 v40, s[40:43], s90 offen offset:3072 lds\n"
"v_mfma_f32_16x16x32_bf16 v[144:147], a[48:51], v[76:79], v[144:147]\n"
"v_mfma_f32_16x16x32_bf16 v[148:151], a[36:39], v[72:75], v[148:151]\n"
"v_mfma_f32_16x16x32_bf16 v[148:151], a[52:55], v[76:79], v[148:151]\n"
"s_add_u32 s77, s90, 0x40000\n"
"s_add_u32 m0, s30, 0x7000\n"
"v_mfma_f32_16x16x32_bf16 v[152:155], a[40:43], v[72:75], v[152:155]\n"
"buffer_load_dwordx4 v37, s[40:43], s77 offen offset:0 lds\n"
"v_mfma_f32_16x16x32_bf16 v[152:155], a[56:59], v[76:79], v[152:155]\n"
"v_mfma_f32_16x16x32_bf16 v[156:159], a[44:47], v[72:75], v[156:159]\n"
"ds_read_b128 v[72:75], v43 offset:12288\n"
"v_mfma_f32_16x16x32_bf16 v[156:159], a[60:63], v[76:79], v[156:159]\n"
"ds_read_b128 v[76:79], v43 offset:12800\n"
"v_mfma_f32_16x16x32_bf16 v[160:163], a[32:35], v[48:51], v[160:163]\n"
"s_add_u32 s88, s72, 7\n"
"buffer_load_dwordx4 v38, s[40:43], s77 offen offset:1024 lds\n"
"v_mfma_f32_16x16x32_bf16 v[160:163], a[48:51], v[52:55], v[160:163]\n"
"s_and_b32 s88, s88, 15\n"
"v_mfma_f32_16x16x32_bf16 v[164:167], a[36:39], v[48:51], v[164:167]\n"
"s_lshl_b32 s88, s88, 7\n"
"v_mfma_f32_16x16x32_bf16 v[164:167], a[52:55], v[52:55], v[164:167]\n"
"v_mfma_f32_16x16x32_bf16 v[168:171], a[40:43], v[48:51], v[168:171]\n"
"buffer_load_dwordx4 v39, s[40:43], s77 offen offset:2048 lds\n"
"v_mfma_f32_16x16x32_bf16 v[168:171], a[56:59], v[52:55], v[168:171]\n"
"v_mfma_f32_16x16x32_bf16 v[172:175], a[44:47], v[48:51], v[172:175]\n"
"ds_read_b128 v[48:51], v43 offset:14336\n"
"v_mfma_f32_16x16x32_bf16 v[172:175], a[60:63], v[52:55], v[172:175]\n"
"s_waitcnt lgkmcnt(3)\n"
"ds_read_b128 v[52:55], v43 offset:14848\n"
"v_mfma_f32_16x16x32_bf16 v[176:179], a[32:35], v[56:59], v[176:179]\n"
"s_waitcnt vmcnt(12)\n"
"buffer_load_dwordx4 v40, s[40:43], s77 offen offset:3072 lds\n"
"ds_read_b64_tr_b16 a[16:17], v45 offset:20480\n"
"v_mfma_f32_16x16x32_bf16 v[176:179], a[48:51], v[60:63], v[176:179]\n"
"ds_read_b64_tr_b16 a[18:19], v45 offset:20736\n"
"v_mfma_f32_16x16x32_bf16 v[180:183], a[36:39], v[56:59], v[180:183]\n"
"ds_read_b64_tr_b16 a[20:21], v46 offset:20480\n"
"v_mfma_f32_16x16x32_bf16 v[180:183], a[52:55], v[60:63], v[180:183]\n"
"s_add_u32 m0, s30, 0x8000\n"
"ds_read_b64_tr_b16 a[22:23], v46 offset:20736\n"
"v_mfma_f32_16x16x32_bf16 v[184:187], a[40:43], v[56:59], v[184:187]\n"
"buffer_load_dwordx4 v32, s[36:39], s88 offen offset:0 lds\n"
"ds_read_b64_tr_b16 a[24:25], v45 offset:20992\n"
"v_mfma_f32_16x16x32_bf16 v[184:187], a[56:59], v[60:63], v[184:187]\n"
"ds_read_b64_tr_b16 a[26:27], v45 offset:21248\n"
"v_mfma_f32_16x16x32_bf16 v[188:191], a[44:47], v[56:59], v[188:191]\n"
"ds_read_b128 v[56:59], v30 offset:0\n"
"v_mfma_f32_16x16x32_bf16 v[188:191], a[60:63], v[60:63], v[188:191]\n"
"s_add_u32 s91, s72, 8\n"
"ds_read_b128 v[60:63], v30 offset:512\n"
"v_mfma_f32_16x16x32_bf16 v[192:195], a[32:35], v[64:67], v[192:195]\n"
"s_and_b32 s91, s91, 15\n"
"buffer_load_dwordx4 v33, s[36:39], s88 offen offset:1024 lds\n"
"ds_read_b64_tr_b16 a[28:29], v46 offset:20992\n"
"v_mfma_f32_16x16x32_bf16 v[192:195], a[48:51], v[68:71], v[192:195]\n"
"s_lshl_b32 s91, s91, 19\n"
"ds_read_b64_tr_b16 a[30:31], v46 offset:21248\n"
"v_mfma_f32_16x16x32_bf16 v[196:199], a[36:39], v[64:67], v[196:199]\n"
"v_mfma_f32_16x16x32_bf16 v[196:199], a[52:55], v[68:71], v[196:199]\n"
"v_mfma_f32_16x16x32_bf16 v[200:203], a[40:43], v[64:67], v[200:203]\n"
"buffer_load_dwordx4 v34, s[36:39], s88 offen offset:2048 lds\n"
"v_mfma_f32_16x16x32_bf16 v[200:203], a[56:59], v[68:71], v[200:203]\n"
"v_mfma_f32_16x16x32_bf16 v[204:207], a[44:47], v[64:67], v[204:207]\n"
"v_mfma_f32_16x16x32_bf16 v[204:207], a[60:63], v[68:71], v[204:207]\n"
"s_waitcnt lgkmcnt(6)\n"
"v_mfma_f32_16x16x32_bf16 v[208:211], a[32:35], v[72:75], v[208:211]\n"
"buffer_load_dwordx4 v35, s[36:39], s88 offen offset:3072 lds\n"
"v_mfma_f32_16x16x32_bf16 v[208:211], a[48:51], v[76:79], v[208:211]\n"
"v_mfma_f32_16x16x32_bf16 v[212:215], a[36:39], v[72:75], v[212:215]\n"
"s_add_u32 m0, s30, 0x4000\n"
"v_mfma_f32_16x16x32_bf16 v[212:215], a[52:55], v[76:79], v[212:215]\n"
"buffer_load_dwordx4 v37, s[40:43], s91 offen offset:0 lds\n"
"v_mfma_f32_16x16x32_bf16 v[216:219], a[40:43], v[72:75], v[216:219]\n"
"v_mfma_f32_16x16x32_bf16 v[216:219], a[56:59], v[76:79], v[216:219]\n"
"v_mfma_f32_16x16x32_bf16 v[220:223], a[44:47], v[72:75], v[220:223]\n"
"v_mfma_f32_16x16x32_bf16 v[220:223], a[60:63], v[76:79], v[220:223]\n"
"buffer_load_dwordx4 v38, s[40:43], s91 offen offset:1024 lds\n"
"v_mfma_f32_16x16x32_bf16 v[224:227], a[32:35], v[48:51], v[224:227]\n"
"v_mfma_f32_16x16x32_bf16 v[224:227], a[48:51], v[52:55], v[224:227]\n"
"s_waitcnt vmcnt(14) lgkmcnt(0)\n"
"s_barrier\n"
"v_mfma_f32_16x16x32_bf16 v[228:231], a[36:39], v[48:51], v[228:231]\n"
"ds_read_b128 v[64:67], v27 offset:8192\n"
"ds_read_b128 v[68:71], v27 offset:8704\n"
"v_mfma_f32_16x16x32_bf16 v[228:231], a[52:55], v[52:55], v[228:231]\n"
"buffer_load_dwordx4 v39, s[40:43], s91 offen offset:2048 lds\n"
"ds_read_b128 v[72:75], v27 offset:10240\n"
"v_mfma_f32_16x16x32_bf16 v[232:235], a[40:43], v[48:51], v[232:235]\n"
"ds_read_b128 v[76:79], v27 offset:10752\n"
"v_mfma_f32_16x16x32_bf16 v[232:235], a[56:59], v[52:55], v[232:235]\n"
"v_mfma_f32_16x16x32_bf16 v[236:239], a[44:47], v[48:51], v[236:239]\n"
"ds_read_b128 v[48:51], v41 offset:8192\n"
"v_mfma_f32_16x16x32_bf16 v[236:239], a[60:63], v[52:55], v[236:239]\n"
"buffer_load_dwordx4 v40, s[40:43], s91 offen offset:3072 lds\n"
"ds_read_b128 v[52:55], v41 offset:8704\n"
"v_mfma_f32_16x16x32_bf16 v[240:243], a[32:35], v[56:59], v[240:243]\n"
"v_mfma_f32_16x16x32_bf16 v[240:243], a[48:51], v[60:63], v[240:243]\n"
"buffer_load_dwordx4 a[64:67], v19, s[48:51], 0 offen offset:0 nt\n"
"v_mfma_f32_16x16x32_bf16 v[244:247], a[36:39], v[56:59], v[244:247]\n"
"s_waitcnt vmcnt(13)\n"
"ds_read_b64_tr_b16 a[32:33], v45 offset:24576\n"
"v_mfma_f32_16x16x32_bf16 v[244:247], a[52:55], v[60:63], v[244:247]\n"
"ds_read_b64_tr_b16 a[34:35], v45 offset:24832\n"
"v_mfma_f32_16x16x32_bf16 v[248:251], a[40:43], v[56:59], v[248:251]\n"
"ds_read_b64_tr_b16 a[36:37], v46 offset:24576\n"
"v_mfma_f32_16x16x32_bf16 v[248:251], a[56:59], v[60:63], v[248:251]\n"
"buffer_load_dwordx4 a[68:71], v20, s[48:51], 0 offen offset:1024 nt\n"
"ds_read_b64_tr_b16 a[38:39], v46 offset:24832\n"
"v_mfma_f32_16x16x32_bf16 v[252:255], a[44:47], v[56:59], v[252:255]\n"
"ds_read_b128 v[56:59], v41 offset:10240\n"
"v_mfma_f32_16x16x32_bf16 v[252:255], a[60:63], v[60:63], v[252:255]\n"
"s_waitcnt lgkmcnt(7)\n"
"ds_read_b128 v[60:63], v41 offset:10752\n"
"v_mfma_f32_16x16x32_bf16 v[112:115], a[0:3], v[64:67], v[112:115]\n"
"ds_read_b64_tr_b16 a[40:41], v45 offset:25088\n"
"v_mfma_f32_16x16x32_bf16 v[112:115], a[16:19], v[68:71], v[112:115]\n"
"buffer_load_dwordx4 a[72:75], v19, s[48:51], 0 offen offset:2048 nt\n"
"ds_read_b64_tr_b16 a[42:43], v45 offset:25344\n"
"v_mfma_f32_16x16x32_bf16 v[116:119], a[4:7], v[64:67], v[116:119]\n"
"ds_read_b64_tr_b16 a[44:45], v46 offset:25088\n"
"v_mfma_f32_16x16x32_bf16 v[116:119], a[20:23], v[68:71], v[116:119]\n"
"ds_read_b64_tr_b16 a[46:47], v46 offset:25344\n"
"v_mfma_f32_16x16x32_bf16 v[120:123], a[8:11], v[64:67], v[120:123]\n"
"v_mfma_f32_16x16x32_bf16 v[120:123], a[24:27], v[68:71], v[120:123]\n"
"buffer_load_dwordx4 a[76:79], v20, s[48:51], 0 offen offset:3072 nt\n"
"v_mfma_f32_16x16x32_bf16 v[124:127], a[12:15], v[64:67], v[124:127]\n"
"ds_read_b128 v[64:67], v42 offset:8192\n"
"v_mfma_f32_16x16x32_bf16 v[124:127], a[28:31], v[68:71], v[124:127]\n"
"ds_read_b128 v[68:71], v42 offset:8704\n"
"v_mfma_f32_16x16x32_bf16 v[128:131], a[0:3], v[72:75], v[128:131]\n"
"s_mov_b32 s78, 0x8000\n"
"v_mfma_f32_16x16x32_bf16 v[128:131], a[16:19], v[76:79], v[128:131]\n"
"buffer_load_dwordx4 a[80:83], v17, s[44:47], s78 offen offset:0 nt\n"
"v_mfma_f32_16x16x32_bf16 v[132:135], a[4:7], v[72:75], v[132:135]\n"
"s_waitcnt vmcnt(13)\n"
"ds_read_b64_tr_b16 a[48:49], v45 offset:28672\n"
"v_mfma_f32_16x16x32_bf16 v[132:135], a[20:23], v[76:79], v[132:135]\n"
"ds_read_b64_tr_b16 a[50:51], v45 offset:28928\n"
"v_mfma_f32_16x16x32_bf16 v[136:139], a[8:11], v[72:75], v[136:139]\n"
"ds_read_b64_tr_b16 a[52:53], v46 offset:28672\n"
"v_mfma_f32_16x16x32_bf16 v[136:139], a[24:27], v[76:79], v[136:139]\n"
"buffer_load_dwordx4 a[84:87], v17, s[44:47], s78 offen offset:1024 nt\n"
"ds_read_b64_tr_b16 a[54:55], v46 offset:28928\n"
"v_mfma_f32_16x16x32_bf16 v[140:143], a[12:15], v[72:75], v[140:143]\n"
"ds_read_b128 v[72:75], v42 offset:10240\n"
"v_mfma_f32_16x16x32_bf16 v[140:143], a[28:31], v[76:79], v[140:143]\n"
"s_waitcnt lgkmcnt(7)\n"
"ds_read_b128 v[76:79], v42 offset:10752\n"
"v_mfma_f32_16x16x32_bf16 v[144:147], a[0:3], v[48:51], v[144:147]\n"
"ds_read_b64_tr_b16 a[56:57], v45 offset:29184\n"
"v_mfma_f32_16x16x32_bf16 v[144:147], a[16:19], v[52:55], v[144:147]\n"
"buffer_load_dwordx4 a[88:91], v18, s[44:47], s78 offen offset:2048 nt\n"
"ds_read_b64_tr_b16 a[58:59], v45 offset:29440\n"
"v_mfma_f32_16x16x32_bf16 v[148:151], a[4:7], v[48:51], v[148:151]\n"
"ds_read_b64_tr_b16 a[60:61], v46 offset:29184\n"
"v_mfma_f32_16x16x32_bf16 v[148:151], a[20:23], v[52:55], v[148:151]\n"
"ds_read_b64_tr_b16 a[62:63], v46 offset:29440\n"
"v_mfma_f32_16x16x32_bf16 v[152:155], a[8:11], v[48:51], v[152:155]\n"
"v_mfma_f32_16x16x32_bf16 v[152:155], a[24:27], v[52:55], v[152:155]\n"
"buffer_load_dwordx4 a[92:95], v18, s[44:47], s78 offen offset:3072 nt\n"
"v_mfma_f32_16x16x32_bf16 v[156:159], a[12:15], v[48:51], v[156:159]\n"
"ds_read_b128 v[48:51], v43 offset:8192\n"
"v_mfma_f32_16x16x32_bf16 v[156:159], a[28:31], v[52:55], v[156:159]\n"
"ds_read_b128 v[52:55], v43 offset:8704\n"
"v_mfma_f32_16x16x32_bf16 v[160:163], a[0:3], v[56:59], v[160:163]\n"
"v_mfma_f32_16x16x32_bf16 v[160:163], a[16:19], v[60:63], v[160:163]\n"
"v_mfma_f32_16x16x32_bf16 v[164:167], a[4:7], v[56:59], v[164:167]\n"
"v_mfma_f32_16x16x32_bf16 v[164:167], a[20:23], v[60:63], v[164:167]\n"
"v_mfma_f32_16x16x32_bf16 v[168:171], a[8:11], v[56:59], v[168:171]\n"
"v_mfma_f32_16x16x32_bf16 v[168:171], a[24:27], v[60:63], v[168:171]\n"
"v_mfma_f32_16x16x32_bf16 v[172:175], a[12:15], v[56:59], v[172:175]\n"
"ds_read_b128 v[56:59], v43 offset:10240\n"
"v_mfma_f32_16x16x32_bf16 v[172:175], a[28:31], v[60:63], v[172:175]\n"
"s_waitcnt lgkmcnt(3)\n"
"ds_read_b128 v[60:63], v43 offset:10752\n"
"v_mfma_f32_16x16x32_bf16 v[176:179], a[0:3], v[64:67], v[176:179]\n"
"v_mfma_f32_16x16x32_bf16 v[176:179], a[16:19], v[68:71], v[176:179]\n"
"v_mfma_f32_16x16x32_bf16 v[180:183], a[4:7], v[64:67], v[180:183]\n"
"v_mfma_f32_16x16x32_bf16 v[180:183], a[20:23], v[68:71], v[180:183]\n"
"v_mfma_f32_16x16x32_bf16 v[184:187], a[8:11], v[64:67], v[184:187]\n"
"v_mfma_f32_16x16x32_bf16 v[184:187], a[24:27], v[68:71], v[184:187]\n"
"v_mfma_f32_16x16x32_bf16 v[188:191], a[12:15], v[64:67], v[188:191]\n"
"ds_read_b128 v[64:67], v44 offset:1024\n"
"v_mfma_f32_16x16x32_bf16 v[188:191], a[28:31], v[68:71], v[188:191]\n"
"ds_read_b128 v[68:71], v44 offset:1536\n"
"v_mfma_f32_16x16x32_bf16 v[192:195], a[0:3], v[72:75], v[192:195]\n"
"v_mfma_f32_16x16x32_bf16 v[192:195], a[16:19], v[76:79], v[192:195]\n"
"v_mfma_f32_16x16x32_bf16 v[196:199], a[4:7], v[72:75], v[196:199]\n"
"v_mfma_f32_16x16x32_bf16 v[196:199], a[20:23], v[76:79], v[196:199]\n"
"v_mfma_f32_16x16x32_bf16 v[200:203], a[8:11], v[72:75], v[200:203]\n"
"s_add_u32 s84, s91, 0x40000\n"
"s_add_u32 m0, s30, 0x5000\n"
"v_mfma_f32_16x16x32_bf16 v[200:203], a[24:27], v[76:79], v[200:203]\n"
"buffer_load_dwordx4 v37, s[40:43], s84 offen offset:0 lds\n"
"v_mfma_f32_16x16x32_bf16 v[204:207], a[12:15], v[72:75], v[204:207]\n"
"v_mfma_f32_16x16x32_bf16 v[204:207], a[28:31], v[76:79], v[204:207]\n"
"s_waitcnt lgkmcnt(2)\n"
"v_mfma_f32_16x16x32_bf16 v[208:211], a[0:3], v[48:51], v[208:211]\n"
"v_mfma_f32_16x16x32_bf16 v[208:211], a[16:19], v[52:55], v[208:211]\n"
"s_add_u32 s89, s72, 8\n"
"buffer_load_dwordx4 v38, s[40:43], s84 offen offset:1024 lds\n"
"v_mfma_f32_16x16x32_bf16 v[212:215], a[4:7], v[48:51], v[212:215]\n"
"s_and_b32 s89, s89, 15\n"
"v_mfma_f32_16x16x32_bf16 v[212:215], a[20:23], v[52:55], v[212:215]\n"
"s_lshl_b32 s89, s89, 7\n"
"v_mfma_f32_16x16x32_bf16 v[216:219], a[8:11], v[48:51], v[216:219]\n"
"v_mfma_f32_16x16x32_bf16 v[216:219], a[24:27], v[52:55], v[216:219]\n"
"buffer_load_dwordx4 v39, s[40:43], s84 offen offset:2048 lds\n"
"v_mfma_f32_16x16x32_bf16 v[220:223], a[12:15], v[48:51], v[220:223]\n"
"v_mfma_f32_16x16x32_bf16 v[220:223], a[28:31], v[52:55], v[220:223]\n"
"v_mfma_f32_16x16x32_bf16 v[224:227], a[0:3], v[56:59], v[224:227]\n"
"v_mfma_f32_16x16x32_bf16 v[224:227], a[16:19], v[60:63], v[224:227]\n"
"s_waitcnt vmcnt(15) lgkmcnt(0)\n"
"s_barrier\n"
"buffer_load_dwordx4 v40, s[40:43], s84 offen offset:3072 lds\n"
"v_mfma_f32_16x16x32_bf16 v[228:231], a[4:7], v[56:59], v[228:231]\n"
"ds_read_b128 v[72:75], v27 offset:32768\n"
"ds_read_b128 v[76:79], v27 offset:33280\n"
"v_mfma_f32_16x16x32_bf16 v[228:231], a[20:23], v[60:63], v[228:231]\n"
"ds_read_b128 v[48:51], v27 offset:34816\n"
"ds_read_b128 v[52:55], v27 offset:35328\n"
"v_mfma_f32_16x16x32_bf16 v[232:235], a[8:11], v[56:59], v[232:235]\n"
"s_add_u32 m0, s30, 0x3000\n"
"v_mfma_f32_16x16x32_bf16 v[232:235], a[24:27], v[60:63], v[232:235]\n"
"buffer_load_dwordx4 v32, s[36:39], s89 offen offset:0 lds\n"
"v_mfma_f32_16x16x32_bf16 v[236:239], a[12:15], v[56:59], v[236:239]\n"
"ds_read_b128 v[56:59], v41 offset:32768\n"
"v_mfma_f32_16x16x32_bf16 v[236:239], a[28:31], v[60:63], v[236:239]\n"
"ds_read_b128 v[60:63], v41 offset:33280\n"
"v_mfma_f32_16x16x32_bf16 v[240:243], a[0:3], v[64:67], v[240:243]\n"
"s_waitcnt vmcnt(13)\n"
"ds_read_b64_tr_b16 a[0:1], v45 offset:16384\n"
"v_mfma_f32_16x16x32_bf16 v[240:243], a[16:19], v[68:71], v[240:243]\n"
"buffer_load_dwordx4 v33, s[36:39], s89 offen offset:1024 lds\n"
"ds_read_b64_tr_b16 a[2:3], v45 offset:16640\n"
"v_mfma_f32_16x16x32_bf16 v[244:247], a[4:7], v[64:67], v[244:247]\n"
"ds_read_b64_tr_b16 a[4:5], v46 offset:16384\n"
"v_mfma_f32_16x16x32_bf16 v[244:247], a[20:23], v[68:71], v[244:247]\n"
"ds_read_b64_tr_b16 a[6:7], v46 offset:16640\n"
"v_mfma_f32_16x16x32_bf16 v[248:251], a[8:11], v[64:67], v[248:251]\n"
"ds_read_b64_tr_b16 a[8:9], v45 offset:16896\n"
"v_mfma_f32_16x16x32_bf16 v[248:251], a[24:27], v[68:71], v[248:251]\n"
"s_add_u32 s74, s72, 9\n"
"buffer_load_dwordx4 v34, s[36:39], s89 offen offset:2048 lds\n"
"ds_read_b64_tr_b16 a[10:11], v45 offset:17152\n"
"v_mfma_f32_16x16x32_bf16 v[252:255], a[12:15], v[64:67], v[252:255]\n"
"s_and_b32 s74, s74, 15\n"
"ds_read_b128 v[64:67], v41 offset:34816\n"
"v_mfma_f32_16x16x32_bf16 v[252:255], a[28:31], v[68:71], v[252:255]\n"
"s_waitcnt lgkmcnt(9)\n"
"s_lshl_b32 s74, s74, 19\n"
"ds_read_b128 v[68:71], v41 offset:35328\n"
"v_mfma_f32_16x16x32_bf16 v[112:115], a[32:35], v[72:75], v[112:115]\n"
"ds_read_b64_tr_b16 a[12:13], v46 offset:16896\n"
"v_mfma_f32_16x16x32_bf16 v[112:115], a[48:51], v[76:79], v[112:115]\n"
"buffer_load_dwordx4 v35, s[36:39], s89 offen offset:3072 lds\n"
"ds_read_b64_tr_b16 a[14:15], v46 offset:17152\n"
"v_mfma_f32_16x16x32_bf16 v[116:119], a[36:39], v[72:75], v[116:119]\n"
"v_mfma_f32_16x16x32_bf16 v[116:119], a[52:55], v[76:79], v[116:119]\n"
"v_mfma_f32_16x16x32_bf16 v[120:123], a[40:43], v[72:75], v[120:123]\n"
"s_add_u32 m0, s30, 0x9000\n"
"v_mfma_f32_16x16x32_bf16 v[120:123], a[56:59], v[76:79], v[120:123]\n"
"buffer_load_dwordx4 v36, s[36:39], s89 offen offset:0 lds\n"
"v_mfma_f32_16x16x32_bf16 v[124:127], a[44:47], v[72:75], v[124:127]\n"
"ds_read_b128 v[72:75], v42 offset:32768\n"
"v_mfma_f32_16x16x32_bf16 v[124:127], a[60:63], v[76:79], v[124:127]\n"
"ds_read_b128 v[76:79], v42 offset:33280\n"
"v_mfma_f32_16x16x32_bf16 v[128:131], a[32:35], v[48:51], v[128:131]\n"
"s_add_u32 m0, s30, 0x6000\n"
"v_mfma_f32_16x16x32_bf16 v[128:131], a[48:51], v[52:55], v[128:131]\n"
"buffer_load_dwordx4 v37, s[40:43], s74 offen offset:0 lds\n"
"v_mfma_f32_16x16x32_bf16 v[132:135], a[36:39], v[48:51], v[132:135]\n"
"v_mfma_f32_16x16x32_bf16 v[132:135], a[52:55], v[52:55], v[132:135]\n"
"v_mfma_f32_16x16x32_bf16 v[136:139], a[40:43], v[48:51], v[136:139]\n"
"v_mfma_f32_16x16x32_bf16 v[136:139], a[56:59], v[52:55], v[136:139]\n"
"buffer_load_dwordx4 v38, s[40:43], s74 offen offset:1024 lds\n"
"v_mfma_f32_16x16x32_bf16 v[140:143], a[44:47], v[48:51], v[140:143]\n"
"ds_read_b128 v[48:51], v42 offset:34816\n"
"v_mfma_f32_16x16x32_bf16 v[140:143], a[60:63], v[52:55], v[140:143]\n"
"s_waitcnt lgkmcnt(3)\n"
"ds_read_b128 v[52:55], v42 offset:35328\n"
"v_mfma_f32_16x16x32_bf16 v[144:147], a[32:35], v[56:59], v[144:147]\n"
"v_mfma_f32_16x16x32_bf16 v[144:147], a[48:51], v[60:63], v[144:147]\n"
"buffer_load_dwordx4 v39, s[40:43], s74 offen offset:2048 lds\n"
"v_mfma_f32_16x16x32_bf16 v[148:151], a[36:39], v[56:59], v[148:151]\n"
"v_mfma_f32_16x16x32_bf16 v[148:151], a[52:55], v[60:63], v[148:151]\n"
"v_mfma_f32_16x16x32_bf16 v[152:155], a[40:43], v[56:59], v[152:155]\n"
"v_mfma_f32_16x16x32_bf16 v[152:155], a[56:59], v[60:63], v[152:155]\n"
"buffer_load_dwordx4 v40, s[40:43], s74 offen offset:3072 lds\n"
"v_mfma_f32_16x16x32_bf16 v[156:159], a[44:47], v[56:59], v[156:159]\n"
"ds_read_b128 v[56:59], v43 offset:32768\n"
"v_mfma_f32_16x16x32_bf16 v[156:159], a[60:63], v[60:63], v[156:159]\n"
"ds_read_b128 v[60:63], v43 offset:33280\n"
"v_mfma_f32_16x16x32_bf16 v[160:163], a[32:35], v[64:67], v[160:163]\n"
"s_add_u32 s96, s74, 0x40000\n"
"s_add_u32 m0, s30, 0x7000\n"
"v_mfma_f32_16x16x32_bf16 v[160:163], a[48:51], v[68:71], v[160:163]\n"
"buffer_load_dwordx4 v37, s[40:43], s96 offen offset:0 lds\n"
"v_mfma_f32_16x16x32_bf16 v[164:167], a[36:39], v[64:67], v[164:167]\n"
"v_mfma_f32_16x16x32_bf16 v[164:167], a[52:55], v[68:71], v[164:167]\n"
"v_mfma_f32_16x16x32_bf16 v[168:171], a[40:43], v[64:67], v[168:171]\n"
"v_mfma_f32_16x16x32_bf16 v[168:171], a[56:59], v[68:71], v[168:171]\n"
"s_add_u32 s73, s72, 9\n"
"buffer_load_dwordx4 v38, s[40:43], s96 offen offset:1024 lds\n"
"v_mfma_f32_16x16x32_bf16 v[172:175], a[44:47], v[64:67], v[172:175]\n"
"s_and_b32 s73, s73, 15\n"
"ds_read_b128 v[64:67], v43 offset:34816\n"
"v_mfma_f32_16x16x32_bf16 v[172:175], a[60:63], v[68:71], v[172:175]\n"
"s_waitcnt lgkmcnt(3)\n"
"s_lshl_b32 s73, s73, 7\n"
"ds_read_b128 v[68:71], v43 offset:35328\n"
"v_mfma_f32_16x16x32_bf16 v[176:179], a[32:35], v[72:75], v[176:179]\n"
"v_mfma_f32_16x16x32_bf16 v[176:179], a[48:51], v[76:79], v[176:179]\n"
"buffer_load_dwordx4 v39, s[40:43], s96 offen offset:2048 lds\n"
"v_mfma_f32_16x16x32_bf16 v[180:183], a[36:39], v[72:75], v[180:183]\n"
"v_mfma_f32_16x16x32_bf16 v[180:183], a[52:55], v[76:79], v[180:183]\n"
"s_waitcnt vmcnt(12)\n"
"ds_read_b64_tr_b16 a[16:17], v45 offset:20480\n"
"v_mfma_f32_16x16x32_bf16 v[184:187], a[40:43], v[72:75], v[184:187]\n"
"ds_read_b64_tr_b16 a[18:19], v45 offset:20736\n"
"v_mfma_f32_16x16x32_bf16 v[184:187], a[56:59], v[76:79], v[184:187]\n"
"buffer_load_dwordx4 v40, s[40:43], s96 offen offset:3072 lds\n"
"ds_read_b64_tr_b16 a[20:21], v46 offset:20480\n"
"v_mfma_f32_16x16x32_bf16 v[188:191], a[44:47], v[72:75], v[188:191]\n"
"ds_read_b128 v[72:75], v30 offset:1024\n"
"v_mfma_f32_16x16x32_bf16 v[188:191], a[60:63], v[76:79], v[188:191]\n"
"ds_read_b128 v[76:79], v30 offset:1536\n"
"v_mfma_f32_16x16x32_bf16 v[192:195], a[32:35], v[48:51], v[192:195]\n"
"s_add_u32 m0, s30, 0x2000\n"
"ds_read_b64_tr_b16 a[22:23], v46 offset:20736\n"
"v_mfma_f32_16x16x32_bf16 v[192:195], a[48:51], v[52:55], v[192:195]\n"
"buffer_load_dwordx4 v32, s[36:39], s73 offen offset:0 lds\n"
"ds_read_b64_tr_b16 a[24:25], v45 offset:20992\n"
"v_mfma_f32_16x16x32_bf16 v[196:199], a[36:39], v[48:51], v[196:199]\n"
"ds_read_b64_tr_b16 a[26:27], v45 offset:21248\n"
"v_mfma_f32_16x16x32_bf16 v[196:199], a[52:55], v[52:55], v[196:199]\n"
"ds_read_b64_tr_b16 a[28:29], v46 offset:20992\n"
"v_mfma_f32_16x16x32_bf16 v[200:203], a[40:43], v[48:51], v[200:203]\n"
"s_add_u32 s90, s72, 10\n"
"ds_read_b64_tr_b16 a[30:31], v46 offset:21248\n"
"v_mfma_f32_16x16x32_bf16 v[200:203], a[56:59], v[52:55], v[200:203]\n"
"s_and_b32 s90, s90, 15\n"
"buffer_load_dwordx4 v33, s[36:39], s73 offen offset:1024 lds\n"
"v_mfma_f32_16x16x32_bf16 v[204:207], a[44:47], v[48:51], v[204:207]\n"
"s_lshl_b32 s90, s90, 19\n"
"v_mfma_f32_16x16x32_bf16 v[204:207], a[60:63], v[52:55], v[204:207]\n"
"s_waitcnt lgkmcnt(8)\n"
"v_mfma_f32_16x16x32_bf16 v[208:211], a[32:35], v[56:59], v[208:211]\n"
"v_mfma_f32_16x16x32_bf16 v[208:211], a[48:51], v[60:63], v[208:211]\n"
"buffer_load_dwordx4 v34, s[36:39], s73 offen offset:2048 lds\n"
"v_mfma_f32_16x16x32_bf16 v[212:215], a[36:39], v[56:59], v[212:215]\n"
"v_mfma_f32_16x16x32_bf16 v[212:215], a[52:55], v[60:63], v[212:215]\n"
"v_mfma_f32_16x16x32_bf16 v[216:219], a[40:43], v[56:59], v[216:219]\n"
"v_mfma_f32_16x16x32_bf16 v[216:219], a[56:59], v[60:63], v[216:219]\n"
"s_getpc_b64 s[98:99]\n"
"buffer_load_dwordx4 v35, s[36:39], s73 offen offset:3072 lds\n"
"v_mfma_f32_16x16x32_bf16 v[220:223], a[44:47], v[56:59], v[220:223]\n"
"v_mfma_f32_16x16x32_bf16 v[220:223], a[60:63], v[60:63], v[220:223]\n"
"s_add_u32 m0, s30, 0x4000\n"
"v_mfma_f32_16x16x32_bf16 v[224:227], a[32:35], v[64:67], v[224:227]\n"
"buffer_load_dwordx4 v37, s[40:43], s90 offen offset:0 lds\n"
"v_mfma_f32_16x16x32_bf16 v[224:227], a[48:51], v[68:71], v[224:227]\n"
"s_waitcnt vmcnt(13) lgkmcnt(0)\n"
"s_barrier\n"
"global_load_dword v26, v0, s[98:99] offset:1536\n"
"v_mfma_f32_16x16x32_bf16 v[228:231], a[36:39], v[64:67], v[228:231]\n"
"ds_read_b128 v[48:51], v27 offset:12288\n"
"ds_read_b128 v[52:55], v27 offset:12800\n"
"v_mfma_f32_16x16x32_bf16 v[228:231], a[52:55], v[68:71], v[228:231]\n"
"ds_read_b128 v[56:59], v27 offset:14336\n"
"ds_read_b128 v[60:63], v27 offset:14848\n"
"v_mfma_f32_16x16x32_bf16 v[232:235], a[40:43], v[64:67], v[232:235]\n"
"buffer_load_dwordx4 v38, s[40:43], s90 offen offset:1024 lds\n"
"v_mfma_f32_16x16x32_bf16 v[232:235], a[56:59], v[68:71], v[232:235]\n"
"v_mfma_f32_16x16x32_bf16 v[236:239], a[44:47], v[64:67], v[236:239]\n"
"ds_read_b128 v[64:67], v41 offset:12288\n"
"v_mfma_f32_16x16x32_bf16 v[236:239], a[60:63], v[68:71], v[236:239]\n"
"ds_read_b128 v[68:71], v41 offset:12800\n"
"v_mfma_f32_16x16x32_bf16 v[240:243], a[32:35], v[72:75], v[240:243]\n"
"buffer_load_dwordx4 v39, s[40:43], s90 offen offset:2048 lds\n"
"v_mfma_f32_16x16x32_bf16 v[240:243], a[48:51], v[76:79], v[240:243]\n"
"v_mfma_f32_16x16x32_bf16 v[244:247], a[36:39], v[72:75], v[244:247]\n"
"buffer_load_dwordx4 a[128:131], v19, s[48:51], s76 offen offset:0 nt\n"
"v_mfma_f32_16x16x32_bf16 v[244:247], a[52:55], v[76:79], v[244:247]\n"
"v_mfma_f32_16x16x32_bf16 v[248:251], a[40:43], v[72:75], v[248:251]\n"
"v_mfma_f32_16x16x32_bf16 v[248:251], a[56:59], v[76:79], v[248:251]\n"
"v_mfma_f32_16x16x32_bf16 v[252:255], a[44:47], v[72:75], v[252:255]\n"
"buffer_load_dwordx4 a[132:135], v20, s[48:51], s76 offen offset:1024 nt\n"
"ds_read_b128 v[72:75], v41 offset:14336\n"
"v_mfma_f32_16x16x32_bf16 v[252:255], a[60:63], v[76:79], v[252:255]\n"
"s_waitcnt lgkmcnt(3)\n"
"ds_read_b128 v[76:79], v41 offset:14848\n"
"v_mfma_f32_16x16x32_bf16 v[112:115], a[0:3], v[48:51], v[112:115]\n"
"s_waitcnt vmcnt(14)\n"
"ds_read_b64_tr_b16 a[32:33], v45 offset:24576\n"
"v_mfma_f32_16x16x32_bf16 v[112:115], a[16:19], v[52:55], v[112:115]\n"
"ds_read_b64_tr_b16 a[34:35], v45 offset:24832\n"
"v_mfma_f32_16x16x32_bf16 v[116:119], a[4:7], v[48:51], v[116:119]\n"
"buffer_load_dwordx4 a[136:139], v19, s[48:51], s76 offen offset:2048 nt\n"
"ds_read_b64_tr_b16 a[36:37], v46 offset:24576\n"
"v_mfma_f32_16x16x32_bf16 v[116:119], a[20:23], v[52:55], v[116:119]\n"
"ds_read_b64_tr_b16 a[38:39], v46 offset:24832\n"
"v_mfma_f32_16x16x32_bf16 v[120:123], a[8:11], v[48:51], v[120:123]\n"
"ds_read_b64_tr_b16 a[40:41], v45 offset:25088\n"
"v_mfma_f32_16x16x32_bf16 v[120:123], a[24:27], v[52:55], v[120:123]\n"
"ds_read_b64_tr_b16 a[42:43], v45 offset:25344\n"
"v_mfma_f32_16x16x32_bf16 v[124:127], a[12:15], v[48:51], v[124:127]\n"
"buffer_load_dwordx4 a[140:143], v20, s[48:51], s76 offen offset:3072 nt\n"
"ds_read_b128 v[48:51], v42 offset:12288\n"
"v_mfma_f32_16x16x32_bf16 v[124:127], a[28:31], v[52:55], v[124:127]\n"
"ds_read_b128 v[52:55], v42 offset:12800\n"
"v_mfma_f32_16x16x32_bf16 v[128:131], a[0:3], v[56:59], v[128:131]\n"
"ds_read_b64_tr_b16 a[44:45], v46 offset:25088\n"
"v_mfma_f32_16x16x32_bf16 v[128:131], a[16:19], v[60:63], v[128:131]\n"
"s_mov_b32 s75, 0xc000\n"
"ds_read_b64_tr_b16 a[46:47], v46 offset:25344\n"
"v_mfma_f32_16x16x32_bf16 v[132:135], a[4:7], v[56:59], v[132:135]\n"
"buffer_load_dwordx4 a[144:147], v17, s[44:47], s75 offen offset:0 nt\n"
"v_mfma_f32_16x16x32_bf16 v[132:135], a[20:23], v[60:63], v[132:135]\n"
"v_mfma_f32_16x16x32_bf16 v[136:139], a[8:11], v[56:59], v[136:139]\n"
"v_mfma_f32_16x16x32_bf16 v[136:139], a[24:27], v[60:63], v[136:139]\n"
"v_mfma_f32_16x16x32_bf16 v[140:143], a[12:15], v[56:59], v[140:143]\n"
"buffer_load_dwordx4 a[148:151], v17, s[44:47], s75 offen offset:1024 nt\n"
"ds_read_b128 v[56:59], v42 offset:14336\n"
"v_mfma_f32_16x16x32_bf16 v[140:143], a[28:31], v[60:63], v[140:143]\n"
"s_waitcnt lgkmcnt(6)\n"
"ds_read_b128 v[60:63], v42 offset:14848\n"
"v_mfma_f32_16x16x32_bf16 v[144:147], a[0:3], v[64:67], v[144:147]\n"
"s_waitcnt vmcnt(14)\n"
"ds_read_b64_tr_b16 a[48:49], v45 offset:28672\n"
"v_mfma_f32_16x16x32_bf16 v[144:147], a[16:19], v[68:71], v[144:147]\n"
"ds_read_b64_tr_b16 a[50:51], v45 offset:28928\n"
"v_mfma_f32_16x16x32_bf16 v[148:151], a[4:7], v[64:67], v[148:151]\n"
"buffer_load_dwordx4 a[152:155], v18, s[44:47], s75 offen offset:2048 nt\n"
"ds_read_b64_tr_b16 a[52:53], v46 offset:28672\n"
"v_mfma_f32_16x16x32_bf16 v[148:151], a[20:23], v[68:71], v[148:151]\n"
"ds_read_b64_tr_b16 a[54:55], v46 offset:28928\n"
"v_mfma_f32_16x16x32_bf16 v[152:155], a[8:11], v[64:67], v[152:155]\n"
"ds_read_b64_tr_b16 a[56:57], v45 offset:29184\n"
"v_mfma_f32_16x16x32_bf16 v[152:155], a[24:27], v[68:71], v[152:155]\n"
"ds_read_b64_tr_b16 a[58:59], v45 offset:29440\n"
"v_mfma_f32_16x16x32_bf16 v[156:159], a[12:15], v[64:67], v[156:159]\n"
"buffer_load_dwordx4 a[156:159], v18, s[44:47], s75 offen offset:3072 nt\n"
"ds_read_b128 v[64:67], v43 offset:12288\n"
"v_mfma_f32_16x16x32_bf16 v[156:159], a[28:31], v[68:71], v[156:159]\n"
"ds_read_b128 v[68:71], v43 offset:12800\n"
"v_mfma_f32_16x16x32_bf16 v[160:163], a[0:3], v[72:75], v[160:163]\n"
"ds_read_b64_tr_b16 a[60:61], v46 offset:29184\n"
"v_mfma_f32_16x16x32_bf16 v[160:163], a[16:19], v[76:79], v[160:163]\n"
"ds_read_b64_tr_b16 a[62:63], v46 offset:29440\n"
"v_mfma_f32_16x16x32_bf16 v[164:167], a[4:7], v[72:75], v[164:167]\n"
"v_mfma_f32_16x16x32_bf16 v[164:167], a[20:23], v[76:79], v[164:167]\n"
"v_mfma_f32_16x16x32_bf16 v[168:171], a[8:11], v[72:75], v[168:171]\n"
"v_mfma_f32_16x16x32_bf16 v[168:171], a[24:27], v[76:79], v[168:171]\n"
"v_mfma_f32_16x16x32_bf16 v[172:175], a[12:15], v[72:75], v[172:175]\n"
"ds_read_b128 v[72:75], v43 offset:14336\n"
"v_mfma_f32_16x16x32_bf16 v[172:175], a[28:31], v[76:79], v[172:175]\n"
"s_waitcnt lgkmcnt(6)\n"
"ds_read_b128 v[76:79], v43 offset:14848\n"
"v_mfma_f32_16x16x32_bf16 v[176:179], a[0:3], v[48:51], v[176:179]\n"
"v_mfma_f32_16x16x32_bf16 v[176:179], a[16:19], v[52:55], v[176:179]\n"
"v_mfma_f32_16x16x32_bf16 v[180:183], a[4:7], v[48:51], v[180:183]\n"
"v_mfma_f32_16x16x32_bf16 v[180:183], a[20:23], v[52:55], v[180:183]\n"
"v_mfma_f32_16x16x32_bf16 v[184:187], a[8:11], v[48:51], v[184:187]\n"
"v_mfma_f32_16x16x32_bf16 v[184:187], a[24:27], v[52:55], v[184:187]\n"
"v_mfma_f32_16x16x32_bf16 v[188:191], a[12:15], v[48:51], v[188:191]\n"
"ds_read_b128 v[48:51], v44 offset:0\n"
"v_mfma_f32_16x16x32_bf16 v[188:191], a[28:31], v[52:55], v[188:191]\n"
"ds_read_b128 v[52:55], v44 offset:512\n"
"v_mfma_f32_16x16x32_bf16 v[192:195], a[0:3], v[56:59], v[192:195]\n"
"v_mfma_f32_16x16x32_bf16 v[192:195], a[16:19], v[60:63], v[192:195]\n"
"v_mfma_f32_16x16x32_bf16 v[196:199], a[4:7], v[56:59], v[196:199]\n"
"v_mfma_f32_16x16x32_bf16 v[196:199], a[20:23], v[60:63], v[196:199]\n"
"v_mfma_f32_16x16x32_bf16 v[200:203], a[8:11], v[56:59], v[200:203]\n"
"v_mfma_f32_16x16x32_bf16 v[200:203], a[24:27], v[60:63], v[200:203]\n"
"v_mfma_f32_16x16x32_bf16 v[204:207], a[12:15], v[56:59], v[204:207]\n"
"buffer_load_dwordx4 v40, s[40:43], s90 offen offset:3072 lds\n"
"v_mfma_f32_16x16x32_bf16 v[204:207], a[28:31], v[60:63], v[204:207]\n"
"s_waitcnt lgkmcnt(2)\n"
"v_mfma_f32_16x16x32_bf16 v[208:211], a[0:3], v[64:67], v[208:211]\n"
"v_mfma_f32_16x16x32_bf16 v[208:211], a[16:19], v[68:71], v[208:211]\n"
"s_add_u32 s77, s90, 0x40000\n"
"s_add_u32 m0, s30, 0x5000\n"
"v_mfma_f32_16x16x32_bf16 v[212:215], a[4:7], v[64:67], v[212:215]\n"
"buffer_load_dwordx4 v37, s[40:43], s77 offen offset:0 lds\n"
"v_mfma_f32_16x16x32_bf16 v[212:215], a[20:23], v[68:71], v[212:215]\n"
"v_mfma_f32_16x16x32_bf16 v[216:219], a[8:11], v[64:67], v[216:219]\n"
"v_mfma_f32_16x16x32_bf16 v[216:219], a[24:27], v[68:71], v[216:219]\n"
"v_mfma_f32_16x16x32_bf16 v[220:223], a[12:15], v[64:67], v[220:223]\n"
"s_add_u32 s88, s72, 10\n"
"buffer_load_dwordx4 v38, s[40:43], s77 offen offset:1024 lds\n"
"v_mfma_f32_16x16x32_bf16 v[220:223], a[28:31], v[68:71], v[220:223]\n"
"s_and_b32 s88, s88, 15\n"
"v_mfma_f32_16x16x32_bf16 v[224:227], a[0:3], v[72:75], v[224:227]\n"
"s_lshl_b32 s88, s88, 7\n"
"v_mfma_f32_16x16x32_bf16 v[224:227], a[16:19], v[76:79], v[224:227]\n"
"s_waitcnt vmcnt(15) lgkmcnt(0)\n"
"s_barrier\n"
"v_mfma_f32_16x16x32_bf16 v[228:231], a[4:7], v[72:75], v[228:231]\n"
"buffer_load_dwordx4 v39, s[40:43], s77 offen offset:2048 lds\n"
"ds_read_b128 v[56:59], v27 offset:8192\n"
"v_mfma_f32_16x16x32_bf16 v[228:231], a[20:23], v[76:79], v[228:231]\n"
"ds_read_b128 v[60:63], v27 offset:8704\n"
"ds_read_b128 v[64:67], v27 offset:10240\n"
"v_mfma_f32_16x16x32_bf16 v[232:235], a[8:11], v[72:75], v[232:235]\n"
"ds_read_b128 v[68:71], v27 offset:10752\n"
"v_mfma_f32_16x16x32_bf16 v[232:235], a[24:27], v[76:79], v[232:235]\n"
"v_mfma_f32_16x16x32_bf16 v[236:239], a[12:15], v[72:75], v[236:239]\n"
"buffer_load_dwordx4 v40, s[40:43], s77 offen offset:3072 lds\n"
"ds_read_b128 v[72:75], v41 offset:8192\n"
"v_mfma_f32_16x16x32_bf16 v[236:239], a[28:31], v[76:79], v[236:239]\n"
"ds_read_b128 v[76:79], v41 offset:8704\n"
"v_mfma_f32_16x16x32_bf16 v[240:243], a[0:3], v[48:51], v[240:243]\n"
"v_mfma_f32_16x16x32_bf16 v[240:243], a[16:19], v[52:55], v[240:243]\n"
"s_add_u32 m0, s30, 0x8000\n"
"v_mfma_f32_16x16x32_bf16 v[244:247], a[4:7], v[48:51], v[244:247]\n"
"buffer_load_dwordx4 v32, s[36:39], s88 offen offset:0 lds\n"
"v_mfma_f32_16x16x32_bf16 v[244:247], a[20:23], v[52:55], v[244:247]\n"
"v_mfma_f32_16x16x32_bf16 v[248:251], a[8:11], v[48:51], v[248:251]\n"
"v_mfma_f32_16x16x32_bf16 v[248:251], a[24:27], v[52:55], v[248:251]\n"
"v_mfma_f32_16x16x32_bf16 v[252:255], a[12:15], v[48:51], v[252:255]\n"
"buffer_load_dwordx4 v33, s[36:39], s88 offen offset:1024 lds\n"
"ds_read_b128 v[48:51], v41 offset:10240\n"
"v_mfma_f32_16x16x32_bf16 v[252:255], a[28:31], v[52:55], v[252:255]\n"
"s_waitcnt lgkmcnt(3)\n"
"ds_read_b128 v[52:55], v41 offset:10752\n"
"v_mfma_f32_16x16x32_bf16 v[112:115], a[32:35], v[56:59], v[112:115]\n"
"v_mfma_f32_16x16x32_bf16 v[112:115], a[48:51], v[60:63], v[112:115]\n"
"v_mfma_f32_16x16x32_bf16 v[116:119], a[36:39], v[56:59], v[116:119]\n"
"s_add_u32 s91, s72, 11\n"
"buffer_load_dwordx4 v34, s[36:39], s88 offen offset:2048 lds\n"
"v_mfma_f32_16x16x32_bf16 v[116:119], a[52:55], v[60:63], v[116:119]\n"
"s_and_b32 s91, s91, 15\n"
"v_mfma_f32_16x16x32_bf16 v[120:123], a[40:43], v[56:59], v[120:123]\n"
"s_lshl_b32 s91, s91, 19\n"
"v_mfma_f32_16x16x32_bf16 v[120:123], a[56:59], v[60:63], v[120:123]\n"
"v_mfma_f32_16x16x32_bf16 v[124:127], a[44:47], v[56:59], v[124:127]\n"
"buffer_load_dwordx4 v35, s[36:39], s88 offen offset:3072 lds\n"
"ds_read_b128 v[56:59], v42 offset:8192\n"
"v_mfma_f32_16x16x32_bf16 v[124:127], a[60:63], v[60:63], v[124:127]\n"
"ds_read_b128 v[60:63], v42 offset:8704\n"
"v_mfma_f32_16x16x32_bf16 v[128:131], a[32:35], v[64:67], v[128:131]\n"
"v_mfma_f32_16x16x32_bf16 v[128:131], a[48:51], v[68:71], v[128:131]\n"
"s_add_u32 m0, s30, 0x9400\n"
"v_mfma_f32_16x16x32_bf16 v[132:135], a[36:39], v[64:67], v[132:135]\n"
"buffer_load_dwordx4 v36, s[36:39], s88 offen offset:0 lds\n"
"v_mfma_f32_16x16x32_bf16 v[132:135], a[52:55], v[68:71], v[132:135]\n"
"v_mfma_f32_16x16x32_bf16 v[136:139], a[40:43], v[64:67], v[136:139]\n"
"v_mfma_f32_16x16x32_bf16 v[136:139], a[56:59], v[68:71], v[136:139]\n"
"s_add_u32 m0, s30, 0x6000\n"
"v_mfma_f32_16x16x32_bf16 v[140:143], a[44:47], v[64:67], v[140:143]\n"
"buffer_load_dwordx4 v37, s[40:43], s91 offen offset:0 lds\n"
"ds_read_b128 v[64:67], v42 offset:10240\n"
"v_mfma_f32_16x16x32_bf16 v[140:143], a[60:63], v[68:71], v[140:143]\n"
"s_waitcnt lgkmcnt(3)\n"
"ds_read_b128 v[68:71], v42 offset:10752\n"
"v_mfma_f32_16x16x32_bf16 v[144:147], a[32:35], v[72:75], v[144:147]\n"
"v_mfma_f32_16x16x32_bf16 v[144:147], a[48:51], v[76:79], v[144:147]\n"
"v_mfma_f32_16x16x32_bf16 v[148:151], a[36:39], v[72:75], v[148:151]\n"
"buffer_load_dwordx4 v38, s[40:43], s91 offen offset:1024 lds\n"
"v_mfma_f32_16x16x32_bf16 v[148:151], a[52:55], v[76:79], v[148:151]\n"
"v_mfma_f32_16x16x32_bf16 v[152:155], a[40:43], v[72:75], v[152:155]\n"
"v_mfma_f32_16x16x32_bf16 v[152:155], a[56:59], v[76:79], v[152:155]\n"
"v_mfma_f32_16x16x32_bf16 v[156:159], a[44:47], v[72:75], v[156:159]\n"
"buffer_load_dwordx4 v39, s[40:43], s91 offen offset:2048 lds\n"
"ds_read_b128 v[72:75], v43 offset:8192\n"
"v_mfma_f32_16x16x32_bf16 v[156:159], a[60:63], v[76:79], v[156:159]\n"
"ds_read_b128 v[76:79], v43 offset:8704\n"
"v_mfma_f32_16x16x32_bf16 v[160:163], a[32:35], v[48:51], v[160:163]\n"
"s_waitcnt vmcnt(12)\n"
"ds_read_b64_tr_b16 a[0:1], v45 offset:16384\n"
"v_mfma_f32_16x16x32_bf16 v[160:163], a[48:51], v[52:55], v[160:163]\n"
"ds_read_b64_tr_b16 a[2:3], v45 offset:16640\n"
"v_mfma_f32_16x16x32_bf16 v[164:167], a[36:39], v[48:51], v[164:167]\n"
"buffer_load_dwordx4 v40, s[40:43], s91 offen offset:3072 lds\n"
"ds_read_b64_tr_b16 a[4:5], v46 offset:16384\n"
"v_mfma_f32_16x16x32_bf16 v[164:167], a[52:55], v[52:55], v[164:167]\n"
"ds_read_b64_tr_b16 a[6:7], v46 offset:16640\n"
"v_mfma_f32_16x16x32_bf16 v[168:171], a[40:43], v[48:51], v[168:171]\n"
"ds_read_b64_tr_b16 a[8:9], v45 offset:16896\n"
"v_mfma_f32_16x16x32_bf16 v[168:171], a[56:59], v[52:55], v[168:171]\n"
"s_add_u32 s78, s91, 0x40000\n"
"s_add_u32 m0, s30, 0x7000\n"
"ds_read_b64_tr_b16 a[10:11], v45 offset:17152\n"
"v_mfma_f32_16x16x32_bf16 v[172:175], a[44:47], v[48:51], v[172:175]\n"
"buffer_load_dwordx4 v37, s[40:43], s78 offen offset:0 lds\n"
"ds_read_b128 v[48:51], v43 offset:10240\n"
"v_mfma_f32_16x16x32_bf16 v[172:175], a[60:63], v[52:55], v[172:175]\n"
"s_waitcnt lgkmcnt(9)\n"
"ds_read_b128 v[52:55], v43 offset:10752\n"
"v_mfma_f32_16x16x32_bf16 v[176:179], a[32:35], v[56:59], v[176:179]\n"
"ds_read_b64_tr_b16 a[12:13], v46 offset:16896\n"
"v_mfma_f32_16x16x32_bf16 v[176:179], a[48:51], v[60:63], v[176:179]\n"
"ds_read_b64_tr_b16 a[14:15], v46 offset:17152\n"
"v_mfma_f32_16x16x32_bf16 v[180:183], a[36:39], v[56:59], v[180:183]\n"
"s_add_u32 s89, s72, 11\n"
"buffer_load_dwordx4 v38, s[40:43], s78 offen offset:1024 lds\n"
"v_mfma_f32_16x16x32_bf16 v[180:183], a[52:55], v[60:63], v[180:183]\n"
"s_and_b32 s89, s89, 15\n"
"v_mfma_f32_16x16x32_bf16 v[184:187], a[40:43], v[56:59], v[184:187]\n"
"s_lshl_b32 s89, s89, 7\n"
"v_mfma_f32_16x16x32_bf16 v[184:187], a[56:59], v[60:63], v[184:187]\n"
"v_mfma_f32_16x16x32_bf16 v[188:191], a[44:47], v[56:59], v[188:191]\n"
"buffer_load_dwordx4 v39, s[40:43], s78 offen offset:2048 lds\n"
"ds_read_b128 v[56:59], v30 offset:0\n"
"v_mfma_f32_16x16x32_bf16 v[188:191], a[60:63], v[60:63], v[188:191]\n"
"ds_read_b128 v[60:63], v30 offset:512\n"
"v_mfma_f32_16x16x32_bf16 v[192:195], a[32:35], v[64:67], v[192:195]\n"
"s_waitcnt vmcnt(12)\n"
"ds_read_b64_tr_b16 a[16:17], v45 offset:20480\n"
"v_mfma_f32_16x16x32_bf16 v[192:195], a[48:51], v[68:71], v[192:195]\n"
"ds_read_b64_tr_b16 a[18:19], v45 offset:20736\n"
"v_mfma_f32_16x16x32_bf16 v[196:199], a[36:39], v[64:67], v[196:199]\n"
"buffer_load_dwordx4 v40, s[40:43], s78 offen offset:3072 lds\n"
"ds_read_b64_tr_b16 a[20:21], v46 offset:20480\n"
"v_mfma_f32_16x16x32_bf16 v[196:199], a[52:55], v[68:71], v[196:199]\n"
"ds_read_b64_tr_b16 a[22:23], v46 offset:20736\n"
"v_mfma_f32_16x16x32_bf16 v[200:203], a[40:43], v[64:67], v[200:203]\n"
"ds_read_b64_tr_b16 a[24:25], v45 offset:20992\n"
"v_mfma_f32_16x16x32_bf16 v[200:203], a[56:59], v[68:71], v[200:203]\n"
"s_add_u32 m0, s30, 0x3000\n"
"ds_read_b64_tr_b16 a[26:27], v45 offset:21248\n"
"v_mfma_f32_16x16x32_bf16 v[204:207], a[44:47], v[64:67], v[204:207]\n"
"buffer_load_dwordx4 v32, s[36:39], s89 offen offset:0 lds\n"
"ds_read_b64_tr_b16 a[28:29], v46 offset:20992\n"
"v_mfma_f32_16x16x32_bf16 v[204:207], a[60:63], v[68:71], v[204:207]\n"
"s_waitcnt lgkmcnt(9)\n"
"ds_read_b64_tr_b16 a[30:31], v46 offset:21248\n"
"v_mfma_f32_16x16x32_bf16 v[208:211], a[32:35], v[72:75], v[208:211]\n"
"v_mfma_f32_16x16x32_bf16 v[208:211], a[48:51], v[76:79], v[208:211]\n"
"s_add_u32 s74, s72, 12\n"
"v_mfma_f32_16x16x32_bf16 v[212:215], a[36:39], v[72:75], v[212:215]\n"
"s_and_b32 s74, s74, 15\n"
"buffer_load_dwordx4 v33, s[36:39], s89 offen offset:1024 lds\n"
"v_mfma_f32_16x16x32_bf16 v[212:215], a[52:55], v[76:79], v[212:215]\n"
"s_lshl_b32 s74, s74, 19\n"
"v_mfma_f32_16x16x32_bf16 v[216:219], a[40:43], v[72:75], v[216:219]\n"
"v_mfma_f32_16x16x32_bf16 v[216:219], a[56:59], v[76:79], v[216:219]\n"
"v_mfma_f32_16x16x32_bf16 v[220:223], a[44:47], v[72:75], v[220:223]\n"
"buffer_load_dwordx4 v34, s[36:39], s89 offen offset:2048 lds\n"
"v_mfma_f32_16x16x32_bf16 v[220:223], a[60:63], v[76:79], v[220:223]\n"
"v_mfma_f32_16x16x32_bf16 v[224:227], a[32:35], v[48:51], v[224:227]\n"
"v_mfma_f32_16x16x32_bf16 v[224:227], a[48:51], v[52:55], v[224:227]\n"
"buffer_load_dwordx4 v35, s[36:39], s89 offen offset:3072 lds\n"
"v_mfma_f32_16x16x32_bf16 v[228:231], a[36:39], v[48:51], v[228:231]\n"
"v_mfma_f32_16x16x32_bf16 v[228:231], a[52:55], v[52:55], v[228:231]\n"
"v_mfma_f32_16x16x32_bf16 v[232:235], a[40:43], v[48:51], v[232:235]\n"
"s_add_u32 m0, s30, 0x4000\n"
"v_mfma_f32_16x16x32_bf16 v[232:235], a[56:59], v[52:55], v[232:235]\n"
"s_waitcnt vmcnt(12) lgkmcnt(0)\n"
"s_barrier\n"
"buffer_load_dwordx4 v37, s[40:43], s74 offen offset:0 lds\n"
"v_mfma_f32_16x16x32_bf16 v[236:239], a[44:47], v[48:51], v[236:239]\n"
"ds_read_b128 v[64:67], v27 offset:32768\n"
"ds_read_b128 v[68:71], v27 offset:33280\n"
"v_mfma_f32_16x16x32_bf16 v[236:239], a[60:63], v[52:55], v[236:239]\n"
"ds_read_b128 v[72:75], v27 offset:34816\n"
"ds_read_b128 v[76:79], v27 offset:35328\n"
"v_mfma_f32_16x16x32_bf16 v[240:243], a[32:35], v[56:59], v[240:243]\n"
"ds_read_b128 v[48:51], v41 offset:32768\n"
"v_mfma_f32_16x16x32_bf16 v[240:243], a[48:51], v[60:63], v[240:243]\n"
"buffer_load_dwordx4 v38, s[40:43], s74 offen offset:1024 lds\n"
"ds_read_b128 v[52:55], v41 offset:33280\n"
"v_mfma_f32_16x16x32_bf16 v[244:247], a[36:39], v[56:59], v[244:247]\n"
"v_mfma_f32_16x16x32_bf16 v[244:247], a[52:55], v[60:63], v[244:247]\n"
"buffer_load_dword v26, v22, s[52:55], s31 offen offset:0\n"
"v_mfma_f32_16x16x32_bf16 v[248:251], a[40:43], v[56:59], v[248:251]\n"
"v_mfma_f32_16x16x32_bf16 v[248:251], a[56:59], v[60:63], v[248:251]\n"
"v_mfma_f32_16x16x32_bf16 v[252:255], a[44:47], v[56:59], v[252:255]\n"
"ds_read_b128 v[56:59], v41 offset:34816\n"
"v_mfma_f32_16x16x32_bf16 v[252:255], a[60:63], v[60:63], v[252:255]\n"
"s_waitcnt lgkmcnt(6)\n"
"buffer_load_dword v26, v22, s[52:55], s31 offen offset:1024\n"
"ds_read_b128 v[60:63], v41 offset:35328\n"
"v_mfma_f32_16x16x32_bf16 v[112:115], a[0:3], v[64:67], v[112:115]\n"
"s_waitcnt lgkmcnt(6)\n"
"v_mfma_f32_16x16x32_bf16 v[112:115], a[16:19], v[68:71], v[112:115]\n"
"v_mfma_f32_16x16x32_bf16 v[116:119], a[4:7], v[64:67], v[116:119]\n"
"s_add_u32 s84, s31, 0x200000\n"
"v_mfma_f32_16x16x32_bf16 v[116:119], a[20:23], v[68:71], v[116:119]\n"
"buffer_load_dword v26, v22, s[52:55], s84 offen offset:0\n"
"v_mfma_f32_16x16x32_bf16 v[120:123], a[8:11], v[64:67], v[120:123]\n"
"s_waitcnt vmcnt(13)\n"
"ds_read_b64_tr_b16 a[32:33], v45 offset:24576\n"
"v_mfma_f32_16x16x32_bf16 v[120:123], a[24:27], v[68:71], v[120:123]\n"
"ds_read_b64_tr_b16 a[34:35], v45 offset:24832\n"
"v_mfma_f32_16x16x32_bf16 v[124:127], a[12:15], v[64:67], v[124:127]\n"
"ds_read_b128 v[64:67], v42 offset:32768\n"
"v_mfma_f32_16x16x32_bf16 v[124:127], a[28:31], v[68:71], v[124:127]\n"
"s_waitcnt lgkmcnt(5)\n"
"buffer_load_dword v26, v22, s[52:55], s84 offen offset:1024\n"
"ds_read_b128 v[68:71], v42 offset:33280\n"
"v_mfma_f32_16x16x32_bf16 v[128:131], a[0:3], v[72:75], v[128:131]\n"
"ds_read_b64_tr_b16 a[36:37], v46 offset:24576\n"
"v_mfma_f32_16x16x32_bf16 v[128:131], a[16:19], v[76:79], v[128:131]\n"
"ds_read_b64_tr_b16 a[38:39], v46 offset:24832\n"
"v_mfma_f32_16x16x32_bf16 v[132:135], a[4:7], v[72:75], v[132:135]\n"
"s_add_u32 s96, s31, 0x40000\n"
"ds_read_b64_tr_b16 a[40:41], v45 offset:25088\n"
"v_mfma_f32_16x16x32_bf16 v[132:135], a[20:23], v[76:79], v[132:135]\n"
"buffer_load_dword v26, v22, s[52:55], s96 offen offset:0\n"
"ds_read_b64_tr_b16 a[42:43], v45 offset:25344\n"
"v_mfma_f32_16x16x32_bf16 v[136:139], a[8:11], v[72:75], v[136:139]\n"
"ds_read_b64_tr_b16 a[44:45], v46 offset:25088\n"
"v_mfma_f32_16x16x32_bf16 v[136:139], a[24:27], v[76:79], v[136:139]\n"
"ds_read_b64_tr_b16 a[46:47], v46 offset:25344\n"
"v_mfma_f32_16x16x32_bf16 v[140:143], a[12:15], v[72:75], v[140:143]\n"
"ds_read_b128 v[72:75], v42 offset:34816\n"
"v_mfma_f32_16x16x32_bf16 v[140:143], a[28:31], v[76:79], v[140:143]\n"
"buffer_load_dword v26, v22, s[52:55], s96 offen offset:1024\n"
"ds_read_b128 v[76:79], v42 offset:35328\n"
"v_mfma_f32_16x16x32_bf16 v[144:147], a[0:3], v[48:51], v[144:147]\n"
"v_mfma_f32_16x16x32_bf16 v[144:147], a[16:19], v[52:55], v[144:147]\n"
"v_mfma_f32_16x16x32_bf16 v[148:151], a[4:7], v[48:51], v[148:151]\n"
"s_add_u32 s76, s31, 0x240000\n"
"v_mfma_f32_16x16x32_bf16 v[148:151], a[20:23], v[52:55], v[148:151]\n"
"buffer_load_dword v26, v22, s[52:55], s76 offen offset:0\n"
"v_mfma_f32_16x16x32_bf16 v[152:155], a[8:11], v[48:51], v[152:155]\n"
"s_waitcnt vmcnt(13)\n"
"ds_read_b64_tr_b16 a[48:49], v45 offset:28672\n"
"v_mfma_f32_16x16x32_bf16 v[152:155], a[24:27], v[52:55], v[152:155]\n"
"ds_read_b64_tr_b16 a[50:51], v45 offset:28928\n"
"v_mfma_f32_16x16x32_bf16 v[156:159], a[12:15], v[48:51], v[156:159]\n"
"ds_read_b128 v[48:51], v43 offset:32768\n"
"v_mfma_f32_16x16x32_bf16 v[156:159], a[28:31], v[52:55], v[156:159]\n"
"s_waitcnt lgkmcnt(6)\n"
"buffer_load_dword v26, v22, s[52:55], s76 offen offset:1024\n"
"ds_read_b128 v[52:55], v43 offset:33280\n"
"v_mfma_f32_16x16x32_bf16 v[160:163], a[0:3], v[56:59], v[160:163]\n"
"ds_read_b64_tr_b16 a[52:53], v46 offset:28672\n"
"v_mfma_f32_16x16x32_bf16 v[160:163], a[16:19], v[60:63], v[160:163]\n"
"ds_read_b64_tr_b16 a[54:55], v46 offset:28928\n"
"v_mfma_f32_16x16x32_bf16 v[164:167], a[4:7], v[56:59], v[164:167]\n"
"ds_read_b64_tr_b16 a[56:57], v45 offset:29184\n"
"v_mfma_f32_16x16x32_bf16 v[164:167], a[20:23], v[60:63], v[164:167]\n"
"ds_read_b64_tr_b16 a[58:59], v45 offset:29440\n"
"v_mfma_f32_16x16x32_bf16 v[168:171], a[8:11], v[56:59], v[168:171]\n"
"ds_read_b64_tr_b16 a[60:61], v46 offset:29184\n"
"v_mfma_f32_16x16x32_bf16 v[168:171], a[24:27], v[60:63], v[168:171]\n"
"ds_read_b64_tr_b16 a[62:63], v46 offset:29440\n"
"v_mfma_f32_16x16x32_bf16 v[172:175], a[12:15], v[56:59], v[172:175]\n"
"ds_read_b128 v[56:59], v43 offset:34816\n"
"v_mfma_f32_16x16x32_bf16 v[172:175], a[28:31], v[60:63], v[172:175]\n"
"ds_read_b128 v[60:63], v43 offset:35328\n"
"v_mfma_f32_16x16x32_bf16 v[176:179], a[0:3], v[64:67], v[176:179]\n"
"v_mfma_f32_16x16x32_bf16 v[176:179], a[16:19], v[68:71], v[176:179]\n"
"v_mfma_f32_16x16x32_bf16 v[180:183], a[4:7], v[64:67], v[180:183]\n"
"v_mfma_f32_16x16x32_bf16 v[180:183], a[20:23], v[68:71], v[180:183]\n"
"v_mfma_f32_16x16x32_bf16 v[184:187], a[8:11], v[64:67], v[184:187]\n"
"v_mfma_f32_16x16x32_bf16 v[184:187], a[24:27], v[68:71], v[184:187]\n"
"v_mfma_f32_16x16x32_bf16 v[188:191], a[12:15], v[64:67], v[188:191]\n"
"ds_read_b128 v[64:67], v44 offset:1024\n"
"v_mfma_f32_16x16x32_bf16 v[188:191], a[28:31], v[68:71], v[188:191]\n"
"s_waitcnt lgkmcnt(4)\n"
"ds_read_b128 v[68:71], v44 offset:1536\n"
"v_mfma_f32_16x16x32_bf16 v[192:195], a[0:3], v[72:75], v[192:195]\n"
"v_mfma_f32_16x16x32_bf16 v[192:195], a[16:19], v[76:79], v[192:195]\n"
"v_mfma_f32_16x16x32_bf16 v[196:199], a[4:7], v[72:75], v[196:199]\n"
"v_mfma_f32_16x16x32_bf16 v[196:199], a[20:23], v[76:79], v[196:199]\n"
"v_mfma_f32_16x16x32_bf16 v[200:203], a[8:11], v[72:75], v[200:203]\n"
"v_mfma_f32_16x16x32_bf16 v[200:203], a[24:27], v[76:79], v[200:203]\n"
"v_mfma_f32_16x16x32_bf16 v[204:207], a[12:15], v[72:75], v[204:207]\n"
"v_mfma_f32_16x16x32_bf16 v[204:207], a[28:31], v[76:79], v[204:207]\n"
"buffer_load_dwordx4 v39, s[40:43], s74 offen offset:2048 lds\n"
"v_mfma_f32_16x16x32_bf16 v[208:211], a[0:3], v[48:51], v[208:211]\n"
"v_mfma_f32_16x16x32_bf16 v[208:211], a[16:19], v[52:55], v[208:211]\n"
"v_mfma_f32_16x16x32_bf16 v[212:215], a[4:7], v[48:51], v[212:215]\n"
"v_mfma_f32_16x16x32_bf16 v[212:215], a[20:23], v[52:55], v[212:215]\n"
"buffer_load_dwordx4 v40, s[40:43], s74 offen offset:3072 lds\n"
"v_mfma_f32_16x16x32_bf16 v[216:219], a[8:11], v[48:51], v[216:219]\n"
"v_mfma_f32_16x16x32_bf16 v[216:219], a[24:27], v[52:55], v[216:219]\n"
"v_mfma_f32_16x16x32_bf16 v[220:223], a[12:15], v[48:51], v[220:223]\n"
"s_add_u32 s75, s74, 0x40000\n"
"s_add_u32 m0, s30, 0x5000\n"
"v_mfma_f32_16x16x32_bf16 v[220:223], a[28:31], v[52:55], v[220:223]\n"
"s_waitcnt lgkmcnt(0)\n"
"buffer_load_dwordx4 v37, s[40:43], s75 offen offset:0 lds\n"
"v_mfma_f32_16x16x32_bf16 v[224:227], a[0:3], v[56:59], v[224:227]\n"
"v_mfma_f32_16x16x32_bf16 v[224:227], a[16:19], v[60:63], v[224:227]\n"
"s_waitcnt vmcnt(13)\n"
"s_barrier\n"
"v_mfma_f32_16x16x32_bf16 v[228:231], a[4:7], v[56:59], v[228:231]\n"
"ds_read_b128 v[72:75], v27 offset:12288\n"
"ds_read_b128 v[76:79], v27 offset:12800\n"
"v_mfma_f32_16x16x32_bf16 v[228:231], a[20:23], v[60:63], v[228:231]\n"
"s_add_u32 s73, s72, 12\n"
"buffer_load_dwordx4 v38, s[40:43], s75 offen offset:1024 lds\n"
"ds_read_b128 v[48:51], v27 offset:14336\n"
"v_mfma_f32_16x16x32_bf16 v[232:235], a[8:11], v[56:59], v[232:235]\n"
"s_and_b32 s73, s73, 15\n"
"ds_read_b128 v[52:55], v27 offset:14848\n"
"v_mfma_f32_16x16x32_bf16 v[232:235], a[24:27], v[60:63], v[232:235]\n"
"s_lshl_b32 s73, s73, 7\n"
"v_mfma_f32_16x16x32_bf16 v[236:239], a[12:15], v[56:59], v[236:239]\n"
"ds_read_b128 v[56:59], v41 offset:12288\n"
"v_mfma_f32_16x16x32_bf16 v[236:239], a[28:31], v[60:63], v[236:239]\n"
"buffer_load_dwordx4 v39, s[40:43], s75 offen offset:2048 lds\n"
"ds_read_b128 v[60:63], v41 offset:12800\n"
"v_mfma_f32_16x16x32_bf16 v[240:243], a[0:3], v[64:67], v[240:243]\n"
"v_mfma_f32_16x16x32_bf16 v[240:243], a[16:19], v[68:71], v[240:243]\n"
"v_mfma_f32_16x16x32_bf16 v[244:247], a[4:7], v[64:67], v[244:247]\n"
"v_mfma_f32_16x16x32_bf16 v[244:247], a[20:23], v[68:71], v[244:247]\n"
"buffer_load_dwordx4 v40, s[40:43], s75 offen offset:3072 lds\n"
"v_mfma_f32_16x16x32_bf16 v[248:251], a[8:11], v[64:67], v[248:251]\n"
"v_mfma_f32_16x16x32_bf16 v[248:251], a[24:27], v[68:71], v[248:251]\n"
"v_mfma_f32_16x16x32_bf16 v[252:255], a[12:15], v[64:67], v[252:255]\n"
"s_add_u32 m0, s30, 0x2000\n"
"ds_read_b128 v[64:67], v41 offset:14336\n"
"v_mfma_f32_16x16x32_bf16 v[252:255], a[28:31], v[68:71], v[252:255]\n"
"s_waitcnt lgkmcnt(3)\n"
"buffer_load_dwordx4 v32, s[36:39], s73 offen offset:0 lds\n"
"ds_read_b128 v[68:71], v41 offset:14848\n"
"v_mfma_f32_16x16x32_bf16 v[112:115], a[32:35], v[72:75], v[112:115]\n"
"v_mfma_f32_16x16x32_bf16 v[112:115], a[48:51], v[76:79], v[112:115]\n"
"v_mfma_f32_16x16x32_bf16 v[116:119], a[36:39], v[72:75], v[116:119]\n"
"v_mfma_f32_16x16x32_bf16 v[116:119], a[52:55], v[76:79], v[116:119]\n"
"buffer_load_dwordx4 v33, s[36:39], s73 offen offset:1024 lds\n"
"v_mfma_f32_16x16x32_bf16 v[120:123], a[40:43], v[72:75], v[120:123]\n"
"v_mfma_f32_16x16x32_bf16 v[120:123], a[56:59], v[76:79], v[120:123]\n"
"v_mfma_f32_16x16x32_bf16 v[124:127], a[44:47], v[72:75], v[124:127]\n"
"ds_read_b128 v[72:75], v42 offset:12288\n"
"v_mfma_f32_16x16x32_bf16 v[124:127], a[60:63], v[76:79], v[124:127]\n"
"s_add_u32 s90, s72, 13\n"
"buffer_load_dwordx4 v34, s[36:39], s73 offen offset:2048 lds\n"
"ds_read_b128 v[76:79], v42 offset:12800\n"
"v_mfma_f32_16x16x32_bf16 v[128:131], a[32:35], v[48:51], v[128:131]\n"
"s_and_b32 s90, s90, 15\n"
"v_mfma_f32_16x16x32_bf16 v[128:131], a[48:51], v[52:55], v[128:131]\n"
"s_lshl_b32 s90, s90, 19\n"
"v_mfma_f32_16x16x32_bf16 v[132:135], a[36:39], v[48:51], v[132:135]\n"
"v_mfma_f32_16x16x32_bf16 v[132:135], a[52:55], v[52:55], v[132:135]\n"
"buffer_load_dwordx4 v35, s[36:39], s73 offen offset:3072 lds\n"
"v_mfma_f32_16x16x32_bf16 v[136:139], a[40:43], v[48:51], v[136:139]\n"
"s_add_u32 m0, s30, 0x9000\n"
"v_mfma_f32_16x16x32_bf16 v[136:139], a[56:59], v[52:55], v[136:139]\n"
"buffer_load_dwordx4 v36, s[36:39], s73 offen offset:0 lds\n"
"v_mfma_f32_16x16x32_bf16 v[140:143], a[44:47], v[48:51], v[140:143]\n"
"ds_read_b128 v[48:51], v42 offset:14336\n"
"v_mfma_f32_16x16x32_bf16 v[140:143], a[60:63], v[52:55], v[140:143]\n"
"s_waitcnt lgkmcnt(3)\n"
"ds_read_b128 v[52:55], v42 offset:14848\n"
"v_mfma_f32_16x16x32_bf16 v[144:147], a[32:35], v[56:59], v[144:147]\n"
"v_mfma_f32_16x16x32_bf16 v[144:147], a[48:51], v[60:63], v[144:147]\n"
"v_mfma_f32_16x16x32_bf16 v[148:151], a[36:39], v[56:59], v[148:151]\n"
"s_add_u32 m0, s30, 0x6000\n"
"v_mfma_f32_16x16x32_bf16 v[148:151], a[52:55], v[60:63], v[148:151]\n"
"buffer_load_dwordx4 v37, s[40:43], s90 offen offset:0 lds\n"
"v_mfma_f32_16x16x32_bf16 v[152:155], a[40:43], v[56:59], v[152:155]\n"
"v_mfma_f32_16x16x32_bf16 v[152:155], a[56:59], v[60:63], v[152:155]\n"
"v_mfma_f32_16x16x32_bf16 v[156:159], a[44:47], v[56:59], v[156:159]\n"
"ds_read_b128 v[56:59], v43 offset:12288\n"
"v_mfma_f32_16x16x32_bf16 v[156:159], a[60:63], v[60:63], v[156:159]\n"
"buffer_load_dwordx4 v38, s[40:43], s90 offen offset:1024 lds\n"
"ds_read_b128 v[60:63], v43 offset:12800\n"
"v_mfma_f32_16x16x32_bf16 v[160:163], a[32:35], v[64:67], v[160:163]\n"
"v_mfma_f32_16x16x32_bf16 v[160:163], a[48:51], v[68:71], v[160:163]\n"
"v_mfma_f32_16x16x32_bf16 v[164:167], a[36:39], v[64:67], v[164:167]\n"
"v_mfma_f32_16x16x32_bf16 v[164:167], a[52:55], v[68:71], v[164:167]\n"
"buffer_load_dwordx4 v39, s[40:43], s90 offen offset:2048 lds\n"
"v_mfma_f32_16x16x32_bf16 v[168:171], a[40:43], v[64:67], v[168:171]\n"
"v_mfma_f32_16x16x32_bf16 v[168:171], a[56:59], v[68:71], v[168:171]\n"
"s_waitcnt vmcnt(12)\n"
"ds_read_b64_tr_b16 a[0:1], v45 offset:16384\n"
"v_mfma_f32_16x16x32_bf16 v[172:175], a[44:47], v[64:67], v[172:175]\n"
"ds_read_b128 v[64:67], v43 offset:14336\n"
"v_mfma_f32_16x16x32_bf16 v[172:175], a[60:63], v[68:71], v[172:175]\n"
"s_waitcnt lgkmcnt(4)\n"
"buffer_load_dwordx4 v40, s[40:43], s90 offen offset:3072 lds\n"
"ds_read_b128 v[68:71], v43 offset:14848\n"
"v_mfma_f32_16x16x32_bf16 v[176:179], a[32:35], v[72:75], v[176:179]\n"
"ds_read_b64_tr_b16 a[2:3], v45 offset:16640\n"
"v_mfma_f32_16x16x32_bf16 v[176:179], a[48:51], v[76:79], v[176:179]\n"
"ds_read_b64_tr_b16 a[4:5], v46 offset:16384\n"
"v_mfma_f32_16x16x32_bf16 v[180:183], a[36:39], v[72:75], v[180:183]\n"
"s_add_u32 s77, s90, 0x40000\n"
"s_add_u32 m0, s30, 0x7000\n"
"ds_read_b64_tr_b16 a[6:7], v46 offset:16640\n"
"v_mfma_f32_16x16x32_bf16 v[180:183], a[52:55], v[76:79], v[180:183]\n"
"buffer_load_dwordx4 v37, s[40:43], s77 offen offset:0 lds\n"
"ds_read_b64_tr_b16 a[8:9], v45 offset:16896\n"
"v_mfma_f32_16x16x32_bf16 v[184:187], a[40:43], v[72:75], v[184:187]\n"
"ds_read_b64_tr_b16 a[10:11], v45 offset:17152\n"
"v_mfma_f32_16x16x32_bf16 v[184:187], a[56:59], v[76:79], v[184:187]\n"
"ds_read_b64_tr_b16 a[12:13], v46 offset:16896\n"
"v_mfma_f32_16x16x32_bf16 v[188:191], a[44:47], v[72:75], v[188:191]\n"
"ds_read_b128 v[72:75], v30 offset:1024\n"
"v_mfma_f32_16x16x32_bf16 v[188:191], a[60:63], v[76:79], v[188:191]\n"
"s_add_u32 s88, s72, 13\n"
"buffer_load_dwordx4 v38, s[40:43], s77 offen offset:1024 lds\n"
"ds_read_b128 v[76:79], v30 offset:1536\n"
"v_mfma_f32_16x16x32_bf16 v[192:195], a[32:35], v[48:51], v[192:195]\n"
"s_and_b32 s88, s88, 15\n"
"ds_read_b64_tr_b16 a[14:15], v46 offset:17152\n"
"v_mfma_f32_16x16x32_bf16 v[192:195], a[48:51], v[52:55], v[192:195]\n"
"s_lshl_b32 s88, s88, 7\n"
"v_mfma_f32_16x16x32_bf16 v[196:199], a[36:39], v[48:51], v[196:199]\n"
"v_mfma_f32_16x16x32_bf16 v[196:199], a[52:55], v[52:55], v[196:199]\n"
"buffer_load_dwordx4 v39, s[40:43], s77 offen offset:2048 lds\n"
"v_mfma_f32_16x16x32_bf16 v[200:203], a[40:43], v[48:51], v[200:203]\n"
"v_mfma_f32_16x16x32_bf16 v[200:203], a[56:59], v[52:55], v[200:203]\n"
"s_waitcnt vmcnt(12)\n"
"ds_read_b64_tr_b16 a[16:17], v45 offset:20480\n"
"v_mfma_f32_16x16x32_bf16 v[204:207], a[44:47], v[48:51], v[204:207]\n"
"ds_read_b64_tr_b16 a[18:19], v45 offset:20736\n"
"v_mfma_f32_16x16x32_bf16 v[204:207], a[60:63], v[52:55], v[204:207]\n"
"s_waitcnt lgkmcnt(6)\n"
"buffer_load_dwordx4 v40, s[40:43], s77 offen offset:3072 lds\n"
"ds_read_b64_tr_b16 a[20:21], v46 offset:20480\n"
"v_mfma_f32_16x16x32_bf16 v[208:211], a[32:35], v[56:59], v[208:211]\n"
"ds_read_b64_tr_b16 a[22:23], v46 offset:20736\n"
"v_mfma_f32_16x16x32_bf16 v[208:211], a[48:51], v[60:63], v[208:211]\n"
"ds_read_b64_tr_b16 a[24:25], v45 offset:20992\n"
"v_mfma_f32_16x16x32_bf16 v[212:215], a[36:39], v[56:59], v[212:215]\n"
"s_add_u32 m0, s30, 0x8000\n"
"ds_read_b64_tr_b16 a[26:27], v45 offset:21248\n"
"v_mfma_f32_16x16x32_bf16 v[212:215], a[52:55], v[60:63], v[212:215]\n"
"buffer_load_dwordx4 v32, s[36:39], s88 offen offset:0 lds\n"
"ds_read_b64_tr_b16 a[28:29], v46 offset:20992\n"
"v_mfma_f32_16x16x32_bf16 v[216:219], a[40:43], v[56:59], v[216:219]\n"
"ds_read_b64_tr_b16 a[30:31], v46 offset:21248\n"
"v_mfma_f32_16x16x32_bf16 v[216:219], a[56:59], v[60:63], v[216:219]\n"
"v_mfma_f32_16x16x32_bf16 v[220:223], a[44:47], v[56:59], v[220:223]\n"
"s_add_u32 s91, s72, 14\n"
"v_mfma_f32_16x16x32_bf16 v[220:223], a[60:63], v[60:63], v[220:223]\n"
"s_and_b32 s91, s91, 15\n"
"buffer_load_dwordx4 v33, s[36:39], s88 offen offset:1024 lds\n"
"v_mfma_f32_16x16x32_bf16 v[224:227], a[32:35], v[64:67], v[224:227]\n"
"s_lshl_b32 s91, s91, 19\n"
"v_mfma_f32_16x16x32_bf16 v[224:227], a[48:51], v[68:71], v[224:227]\n"
"v_mfma_f32_16x16x32_bf16 v[228:231], a[36:39], v[64:67], v[228:231]\n"
"buffer_load_dwordx4 v34, s[36:39], s88 offen offset:2048 lds\n"
"v_mfma_f32_16x16x32_bf16 v[228:231], a[52:55], v[68:71], v[228:231]\n"
"v_mfma_f32_16x16x32_bf16 v[232:235], a[40:43], v[64:67], v[232:235]\n"
"v_mfma_f32_16x16x32_bf16 v[232:235], a[56:59], v[68:71], v[232:235]\n"
"v_mfma_f32_16x16x32_bf16 v[236:239], a[44:47], v[64:67], v[236:239]\n"
"buffer_load_dwordx4 v35, s[36:39], s88 offen offset:3072 lds\n"
"v_mfma_f32_16x16x32_bf16 v[236:239], a[60:63], v[68:71], v[236:239]\n"
"s_waitcnt vmcnt(12) lgkmcnt(0)\n"
"s_barrier\n"
"s_mul_i32 s88, s70, 0x2400\n"
"v_mfma_f32_16x16x32_bf16 v[240:243], a[32:35], v[72:75], v[240:243]\n"
"s_add_u32 m0, s30, 0x4000\n"
"ds_read_b128 v[48:51], v27 offset:8192\n"
"ds_read_b128 v[52:55], v27 offset:8704\n"
"v_mfma_f32_16x16x32_bf16 v[240:243], a[48:51], v[76:79], v[240:243]\n"
"s_getpc_b64 s[98:99]\n"
"7:\n"
"s_add_u32 s98, s98, .Lmha_VARJ-7b\n"
"s_addc_u32 s99, s99, (.Lmha_VARJ-7b)>>32\n"
"ds_read_b128 v[56:59], v27 offset:10240\n"
"ds_read_b128 v[60:63], v27 offset:10752\n"
"v_mfma_f32_16x16x32_bf16 v[244:247], a[36:39], v[72:75], v[244:247]\n"
"buffer_load_dwordx4 v37, s[40:43], s91 offen offset:0 lds\n"
"ds_read_b128 v[64:67], v41 offset:8192\n"
"v_mfma_f32_16x16x32_bf16 v[244:247], a[52:55], v[76:79], v[244:247]\n"
"ds_read_b128 v[68:71], v41 offset:8704\n"
"v_mfma_f32_16x16x32_bf16 v[248:251], a[40:43], v[72:75], v[248:251]\n"
"global_load_dword v26, v0, s[98:99] offset:0\n"
"v_mfma_f32_16x16x32_bf16 v[248:251], a[56:59], v[76:79], v[248:251]\n"
"v_mfma_f32_16x16x32_bf16 v[252:255], a[44:47], v[72:75], v[252:255]\n"
"buffer_load_dwordx4 v38, s[40:43], s91 offen offset:1024 lds\n"
"ds_read_b128 v[72:75], v41 offset:10240\n"
"v_mfma_f32_16x16x32_bf16 v[252:255], a[60:63], v[76:79], v[252:255]\n"
"s_waitcnt lgkmcnt(6)\n"
"ds_read_b128 v[76:79], v41 offset:10752\n"
"v_mfma_f32_16x16x32_bf16 v[112:115], a[0:3], v[48:51], v[112:115]\n"
"s_waitcnt lgkmcnt(6)\n"
"v_mfma_f32_16x16x32_bf16 v[112:115], a[16:19], v[52:55], v[112:115]\n"
"v_mfma_f32_16x16x32_bf16 v[116:119], a[4:7], v[48:51], v[116:119]\n"
"buffer_load_dwordx4 v39, s[40:43], s91 offen offset:2048 lds\n"
"v_mfma_f32_16x16x32_bf16 v[116:119], a[20:23], v[52:55], v[116:119]\n"
"v_mfma_f32_16x16x32_bf16 v[120:123], a[8:11], v[48:51], v[120:123]\n"
"v_mfma_f32_16x16x32_bf16 v[120:123], a[24:27], v[52:55], v[120:123]\n"
"v_mfma_f32_16x16x32_bf16 v[124:127], a[12:15], v[48:51], v[124:127]\n"
"buffer_load_dwordx4 v40, s[40:43], s91 offen offset:3072 lds\n"
"ds_read_b128 v[48:51], v42 offset:8192\n"
"v_mfma_f32_16x16x32_bf16 v[124:127], a[28:31], v[52:55], v[124:127]\n"
"s_waitcnt lgkmcnt(3)\n"
"ds_read_b128 v[52:55], v42 offset:8704\n"
"v_mfma_f32_16x16x32_bf16 v[128:131], a[0:3], v[56:59], v[128:131]\n"
"s_waitcnt vmcnt(13)\n"
"ds_read_b64_tr_b16 a[32:33], v45 offset:24576\n"
"v_mfma_f32_16x16x32_bf16 v[128:131], a[16:19], v[60:63], v[128:131]\n"
"s_add_u32 s78, s91, 0x40000\n"
"s_add_u32 m0, s30, 0x5000\n"
"ds_read_b64_tr_b16 a[34:35], v45 offset:24832\n"
"v_mfma_f32_16x16x32_bf16 v[132:135], a[4:7], v[56:59], v[132:135]\n"
"buffer_load_dwordx4 v37, s[40:43], s78 offen offset:0 lds\n"
"ds_read_b64_tr_b16 a[36:37], v46 offset:24576\n"
"v_mfma_f32_16x16x32_bf16 v[132:135], a[20:23], v[60:63], v[132:135]\n"
"ds_read_b64_tr_b16 a[38:39], v46 offset:24832\n"
"v_mfma_f32_16x16x32_bf16 v[136:139], a[8:11], v[56:59], v[136:139]\n"
"ds_read_b64_tr_b16 a[40:41], v45 offset:25088\n"
"v_mfma_f32_16x16x32_bf16 v[136:139], a[24:27], v[60:63], v[136:139]\n"
"ds_read_b64_tr_b16 a[42:43], v45 offset:25344\n"
"v_mfma_f32_16x16x32_bf16 v[140:143], a[12:15], v[56:59], v[140:143]\n"
"s_add_u32 s74, s72, 15\n"
"buffer_load_dwordx4 v38, s[40:43], s78 offen offset:1024 lds\n"
"ds_read_b128 v[56:59], v42 offset:10240\n"
"v_mfma_f32_16x16x32_bf16 v[140:143], a[28:31], v[60:63], v[140:143]\n"
"s_and_b32 s74, s74, 15\n"
"ds_read_b128 v[60:63], v42 offset:10752\n"
"v_mfma_f32_16x16x32_bf16 v[144:147], a[0:3], v[64:67], v[144:147]\n"
"s_lshl_b32 s74, s74, 19\n"
"ds_read_b64_tr_b16 a[44:45], v46 offset:25088\n"
"v_mfma_f32_16x16x32_bf16 v[144:147], a[16:19], v[68:71], v[144:147]\n"
"ds_read_b64_tr_b16 a[46:47], v46 offset:25344\n"
"v_mfma_f32_16x16x32_bf16 v[148:151], a[4:7], v[64:67], v[148:151]\n"
"buffer_load_dwordx4 v39, s[40:43], s78 offen offset:2048 lds\n"
"v_mfma_f32_16x16x32_bf16 v[148:151], a[20:23], v[68:71], v[148:151]\n"
"v_mfma_f32_16x16x32_bf16 v[152:155], a[8:11], v[64:67], v[152:155]\n"
"v_mfma_f32_16x16x32_bf16 v[152:155], a[24:27], v[68:71], v[152:155]\n"
"v_mfma_f32_16x16x32_bf16 v[156:159], a[12:15], v[64:67], v[156:159]\n"
"buffer_load_dwordx4 v40, s[40:43], s78 offen offset:3072 lds\n"
"ds_read_b128 v[64:67], v43 offset:8192\n"
"v_mfma_f32_16x16x32_bf16 v[156:159], a[28:31], v[68:71], v[156:159]\n"
"s_waitcnt lgkmcnt(6)\n"
"ds_read_b128 v[68:71], v43 offset:8704\n"
"v_mfma_f32_16x16x32_bf16 v[160:163], a[0:3], v[72:75], v[160:163]\n"
"s_waitcnt vmcnt(13)\n"
"ds_read_b64_tr_b16 a[48:49], v45 offset:28672\n"
"v_mfma_f32_16x16x32_bf16 v[160:163], a[16:19], v[76:79], v[160:163]\n"
"s_add_u32 m0, s30, 0x6000\n"
"ds_read_b64_tr_b16 a[50:51], v45 offset:28928\n"
"v_mfma_f32_16x16x32_bf16 v[164:167], a[4:7], v[72:75], v[164:167]\n"
"s_waitcnt lgkmcnt(4)\n"
"buffer_load_dwordx4 v37, s[40:43], s74 offen offset:0 lds\n"
"ds_read_b64_tr_b16 a[52:53], v46 offset:28672\n"
"v_mfma_f32_16x16x32_bf16 v[164:167], a[20:23], v[76:79], v[164:167]\n"
"ds_read_b64_tr_b16 a[54:55], v46 offset:28928\n"
"v_mfma_f32_16x16x32_bf16 v[168:171], a[8:11], v[72:75], v[168:171]\n"
"ds_read_b64_tr_b16 a[56:57], v45 offset:29184\n"
"v_mfma_f32_16x16x32_bf16 v[168:171], a[24:27], v[76:79], v[168:171]\n"
"ds_read_b64_tr_b16 a[58:59], v45 offset:29440\n"
"v_mfma_f32_16x16x32_bf16 v[172:175], a[12:15], v[72:75], v[172:175]\n"
"s_add_u32 s89, s72, 14\n"
"buffer_load_dwordx4 v38, s[40:43], s74 offen offset:1024 lds\n"
"ds_read_b128 v[72:75], v43 offset:10240\n"
"v_mfma_f32_16x16x32_bf16 v[172:175], a[28:31], v[76:79], v[172:175]\n"
"s_and_b32 s89, s89, 15\n"
"ds_read_b128 v[76:79], v43 offset:10752\n"
"v_mfma_f32_16x16x32_bf16 v[176:179], a[0:3], v[48:51], v[176:179]\n"
"s_lshl_b32 s89, s89, 7\n"
"ds_read_b64_tr_b16 a[60:61], v46 offset:29184\n"
"v_mfma_f32_16x16x32_bf16 v[176:179], a[16:19], v[52:55], v[176:179]\n"
"ds_read_b64_tr_b16 a[62:63], v46 offset:29440\n"
"v_mfma_f32_16x16x32_bf16 v[180:183], a[4:7], v[48:51], v[180:183]\n"
"buffer_load_dwordx4 v39, s[40:43], s74 offen offset:2048 lds\n"
"v_mfma_f32_16x16x32_bf16 v[180:183], a[20:23], v[52:55], v[180:183]\n"
"v_mfma_f32_16x16x32_bf16 v[184:187], a[8:11], v[48:51], v[184:187]\n"
"v_mfma_f32_16x16x32_bf16 v[184:187], a[24:27], v[52:55], v[184:187]\n"
"v_mfma_f32_16x16x32_bf16 v[188:191], a[12:15], v[48:51], v[188:191]\n"
"buffer_load_dwordx4 v40, s[40:43], s74 offen offset:3072 lds\n"
"ds_read_b128 v[48:51], v44 offset:0\n"
"v_mfma_f32_16x16x32_bf16 v[188:191], a[28:31], v[52:55], v[188:191]\n"
"ds_read_b128 v[52:55], v44 offset:512\n"
"v_mfma_f32_16x16x32_bf16 v[192:195], a[0:3], v[56:59], v[192:195]\n"
"v_mfma_f32_16x16x32_bf16 v[192:195], a[16:19], v[60:63], v[192:195]\n"
"s_add_u32 m0, s30, 0x3000\n"
"v_mfma_f32_16x16x32_bf16 v[196:199], a[4:7], v[56:59], v[196:199]\n"
"buffer_load_dwordx4 v32, s[36:39], s89 offen offset:0 lds\n"
"v_mfma_f32_16x16x32_bf16 v[196:199], a[20:23], v[60:63], v[196:199]\n"
"v_mfma_f32_16x16x32_bf16 v[200:203], a[8:11], v[56:59], v[200:203]\n"
"v_mfma_f32_16x16x32_bf16 v[200:203], a[24:27], v[60:63], v[200:203]\n"
"v_mfma_f32_16x16x32_bf16 v[204:207], a[12:15], v[56:59], v[204:207]\n"
"buffer_load_dwordx4 v33, s[36:39], s89 offen offset:1024 lds\n"
"v_mfma_f32_16x16x32_bf16 v[204:207], a[28:31], v[60:63], v[204:207]\n"
"s_waitcnt lgkmcnt(2)\n"
"v_mfma_f32_16x16x32_bf16 v[208:211], a[0:3], v[64:67], v[208:211]\n"
"v_mfma_f32_16x16x32_bf16 v[208:211], a[16:19], v[68:71], v[208:211]\n"
"v_mfma_f32_16x16x32_bf16 v[212:215], a[4:7], v[64:67], v[212:215]\n"
"buffer_load_dwordx4 v34, s[36:39], s89 offen offset:2048 lds\n"
"v_mfma_f32_16x16x32_bf16 v[212:215], a[20:23], v[68:71], v[212:215]\n"
"v_mfma_f32_16x16x32_bf16 v[216:219], a[8:11], v[64:67], v[216:219]\n"
"v_mfma_f32_16x16x32_bf16 v[216:219], a[24:27], v[68:71], v[216:219]\n"
"v_mfma_f32_16x16x32_bf16 v[220:223], a[12:15], v[64:67], v[220:223]\n"
"buffer_load_dwordx4 v35, s[36:39], s89 offen offset:3072 lds\n"
"v_mfma_f32_16x16x32_bf16 v[220:223], a[28:31], v[68:71], v[220:223]\n"
"v_mfma_f32_16x16x32_bf16 v[224:227], a[0:3], v[72:75], v[224:227]\n"
"v_mfma_f32_16x16x32_bf16 v[224:227], a[16:19], v[76:79], v[224:227]\n"
"s_waitcnt vmcnt(17) lgkmcnt(0)\n"
"s_barrier\n"
"s_add_u32 m0, s30, 0x9400\n"
"v_mfma_f32_16x16x32_bf16 v[228:231], a[4:7], v[72:75], v[228:231]\n"
"buffer_load_dwordx4 v36, s[36:39], s89 offen offset:0 lds\n"
"ds_read_b128 v[56:59], v27 offset:32768\n"
"v_mfma_f32_16x16x32_bf16 v[228:231], a[20:23], v[76:79], v[228:231]\n"
"s_lshl_b32 s89, s70, 16\n"
"ds_read_b128 v[60:63], v27 offset:33280\n"
"ds_read_b128 v[64:67], v27 offset:34816\n"
"v_mfma_f32_16x16x32_bf16 v[232:235], a[8:11], v[72:75], v[232:235]\n"
"s_and_b32 s70, s27, 7\n"
"ds_read_b128 v[68:71], v27 offset:35328\n"
"v_mfma_f32_16x16x32_bf16 v[232:235], a[24:27], v[76:79], v[232:235]\n"
"s_sub_u32 s70, s70, 7\n"
"s_add_u32 s84, s74, 0x40000\n"
"s_add_u32 m0, s30, 0x7000\n"
"v_mfma_f32_16x16x32_bf16 v[236:239], a[12:15], v[72:75], v[236:239]\n"
"s_lshr_b32 s70, s70, 31\n"
"buffer_load_dwordx4 v37, s[40:43], s84 offen offset:0 lds\n"
"ds_read_b128 v[72:75], v41 offset:32768\n"
"v_mfma_f32_16x16x32_bf16 v[236:239], a[28:31], v[76:79], v[236:239]\n"
"s_add_u32 s70, s70, s71\n"
"ds_read_b128 v[76:79], v41 offset:33280\n"
"v_mfma_f32_16x16x32_bf16 v[240:243], a[0:3], v[48:51], v[240:243]\n"
"s_waitcnt vmcnt(14)\n"
"s_add_u32 s70, s70, 6\n"
"ds_read_b64_tr_b16 a[0:1], v45 offset:16384\n"
"v_mfma_f32_16x16x32_bf16 v[240:243], a[16:19], v[52:55], v[240:243]\n"
"s_add_u32 s73, s72, 15\n"
"buffer_load_dwordx4 v38, s[40:43], s84 offen offset:1024 lds\n"
"ds_read_b64_tr_b16 a[2:3], v45 offset:16640\n"
"v_mfma_f32_16x16x32_bf16 v[244:247], a[4:7], v[48:51], v[244:247]\n"
"s_and_b32 s73, s73, 15\n"
"s_lshl_b32 s70, s70, 3\n"
"ds_read_b64_tr_b16 a[4:5], v46 offset:16384\n"
"v_mfma_f32_16x16x32_bf16 v[244:247], a[20:23], v[52:55], v[244:247]\n"
"s_lshl_b32 s73, s73, 7\n"
"s_add_u32 s70, s70, s11\n"
"ds_read_b64_tr_b16 a[6:7], v46 offset:16640\n"
"v_mfma_f32_16x16x32_bf16 v[248:251], a[8:11], v[48:51], v[248:251]\n"
"s_mul_i32 s90, s70, 0x2400\n"
"s_lshl_b32 s91, s70, 16\n"
"ds_read_b64_tr_b16 a[8:9], v45 offset:16896\n"
"v_mfma_f32_16x16x32_bf16 v[248:251], a[24:27], v[52:55], v[248:251]\n"
"buffer_load_dwordx4 v39, s[40:43], s84 offen offset:2048 lds\n"
"ds_read_b64_tr_b16 a[10:11], v45 offset:17152\n"
"v_mfma_f32_16x16x32_bf16 v[252:255], a[12:15], v[48:51], v[252:255]\n"
"ds_read_b128 v[48:51], v41 offset:34816\n"
"v_mfma_f32_16x16x32_bf16 v[252:255], a[28:31], v[52:55], v[252:255]\n"
"s_waitcnt lgkmcnt(9)\n"
"ds_read_b128 v[52:55], v41 offset:35328\n"
"v_mfma_f32_16x16x32_bf16 v[112:115], a[32:35], v[56:59], v[112:115]\n"
"s_waitcnt vmcnt(12)\n"
"ds_read_b64_tr_b16 a[16:17], v45 offset:20480\n"
"v_mfma_f32_16x16x32_bf16 v[112:115], a[48:51], v[60:63], v[112:115]\n"
"buffer_load_dwordx4 v40, s[40:43], s84 offen offset:3072 lds\n"
"ds_read_b64_tr_b16 a[18:19], v45 offset:20736\n"
"v_mfma_f32_16x16x32_bf16 v[116:119], a[36:39], v[56:59], v[116:119]\n"
"s_mul_i32 s41, s68, 0x2400\n"
"ds_read_b64_tr_b16 a[20:21], v46 offset:20480\n"
"v_mfma_f32_16x16x32_bf16 v[116:119], a[52:55], v[60:63], v[116:119]\n"
"s_xor_b32 s68, s68, 32\n"
"ds_read_b64_tr_b16 a[22:23], v46 offset:20736\n"
"v_mfma_f32_16x16x32_bf16 v[120:123], a[40:43], v[56:59], v[120:123]\n"
"s_mul_i32 s40, s68, 0x2400\n"
"s_add_u32 m0, s30, 0x2000\n"
"ds_read_b64_tr_b16 a[24:25], v45 offset:20992\n"
"v_mfma_f32_16x16x32_bf16 v[120:123], a[56:59], v[60:63], v[120:123]\n"
"s_lshl_b32 s68, s29, 1\n"
"buffer_load_dwordx4 v32, s[36:39], s73 offen offset:0 lds\n"
"ds_read_b64_tr_b16 a[26:27], v45 offset:21248\n"
"v_mfma_f32_16x16x32_bf16 v[124:127], a[44:47], v[56:59], v[124:127]\n"
"s_add_u32 s68, s68, s28\n"
"ds_read_b128 v[56:59], v42 offset:32768\n"
"v_mfma_f32_16x16x32_bf16 v[124:127], a[60:63], v[60:63], v[124:127]\n"
"s_lshl_b32 s68, s68, 2\n"
"ds_read_b128 v[60:63], v42 offset:33280\n"
"v_mfma_f32_16x16x32_bf16 v[128:131], a[32:35], v[64:67], v[128:131]\n"
"s_add_u32 s68, s68, s11\n"
"ds_read_b64_tr_b16 a[12:13], v46 offset:16896\n"
"v_mfma_f32_16x16x32_bf16 v[128:131], a[48:51], v[68:71], v[128:131]\n"
"s_lshl_b32 s42, s68, 5\n"
"buffer_load_dwordx4 v33, s[36:39], s73 offen offset:1024 lds\n"
"ds_read_b64_tr_b16 a[14:15], v46 offset:17152\n"
"v_mfma_f32_16x16x32_bf16 v[132:135], a[36:39], v[64:67], v[132:135]\n"
"ds_read_b64_tr_b16 a[28:29], v46 offset:20992\n"
"v_mfma_f32_16x16x32_bf16 v[132:135], a[52:55], v[68:71], v[132:135]\n"
"ds_read_b64_tr_b16 a[30:31], v46 offset:21248\n"
"v_mfma_f32_16x16x32_bf16 v[136:139], a[40:43], v[64:67], v[136:139]\n"
"v_mfma_f32_16x16x32_bf16 v[136:139], a[56:59], v[68:71], v[136:139]\n"
"buffer_load_dwordx4 v34, s[36:39], s73 offen offset:2048 lds\n"
"buffer_load_dwordx4 v35, s[36:39], s73 offen offset:3072 lds\n"
"v_mfma_f32_16x16x32_bf16 v[140:143], a[44:47], v[64:67], v[140:143]\n"
"s_lshl_b32 s39, s27, 7\n"
"ds_read_b128 v[64:67], v42 offset:34816\n"
"v_mfma_f32_16x16x32_bf16 v[140:143], a[60:63], v[68:71], v[140:143]\n"
"s_waitcnt lgkmcnt(8)\n"
"s_add_u32 s39, s39, s94\n"
"ds_read_b128 v[68:71], v42 offset:35328\n"
"v_mfma_f32_16x16x32_bf16 v[144:147], a[32:35], v[72:75], v[144:147]\n"
"s_waitcnt vmcnt(13)\n"
"ds_read_b64_tr_b16 v[80:81], v45 offset:24576\n"
"v_mfma_f32_16x16x32_bf16 v[144:147], a[48:51], v[76:79], v[144:147]\n"
"ds_read_b64_tr_b16 v[82:83], v45 offset:24832\n"
"v_mfma_f32_16x16x32_bf16 v[148:151], a[36:39], v[72:75], v[148:151]\n"
"ds_read_b64_tr_b16 v[84:85], v46 offset:24576\n"
"v_mfma_f32_16x16x32_bf16 v[148:151], a[52:55], v[76:79], v[148:151]\n"
"ds_read_b64_tr_b16 v[86:87], v46 offset:24832\n"
"v_mfma_f32_16x16x32_bf16 v[152:155], a[40:43], v[72:75], v[152:155]\n"
"ds_read_b64_tr_b16 v[88:89], v45 offset:25088\n"
"v_mfma_f32_16x16x32_bf16 v[152:155], a[56:59], v[76:79], v[152:155]\n"
"ds_read_b64_tr_b16 v[90:91], v45 offset:25344\n"
"v_mfma_f32_16x16x32_bf16 v[156:159], a[44:47], v[72:75], v[156:159]\n"
"ds_read_b128 v[72:75], v43 offset:32768\n"
"v_mfma_f32_16x16x32_bf16 v[156:159], a[60:63], v[76:79], v[156:159]\n"
"ds_read_b128 v[76:79], v43 offset:33280\n"
"v_mfma_f32_16x16x32_bf16 v[160:163], a[32:35], v[48:51], v[160:163]\n"
"ds_read_b64_tr_b16 v[92:93], v46 offset:25088\n"
"v_mfma_f32_16x16x32_bf16 v[160:163], a[48:51], v[52:55], v[160:163]\n"
"ds_read_b64_tr_b16 v[94:95], v46 offset:25344\n"
"v_mfma_f32_16x16x32_bf16 v[164:167], a[36:39], v[48:51], v[164:167]\n"
"v_mfma_f32_16x16x32_bf16 v[164:167], a[52:55], v[52:55], v[164:167]\n"
"v_mfma_f32_16x16x32_bf16 v[168:171], a[40:43], v[48:51], v[168:171]\n"
"v_mfma_f32_16x16x32_bf16 v[168:171], a[56:59], v[52:55], v[168:171]\n"
"v_mfma_f32_16x16x32_bf16 v[172:175], a[44:47], v[48:51], v[172:175]\n"
"ds_read_b128 v[48:51], v43 offset:34816\n"
"v_mfma_f32_16x16x32_bf16 v[172:175], a[60:63], v[52:55], v[172:175]\n"
"s_waitcnt lgkmcnt(6)\n"
"ds_read_b128 v[52:55], v43 offset:35328\n"
"v_mfma_f32_16x16x32_bf16 v[176:179], a[32:35], v[56:59], v[176:179]\n"
"v_mfma_f32_16x16x32_bf16 v[176:179], a[48:51], v[60:63], v[176:179]\n"
"s_mov_b32 s96, 0x8000\n"
"v_mfma_f32_16x16x32_bf16 v[180:183], a[36:39], v[56:59], v[180:183]\n"
"buffer_load_dwordx4 a[160:163], v19, s[48:51], s96 offen offset:0\n"
"v_mfma_f32_16x16x32_bf16 v[180:183], a[52:55], v[60:63], v[180:183]\n"
"v_mfma_f32_16x16x32_bf16 v[184:187], a[40:43], v[56:59], v[184:187]\n"
"v_mfma_f32_16x16x32_bf16 v[184:187], a[56:59], v[60:63], v[184:187]\n"
"v_mfma_f32_16x16x32_bf16 v[188:191], a[44:47], v[56:59], v[188:191]\n"
"ds_read_b128 v[56:59], v30 offset:0\n"
"v_mfma_f32_16x16x32_bf16 v[188:191], a[60:63], v[60:63], v[188:191]\n"
"ds_read_b128 v[60:63], v30 offset:512\n"
"v_mfma_f32_16x16x32_bf16 v[192:195], a[32:35], v[64:67], v[192:195]\n"
"v_mfma_f32_16x16x32_bf16 v[192:195], a[48:51], v[68:71], v[192:195]\n"
"v_mfma_f32_16x16x32_bf16 v[196:199], a[36:39], v[64:67], v[196:199]\n"
"v_mfma_f32_16x16x32_bf16 v[196:199], a[52:55], v[68:71], v[196:199]\n"
"v_mfma_f32_16x16x32_bf16 v[200:203], a[40:43], v[64:67], v[200:203]\n"
"v_mfma_f32_16x16x32_bf16 v[200:203], a[56:59], v[68:71], v[200:203]\n"
"v_mfma_f32_16x16x32_bf16 v[204:207], a[44:47], v[64:67], v[204:207]\n"
"v_mfma_f32_16x16x32_bf16 v[204:207], a[60:63], v[68:71], v[204:207]\n"
"s_waitcnt lgkmcnt(2)\n"
"buffer_load_dwordx4 a[164:167], v20, s[48:51], s96 offen offset:1024\n"
"v_mfma_f32_16x16x32_bf16 v[208:211], a[32:35], v[72:75], v[208:211]\n"
"v_mfma_f32_16x16x32_bf16 v[208:211], a[48:51], v[76:79], v[208:211]\n"
"v_mfma_f32_16x16x32_bf16 v[212:215], a[36:39], v[72:75], v[212:215]\n"
"v_mfma_f32_16x16x32_bf16 v[212:215], a[52:55], v[76:79], v[212:215]\n"
"s_waitcnt vmcnt(6)\n"
"ds_read_b64_tr_b16 v[96:97], v45 offset:28672\n"
"v_mfma_f32_16x16x32_bf16 v[216:219], a[40:43], v[72:75], v[216:219]\n"
"ds_read_b64_tr_b16 v[98:99], v45 offset:28928\n"
"v_mfma_f32_16x16x32_bf16 v[216:219], a[56:59], v[76:79], v[216:219]\n"
"ds_read_b64_tr_b16 v[100:101], v46 offset:28672\n"
"v_mfma_f32_16x16x32_bf16 v[220:223], a[44:47], v[72:75], v[220:223]\n"
"ds_read_b64_tr_b16 v[102:103], v46 offset:28928\n"
"v_mfma_f32_16x16x32_bf16 v[220:223], a[60:63], v[76:79], v[220:223]\n"
"ds_read_b64_tr_b16 v[104:105], v45 offset:29184\n"
"v_mfma_f32_16x16x32_bf16 v[224:227], a[32:35], v[48:51], v[224:227]\n"
"ds_read_b64_tr_b16 v[106:107], v45 offset:29440\n"
"v_mfma_f32_16x16x32_bf16 v[224:227], a[48:51], v[52:55], v[224:227]\n"
"ds_read_b64_tr_b16 v[108:109], v46 offset:29184\n"
"v_mfma_f32_16x16x32_bf16 v[228:231], a[36:39], v[48:51], v[228:231]\n"
"ds_read_b64_tr_b16 v[110:111], v46 offset:29440\n"
"v_mfma_f32_16x16x32_bf16 v[228:231], a[52:55], v[52:55], v[228:231]\n"
"buffer_load_dwordx4 a[168:171], v19, s[48:51], s96 offen offset:2048\n"
"v_mfma_f32_16x16x32_bf16 v[232:235], a[40:43], v[48:51], v[232:235]\n"
"v_mfma_f32_16x16x32_bf16 v[232:235], a[56:59], v[52:55], v[232:235]\n"
"v_mfma_f32_16x16x32_bf16 v[236:239], a[44:47], v[48:51], v[236:239]\n"
"v_mfma_f32_16x16x32_bf16 v[236:239], a[60:63], v[52:55], v[236:239]\n"
"s_waitcnt lgkmcnt(6)\n"
"v_mfma_f32_16x16x32_bf16 v[240:243], a[32:35], v[56:59], v[240:243]\n"
"s_waitcnt vmcnt(3)\n"
"s_barrier\n"
"ds_read_b128 v[64:67], v44 offset:1024\n"
"v_mfma_f32_16x16x32_bf16 v[240:243], a[48:51], v[60:63], v[240:243]\n"
"ds_read_b128 v[68:71], v44 offset:1536\n"
"ds_read_b128 v[72:75], v30 offset:1024\n"
"v_mfma_f32_16x16x32_bf16 v[244:247], a[36:39], v[56:59], v[244:247]\n"
"ds_read_b128 v[76:79], v30 offset:1536\n"
"ds_read_b128 v[48:51], v27 offset:12288\n"
"v_mfma_f32_16x16x32_bf16 v[244:247], a[52:55], v[60:63], v[244:247]\n"
"ds_read_b128 v[52:55], v27 offset:12800\n"
"v_add_u32 v30, 0x8000, v22\n"
"v_mfma_f32_16x16x32_bf16 v[248:251], a[40:43], v[56:59], v[248:251]\n"
"ds_read_b128 a[32:35], v21 offset:0\n"
"v_mfma_f32_16x16x32_bf16 v[248:251], a[56:59], v[60:63], v[248:251]\n"
"ds_read_b128 a[36:39], v21 offset:256\n"
"v_mfma_f32_16x16x32_bf16 v[252:255], a[44:47], v[56:59], v[252:255]\n"
"ds_read_b128 v[56:59], v27 offset:8192\n"
"v_mfma_f32_16x16x32_bf16 v[252:255], a[60:63], v[60:63], v[252:255]\n"
"s_mov_b32 s75, 0x8000\n"
"ds_read_b128 v[60:63], v27 offset:8704\n"
"ds_read_b128 a[40:43], v21 offset:512\n"
"s_waitcnt lgkmcnt(10)\n"
"v_mfma_f32_16x16x32_bf16 v[240:243], a[0:3], v[64:67], v[240:243]\n"
"s_waitcnt lgkmcnt(9)\n"
"buffer_load_dwordx4 a[172:175], v20, s[48:51], s75 offen offset:3072\n"
"ds_read_b128 a[44:47], v21 offset:768\n"
"v_mfma_f32_16x16x32_bf16 v[240:243], a[16:19], v[68:71], v[240:243]\n"
"s_waitcnt lgkmcnt(9)\n"
"ds_read_b128 a[48:51], v1 offset:0\n"
"v_mfma_f32_16x16x32_bf16 v[240:243], v[80:83], v[72:75], v[240:243]\n"
"s_waitcnt lgkmcnt(9)\n"
"ds_read_b128 a[52:55], v1 offset:256\n"
"v_mfma_f32_16x16x32_bf16 v[240:243], v[96:99], v[76:79], v[240:243]\n"
"ds_read_b128 a[56:59], v1 offset:512\n"
"v_mfma_f32_16x16x32_bf16 v[244:247], a[4:7], v[64:67], v[244:247]\n"
"ds_read_b128 a[60:63], v1 offset:768\n"
"v_mfma_f32_16x16x32_bf16 v[244:247], a[20:23], v[68:71], v[244:247]\n"
"s_nop 0\n"
"s_nop 0\n"
"s_nop 0\n"
"v_mfma_f32_16x16x32_bf16 v[244:247], v[84:87], v[72:75], v[244:247]\n"
"v_mul_f32 v240, s85, v240\n"
"v_mul_f32 v241, s85, v241\n"
"v_mfma_f32_16x16x32_bf16 v[244:247], v[100:103], v[76:79], v[244:247]\n"
"v_mul_f32 v242, s85, v242\n"
"v_mul_f32 v243, s85, v243\n"
"v_mfma_f32_16x16x32_bf16 v[248:251], a[8:11], v[64:67], v[248:251]\n"
"v_cvt_pk_bf16_f32 v240, v240, v241\n"
"v_cvt_pk_bf16_f32 v241, v242, v243\n"
"v_mfma_f32_16x16x32_bf16 v[248:251], a[24:27], v[68:71], v[248:251]\n"
"ds_write_b64 v47, v[240:241] offset:32768\n"
"v_mfma_f32_16x16x32_bf16 v[248:251], v[88:91], v[72:75], v[248:251]\n"
"v_mul_f32 v244, s85, v244\n"
"v_mul_f32 v245, s85, v245\n"
"v_mfma_f32_16x16x32_bf16 v[248:251], v[104:107], v[76:79], v[248:251]\n"
"s_mov_b32 s76, 0x10000\n"
"v_mul_f32 v246, s85, v246\n"
"v_mul_f32 v247, s85, v247\n"
"v_mfma_f32_16x16x32_bf16 v[252:255], a[12:15], v[64:67], v[252:255]\n"
"buffer_load_dwordx4 a[176:179], v17, s[44:47], s76 offen offset:0\n"
"v_cvt_pk_bf16_f32 v244, v244, v245\n"
"v_mfma_f32_16x16x32_bf16 v[252:255], a[28:31], v[68:71], v[252:255]\n"
"ds_read_b128 v[64:67], v27 offset:14336\n"
"v_cvt_pk_bf16_f32 v245, v246, v247\n"
"v_mfma_f32_16x16x32_bf16 v[252:255], v[92:95], v[72:75], v[252:255]\n"
"ds_write_b64 v47, v[244:245] offset:33280\n"
"v_mfma_f32_16x16x32_bf16 v[252:255], v[108:111], v[76:79], v[252:255]\n"
"s_waitcnt lgkmcnt(3)\n"
"v_mul_f32 v248, s85, v248\n"
"v_mul_f32 v249, s85, v249\n"
"v_mfma_f32_16x16x32_bf16 v[112:115], a[0:3], v[48:51], v[112:115]\n"
"ds_read_b128 v[68:71], v27 offset:14848\n"
"v_mul_f32 v250, s85, v250\n"
"v_mfma_f32_16x16x32_bf16 v[112:115], a[16:19], v[52:55], v[112:115]\n"
"ds_read_b128 v[72:75], v27 offset:10240\n"
"v_mul_f32 v251, s85, v251\n"
"v_mfma_f32_16x16x32_bf16 v[112:115], v[80:83], v[56:59], v[112:115]\n"
"ds_read_b128 v[76:79], v27 offset:10752\n"
"v_cvt_pk_bf16_f32 v248, v248, v249\n"
".Lmha_VARJ:\n"
"v_mfma_f32_16x16x32_bf16 v[112:115], v[96:99], v[60:63], v[112:115]\n"
"v_cvt_pk_bf16_f32 v249, v250, v251\n"
"v_mul_f32 v252, s85, v252\n"
"v_mul_f32 v253, s85, v253\n"
"v_mfma_f32_16x16x32_bf16 v[116:119], a[4:7], v[48:51], v[116:119]\n"
"ds_write_b64 v47, v[248:249] offset:33792\n"
"v_mfma_f32_16x16x32_bf16 v[116:119], a[20:23], v[52:55], v[116:119]\n"
"v_mul_f32 v254, s85, v254\n"
"v_mul_f32 v255, s85, v255\n"
"v_cvt_pk_bf16_f32 v252, v252, v253\n"
"v_mfma_f32_16x16x32_bf16 v[116:119], v[84:87], v[56:59], v[116:119]\n"
"v_cvt_pk_bf16_f32 v253, v254, v255\n"
"v_mul_f32 v112, s85, v112\n"
"v_mfma_f32_16x16x32_bf16 v[116:119], v[100:103], v[60:63], v[116:119]\n"
"ds_write_b64 v47, v[252:253] offset:34304\n"
"v_mfma_f32_16x16x32_bf16 v[120:123], a[8:11], v[48:51], v[120:123]\n"
"v_mul_f32 v113, s85, v113\n"
"v_mul_f32 v114, s85, v114\n"
"v_mfma_f32_16x16x32_bf16 v[120:123], a[24:27], v[52:55], v[120:123]\n"
"buffer_load_dwordx4 a[180:183], v17, s[44:47], s76 offen offset:1024\n"
"v_mul_f32 v115, s85, v115\n"
"v_mfma_f32_16x16x32_bf16 v[120:123], v[88:91], v[56:59], v[120:123]\n"
"v_cvt_pk_bf16_f32 v112, v112, v113\n"
"v_cvt_pk_bf16_f32 v113, v114, v115\n"
"v_mfma_f32_16x16x32_bf16 v[120:123], v[104:107], v[60:63], v[120:123]\n"
"ds_write_b64 v47, v[112:113] offset:16384\n"
"v_mfma_f32_16x16x32_bf16 v[124:127], a[12:15], v[48:51], v[124:127]\n"
"v_mul_f32 v116, s85, v116\n"
"v_mul_f32 v117, s85, v117\n"
"v_mfma_f32_16x16x32_bf16 v[124:127], a[28:31], v[52:55], v[124:127]\n"
"ds_read_b128 v[48:51], v41 offset:12288\n"
"v_mul_f32 v118, s85, v118\n"
"v_mul_f32 v119, s85, v119\n"
"v_mfma_f32_16x16x32_bf16 v[124:127], v[92:95], v[56:59], v[124:127]\n"
"ds_read_b128 v[52:55], v41 offset:12800\n"
"v_cvt_pk_bf16_f32 v116, v116, v117\n"
"v_mfma_f32_16x16x32_bf16 v[124:127], v[108:111], v[60:63], v[124:127]\n"
"s_waitcnt lgkmcnt(4)\n"
"ds_read_b128 v[56:59], v41 offset:8192\n"
"v_cvt_pk_bf16_f32 v117, v118, v119\n"
"v_mfma_f32_16x16x32_bf16 v[128:131], a[0:3], v[64:67], v[128:131]\n"
"ds_write_b64 v47, v[116:117] offset:16896\n"
"v_mfma_f32_16x16x32_bf16 v[128:131], a[16:19], v[68:71], v[128:131]\n"
"v_mul_f32 v120, s85, v120\n"
"v_mul_f32 v121, s85, v121\n"
"v_mul_f32 v122, s85, v122\n"
"v_mfma_f32_16x16x32_bf16 v[128:131], v[80:83], v[72:75], v[128:131]\n"
"ds_read_b128 v[60:63], v41 offset:8704\n"
"v_mul_f32 v123, s85, v123\n"
"v_cvt_pk_bf16_f32 v120, v120, v121\n"
"v_mfma_f32_16x16x32_bf16 v[128:131], v[96:99], v[76:79], v[128:131]\n"
"v_cvt_pk_bf16_f32 v121, v122, v123\n"
"v_mul_f32 v124, s85, v124\n"
"v_mul_f32 v125, s85, v125\n"
"v_mfma_f32_16x16x32_bf16 v[132:135], a[4:7], v[64:67], v[132:135]\n"
"ds_write_b64 v47, v[120:121] offset:17408\n"
"v_mfma_f32_16x16x32_bf16 v[132:135], a[20:23], v[68:71], v[132:135]\n"
"buffer_load_dwordx4 a[184:187], v18, s[44:47], s76 offen offset:2048\n"
"v_mul_f32 v126, s85, v126\n"
"v_mul_f32 v127, s85, v127\n"
"v_mfma_f32_16x16x32_bf16 v[132:135], v[84:87], v[72:75], v[132:135]\n"
"v_cvt_pk_bf16_f32 v124, v124, v125\n"
"v_cvt_pk_bf16_f32 v125, v126, v127\n"
"v_mfma_f32_16x16x32_bf16 v[132:135], v[100:103], v[76:79], v[132:135]\n"
"ds_write_b64 v47, v[124:125] offset:17920\n"
"v_mfma_f32_16x16x32_bf16 v[136:139], a[8:11], v[64:67], v[136:139]\n"
"v_mul_f32 v128, s85, v128\n"
"v_mul_f32 v129, s85, v129\n"
"v_mfma_f32_16x16x32_bf16 v[136:139], a[24:27], v[68:71], v[136:139]\n"
"v_mul_f32 v130, s85, v130\n"
"v_mul_f32 v131, s85, v131\n"
"v_cvt_pk_bf16_f32 v128, v128, v129\n"
"v_mfma_f32_16x16x32_bf16 v[136:139], v[88:91], v[72:75], v[136:139]\n"
"v_cvt_pk_bf16_f32 v129, v130, v131\n"
"v_mul_f32 v132, s85, v132\n"
"v_mfma_f32_16x16x32_bf16 v[136:139], v[104:107], v[76:79], v[136:139]\n"
"ds_write_b64 v47, v[128:129] offset:18432\n"
"v_mfma_f32_16x16x32_bf16 v[140:143], a[12:15], v[64:67], v[140:143]\n"
"v_mul_f32 v133, s85, v133\n"
"v_mul_f32 v134, s85, v134\n"
"v_mfma_f32_16x16x32_bf16 v[140:143], a[28:31], v[68:71], v[140:143]\n"
"ds_read_b128 v[64:67], v41 offset:14336\n"
"v_mul_f32 v135, s85, v135\n"
"v_mfma_f32_16x16x32_bf16 v[140:143], v[92:95], v[72:75], v[140:143]\n"
"ds_read_b128 v[68:71], v41 offset:14848\n"
"v_cvt_pk_bf16_f32 v132, v132, v133\n"
"v_mfma_f32_16x16x32_bf16 v[140:143], v[108:111], v[76:79], v[140:143]\n"
"s_waitcnt lgkmcnt(4)\n"
"ds_read_b128 v[72:75], v41 offset:10240\n"
"v_cvt_pk_bf16_f32 v133, v134, v135\n"
"v_mfma_f32_16x16x32_bf16 v[144:147], a[0:3], v[48:51], v[144:147]\n"
"ds_write_b64 v47, v[132:133] offset:18944\n"
"v_mfma_f32_16x16x32_bf16 v[144:147], a[16:19], v[52:55], v[144:147]\n"
"v_mul_f32 v136, s85, v136\n"
"v_mul_f32 v137, s85, v137\n"
"v_mul_f32 v138, s85, v138\n"
"v_mfma_f32_16x16x32_bf16 v[144:147], v[80:83], v[56:59], v[144:147]\n"
"buffer_load_dwordx4 a[188:191], v18, s[44:47], s76 offen offset:3072\n"
"v_mul_f32 v139, s85, v139\n"
"v_cvt_pk_bf16_f32 v136, v136, v137\n"
"v_mfma_f32_16x16x32_bf16 v[144:147], v[96:99], v[60:63], v[144:147]\n"
"ds_read_b128 v[76:79], v41 offset:10752\n"
"v_cvt_pk_bf16_f32 v137, v138, v139\n"
"v_mul_f32 v140, s85, v140\n"
"v_mfma_f32_16x16x32_bf16 v[148:151], a[4:7], v[48:51], v[148:151]\n"
"ds_write_b64 v47, v[136:137] offset:19456\n"
"v_mfma_f32_16x16x32_bf16 v[148:151], a[20:23], v[52:55], v[148:151]\n"
"v_mul_f32 v141, s85, v141\n"
"v_mul_f32 v142, s85, v142\n"
"v_mul_f32 v143, s85, v143\n"
"v_mfma_f32_16x16x32_bf16 v[148:151], v[84:87], v[56:59], v[148:151]\n"
"v_cvt_pk_bf16_f32 v140, v140, v141\n"
"v_cvt_pk_bf16_f32 v141, v142, v143\n"
"v_mul_f32 v144, s85, v144\n"
"v_mfma_f32_16x16x32_bf16 v[148:151], v[100:103], v[60:63], v[148:151]\n"
"ds_write_b64 v47, v[140:141] offset:19968\n"
"v_mfma_f32_16x16x32_bf16 v[152:155], a[8:11], v[48:51], v[152:155]\n"
"v_mul_f32 v145, s85, v145\n"
"v_mul_f32 v146, s85, v146\n"
"v_mfma_f32_16x16x32_bf16 v[152:155], a[24:27], v[52:55], v[152:155]\n"
"v_mul_f32 v147, s85, v147\n"
"v_cvt_pk_bf16_f32 v144, v144, v145\n"
"v_mfma_f32_16x16x32_bf16 v[152:155], v[88:91], v[56:59], v[152:155]\n"
"v_cvt_pk_bf16_f32 v145, v146, v147\n"
"v_mul_f32 v148, s85, v148\n"
"v_mfma_f32_16x16x32_bf16 v[152:155], v[104:107], v[60:63], v[152:155]\n"
"ds_write_b64 v47, v[144:145] offset:20480\n"
"v_mfma_f32_16x16x32_bf16 v[156:159], a[12:15], v[48:51], v[156:159]\n"
"v_mul_f32 v149, s85, v149\n"
"v_mul_f32 v150, s85, v150\n"
"v_mfma_f32_16x16x32_bf16 v[156:159], a[28:31], v[52:55], v[156:159]\n"
"s_mov_b32 s75, 0xc000\n"
"ds_read_b128 v[48:51], v42 offset:12288\n"
"v_mul_f32 v151, s85, v151\n"
"v_mfma_f32_16x16x32_bf16 v[156:159], v[92:95], v[56:59], v[156:159]\n"
"buffer_load_dwordx4 a[192:195], v19, s[48:51], s75 offen offset:0\n"
"v_cvt_pk_bf16_f32 v148, v148, v149\n"
"v_mfma_f32_16x16x32_bf16 v[156:159], v[108:111], v[60:63], v[156:159]\n"
"s_waitcnt lgkmcnt(3)\n"
"ds_read_b128 v[52:55], v42 offset:12800\n"
"v_cvt_pk_bf16_f32 v149, v150, v151\n"
"v_mfma_f32_16x16x32_bf16 v[160:163], a[0:3], v[64:67], v[160:163]\n"
"ds_write_b64 v47, v[148:149] offset:20992\n"
"v_mfma_f32_16x16x32_bf16 v[160:163], a[16:19], v[68:71], v[160:163]\n"
"v_mul_f32 v152, s85, v152\n"
"v_mul_f32 v153, s85, v153\n"
"v_mul_f32 v154, s85, v154\n"
"v_mfma_f32_16x16x32_bf16 v[160:163], v[80:83], v[72:75], v[160:163]\n"
"ds_read_b128 v[56:59], v42 offset:8192\n"
"v_mul_f32 v155, s85, v155\n"
"v_cvt_pk_bf16_f32 v152, v152, v153\n"
"v_mfma_f32_16x16x32_bf16 v[160:163], v[96:99], v[76:79], v[160:163]\n"
"ds_read_b128 v[60:63], v42 offset:8704\n"
"v_cvt_pk_bf16_f32 v153, v154, v155\n"
"v_mul_f32 v156, s85, v156\n"
"v_mfma_f32_16x16x32_bf16 v[164:167], a[4:7], v[64:67], v[164:167]\n"
"ds_write_b64 v47, v[152:153] offset:21504\n"
"v_mfma_f32_16x16x32_bf16 v[164:167], a[20:23], v[68:71], v[164:167]\n"
"v_mul_f32 v157, s85, v157\n"
"v_mul_f32 v158, s85, v158\n"
"v_mul_f32 v159, s85, v159\n"
"v_mfma_f32_16x16x32_bf16 v[164:167], v[84:87], v[72:75], v[164:167]\n"
"v_cvt_pk_bf16_f32 v156, v156, v157\n"
"v_cvt_pk_bf16_f32 v157, v158, v159\n"
"v_mul_f32 v160, s85, v160\n"
"v_mfma_f32_16x16x32_bf16 v[164:167], v[100:103], v[76:79], v[164:167]\n"
"ds_write_b64 v47, v[156:157] offset:22016\n"
"v_mfma_f32_16x16x32_bf16 v[168:171], a[8:11], v[64:67], v[168:171]\n"
"v_mul_f32 v161, s85, v161\n"
"v_mul_f32 v162, s85, v162\n"
"v_mfma_f32_16x16x32_bf16 v[168:171], a[24:27], v[68:71], v[168:171]\n"
"v_mul_f32 v163, s85, v163\n"
"v_cvt_pk_bf16_f32 v160, v160, v161\n"
"v_mfma_f32_16x16x32_bf16 v[168:171], v[88:91], v[72:75], v[168:171]\n"
"v_cvt_pk_bf16_f32 v161, v162, v163\n"
"v_mul_f32 v164, s85, v164\n"
"v_mfma_f32_16x16x32_bf16 v[168:171], v[104:107], v[76:79], v[168:171]\n"
"buffer_load_dwordx4 a[196:199], v20, s[48:51], s75 offen offset:1024\n"
"v_mul_f32 v165, s85, v165\n"
"v_mfma_f32_16x16x32_bf16 v[172:175], a[12:15], v[64:67], v[172:175]\n"
"ds_write_b64 v47, v[160:161] offset:22528\n"
"v_mfma_f32_16x16x32_bf16 v[172:175], a[28:31], v[68:71], v[172:175]\n"
"v_mul_f32 v166, s85, v166\n"
"v_mul_f32 v167, s85, v167\n"
"v_cvt_pk_bf16_f32 v164, v164, v165\n"
"v_mfma_f32_16x16x32_bf16 v[172:175], v[92:95], v[72:75], v[172:175]\n"
"ds_read_b128 v[64:67], v42 offset:14336\n"
"v_cvt_pk_bf16_f32 v165, v166, v167\n"
"v_mfma_f32_16x16x32_bf16 v[172:175], v[108:111], v[76:79], v[172:175]\n"
"s_waitcnt lgkmcnt(3)\n"
"ds_write_b64 v47, v[164:165] offset:23040\n"
"v_mfma_f32_16x16x32_bf16 v[176:179], a[0:3], v[48:51], v[176:179]\n"
"v_mul_f32 v168, s85, v168\n"
"v_mul_f32 v169, s85, v169\n"
"v_mfma_f32_16x16x32_bf16 v[176:179], a[16:19], v[52:55], v[176:179]\n"
"ds_read_b128 v[68:71], v42 offset:14848\n"
"v_mul_f32 v170, s85, v170\n"
"v_mul_f32 v171, s85, v171\n"
"v_mfma_f32_16x16x32_bf16 v[176:179], v[80:83], v[56:59], v[176:179]\n"
"ds_read_b128 v[72:75], v42 offset:10240\n"
"v_cvt_pk_bf16_f32 v168, v168, v169\n"
"v_mul_f32 v172, s85, v172\n"
"v_mfma_f32_16x16x32_bf16 v[176:179], v[96:99], v[60:63], v[176:179]\n"
"ds_read_b128 v[76:79], v42 offset:10752\n"
"v_cvt_pk_bf16_f32 v169, v170, v171\n"
"v_mul_f32 v173, s85, v173\n"
"v_mfma_f32_16x16x32_bf16 v[180:183], a[4:7], v[48:51], v[180:183]\n"
"ds_write_b64 v47, v[168:169] offset:23552\n"
"v_mfma_f32_16x16x32_bf16 v[180:183], a[20:23], v[52:55], v[180:183]\n"
"v_mul_f32 v174, s85, v174\n"
"v_mul_f32 v175, s85, v175\n"
"v_cvt_pk_bf16_f32 v172, v172, v173\n"
"v_mfma_f32_16x16x32_bf16 v[180:183], v[84:87], v[56:59], v[180:183]\n"
"v_cvt_pk_bf16_f32 v173, v174, v175\n"
"v_mul_f32 v176, s85, v176\n"
"v_mfma_f32_16x16x32_bf16 v[180:183], v[100:103], v[60:63], v[180:183]\n"
"buffer_load_dwordx4 a[200:203], v19, s[48:51], s75 offen offset:2048\n"
"v_mul_f32 v177, s85, v177\n"
"v_mfma_f32_16x16x32_bf16 v[184:187], a[8:11], v[48:51], v[184:187]\n"
"ds_write_b64 v47, v[172:173] offset:24064\n"
"v_mfma_f32_16x16x32_bf16 v[184:187], a[24:27], v[52:55], v[184:187]\n"
"v_mul_f32 v178, s85, v178\n"
"v_mul_f32 v179, s85, v179\n"
"v_cvt_pk_bf16_f32 v176, v176, v177\n"
"v_mfma_f32_16x16x32_bf16 v[184:187], v[88:91], v[56:59], v[184:187]\n"
"v_cvt_pk_bf16_f32 v177, v178, v179\n"
"v_mul_f32 v180, s85, v180\n"
"v_mfma_f32_16x16x32_bf16 v[184:187], v[104:107], v[60:63], v[184:187]\n"
"ds_write_b64 v47, v[176:177] offset:24576\n"
"v_mfma_f32_16x16x32_bf16 v[188:191], a[12:15], v[48:51], v[188:191]\n"
"v_mul_f32 v181, s85, v181\n"
"v_mul_f32 v182, s85, v182\n"
"v_mfma_f32_16x16x32_bf16 v[188:191], a[28:31], v[52:55], v[188:191]\n"
"ds_read_b128 v[48:51], v43 offset:12288\n"
"v_mul_f32 v183, s85, v183\n"
"v_mfma_f32_16x16x32_bf16 v[188:191], v[92:95], v[56:59], v[188:191]\n"
"ds_read_b128 v[52:55], v43 offset:12800\n"
"v_cvt_pk_bf16_f32 v180, v180, v181\n"
"v_mfma_f32_16x16x32_bf16 v[188:191], v[108:111], v[60:63], v[188:191]\n"
"s_waitcnt lgkmcnt(4)\n"
"ds_read_b128 v[56:59], v43 offset:8192\n"
"v_cvt_pk_bf16_f32 v181, v182, v183\n"
"v_mfma_f32_16x16x32_bf16 v[192:195], a[0:3], v[64:67], v[192:195]\n"
"ds_write_b64 v47, v[180:181] offset:25088\n"
"v_mfma_f32_16x16x32_bf16 v[192:195], a[16:19], v[68:71], v[192:195]\n"
"v_mul_f32 v184, s85, v184\n"
"v_mul_f32 v185, s85, v185\n"
"v_mul_f32 v186, s85, v186\n"
"v_mfma_f32_16x16x32_bf16 v[192:195], v[80:83], v[72:75], v[192:195]\n"
"ds_read_b128 v[60:63], v43 offset:8704\n"
"v_mul_f32 v187, s85, v187\n"
"v_cvt_pk_bf16_f32 v184, v184, v185\n"
"v_mfma_f32_16x16x32_bf16 v[192:195], v[96:99], v[76:79], v[192:195]\n"
"buffer_load_dwordx4 a[204:207], v20, s[48:51], s75 offen offset:3072\n"
"v_cvt_pk_bf16_f32 v185, v186, v187\n"
"v_mul_f32 v188, s85, v188\n"
"v_mfma_f32_16x16x32_bf16 v[196:199], a[4:7], v[64:67], v[196:199]\n"
"ds_write_b64 v47, v[184:185] offset:25600\n"
"v_mfma_f32_16x16x32_bf16 v[196:199], a[20:23], v[68:71], v[196:199]\n"
"v_mul_f32 v189, s85, v189\n"
"v_mul_f32 v190, s85, v190\n"
"v_mul_f32 v191, s85, v191\n"
"v_mfma_f32_16x16x32_bf16 v[196:199], v[84:87], v[72:75], v[196:199]\n"
"v_cvt_pk_bf16_f32 v188, v188, v189\n"
"v_cvt_pk_bf16_f32 v189, v190, v191\n"
"v_mul_f32 v192, s85, v192\n"
"v_mfma_f32_16x16x32_bf16 v[196:199], v[100:103], v[76:79], v[196:199]\n"
"ds_write_b64 v47, v[188:189] offset:26112\n"
"v_mfma_f32_16x16x32_bf16 v[200:203], a[8:11], v[64:67], v[200:203]\n"
"v_mul_f32 v193, s85, v193\n"
"v_mul_f32 v194, s85, v194\n"
"v_mfma_f32_16x16x32_bf16 v[200:203], a[24:27], v[68:71], v[200:203]\n"
"v_mul_f32 v195, s85, v195\n"
"v_cvt_pk_bf16_f32 v192, v192, v193\n"
"v_mfma_f32_16x16x32_bf16 v[200:203], v[88:91], v[72:75], v[200:203]\n"
"v_cvt_pk_bf16_f32 v193, v194, v195\n"
"v_mul_f32 v196, s85, v196\n"
"v_mfma_f32_16x16x32_bf16 v[200:203], v[104:107], v[76:79], v[200:203]\n"
"ds_write_b64 v47, v[192:193] offset:26624\n"
"v_mfma_f32_16x16x32_bf16 v[204:207], a[12:15], v[64:67], v[204:207]\n"
"v_mul_f32 v197, s85, v197\n"
"v_mul_f32 v198, s85, v198\n"
"v_mfma_f32_16x16x32_bf16 v[204:207], a[28:31], v[68:71], v[204:207]\n"
"s_mov_b32 s76, 0x14000\n"
"ds_read_b128 v[64:67], v43 offset:14336\n"
"v_mul_f32 v199, s85, v199\n"
"v_mfma_f32_16x16x32_bf16 v[204:207], v[92:95], v[72:75], v[204:207]\n"
"buffer_load_dwordx4 a[208:211], v17, s[44:47], s76 offen offset:0\n"
"v_cvt_pk_bf16_f32 v196, v196, v197\n"
"v_mfma_f32_16x16x32_bf16 v[204:207], v[108:111], v[76:79], v[204:207]\n"
"s_waitcnt lgkmcnt(3)\n"
"ds_read_b128 v[68:71], v43 offset:14848\n"
"v_cvt_pk_bf16_f32 v197, v198, v199\n"
"v_mfma_f32_16x16x32_bf16 v[208:211], a[0:3], v[48:51], v[208:211]\n"
"ds_write_b64 v47, v[196:197] offset:27136\n"
"v_mfma_f32_16x16x32_bf16 v[208:211], a[16:19], v[52:55], v[208:211]\n"
"v_mul_f32 v200, s85, v200\n"
"v_mul_f32 v201, s85, v201\n"
"v_mul_f32 v202, s85, v202\n"
"v_mfma_f32_16x16x32_bf16 v[208:211], v[80:83], v[56:59], v[208:211]\n"
"ds_read_b128 v[72:75], v43 offset:10240\n"
"v_mul_f32 v203, s85, v203\n"
"v_cvt_pk_bf16_f32 v200, v200, v201\n"
"v_mfma_f32_16x16x32_bf16 v[208:211], v[96:99], v[60:63], v[208:211]\n"
"ds_read_b128 v[76:79], v43 offset:10752\n"
"v_cvt_pk_bf16_f32 v201, v202, v203\n"
"v_mul_f32 v204, s85, v204\n"
"v_mul_f32 v205, s85, v205\n"
"v_mul_f32 v206, s85, v206\n"
"v_mfma_f32_16x16x32_bf16 v[212:215], a[4:7], v[48:51], v[212:215]\n"
"ds_write_b64 v47, v[200:201] offset:27648\n"
"v_mfma_f32_16x16x32_bf16 v[212:215], a[20:23], v[52:55], v[212:215]\n"
"v_mul_f32 v207, s85, v207\n"
"v_cvt_pk_bf16_f32 v204, v204, v205\n"
"v_mul_f32 v208, s85, v208\n"
"v_mul_f32 v209, s85, v209\n"
"v_mul_f32 v210, s85, v210\n"
"v_mfma_f32_16x16x32_bf16 v[212:215], v[84:87], v[56:59], v[212:215]\n"
"v_cvt_pk_bf16_f32 v205, v206, v207\n"
"v_mul_f32 v211, s85, v211\n"
"v_cvt_pk_bf16_f32 v208, v208, v209\n"
"v_mfma_f32_16x16x32_bf16 v[212:215], v[100:103], v[60:63], v[212:215]\n"
"ds_write_b64 v47, v[204:205] offset:28160\n"
"v_mfma_f32_16x16x32_bf16 v[216:219], a[8:11], v[48:51], v[216:219]\n"
"v_cvt_pk_bf16_f32 v209, v210, v211\n"
"v_add_u32 v252, 0x14000, v30\n"
"v_mfma_f32_16x16x32_bf16 v[216:219], a[24:27], v[52:55], v[216:219]\n"
"ds_write_b64 v47, v[208:209] offset:28672\n"
"v_mfma_f32_16x16x32_bf16 v[216:219], v[88:91], v[56:59], v[216:219]\n"
"buffer_load_dwordx4 a[212:215], v17, s[44:47], s76 offen offset:1024\n"
"v_mul_f32 v212, s85, v212\n"
"v_mul_f32 v213, s85, v213\n"
"v_mul_f32 v214, s85, v214\n"
"v_mul_f32 v215, s85, v215\n"
"v_mfma_f32_16x16x32_bf16 v[216:219], v[104:107], v[60:63], v[216:219]\n"
"v_cvt_pk_bf16_f32 v212, v212, v213\n"
"v_cvt_pk_bf16_f32 v213, v214, v215\n"
"v_mfma_f32_16x16x32_bf16 v[220:223], a[12:15], v[48:51], v[220:223]\n"
"ds_write_b64 v47, v[212:213] offset:29184\n"
"v_mfma_f32_16x16x32_bf16 v[220:223], a[28:31], v[52:55], v[220:223]\n"
"s_nop 0\n"
"s_nop 0\n"
"s_nop 0\n"
"v_mfma_f32_16x16x32_bf16 v[220:223], v[92:95], v[56:59], v[220:223]\n"
"v_mul_f32 v216, s85, v216\n"
"v_mul_f32 v217, s85, v217\n"
"v_mul_f32 v218, s85, v218\n"
"v_mul_f32 v219, s85, v219\n"
"v_mfma_f32_16x16x32_bf16 v[220:223], v[108:111], v[60:63], v[220:223]\n"
"s_waitcnt lgkmcnt(3)\n"
"v_cvt_pk_bf16_f32 v216, v216, v217\n"
"v_cvt_pk_bf16_f32 v217, v218, v219\n"
"v_mfma_f32_16x16x32_bf16 v[224:227], a[0:3], v[64:67], v[224:227]\n"
"ds_write_b64 v47, v[216:217] offset:29696\n"
"v_mfma_f32_16x16x32_bf16 v[224:227], a[16:19], v[68:71], v[224:227]\n"
"s_nop 0\n"
"s_nop 0\n"
"s_nop 0\n"
"v_mfma_f32_16x16x32_bf16 v[224:227], v[80:83], v[72:75], v[224:227]\n"
"ds_read_b128 a[0:3], v2 offset:0\n"
"v_mul_f32 v220, s85, v220\n"
"v_mul_f32 v221, s85, v221\n"
"v_mul_f32 v222, s85, v222\n"
"v_mul_f32 v223, s85, v223\n"
"v_mfma_f32_16x16x32_bf16 v[224:227], v[96:99], v[76:79], v[224:227]\n"
"ds_read_b128 a[16:19], v3 offset:0\n"
"v_cvt_pk_bf16_f32 v220, v220, v221\n"
"v_mfma_f32_16x16x32_bf16 v[228:231], a[4:7], v[64:67], v[228:231]\n"
"ds_read_b128 a[4:7], v2 offset:256\n"
"v_cvt_pk_bf16_f32 v221, v222, v223\n"
"v_mfma_f32_16x16x32_bf16 v[228:231], a[20:23], v[68:71], v[228:231]\n"
"buffer_load_dwordx4 a[216:219], v18, s[44:47], s76 offen offset:2048\n"
"ds_read_b128 a[20:23], v3 offset:256\n"
"v_mul_f32 v224, s85, v224\n"
"v_mul_f32 v225, s85, v225\n"
"v_mul_f32 v226, s85, v226\n"
"v_mfma_f32_16x16x32_bf16 v[228:231], v[84:87], v[72:75], v[228:231]\n"
"ds_write_b64 v47, v[220:221] offset:30208\n"
"v_mfma_f32_16x16x32_bf16 v[228:231], v[100:103], v[76:79], v[228:231]\n"
"v_mul_f32 v227, s85, v227\n"
"v_cvt_pk_bf16_f32 v224, v224, v225\n"
"v_mfma_f32_16x16x32_bf16 v[232:235], a[8:11], v[64:67], v[232:235]\n"
"ds_read_b128 a[8:11], v2 offset:512\n"
"v_cvt_pk_bf16_f32 v225, v226, v227\n"
"v_mfma_f32_16x16x32_bf16 v[232:235], a[24:27], v[68:71], v[232:235]\n"
"ds_write_b64 v47, v[224:225] offset:30720\n"
"v_mfma_f32_16x16x32_bf16 v[232:235], v[88:91], v[72:75], v[232:235]\n"
"v_mul_f32 v228, s85, v228\n"
"v_mul_f32 v229, s85, v229\n"
"v_mul_f32 v230, s85, v230\n"
"v_mul_f32 v231, s85, v231\n"
"v_mfma_f32_16x16x32_bf16 v[232:235], v[104:107], v[76:79], v[232:235]\n"
"ds_read_b128 a[24:27], v3 offset:512\n"
"v_cvt_pk_bf16_f32 v228, v228, v229\n"
"v_mfma_f32_16x16x32_bf16 v[236:239], a[12:15], v[64:67], v[236:239]\n"
"ds_read_b128 a[12:15], v2 offset:768\n"
"v_cvt_pk_bf16_f32 v229, v230, v231\n"
"v_mfma_f32_16x16x32_bf16 v[236:239], a[28:31], v[68:71], v[236:239]\n"
"ds_write_b64 v47, v[228:229] offset:31232\n"
"v_mfma_f32_16x16x32_bf16 v[236:239], v[92:95], v[72:75], v[236:239]\n"
"v_mul_f32 v232, s85, v232\n"
"v_mul_f32 v233, s85, v233\n"
"v_mul_f32 v234, s85, v234\n"
"v_mul_f32 v235, s85, v235\n"
"v_cvt_pk_bf16_f32 v232, v232, v233\n"
"v_cvt_pk_bf16_f32 v233, v234, v235\n"
"v_mfma_f32_16x16x32_bf16 v[236:239], v[108:111], v[76:79], v[236:239]\n"
"ds_write_b64 v47, v[232:233] offset:31744\n"
"ds_read_b128 a[28:31], v3 offset:768\n"
"v_mov_b32 v228, 0x3f803f80\n"
"v_mov_b32 v229, 0x3f803f80\n"
"v_mov_b32 v230, 0x3f803f80\n"
"v_mov_b32 v231, 0x3f803f80\n"
"s_nop 1\n"
"v_mul_f32 v236, s85, v236\n"
"v_mul_f32 v237, s85, v237\n"
"v_mul_f32 v238, s85, v238\n"
"v_mul_f32 v239, s85, v239\n"
"v_cvt_pk_bf16_f32 v236, v236, v237\n"
"v_cvt_pk_bf16_f32 v237, v238, v239\n"
"ds_write_b64 v47, v[236:237] offset:32256\n"
"buffer_load_dwordx4 a[220:223], v18, s[44:47], s76 offen offset:3072\n"
"s_waitcnt lgkmcnt(0)\n"
"s_barrier\n"
"ds_read_b128 v[64:67], v30 offset:0\n"
"ds_read_b128 v[68:71], v30 offset:1024\n"
"ds_read_b128 v[72:75], v30 offset:40960\n"
"ds_read_b128 v[76:79], v30 offset:41984\n"
"ds_read_b128 v[32:35], v28 offset:0\n"
"ds_read_b128 v[36:39], v28 offset:1024\n"
"ds_read_b128 v[40:43], v28 offset:40960\n"
"ds_read_b128 v[44:47], v28 offset:41984\n"
"ds_read_b128 v[48:51], v28 offset:2048\n"
"ds_read_b128 v[52:55], v28 offset:3072\n"
"s_waitcnt lgkmcnt(9)\n"
"v_mfma_f32_16x16x32_bf16 v[152:155], a[32:35], v[64:67], 0\n"
"s_waitcnt lgkmcnt(8)\n"
"v_mov_b32 v30, 0\n"
"v_mfma_f32_16x16x32_bf16 v[152:155], a[36:39], v[68:71], v[152:155]\n"
"s_waitcnt lgkmcnt(7)\n"
"ds_read_b128 v[56:59], v28 offset:43008\n"
"ds_read_b128 v[60:63], v28 offset:44032\n"
"v_mfma_f32_16x16x32_bf16 v[152:155], a[40:43], v[72:75], v[152:155]\n"
"s_waitcnt lgkmcnt(8)\n"
"ds_write_b128 v13, a[64:67] offset:8192\n"
"v_mfma_f32_16x16x32_bf16 v[152:155], a[44:47], v[76:79], v[152:155]\n"
"s_waitcnt lgkmcnt(8)\n"
"v_mfma_f32_16x16x32_bf16 v[156:159], a[48:51], v[32:35], 0\n"
"s_waitcnt lgkmcnt(7)\n"
"v_mfma_f32_16x16x32_bf16 v[156:159], a[52:55], v[36:39], v[156:159]\n"
"s_waitcnt lgkmcnt(6)\n"
"ds_read_b128 a[32:35], v4 offset:0\n"
"v_mfma_f32_16x16x32_bf16 v[156:159], a[56:59], v[40:43], v[156:159]\n"
"s_waitcnt lgkmcnt(6)\n"
"ds_read_b128 a[36:39], v4 offset:256\n"
"ds_read_b128 a[40:43], v4 offset:512\n"
"v_max3_f32 v244, v152, v153, v154\n"
"v_mfma_f32_16x16x32_bf16 v[156:159], a[60:63], v[44:47], v[156:159]\n"
"ds_read_b128 a[44:47], v4 offset:768\n"
"v_max_f32 v244, v244, v155\n"
"v_mfma_f32_16x16x32_bf16 v[160:163], a[0:3], v[32:35], 0\n"
"ds_read_b128 a[96:99], v252 offset:0\n"
"v_mov_b32 v245, v244\n"
"v_mfma_f32_16x16x32_bf16 v[160:163], a[4:7], v[36:39], v[160:163]\n"
"ds_write_b128 v13, a[68:71] offset:9216\n"
"v_mfma_f32_16x16x32_bf16 v[160:163], a[8:11], v[40:43], v[160:163]\n"
"v_permlane32_swap_b32 v244, v245\n"
"v_max_f32 v244, v244, v245\n"
"v_max3_f32 v246, v156, v157, v158\n"
"v_mfma_f32_16x16x32_bf16 v[160:163], a[12:15], v[44:47], v[160:163]\n"
"s_waitcnt lgkmcnt(6)\n"
"s_mov_b32 s77, 0x10000\n"
"v_mov_b32 v245, v244\n"
"v_mfma_f32_16x16x32_bf16 v[164:167], a[48:51], v[48:51], 0\n"
"buffer_load_dwordx4 a[224:227], v19, s[48:51], s77 offen offset:0\n"
"v_permlane16_swap_b32 v244, v245\n"
"v_mfma_f32_16x16x32_bf16 v[164:167], a[52:55], v[52:55], v[164:167]\n"
"ds_read_b128 a[100:103], v252 offset:1024\n"
"v_max_f32 v244, v244, v245\n"
"v_max3_f32 v246, v246, v159, v160\n"
"v_mfma_f32_16x16x32_bf16 v[164:167], a[56:59], v[56:59], v[164:167]\n"
"ds_read_b128 a[104:107], v252 offset:40960\n"
"v_max3_f32 v246, v246, v161, v162\n"
"v_mul_f32 v240, -1.0, v244\n"
"v_mul_f32 v241, -1.0, v244\n"
"v_mfma_f32_16x16x32_bf16 v[164:167], a[60:63], v[60:63], v[164:167]\n"
"ds_read_b128 a[108:111], v252 offset:41984\n"
"v_mul_f32 v242, -1.0, v244\n"
"v_mul_f32 v243, -1.0, v244\n"
"v_mfma_f32_16x16x32_bf16 v[168:171], a[0:3], v[48:51], 0\n"
"ds_read_b128 a[0:3], v2 offset:4096\n"
"v_max_f32 v246, v246, v163\n"
"v_mfma_f32_16x16x32_bf16 v[168:171], a[4:7], v[52:55], v[168:171]\n"
"ds_read_b128 a[64:67], v29 offset:0\n"
"v_mov_b32 v247, v246\n"
"v_max3_f32 v248, v164, v165, v166\n"
"v_mfma_f32_16x16x32_bf16 v[168:171], a[8:11], v[56:59], v[168:171]\n"
"ds_write_b128 v13, a[72:75] offset:10240\n"
"v_mfma_f32_16x16x32_bf16 v[168:171], a[12:15], v[60:63], v[168:171]\n"
"v_permlane32_swap_b32 v246, v247\n"
"v_max_f32 v246, v246, v247\n"
"v_mfma_f32_16x16x32_bf16 v[172:175], a[48:51], v[64:67], v[240:243]\n"
"v_mov_b32 v247, v246\n"
"v_mfma_f32_16x16x32_bf16 v[172:175], a[52:55], v[68:71], v[172:175]\n"
"ds_read_b128 a[48:51], v1 offset:4096\n"
"ds_read_b128 a[52:55], v1 offset:4352\n"
"v_permlane16_swap_b32 v246, v247\n"
"v_max3_f32 v248, v248, v167, v168\n"
"v_mfma_f32_16x16x32_bf16 v[172:175], a[56:59], v[72:75], v[172:175]\n"
"ds_read_b128 a[56:59], v1 offset:4608\n"
"ds_read_b128 a[68:71], v29 offset:1024\n"
"v_max_f32 v246, v246, v247\n"
"v_max3_f32 v248, v248, v169, v170\n"
"v_mfma_f32_16x16x32_bf16 v[172:175], a[60:63], v[76:79], v[172:175]\n"
"ds_read_b128 a[60:63], v1 offset:4864\n"
"v_mul_f32 v232, -1.0, v246\n"
"v_mul_f32 v233, -1.0, v246\n"
"v_mul_f32 v234, -1.0, v246\n"
"v_mul_f32 v235, -1.0, v246\n"
"v_max_f32 v248, v248, v171\n"
"v_mov_b32 v249, v248\n"
"v_mfma_f32_16x16x32_bf16 v[176:179], a[16:19], v[32:35], v[232:235]\n"
"buffer_load_dwordx4 a[228:231], v20, s[48:51], s77 offen offset:1024\n"
"v_permlane32_swap_b32 v248, v249\n"
"v_exp_f32 v172, v172\n"
"v_mfma_f32_16x16x32_bf16 v[176:179], a[20:23], v[36:39], v[176:179]\n"
"ds_read_b128 a[72:75], v29 offset:40960\n"
"v_max_f32 v248, v248, v249\n"
"v_exp_f32 v173, v173\n"
"v_mov_b32 v249, v248\n"
"v_mfma_f32_16x16x32_bf16 v[176:179], a[24:27], v[40:43], v[176:179]\n"
"ds_write_b128 v13, a[76:79] offset:11264\n"
"v_permlane16_swap_b32 v248, v249\n"
"v_mfma_f32_16x16x32_bf16 v[176:179], a[28:31], v[44:47], v[176:179]\n"
"v_max_f32 v248, v248, v249\n"
"v_mul_f32 v236, -1.0, v248\n"
"v_add_f32 v254, v172, v173\n"
"v_mul_f32 v237, -1.0, v248\n"
"v_mul_f32 v238, -1.0, v248\n"
"v_mul_f32 v239, -1.0, v248\n"
"v_exp_f32 v174, v174\n"
"v_cvt_pk_bf16_f32 v172, v172, v173\n"
"v_mfma_f32_16x16x32_bf16 v[180:183], a[16:19], v[48:51], v[236:239]\n"
"v_exp_f32 v175, v175\n"
"v_mfma_f32_16x16x32_bf16 v[180:183], a[20:23], v[52:55], v[180:183]\n"
"ds_read_b128 a[76:79], v29 offset:41984\n"
"v_cvt_pk_bf16_f32 v173, v174, v175\n"
"v_mfma_f32_16x16x32_bf16 v[180:183], a[24:27], v[56:59], v[180:183]\n"
"ds_write_b128 v13, a[80:83] offset:12288\n"
"v_mfma_f32_16x16x32_bf16 v[180:183], a[28:31], v[60:63], v[180:183]\n"
"s_waitcnt lgkmcnt(4)\n"
"v_add_f32 v156, v156, v232\n"
"v_add_f32 v157, v157, v232\n"
"v_mfma_f32_16x16x32_bf16 v[184:187], a[32:35], v[32:35], v[232:235]\n"
"v_add_f32 v158, v158, v232\n"
"v_add_f32 v159, v159, v232\n"
"v_add_f32 v160, v160, v232\n"
"v_mfma_f32_16x16x32_bf16 v[184:187], a[36:39], v[36:39], v[184:187]\n"
"ds_read_b128 a[80:83], v29 offset:2048\n"
"ds_read_b128 a[4:7], v2 offset:4352\n"
"v_add_f32 v161, v161, v232\n"
"v_mfma_f32_16x16x32_bf16 v[184:187], a[40:43], v[40:43], v[184:187]\n"
"ds_write_b64 v13, v[172:173] offset:38912\n"
"v_mfma_f32_16x16x32_bf16 v[184:187], a[44:47], v[44:47], v[184:187]\n"
"buffer_load_dwordx4 a[232:235], v19, s[48:51], s77 offen offset:2048\n"
"v_add_f32 v162, v162, v232\n"
"v_exp_f32 v156, v156\n"
"v_exp_f32 v157, v157\n"
"v_exp_f32 v158, v158\n"
"v_mfma_f32_16x16x32_bf16 v[188:191], a[32:35], v[48:51], v[236:239]\n"
"ds_read_b128 a[8:11], v2 offset:4608\n"
"ds_read_b128 a[12:15], v2 offset:4864\n"
"v_exp_f32 v159, v159\n"
"v_cvt_pk_bf16_f32 v200, v156, v157\n"
"v_add_f32 v163, v163, v232\n"
"v_mfma_f32_16x16x32_bf16 v[188:191], a[36:39], v[52:55], v[188:191]\n"
"ds_write_b128 v13, a[84:87] offset:13312\n"
"v_mfma_f32_16x16x32_bf16 v[188:191], a[40:43], v[56:59], v[188:191]\n"
"v_cvt_pk_bf16_f32 v201, v158, v159\n"
"v_add_f32 v164, v164, v236\n"
"v_exp_f32 v160, v160\n"
"v_exp_f32 v161, v161\n"
"v_exp_f32 v162, v162\n"
"v_mfma_f32_16x16x32_bf16 v[188:191], a[44:47], v[60:63], v[188:191]\n"
"v_exp_f32 v163, v163\n"
"v_cvt_pk_bf16_f32 v202, v160, v161\n"
"v_add_f32 v165, v165, v236\n"
"v_mfma_f32_16x16x32_bf16 v[192:195], a[48:51], v[64:67], v[240:243]\n"
"s_memrealtime s[24:25]\n"
"ds_read_b128 a[84:87], v29 offset:3072\n"
"v_cvt_pk_bf16_f32 v203, v162, v163\n"
"v_add_f32 v166, v166, v236\n"
"v_mfma_f32_16x16x32_bf16 v[192:195], a[52:55], v[68:71], v[192:195]\n"
"v_add_f32 v167, v167, v236\n"
"v_add_f32 v168, v168, v236\n"
"v_add_f32 v169, v169, v236\n"
"v_mfma_f32_16x16x32_bf16 v[192:195], a[56:59], v[72:75], v[192:195]\n"
"v_add_f32 v170, v170, v236\n"
"v_add_f32 v171, v171, v236\n"
"v_mfma_f32_16x16x32_bf16 v[192:195], a[60:63], v[76:79], v[192:195]\n"
"v_exp_f32 v164, v164\n"
"v_mfma_f32_16x16x32_bf16 v[196:199], a[48:51], v[32:35], v[232:235]\n"
"ds_write_b128 v13, a[88:91] offset:14336\n"
"ds_write_b128 v13, a[92:95] offset:15360\n"
"v_mfma_f32_16x16x32_bf16 v[196:199], a[52:55], v[36:39], v[196:199]\n"
"s_waitcnt lgkmcnt(0)\n"
"s_add_u32 s24, s24, s92\n"
"ds_read_b128 a[88:91], v29 offset:43008\n"
"ds_read_b128 a[92:95], v29 offset:44032\n"
"v_mfma_f32_16x16x32_bf16 v[196:199], a[56:59], v[40:43], v[196:199]\n"
"s_add_u32 s5, s24, 0x50\n"
"v_exp_f32 v165, v165\n"
"v_mfma_f32_16x16x32_bf16 v[196:199], a[60:63], v[44:47], v[196:199]\n"
"buffer_load_dwordx4 a[236:239], v20, s[48:51], s77 offen offset:3072\n"
"v_cvt_pk_bf16_f32 v204, v164, v165\n"
"v_mfma_f32_16x16x32_bf16 v[152:155], a[48:51], v[48:51], v[236:239]\n"
"ds_read_b128 a[48:51], v3 offset:4096\n"
"v_exp_f32 v166, v166\n"
"v_mfma_f32_16x16x32_bf16 v[152:155], a[52:55], v[52:55], v[152:155]\n"
"ds_read_b128 a[52:55], v3 offset:4352\n"
"v_exp_f32 v167, v167\n"
"v_mfma_f32_16x16x32_bf16 v[152:155], a[56:59], v[56:59], v[152:155]\n"
"ds_read_b128 a[56:59], v3 offset:4608\n"
"v_exp_f32 v168, v168\n"
"v_mfma_f32_16x16x32_bf16 v[152:155], a[60:63], v[60:63], v[152:155]\n"
"ds_read_b128 a[60:63], v3 offset:4864\n"
"v_exp_f32 v169, v169\n"
"v_mfma_f32_16x16x32_bf16 v[156:159], a[0:3], v[32:35], v[232:235]\n"
"v_exp_f32 v170, v170\n"
"v_cvt_pk_bf16_f32 v205, v166, v167\n"
"v_mfma_f32_16x16x32_bf16 v[156:159], a[4:7], v[36:39], v[156:159]\n"
"s_waitcnt lgkmcnt(4)\n"
"s_barrier\n"
"v_mfma_f32_16x16x32_bf16 v[156:159], a[8:11], v[40:43], v[156:159]\n"
"ds_write_b128 v13, a[128:131] offset:0\n"
"ds_read_b64_tr_b16 a[16:17], v5 offset:8448\n"
"ds_read_b64_tr_b16 a[18:19], v7 offset:8192\n"
"v_mfma_f32_16x16x32_bf16 v[156:159], a[12:15], v[44:47], v[156:159]\n"
"ds_read_b64_tr_b16 a[20:21], v6 offset:8448\n"
"ds_read_b64_tr_b16 a[22:23], v8 offset:8192\n"
"v_cvt_pk_bf16_f32 v206, v168, v169\n"
"v_mfma_f32_16x16x32_bf16 v[160:163], a[0:3], v[48:51], v[236:239]\n"
"ds_read_b64_tr_b16 a[24:25], v5 offset:8704\n"
"ds_read_b64_tr_b16 a[26:27], v7 offset:8960\n"
"v_mfma_f32_16x16x32_bf16 v[160:163], a[4:7], v[52:55], v[160:163]\n"
"s_add_u32 m0, s30, 0x4000\n"
"v_exp_f32 v171, v171\n"
"v_mfma_f32_16x16x32_bf16 v[160:163], a[8:11], v[56:59], v[160:163]\n"
"s_mov_b32 s78, 0x18000\n"
"ds_read_b64_tr_b16 a[28:29], v6 offset:8704\n"
"v_cvt_pk_bf16_f32 v207, v170, v171\n"
"v_mfma_f32_16x16x32_bf16 v[160:163], a[12:15], v[60:63], v[160:163]\n"
"ds_read_b64_tr_b16 a[30:31], v8 offset:8960\n"
"buffer_load_dwordx4 v17, s[44:47], s78 offen offset:0 nt lds\n"
"v_mfma_f32_16x16x32_bf16 a[120:123], v[228:231], v[200:203], 0\n"
"ds_write_b128 v13, a[132:135] offset:1024\n"
"v_mfma_f32_16x16x32_bf16 a[124:127], v[228:231], v[204:207], 0\n"
"s_waitcnt lgkmcnt(7)\n"
"ds_read_b64_tr_b16 a[32:33], v5 offset:8960\n"
"ds_read_b64_tr_b16 a[34:35], v7 offset:8704\n"
"v_mfma_f32_16x16x32_bf16 v[88:91], a[16:19], v[200:203], 0\n"
"ds_read_b64_tr_b16 a[36:37], v6 offset:8960\n"
"ds_read_b64_tr_b16 a[38:39], v8 offset:8704\n"
"v_mfma_f32_16x16x32_bf16 v[120:123], a[16:19], v[204:207], 0\n"
"s_waitcnt lgkmcnt(9)\n"
"ds_read_b64_tr_b16 a[40:41], v5 offset:8192\n"
"ds_read_b64_tr_b16 a[42:43], v7 offset:8448\n"
"v_mfma_f32_16x16x32_bf16 v[92:95], a[20:23], v[200:203], 0\n"
"ds_write_b128 v13, a[136:139] offset:2048\n"
"v_mfma_f32_16x16x32_bf16 v[124:127], a[20:23], v[204:207], 0\n"
"s_waitcnt lgkmcnt(10)\n"
"v_add_f32 v254, v254, v174\n"
"v_add_f32 v254, v254, v175\n"
"v_mfma_f32_16x16x32_bf16 v[96:99], a[24:27], v[200:203], 0\n"
"s_getpc_b64 s[98:99]\n"
"ds_read_b64 a[112:113], v13 offset:38912\n"
"ds_read_b64 a[114:115], v14 offset:38912\n"
"v_mfma_f32_16x16x32_bf16 v[128:131], a[24:27], v[204:207], 0\n"
"s_waitcnt lgkmcnt(10)\n"
"ds_read_b64_tr_b16 a[44:45], v6 offset:8192\n"
"ds_read_b64_tr_b16 a[46:47], v8 offset:8448\n"
"v_mfma_f32_16x16x32_bf16 v[100:103], a[28:31], v[200:203], 0\n"
"global_load_dword v250, v0, s[98:99] offset:1536\n"
"ds_read_b128 a[0:3], v4 offset:4096\n"
"v_mfma_f32_16x16x32_bf16 v[132:135], a[28:31], v[204:207], 0\n"
"s_waitcnt lgkmcnt(10)\n"
"ds_read_b128 a[4:7], v4 offset:4352\n"
"ds_read_b128 a[8:11], v4 offset:4608\n"
"v_mfma_f32_16x16x32_bf16 v[104:107], a[32:35], v[200:203], 0\n"
"buffer_load_dwordx4 v17, s[44:47], s78 offen offset:1024 nt lds\n"
"ds_read_b128 a[12:15], v4 offset:4864\n"
"v_mfma_f32_16x16x32_bf16 v[136:139], a[32:35], v[204:207], 0\n"
"s_waitcnt lgkmcnt(11)\n"
"ds_write_b128 v13, a[140:143] offset:3072\n"
"v_mfma_f32_16x16x32_bf16 v[108:111], a[36:39], v[200:203], 0\n"
"v_exp_f32 v176, v176\n"
"v_exp_f32 v177, v177\n"
"v_mfma_f32_16x16x32_bf16 v[140:143], a[36:39], v[204:207], 0\n"
"s_waitcnt lgkmcnt(9)\n"
"v_exp_f32 v178, v178\n"
"v_exp_f32 v179, v179\n"
"v_cvt_pk_bf16_f32 v208, v176, v177\n"
"v_mfma_f32_16x16x32_bf16 v[80:83], a[40:43], v[200:203], 0\n"
"ds_read_b64_tr_b16 a[16:17], v9 offset:8960\n"
"v_cvt_pk_bf16_f32 v209, v178, v179\n"
"v_exp_f32 v184, v184\n"
"v_mfma_f32_16x16x32_bf16 v[112:115], a[40:43], v[204:207], 0\n"
"s_waitcnt lgkmcnt(8)\n"
"ds_read_b64_tr_b16 a[18:19], v11 offset:8704\n"
"v_exp_f32 v185, v185\n"
"v_exp_f32 v186, v186\n"
"v_exp_f32 v187, v187\n"
"v_mfma_f32_16x16x32_bf16 v[144:147], a[40:43], a[112:115], 0\n"
"s_waitcnt lgkmcnt(5)\n"
"ds_read_b64_tr_b16 a[20:21], v10 offset:8960\n"
"v_cvt_pk_bf16_f32 v210, v184, v185\n"
"v_cvt_pk_bf16_f32 v211, v186, v187\n"
"v_mfma_f32_16x16x32_bf16 v[84:87], a[44:47], v[200:203], 0\n"
"ds_read_b64_tr_b16 a[22:23], v12 offset:8704\n"
"ds_read_b64_tr_b16 a[24:25], v9 offset:8192\n"
"v_exp_f32 v180, v180\n"
"v_mfma_f32_16x16x32_bf16 v[116:119], a[44:47], v[204:207], 0\n"
"ds_read_b64_tr_b16 a[26:27], v11 offset:8448\n"
"ds_read_b64_tr_b16 a[28:29], v10 offset:8192\n"
"v_exp_f32 v181, v181\n"
"v_mfma_f32_16x16x32_bf16 v[148:151], a[44:47], a[112:115], 0\n"
"ds_read_b64_tr_b16 a[30:31], v12 offset:8448\n"
"v_exp_f32 v182, v182\n"
"v_mfma_f32_16x16x32_bf16 v[164:167], a[48:51], v[32:35], v[232:235]\n"
"ds_read_b64_tr_b16 a[32:33], v9 offset:8448\n"
"v_exp_f32 v183, v183\n"
"v_mfma_f32_16x16x32_bf16 v[164:167], a[52:55], v[36:39], v[164:167]\n"
"buffer_load_dwordx4 v18, s[44:47], s78 offen offset:2048 nt lds\n"
"v_cvt_pk_bf16_f32 v212, v180, v181\n"
"v_cvt_pk_bf16_f32 v213, v182, v183\n"
"v_mfma_f32_16x16x32_bf16 v[164:167], a[56:59], v[40:43], v[164:167]\n"
"v_exp_f32 v188, v188\n"
"v_mfma_f32_16x16x32_bf16 v[164:167], a[60:63], v[44:47], v[164:167]\n"
"v_exp_f32 v189, v189\n"
"v_mfma_f32_16x16x32_bf16 v[168:171], a[48:51], v[48:51], v[236:239]\n"
"v_exp_f32 v190, v190\n"
"v_mfma_f32_16x16x32_bf16 v[168:171], a[52:55], v[52:55], v[168:171]\n"
"v_exp_f32 v191, v191\n"
"v_mfma_f32_16x16x32_bf16 v[168:171], a[56:59], v[56:59], v[168:171]\n"
"ds_read_b64_tr_b16 a[34:35], v11 offset:8192\n"
"v_cvt_pk_bf16_f32 v214, v188, v189\n"
"v_mfma_f32_16x16x32_bf16 v[168:171], a[60:63], v[60:63], v[168:171]\n"
"ds_read_b64 a[116:117], v15 offset:38912\n"
"v_cvt_pk_bf16_f32 v215, v190, v191\n"
"v_mfma_f32_16x16x32_bf16 v[172:175], a[0:3], v[32:35], v[232:235]\n"
"v_exp_f32 v192, v192\n"
"v_mfma_f32_16x16x32_bf16 v[172:175], a[4:7], v[36:39], v[172:175]\n"
"s_waitcnt lgkmcnt(4)\n"
"v_exp_f32 v193, v193\n"
"v_mfma_f32_16x16x32_bf16 v[172:175], a[8:11], v[40:43], v[172:175]\n"
"ds_read_b64 a[118:119], v16 offset:38912\n"
"v_add_f32 v254, v254, v192\n"
"v_mfma_f32_16x16x32_bf16 v[172:175], a[12:15], v[44:47], v[172:175]\n"
"v_exp_f32 v194, v194\n"
"v_mfma_f32_16x16x32_bf16 v[176:179], a[0:3], v[48:51], v[236:239]\n"
"v_exp_f32 v195, v195\n"
"v_mfma_f32_16x16x32_bf16 v[176:179], a[4:7], v[52:55], v[176:179]\n"
"buffer_load_dwordx4 v18, s[44:47], s78 offen offset:3072 nt lds\n"
"v_add_f32 v254, v254, v193\n"
"v_mfma_f32_16x16x32_bf16 v[176:179], a[8:11], v[56:59], v[176:179]\n"
"ds_read_b64_tr_b16 a[36:37], v10 offset:8448\n"
"v_cvt_pk_bf16_f32 v192, v192, v193\n"
"v_mfma_f32_16x16x32_bf16 v[176:179], a[12:15], v[60:63], v[176:179]\n"
"ds_read_b64_tr_b16 a[38:39], v12 offset:8192\n"
"v_cvt_pk_bf16_f32 v193, v194, v195\n"
"v_mfma_f32_16x16x32_bf16 a[120:123], v[228:231], v[208:211], a[120:123]\n"
"ds_write_b64 v13, v[192:193] offset:38920\n"
"v_mfma_f32_16x16x32_bf16 a[124:127], v[228:231], v[212:215], a[124:127]\n"
"ds_read_b64_tr_b16 a[40:41], v9 offset:8704\n"
"ds_read_b64_tr_b16 a[42:43], v11 offset:8960\n"
"v_mfma_f32_16x16x32_bf16 v[88:91], a[16:19], v[208:211], v[88:91]\n"
"ds_read_b64_tr_b16 a[44:45], v10 offset:8704\n"
"ds_read_b64_tr_b16 a[46:47], v12 offset:8960\n"
"v_mfma_f32_16x16x32_bf16 v[120:123], a[16:19], v[212:215], v[120:123]\n"
"v_add_f32 v254, v254, v194\n"
"v_add_f32 v254, v254, v195\n"
"v_mfma_f32_16x16x32_bf16 v[92:95], a[20:23], v[208:211], v[92:95]\n"
"s_memrealtime s[24:25]\n"
"v_exp_f32 v196, v196\n"
"v_mfma_f32_16x16x32_bf16 v[124:127], a[20:23], v[212:215], v[124:127]\n"
"v_exp_f32 v197, v197\n"
"v_mfma_f32_16x16x32_bf16 v[96:99], a[24:27], v[208:211], v[96:99]\n"
"v_exp_f32 v198, v198\n"
"v_mfma_f32_16x16x32_bf16 v[128:131], a[24:27], v[212:215], v[128:131]\n"
"s_waitcnt lgkmcnt(0)\n"
"v_exp_f32 v199, v199\n"
"v_mfma_f32_16x16x32_bf16 v[100:103], a[28:31], v[208:211], v[100:103]\n"
"s_add_u32 m0, s30, 0x5000\n"
"s_nop 0\n"
"buffer_load_dwordx4 v19, s[48:51], s76 offen offset:0 nt lds\n"
"v_cvt_pk_bf16_f32 v216, v196, v197\n"
"v_mfma_f32_16x16x32_bf16 v[132:135], a[28:31], v[212:215], v[132:135]\n"
"s_sub_u32 s25, s24, s5\n"
"s_cmp_lt_i32 s25, 0\n"
"s_cbranch_scc1 .Lmha_LIM2\n"
".Lmha_LIM2R:\n"
"v_exp_f32 v156, v156\n"
"v_mfma_f32_16x16x32_bf16 v[104:107], a[32:35], v[208:211], v[104:107]\n"
"s_barrier\n"
"v_exp_f32 v157, v157\n"
"v_mfma_f32_16x16x32_bf16 v[136:139], a[32:35], v[212:215], v[136:139]\n"
"s_add_u32 s24, s24, s92\n"
"ds_read_b128 a[48:51], v1 offset:12288\n"
"ds_read_b128 a[52:55], v1 offset:12544\n"
"v_mfma_f32_16x16x32_bf16 v[108:111], a[36:39], v[208:211], v[108:111]\n"
"s_add_u32 s5, s24, 0x66\n"
"ds_read_b128 a[56:59], v1 offset:12800\n"
"ds_read_b128 a[60:63], v1 offset:13056\n"
"v_mfma_f32_16x16x32_bf16 v[140:143], a[36:39], v[212:215], v[140:143]\n"
"ds_read_b128 a[0:3], v2 offset:12288\n"
"ds_read_b128 a[4:7], v2 offset:12544\n"
"v_mfma_f32_16x16x32_bf16 v[80:83], a[40:43], v[208:211], v[80:83]\n"
"ds_write_b128 v13, a[144:147] offset:4096\n"
"v_mfma_f32_16x16x32_bf16 v[112:115], a[40:43], v[212:215], v[112:115]\n"
"v_cvt_pk_bf16_f32 v217, v198, v199\n"
"v_cvt_pk_bf16_f32 v218, v156, v157\n"
"v_mfma_f32_16x16x32_bf16 v[144:147], a[40:43], a[116:119], v[144:147]\n"
"v_exp_f32 v158, v158\n"
"v_mfma_f32_16x16x32_bf16 v[84:87], a[44:47], v[208:211], v[84:87]\n"
"ds_read_b128 a[8:11], v2 offset:12800\n"
"ds_read_b128 a[12:15], v2 offset:13056\n"
"v_mfma_f32_16x16x32_bf16 v[116:119], a[44:47], v[212:215], v[116:119]\n"
"v_exp_f32 v159, v159\n"
"v_mfma_f32_16x16x32_bf16 v[148:151], a[44:47], a[116:119], v[148:151]\n"
"s_waitcnt lgkmcnt(8)\n"
"ds_read_b64_tr_b16 a[16:17], v5 offset:256\n"
"v_cvt_pk_bf16_f32 v219, v158, v159\n"
"v_mfma_f32_16x16x32_bf16 v[180:183], a[48:51], v[64:67], v[240:243]\n"
"s_waitcnt lgkmcnt(8)\n"
"buffer_load_dwordx4 v20, s[48:51], s76 offen offset:1024 nt lds\n"
"ds_read_b64_tr_b16 a[18:19], v7 offset:0\n"
"v_mfma_f32_16x16x32_bf16 v[180:183], a[52:55], v[68:71], v[180:183]\n"
"s_waitcnt lgkmcnt(8)\n"
"ds_write_b128 v13, a[148:151] offset:5120\n"
"v_mfma_f32_16x16x32_bf16 v[180:183], a[56:59], v[72:75], v[180:183]\n"
"s_waitcnt lgkmcnt(8)\n"
"v_exp_f32 v152, v152\n"
"v_mfma_f32_16x16x32_bf16 v[180:183], a[60:63], v[76:79], v[180:183]\n"
"v_exp_f32 v153, v153\n"
"v_mfma_f32_16x16x32_bf16 v[184:187], a[48:51], v[32:35], v[232:235]\n"
"ds_read_b64_tr_b16 a[20:21], v6 offset:256\n"
"ds_read_b64_tr_b16 a[22:23], v8 offset:0\n"
"v_mfma_f32_16x16x32_bf16 v[184:187], a[52:55], v[36:39], v[184:187]\n"
"ds_read_b64_tr_b16 a[24:25], v5 offset:512\n"
"v_cvt_pk_bf16_f32 v220, v152, v153\n"
"v_mfma_f32_16x16x32_bf16 v[184:187], a[56:59], v[40:43], v[184:187]\n"
"ds_read_b64_tr_b16 a[26:27], v7 offset:768\n"
"ds_read_b64_tr_b16 a[28:29], v6 offset:512\n"
"v_mfma_f32_16x16x32_bf16 v[184:187], a[60:63], v[44:47], v[184:187]\n"
"v_exp_f32 v154, v154\n"
"v_mfma_f32_16x16x32_bf16 v[188:191], a[48:51], v[48:51], v[236:239]\n"
"ds_write_b128 v13, a[152:155] offset:6144\n"
"v_mfma_f32_16x16x32_bf16 v[188:191], a[52:55], v[52:55], v[188:191]\n"
"v_exp_f32 v155, v155\n"
"v_mfma_f32_16x16x32_bf16 v[188:191], a[56:59], v[56:59], v[188:191]\n"
"v_exp_f32 v160, v160\n"
"v_mfma_f32_16x16x32_bf16 v[188:191], a[60:63], v[60:63], v[188:191]\n"
"s_waitcnt lgkmcnt(6)\n"
"buffer_load_dwordx4 v19, s[48:51], s76 offen offset:2048 nt lds\n"
"ds_read_b64_tr_b16 a[30:31], v8 offset:768\n"
"v_mfma_f32_16x16x32_bf16 v[192:195], a[0:3], v[32:35], v[232:235]\n"
"v_exp_f32 v161, v161\n"
"v_mfma_f32_16x16x32_bf16 v[192:195], a[4:7], v[36:39], v[192:195]\n"
"v_exp_f32 v162, v162\n"
"v_mfma_f32_16x16x32_bf16 v[192:195], a[8:11], v[40:43], v[192:195]\n"
"v_exp_f32 v163, v163\n"
"v_mfma_f32_16x16x32_bf16 v[192:195], a[12:15], v[44:47], v[192:195]\n"
"ds_write_b128 v13, a[156:159] offset:7168\n"
"v_mfma_f32_16x16x32_bf16 v[196:199], a[0:3], v[48:51], v[236:239]\n"
"v_cvt_pk_bf16_f32 v221, v154, v155\n"
"v_cvt_pk_bf16_f32 v222, v160, v161\n"
"v_mfma_f32_16x16x32_bf16 v[196:199], a[4:7], v[52:55], v[196:199]\n"
"v_exp_f32 v164, v164\n"
"v_mfma_f32_16x16x32_bf16 v[196:199], a[8:11], v[56:59], v[196:199]\n"
"ds_read_b64_tr_b16 a[32:33], v5 offset:768\n"
"v_cvt_pk_bf16_f32 v223, v162, v163\n"
"v_mfma_f32_16x16x32_bf16 v[196:199], a[12:15], v[60:63], v[196:199]\n"
"ds_read_b64_tr_b16 a[34:35], v7 offset:512\n"
"ds_read_b64_tr_b16 a[36:37], v6 offset:768\n"
"v_mfma_f32_16x16x32_bf16 a[120:123], v[228:231], v[216:219], a[120:123]\n"
"ds_read_b64_tr_b16 a[38:39], v8 offset:512\n"
"ds_read_b64_tr_b16 a[40:41], v5 offset:0\n"
"v_mfma_f32_16x16x32_bf16 a[124:127], v[228:231], v[220:223], a[124:127]\n"
"ds_read_b64_tr_b16 a[42:43], v7 offset:256\n"
"ds_read_b64 a[112:113], v13 offset:38920\n"
"v_mfma_f32_16x16x32_bf16 v[88:91], a[16:19], v[216:219], v[88:91]\n"
"s_waitcnt vmcnt(27)\n"
"ds_write_b128 v13, a[160:163] offset:8192\n"
"v_mfma_f32_16x16x32_bf16 v[120:123], a[16:19], v[220:223], v[120:123]\n"
"s_waitcnt lgkmcnt(9)\n"
"buffer_load_dwordx4 v20, s[48:51], s76 offen offset:3072 nt lds\n"
"v_mfma_f32_16x16x32_bf16 v[92:95], a[20:23], v[216:219], v[92:95]\n"
"v_exp_f32 v165, v165\n"
"v_mfma_f32_16x16x32_bf16 v[124:127], a[20:23], v[220:223], v[124:127]\n"
"ds_read_b64 a[114:115], v14 offset:38920\n"
"ds_read_b64_tr_b16 a[44:45], v6 offset:0\n"
"v_mfma_f32_16x16x32_bf16 v[96:99], a[24:27], v[216:219], v[96:99]\n"
"ds_read_b64_tr_b16 a[46:47], v8 offset:256\n"
"ds_read_b128 a[48:51], v3 offset:12288\n"
"v_mfma_f32_16x16x32_bf16 v[128:131], a[24:27], v[220:223], v[128:131]\n"
"ds_read_b128 a[52:55], v3 offset:12544\n"
"ds_read_b128 a[56:59], v3 offset:12800\n"
"v_mfma_f32_16x16x32_bf16 v[100:103], a[28:31], v[216:219], v[100:103]\n"
"ds_read_b128 a[60:63], v3 offset:13056\n"
"ds_read_b128 a[0:3], v4 offset:12288\n"
"v_mfma_f32_16x16x32_bf16 v[132:135], a[28:31], v[220:223], v[132:135]\n"
"s_waitcnt vmcnt(27) lgkmcnt(13)\n"
"ds_write_b128 v13, a[164:167] offset:9216\n"
"v_mfma_f32_16x16x32_bf16 v[104:107], a[32:35], v[216:219], v[104:107]\n"
"v_exp_f32 v166, v166\n"
"v_mfma_f32_16x16x32_bf16 v[136:139], a[32:35], v[220:223], v[136:139]\n"
"s_waitcnt lgkmcnt(9)\n"
"v_exp_f32 v167, v167\n"
"v_mfma_f32_16x16x32_bf16 v[108:111], a[36:39], v[216:219], v[108:111]\n"
"s_add_u32 m0, s30, 0x6000\n"
"ds_read_b128 a[4:7], v4 offset:12544\n"
"ds_read_b128 a[8:11], v4 offset:12800\n"
"v_cvt_pk_bf16_f32 v224, v164, v165\n"
"v_mfma_f32_16x16x32_bf16 v[140:143], a[36:39], v[220:223], v[140:143]\n"
"s_mov_b32 s84, 0x1c000\n"
"ds_read_b128 a[12:15], v4 offset:13056\n"
"v_cvt_pk_bf16_f32 v225, v166, v167\n"
"v_exp_f32 v172, v172\n"
"v_exp_f32 v173, v173\n"
"v_mfma_f32_16x16x32_bf16 v[80:83], a[40:43], v[216:219], v[80:83]\n"
"buffer_load_dwordx4 v17, s[44:47], s84 offen offset:0 nt lds\n"
"ds_read_b64_tr_b16 a[16:17], v9 offset:768\n"
"v_exp_f32 v174, v174\n"
"v_mfma_f32_16x16x32_bf16 v[112:115], a[40:43], v[220:223], v[112:115]\n"
"s_waitcnt lgkmcnt(8)\n"
"buffer_load_dwordx2 v[28:29], v30, s[64:67], s94 offen offset:0 sc1\n"
"ds_read_b64_tr_b16 a[18:19], v11 offset:512\n"
"v_exp_f32 v175, v175\n"
"v_cvt_pk_bf16_f32 v226, v172, v173\n"
"v_exp_f32 v168, v168\n"
"v_mfma_f32_16x16x32_bf16 v[144:147], a[40:43], a[112:115], v[144:147]\n"
"s_waitcnt vmcnt(28)\n"
"ds_write_b128 v13, a[168:171] offset:10240\n"
"v_mfma_f32_16x16x32_bf16 v[84:87], a[44:47], v[216:219], v[84:87]\n"
"v_exp_f32 v169, v169\n"
"v_cvt_pk_bf16_f32 v227, v174, v175\n"
"v_mfma_f32_16x16x32_bf16 v[116:119], a[44:47], v[220:223], v[116:119]\n"
"v_exp_f32 v170, v170\n"
"v_exp_f32 v171, v171\n"
"v_cvt_pk_bf16_f32 v200, v168, v169\n"
"v_mfma_f32_16x16x32_bf16 v[148:151], a[44:47], a[112:115], v[148:151]\n"
"ds_read_b64_tr_b16 a[20:21], v10 offset:768\n"
"ds_read_b64_tr_b16 a[22:23], v12 offset:512\n"
"v_cvt_pk_bf16_f32 v201, v170, v171\n"
"v_mfma_f32_16x16x32_bf16 v[152:155], a[48:51], v[32:35], v[232:235]\n"
"ds_read_b64_tr_b16 a[24:25], v9 offset:0\n"
"ds_read_b64_tr_b16 a[26:27], v11 offset:256\n"
"v_mfma_f32_16x16x32_bf16 v[152:155], a[52:55], v[36:39], v[152:155]\n"
"s_waitcnt lgkmcnt(7)\n"
"ds_read_b64_tr_b16 a[28:29], v10 offset:0\n"
"v_exp_f32 v176, v176\n"
"v_mfma_f32_16x16x32_bf16 v[152:155], a[56:59], v[40:43], v[152:155]\n"
"ds_read_b64_tr_b16 a[30:31], v12 offset:256\n"
"v_exp_f32 v177, v177\n"
"v_mfma_f32_16x16x32_bf16 v[152:155], a[60:63], v[44:47], v[152:155]\n"
"s_waitcnt vmcnt(27)\n"
"ds_write_b128 v13, a[172:175] offset:11264\n"
"v_mfma_f32_16x16x32_bf16 v[156:159], a[48:51], v[48:51], v[236:239]\n"
"v_exp_f32 v178, v178\n"
"v_cvt_pk_bf16_f32 v202, v176, v177\n"
"v_mfma_f32_16x16x32_bf16 v[156:159], a[52:55], v[52:55], v[156:159]\n"
"v_exp_f32 v179, v179\n"
"v_mfma_f32_16x16x32_bf16 v[156:159], a[56:59], v[56:59], v[156:159]\n"
"buffer_load_dwordx4 v17, s[44:47], s84 offen offset:1024 nt lds\n"
"v_cvt_pk_bf16_f32 v203, v178, v179\n"
"v_mfma_f32_16x16x32_bf16 v[156:159], a[60:63], v[60:63], v[156:159]\n"
"v_exp_f32 v180, v180\n"
"v_mfma_f32_16x16x32_bf16 v[160:163], a[0:3], v[32:35], v[232:235]\n"
"v_exp_f32 v181, v181\n"
"v_mfma_f32_16x16x32_bf16 v[160:163], a[4:7], v[36:39], v[160:163]\n"
"ds_read_b64_tr_b16 a[32:33], v9 offset:256\n"
"v_add_f32 v254, v254, v180\n"
"v_mfma_f32_16x16x32_bf16 v[160:163], a[8:11], v[40:43], v[160:163]\n"
"v_exp_f32 v182, v182\n"
"v_mfma_f32_16x16x32_bf16 v[160:163], a[12:15], v[44:47], v[160:163]\n"
"v_exp_f32 v183, v183\n"
"v_mfma_f32_16x16x32_bf16 v[164:167], a[0:3], v[48:51], v[236:239]\n"
"ds_read_b64_tr_b16 a[34:35], v11 offset:0\n"
"v_add_f32 v254, v254, v181\n"
"v_mfma_f32_16x16x32_bf16 v[164:167], a[4:7], v[52:55], v[164:167]\n"
"ds_read_b64 a[116:117], v15 offset:38920\n"
"v_cvt_pk_bf16_f32 v180, v180, v181\n"
"v_mfma_f32_16x16x32_bf16 v[164:167], a[8:11], v[56:59], v[164:167]\n"
"ds_read_b64 a[118:119], v16 offset:38920\n"
"v_cvt_pk_bf16_f32 v181, v182, v183\n"
"v_mfma_f32_16x16x32_bf16 v[164:167], a[12:15], v[60:63], v[164:167]\n"
"ds_write_b64 v13, v[180:181] offset:38912\n"
"v_mfma_f32_16x16x32_bf16 a[120:123], v[228:231], v[224:227], a[120:123]\n"
"v_add_f32 v254, v254, v182\n"
"v_add_f32 v254, v254, v183\n"
"v_mfma_f32_16x16x32_bf16 a[124:127], v[228:231], v[200:203], a[124:127]\n"
"s_waitcnt lgkmcnt(5)\n"
"ds_read_b64_tr_b16 a[36:37], v10 offset:256\n"
"buffer_load_dwordx4 v18, s[44:47], s84 offen offset:2048 nt lds\n"
"v_mfma_f32_16x16x32_bf16 v[88:91], a[16:19], v[224:227], v[88:91]\n"
"ds_read_b64_tr_b16 a[38:39], v12 offset:0\n"
"ds_read_b64_tr_b16 a[40:41], v9 offset:512\n"
"v_mfma_f32_16x16x32_bf16 v[120:123], a[16:19], v[200:203], v[120:123]\n"
"ds_read_b64_tr_b16 a[42:43], v11 offset:768\n"
"ds_read_b64_tr_b16 a[44:45], v10 offset:512\n"
"ds_read_b64_tr_b16 a[46:47], v12 offset:768\n"
"v_mfma_f32_16x16x32_bf16 v[92:95], a[20:23], v[224:227], v[92:95]\n"
"s_memrealtime s[24:25]\n"
"v_exp_f32 v184, v184\n"
"v_mfma_f32_16x16x32_bf16 v[124:127], a[20:23], v[200:203], v[124:127]\n"
"v_exp_f32 v185, v185\n"
"v_mfma_f32_16x16x32_bf16 v[96:99], a[24:27], v[224:227], v[96:99]\n"
"v_exp_f32 v186, v186\n"
"v_mfma_f32_16x16x32_bf16 v[128:131], a[24:27], v[200:203], v[128:131]\n"
"v_exp_f32 v187, v187\n"
"v_mfma_f32_16x16x32_bf16 v[100:103], a[28:31], v[224:227], v[100:103]\n"
"v_cvt_pk_bf16_f32 v204, v184, v185\n"
"v_cvt_pk_bf16_f32 v205, v186, v187\n"
"v_mfma_f32_16x16x32_bf16 v[132:135], a[28:31], v[200:203], v[132:135]\n"
"s_waitcnt lgkmcnt(0)\n"
"s_sub_u32 s25, s24, s5\n"
"s_cmp_lt_i32 s25, 0\n"
"s_cbranch_scc1 .Lmha_LIM3\n"
".Lmha_LIM3R:\n"
"v_mfma_f32_16x16x32_bf16 v[104:107], a[32:35], v[224:227], v[104:107]\n"
"s_add_u32 s24, s24, s92\n"
"v_exp_f32 v192, v192\n"
"v_mfma_f32_16x16x32_bf16 v[136:139], a[32:35], v[200:203], v[136:139]\n"
"s_barrier\n"
"v_exp_f32 v193, v193\n"
"v_mfma_f32_16x16x32_bf16 v[108:111], a[36:39], v[224:227], v[108:111]\n"
"s_add_u32 s5, s24, 0x66\n"
"ds_read_b128 a[48:51], v1 offset:4096\n"
"ds_read_b128 a[52:55], v1 offset:4352\n"
"v_mfma_f32_16x16x32_bf16 v[140:143], a[36:39], v[200:203], v[140:143]\n"
"ds_read_b128 a[56:59], v1 offset:4608\n"
"buffer_load_dwordx4 v18, s[44:47], s84 offen offset:3072 nt lds\n"
"v_mfma_f32_16x16x32_bf16 v[80:83], a[40:43], v[224:227], v[80:83]\n"
"ds_read_b128 a[60:63], v1 offset:4864\n"
"ds_read_b128 a[0:3], v2 offset:4096\n"
"v_mfma_f32_16x16x32_bf16 v[112:115], a[40:43], v[200:203], v[112:115]\n"
"s_waitcnt vmcnt(29)\n"
"ds_write_b128 v13, a[176:179] offset:0\n"
"v_mfma_f32_16x16x32_bf16 v[144:147], a[40:43], a[116:119], v[144:147]\n"
"v_exp_f32 v194, v194\n"
"v_mfma_f32_16x16x32_bf16 v[84:87], a[44:47], v[224:227], v[84:87]\n"
"v_exp_f32 v195, v195\n"
"v_mfma_f32_16x16x32_bf16 v[116:119], a[44:47], v[200:203], v[116:119]\n"
"ds_read_b128 a[4:7], v2 offset:4352\n"
"ds_read_b128 a[8:11], v2 offset:4608\n"
"v_mfma_f32_16x16x32_bf16 v[148:151], a[44:47], a[116:119], v[148:151]\n"
"s_waitcnt lgkmcnt(7)\n"
"ds_read_b128 a[12:15], v2 offset:4864\n"
"v_cvt_pk_bf16_f32 v206, v192, v193\n"
"v_mfma_f32_16x16x32_bf16 v[168:171], a[48:51], v[64:67], v[240:243]\n"
"s_waitcnt lgkmcnt(7)\n"
"ds_read_b64_tr_b16 a[16:17], v5 offset:8448\n"
"ds_read_b64_tr_b16 a[18:19], v7 offset:8192\n"
"v_mfma_f32_16x16x32_bf16 v[168:171], a[52:55], v[68:71], v[168:171]\n"
"s_waitcnt vmcnt(28) lgkmcnt(8)\n"
"ds_write_b128 v13, a[180:183] offset:1024\n"
"v_mfma_f32_16x16x32_bf16 v[168:171], a[56:59], v[72:75], v[168:171]\n"
"s_waitcnt lgkmcnt(8)\n"
"v_exp_f32 v188, v188\n"
"v_mfma_f32_16x16x32_bf16 v[168:171], a[60:63], v[76:79], v[168:171]\n"
"s_add_u32 m0, s30, 0x7000\n"
"v_exp_f32 v189, v189\n"
"v_mfma_f32_16x16x32_bf16 v[172:175], a[48:51], v[32:35], v[232:235]\n"
"buffer_load_dwordx4 v19, s[48:51], s78 offen offset:0 nt lds\n"
"ds_read_b64_tr_b16 a[20:21], v6 offset:8448\n"
"v_mfma_f32_16x16x32_bf16 v[172:175], a[52:55], v[36:39], v[172:175]\n"
"ds_read_b64_tr_b16 a[22:23], v8 offset:8192\n"
"v_cvt_pk_bf16_f32 v207, v194, v195\n"
"v_mfma_f32_16x16x32_bf16 v[172:175], a[56:59], v[40:43], v[172:175]\n"
"ds_read_b64_tr_b16 a[24:25], v5 offset:8704\n"
"v_cvt_pk_bf16_f32 v208, v188, v189\n"
"v_mfma_f32_16x16x32_bf16 v[172:175], a[60:63], v[44:47], v[172:175]\n"
"ds_read_b64_tr_b16 a[26:27], v7 offset:8960\n"
"ds_read_b64_tr_b16 a[28:29], v6 offset:8704\n"
"v_mfma_f32_16x16x32_bf16 v[176:179], a[48:51], v[48:51], v[236:239]\n"
"s_waitcnt vmcnt(28)\n"
"ds_write_b128 v13, a[184:187] offset:2048\n"
"v_mfma_f32_16x16x32_bf16 v[176:179], a[52:55], v[52:55], v[176:179]\n"
"v_exp_f32 v190, v190\n"
"v_mfma_f32_16x16x32_bf16 v[176:179], a[56:59], v[56:59], v[176:179]\n"
"v_exp_f32 v191, v191\n"
"v_mfma_f32_16x16x32_bf16 v[176:179], a[60:63], v[60:63], v[176:179]\n"
"s_waitcnt lgkmcnt(6)\n"
"v_exp_f32 v196, v196\n"
"v_mfma_f32_16x16x32_bf16 v[180:183], a[0:3], v[32:35], v[232:235]\n"
"v_exp_f32 v197, v197\n"
"v_mfma_f32_16x16x32_bf16 v[180:183], a[4:7], v[36:39], v[180:183]\n"
"v_exp_f32 v198, v198\n"
"v_mfma_f32_16x16x32_bf16 v[180:183], a[8:11], v[40:43], v[180:183]\n"
"v_exp_f32 v199, v199\n"
"v_mfma_f32_16x16x32_bf16 v[180:183], a[12:15], v[44:47], v[180:183]\n"
"s_waitcnt vmcnt(27)\n"
"ds_write_b128 v13, a[188:191] offset:3072\n"
"v_mfma_f32_16x16x32_bf16 v[184:187], a[0:3], v[48:51], v[236:239]\n"
"buffer_load_dwordx4 v20, s[48:51], s78 offen offset:1024 nt lds\n"
"v_cvt_pk_bf16_f32 v209, v190, v191\n"
"v_mfma_f32_16x16x32_bf16 v[184:187], a[4:7], v[52:55], v[184:187]\n"
"v_cvt_pk_bf16_f32 v210, v196, v197\n"
"v_cvt_pk_bf16_f32 v211, v198, v199\n"
"v_mfma_f32_16x16x32_bf16 v[184:187], a[8:11], v[56:59], v[184:187]\n"
"ds_read_b64_tr_b16 a[30:31], v8 offset:8960\n"
"ds_read_b64_tr_b16 a[32:33], v5 offset:8960\n"
"v_mfma_f32_16x16x32_bf16 v[184:187], a[12:15], v[60:63], v[184:187]\n"
"ds_read_b64_tr_b16 a[34:35], v7 offset:8704\n"
"ds_read_b64_tr_b16 a[36:37], v6 offset:8960\n"
"v_mfma_f32_16x16x32_bf16 a[120:123], v[228:231], v[204:207], a[120:123]\n"
"ds_read_b64_tr_b16 a[38:39], v8 offset:8704\n"
"ds_read_b64_tr_b16 a[40:41], v5 offset:8192\n"
"v_mfma_f32_16x16x32_bf16 a[124:127], v[228:231], v[208:211], a[124:127]\n"
"ds_read_b64_tr_b16 a[42:43], v7 offset:8448\n"
"ds_read_b64 a[112:113], v13 offset:38912\n"
"v_mfma_f32_16x16x32_bf16 v[88:91], a[16:19], v[204:207], v[88:91]\n"
"s_waitcnt vmcnt(27)\n"
"ds_write_b128 v13, a[192:195] offset:12288\n"
"v_mfma_f32_16x16x32_bf16 v[120:123], a[16:19], v[208:211], v[120:123]\n"
"s_waitcnt lgkmcnt(10)\n"
"v_exp_f32 v152, v152\n"
"v_mfma_f32_16x16x32_bf16 v[92:95], a[20:23], v[204:207], v[92:95]\n"
"v_exp_f32 v153, v153\n"
"v_mfma_f32_16x16x32_bf16 v[124:127], a[20:23], v[208:211], v[124:127]\n"
"ds_read_b64 a[114:115], v14 offset:38912\n"
"ds_read_b64_tr_b16 a[44:45], v6 offset:8192\n"
"v_mfma_f32_16x16x32_bf16 v[96:99], a[24:27], v[204:207], v[96:99]\n"
"ds_read_b64_tr_b16 a[46:47], v8 offset:8448\n"
"ds_read_b128 a[48:51], v3 offset:4096\n"
"v_mfma_f32_16x16x32_bf16 v[128:131], a[24:27], v[208:211], v[128:131]\n"
"s_waitcnt lgkmcnt(12)\n"
"buffer_load_dwordx4 v19, s[48:51], s78 offen offset:2048 nt lds\n"
"ds_read_b128 a[52:55], v3 offset:4352\n"
"v_mfma_f32_16x16x32_bf16 v[100:103], a[28:31], v[204:207], v[100:103]\n"
"ds_read_b128 a[56:59], v3 offset:4608\n"
"ds_read_b128 a[60:63], v3 offset:4864\n"
"v_mfma_f32_16x16x32_bf16 v[132:135], a[28:31], v[208:211], v[132:135]\n"
"s_waitcnt vmcnt(27) lgkmcnt(12)\n"
"ds_write_b128 v13, a[196:199] offset:13312\n"
"v_mfma_f32_16x16x32_bf16 v[104:107], a[32:35], v[204:207], v[104:107]\n"
"v_exp_f32 v154, v154\n"
"v_mfma_f32_16x16x32_bf16 v[136:139], a[32:35], v[208:211], v[136:139]\n"
"s_waitcnt lgkmcnt(8)\n"
"v_exp_f32 v155, v155\n"
"v_mfma_f32_16x16x32_bf16 v[108:111], a[36:39], v[204:207], v[108:111]\n"
"ds_read_b128 a[0:3], v4 offset:4096\n"
"ds_read_b128 a[4:7], v4 offset:4352\n"
"v_cvt_pk_bf16_f32 v212, v152, v153\n"
"v_mfma_f32_16x16x32_bf16 v[140:143], a[36:39], v[208:211], v[140:143]\n"
"ds_read_b128 a[8:11], v4 offset:4608\n"
"ds_read_b128 a[12:15], v4 offset:4864\n"
"v_cvt_pk_bf16_f32 v213, v154, v155\n"
"v_exp_f32 v160, v160\n"
"v_mfma_f32_16x16x32_bf16 v[80:83], a[40:43], v[204:207], v[80:83]\n"
"ds_read_b64_tr_b16 a[16:17], v9 offset:8960\n"
"ds_read_b64_tr_b16 a[18:19], v11 offset:8704\n"
"v_exp_f32 v161, v161\n"
"v_mfma_f32_16x16x32_bf16 v[112:115], a[40:43], v[208:211], v[112:115]\n"
"s_waitcnt lgkmcnt(10)\n"
"ds_read_b64_tr_b16 a[20:21], v10 offset:8960\n"
"v_cvt_pk_bf16_f32 v214, v160, v161\n"
"v_exp_f32 v162, v162\n"
"v_exp_f32 v163, v163\n"
"v_exp_f32 v156, v156\n"
"v_mfma_f32_16x16x32_bf16 v[144:147], a[40:43], a[112:115], v[144:147]\n"
"s_waitcnt vmcnt(26)\n"
"ds_write_b128 v13, a[200:203] offset:14336\n"
"v_mfma_f32_16x16x32_bf16 v[84:87], a[44:47], v[204:207], v[84:87]\n"
"v_exp_f32 v157, v157\n"
"v_cvt_pk_bf16_f32 v215, v162, v163\n"
"v_mfma_f32_16x16x32_bf16 v[116:119], a[44:47], v[208:211], v[116:119]\n"
"v_exp_f32 v158, v158\n"
"v_exp_f32 v159, v159\n"
"v_cvt_pk_bf16_f32 v216, v156, v157\n"
"v_mfma_f32_16x16x32_bf16 v[148:151], a[44:47], a[112:115], v[148:151]\n"
"buffer_load_dwordx4 v20, s[48:51], s78 offen offset:3072 nt lds\n"
"ds_read_b64_tr_b16 a[22:23], v12 offset:8704\n"
"v_cvt_pk_bf16_f32 v217, v158, v159\n"
"v_mfma_f32_16x16x32_bf16 v[188:191], a[48:51], v[32:35], v[232:235]\n"
"s_waitcnt lgkmcnt(5)\n"
"ds_read_b64_tr_b16 a[24:25], v9 offset:8192\n"
"ds_read_b64_tr_b16 a[26:27], v11 offset:8448\n"
"v_mfma_f32_16x16x32_bf16 v[188:191], a[52:55], v[36:39], v[188:191]\n"
"ds_read_b64_tr_b16 a[28:29], v10 offset:8192\n"
"v_exp_f32 v164, v164\n"
"v_mfma_f32_16x16x32_bf16 v[188:191], a[56:59], v[40:43], v[188:191]\n"
"ds_read_b64_tr_b16 a[30:31], v12 offset:8448\n"
"v_exp_f32 v165, v165\n"
"v_mfma_f32_16x16x32_bf16 v[188:191], a[60:63], v[44:47], v[188:191]\n"
"s_waitcnt vmcnt(26)\n"
"ds_write_b128 v13, a[204:207] offset:15360\n"
"v_mfma_f32_16x16x32_bf16 v[192:195], a[48:51], v[48:51], v[236:239]\n"
"v_exp_f32 v166, v166\n"
"v_cvt_pk_bf16_f32 v218, v164, v165\n"
"v_mfma_f32_16x16x32_bf16 v[192:195], a[52:55], v[52:55], v[192:195]\n"
"v_exp_f32 v167, v167\n"
"v_mfma_f32_16x16x32_bf16 v[192:195], a[56:59], v[56:59], v[192:195]\n"
"ds_read_b64_tr_b16 a[32:33], v9 offset:8448\n"
"v_cvt_pk_bf16_f32 v219, v166, v167\n"
"v_mfma_f32_16x16x32_bf16 v[192:195], a[60:63], v[60:63], v[192:195]\n"
"v_exp_f32 v168, v168\n"
"v_mfma_f32_16x16x32_bf16 v[196:199], a[0:3], v[32:35], v[232:235]\n"
"s_add_u32 m0, s30, 0x8000\n"
"v_exp_f32 v169, v169\n"
"v_mfma_f32_16x16x32_bf16 v[196:199], a[4:7], v[36:39], v[196:199]\n"
"s_mov_b32 s96, 0x20000\n"
"ds_read_b64_tr_b16 a[34:35], v11 offset:8192\n"
"v_add_f32 v254, v254, v168\n"
"v_mfma_f32_16x16x32_bf16 v[196:199], a[8:11], v[40:43], v[196:199]\n"
"buffer_load_dwordx4 v17, s[44:47], s96 offen offset:0 nt lds\n"
"v_add_f32 v254, v254, v169\n"
"v_mfma_f32_16x16x32_bf16 v[196:199], a[12:15], v[44:47], v[196:199]\n"
"v_exp_f32 v170, v170\n"
"v_mfma_f32_16x16x32_bf16 v[152:155], a[0:3], v[48:51], v[236:239]\n"
"v_exp_f32 v171, v171\n"
"v_mfma_f32_16x16x32_bf16 v[152:155], a[4:7], v[52:55], v[152:155]\n"
"ds_read_b64 a[116:117], v15 offset:38912\n"
"v_cvt_pk_bf16_f32 v168, v168, v169\n"
"v_mfma_f32_16x16x32_bf16 v[152:155], a[8:11], v[56:59], v[152:155]\n"
"ds_read_b64 a[118:119], v16 offset:38912\n"
"v_cvt_pk_bf16_f32 v169, v170, v171\n"
"v_mfma_f32_16x16x32_bf16 v[152:155], a[12:15], v[60:63], v[152:155]\n"
"ds_write_b64 v13, v[168:169] offset:38920\n"
"v_mfma_f32_16x16x32_bf16 a[120:123], v[228:231], v[212:215], a[120:123]\n"
"v_add_f32 v254, v254, v170\n"
"v_add_f32 v254, v254, v171\n"
"v_mfma_f32_16x16x32_bf16 a[124:127], v[228:231], v[216:219], a[124:127]\n"
"s_waitcnt lgkmcnt(4)\n"
"ds_read_b64_tr_b16 a[36:37], v10 offset:8448\n"
"ds_read_b64_tr_b16 a[38:39], v12 offset:8192\n"
"v_mfma_f32_16x16x32_bf16 v[88:91], a[16:19], v[212:215], v[88:91]\n"
"ds_read_b64_tr_b16 a[40:41], v9 offset:8704\n"
"ds_read_b64_tr_b16 a[42:43], v11 offset:8960\n"
"v_mfma_f32_16x16x32_bf16 v[120:123], a[16:19], v[216:219], v[120:123]\n"
"ds_read_b64_tr_b16 a[44:45], v10 offset:8704\n"
"ds_read_b64_tr_b16 a[46:47], v12 offset:8960\n"
"v_mfma_f32_16x16x32_bf16 v[92:95], a[20:23], v[212:215], v[92:95]\n"
"s_memrealtime s[24:25]\n"
"v_exp_f32 v172, v172\n"
"v_mfma_f32_16x16x32_bf16 v[124:127], a[20:23], v[216:219], v[124:127]\n"
"v_exp_f32 v173, v173\n"
"v_mfma_f32_16x16x32_bf16 v[96:99], a[24:27], v[212:215], v[96:99]\n"
"buffer_load_dwordx4 v17, s[44:47], s96 offen offset:1024 nt lds\n"
"v_cvt_pk_bf16_f32 v220, v172, v173\n"
"v_mfma_f32_16x16x32_bf16 v[128:131], a[24:27], v[216:219], v[128:131]\n"
"v_exp_f32 v174, v174\n"
"v_mfma_f32_16x16x32_bf16 v[100:103], a[28:31], v[212:215], v[100:103]\n"
"v_exp_f32 v175, v175\n"
"v_mfma_f32_16x16x32_bf16 v[132:135], a[28:31], v[216:219], v[132:135]\n"
"s_waitcnt lgkmcnt(0)\n"
"s_sub_u32 s25, s24, s5\n"
"s_cmp_lt_i32 s25, 0\n"
"s_cbranch_scc1 .Lmha_LIM4\n"
".Lmha_LIM4R:\n"
"v_mfma_f32_16x16x32_bf16 v[104:107], a[32:35], v[212:215], v[104:107]\n"
"s_add_u32 s24, s24, s92\n"
"v_exp_f32 v180, v180\n"
"v_mfma_f32_16x16x32_bf16 v[136:139], a[32:35], v[216:219], v[136:139]\n"
"s_add_u32 s5, s24, 0x67\n"
"v_exp_f32 v181, v181\n"
"s_barrier\n"
"v_mfma_f32_16x16x32_bf16 v[108:111], a[36:39], v[212:215], v[108:111]\n"
"ds_read_b128 a[48:51], v1 offset:0\n"
"ds_read_b128 a[52:55], v1 offset:256\n"
"v_mfma_f32_16x16x32_bf16 v[140:143], a[36:39], v[216:219], v[140:143]\n"
"ds_read_b128 a[56:59], v1 offset:512\n"
"ds_read_b128 a[60:63], v1 offset:768\n"
"v_mfma_f32_16x16x32_bf16 v[80:83], a[40:43], v[212:215], v[80:83]\n"
"s_waitcnt vmcnt(27)\n"
"ds_write_b128 v13, a[208:211] offset:4096\n"
"v_mfma_f32_16x16x32_bf16 v[112:115], a[40:43], v[216:219], v[112:115]\n"
"v_cvt_pk_bf16_f32 v221, v174, v175\n"
"v_cvt_pk_bf16_f32 v222, v180, v181\n"
"v_mfma_f32_16x16x32_bf16 v[144:147], a[40:43], a[116:119], v[144:147]\n"
"v_exp_f32 v182, v182\n"
"v_mfma_f32_16x16x32_bf16 v[84:87], a[44:47], v[212:215], v[84:87]\n"
"buffer_load_dwordx4 v18, s[44:47], s96 offen offset:2048 nt lds\n"
"ds_read_b128 a[0:3], v2 offset:0\n"
"v_mfma_f32_16x16x32_bf16 v[116:119], a[44:47], v[216:219], v[116:119]\n"
"ds_read_b128 a[4:7], v2 offset:256\n"
"ds_read_b128 a[8:11], v2 offset:512\n"
"v_mfma_f32_16x16x32_bf16 v[148:151], a[44:47], a[116:119], v[148:151]\n"
"s_waitcnt lgkmcnt(7)\n"
"v_exp_f32 v183, v183\n"
"v_mfma_f32_16x16x32_bf16 v[156:159], a[48:51], v[64:67], v[240:243]\n"
"s_waitcnt lgkmcnt(6)\n"
"ds_read_b128 a[12:15], v2 offset:768\n"
"ds_read_b64_tr_b16 a[16:17], v5 offset:12544\n"
"v_mfma_f32_16x16x32_bf16 v[156:159], a[52:55], v[68:71], v[156:159]\n"
"s_waitcnt vmcnt(27) lgkmcnt(7)\n"
"ds_write_b128 v13, a[212:215] offset:5120\n"
"v_mfma_f32_16x16x32_bf16 v[156:159], a[56:59], v[72:75], v[156:159]\n"
"s_waitcnt lgkmcnt(7)\n"
"v_exp_f32 v176, v176\n"
"v_mfma_f32_16x16x32_bf16 v[156:159], a[60:63], v[76:79], v[156:159]\n"
"v_exp_f32 v177, v177\n"
"v_mfma_f32_16x16x32_bf16 v[160:163], a[48:51], v[32:35], v[232:235]\n"
"ds_read_b64_tr_b16 a[18:19], v7 offset:12288\n"
"ds_read_b64_tr_b16 a[20:21], v6 offset:12544\n"
"v_mfma_f32_16x16x32_bf16 v[160:163], a[52:55], v[36:39], v[160:163]\n"
"ds_read_b64_tr_b16 a[22:23], v8 offset:12288\n"
"v_cvt_pk_bf16_f32 v223, v182, v183\n"
"v_mfma_f32_16x16x32_bf16 v[160:163], a[56:59], v[40:43], v[160:163]\n"
"ds_read_b64_tr_b16 a[24:25], v5 offset:12800\n"
"v_cvt_pk_bf16_f32 v224, v176, v177\n"
"v_mfma_f32_16x16x32_bf16 v[160:163], a[60:63], v[44:47], v[160:163]\n"
"ds_read_b64_tr_b16 a[26:27], v7 offset:13056\n"
"ds_read_b64_tr_b16 a[28:29], v6 offset:12800\n"
"v_mfma_f32_16x16x32_bf16 v[164:167], a[48:51], v[48:51], v[236:239]\n"
"s_waitcnt vmcnt(26)\n"
"ds_write_b128 v13, a[216:219] offset:6144\n"
"v_mfma_f32_16x16x32_bf16 v[164:167], a[52:55], v[52:55], v[164:167]\n"
"buffer_load_dwordx4 v18, s[44:47], s96 offen offset:3072 nt lds\n"
"v_mfma_f32_16x16x32_bf16 v[164:167], a[56:59], v[56:59], v[164:167]\n"
"v_exp_f32 v178, v178\n"
"v_mfma_f32_16x16x32_bf16 v[164:167], a[60:63], v[60:63], v[164:167]\n"
"s_waitcnt lgkmcnt(7)\n"
"v_exp_f32 v179, v179\n"
"v_mfma_f32_16x16x32_bf16 v[168:171], a[0:3], v[32:35], v[232:235]\n"
"v_exp_f32 v184, v184\n"
"v_mfma_f32_16x16x32_bf16 v[168:171], a[4:7], v[36:39], v[168:171]\n"
"v_exp_f32 v185, v185\n"
"v_mfma_f32_16x16x32_bf16 v[168:171], a[8:11], v[40:43], v[168:171]\n"
"v_exp_f32 v186, v186\n"
"v_mfma_f32_16x16x32_bf16 v[168:171], a[12:15], v[44:47], v[168:171]\n"
"s_waitcnt vmcnt(26)\n"
"ds_write_b128 v13, a[220:223] offset:7168\n"
"v_mfma_f32_16x16x32_bf16 v[172:175], a[0:3], v[48:51], v[236:239]\n"
"v_exp_f32 v187, v187\n"
"v_mfma_f32_16x16x32_bf16 v[172:175], a[4:7], v[52:55], v[172:175]\n"
"v_cvt_pk_bf16_f32 v225, v178, v179\n"
"v_cvt_pk_bf16_f32 v226, v184, v185\n"
"v_mfma_f32_16x16x32_bf16 v[172:175], a[8:11], v[56:59], v[172:175]\n"
"ds_read_b64_tr_b16 a[30:31], v8 offset:13056\n"
"v_cvt_pk_bf16_f32 v227, v186, v187\n"
"v_mfma_f32_16x16x32_bf16 v[172:175], a[12:15], v[60:63], v[172:175]\n"
"ds_read_b64_tr_b16 a[32:33], v5 offset:13056\n"
"ds_read_b64_tr_b16 a[34:35], v7 offset:12800\n"
"v_mfma_f32_16x16x32_bf16 a[120:123], v[228:231], v[220:223], a[120:123]\n"
"buffer_load_dwordx4 a[128:131], v19, s[48:51], s84 offen offset:0 nt\n"
"ds_read_b64_tr_b16 a[36:37], v6 offset:13056\n"
"v_mfma_f32_16x16x32_bf16 a[124:127], v[228:231], v[224:227], a[124:127]\n"
"s_waitcnt lgkmcnt(5)\n"
"ds_read_b64_tr_b16 a[38:39], v8 offset:12800\n"
"ds_read_b64_tr_b16 a[40:41], v5 offset:12288\n"
"v_mfma_f32_16x16x32_bf16 v[88:91], a[16:19], v[220:223], v[88:91]\n"
"s_waitcnt vmcnt(26)\n"
"ds_write_b128 v13, a[224:227] offset:8192\n"
"v_mfma_f32_16x16x32_bf16 v[120:123], a[16:19], v[224:227], v[120:123]\n"
"v_exp_f32 v188, v188\n"
"v_mfma_f32_16x16x32_bf16 v[92:95], a[20:23], v[220:223], v[92:95]\n"
"s_waitcnt vmcnt(12)\n"
"ds_read_b64_tr_b16 a[42:43], v7 offset:12544\n"
"v_readfirstlane_b32 s86, v28\n"
"v_mfma_f32_16x16x32_bf16 v[124:127], a[20:23], v[224:227], v[124:127]\n"
"ds_read_b64 a[112:113], v13 offset:38920\n"
"ds_read_b64 a[114:115], v14 offset:38920\n"
"v_mfma_f32_16x16x32_bf16 v[96:99], a[24:27], v[220:223], v[96:99]\n"
"s_mul_i32 s86, s86, 0x9e3779b5\n"
"ds_read_b64_tr_b16 a[44:45], v6 offset:12288\n"
"ds_read_b64_tr_b16 a[46:47], v8 offset:12544\n"
"v_mfma_f32_16x16x32_bf16 v[128:131], a[24:27], v[224:227], v[128:131]\n"
"s_waitcnt lgkmcnt(11)\n"
"ds_read_b128 a[48:51], v3 offset:0\n"
"ds_read_b128 a[52:55], v3 offset:256\n"
"v_mfma_f32_16x16x32_bf16 v[100:103], a[28:31], v[220:223], v[100:103]\n"
"s_add_u32 s86, s86, 0x7f4a7c15\n"
"ds_read_b128 a[56:59], v3 offset:512\n"
"ds_read_b128 a[60:63], v3 offset:768\n"
"v_mfma_f32_16x16x32_bf16 v[132:135], a[28:31], v[224:227], v[132:135]\n"
"s_waitcnt lgkmcnt(13)\n"
"ds_write_b128 v13, a[228:231] offset:9216\n"
"v_mfma_f32_16x16x32_bf16 v[104:107], a[32:35], v[220:223], v[104:107]\n"
"v_exp_f32 v189, v189\n"
"v_mfma_f32_16x16x32_bf16 v[136:139], a[32:35], v[224:227], v[136:139]\n"
"s_waitcnt lgkmcnt(10)\n"
"v_exp_f32 v190, v190\n"
"v_mfma_f32_16x16x32_bf16 v[108:111], a[36:39], v[220:223], v[108:111]\n"
"buffer_load_dwordx4 a[132:135], v20, s[48:51], s84 offen offset:1024 nt\n"
"ds_read_b128 a[0:3], v4 offset:0\n"
"v_exp_f32 v191, v191\n"
"v_mfma_f32_16x16x32_bf16 v[140:143], a[36:39], v[224:227], v[140:143]\n"
"s_waitcnt lgkmcnt(10)\n"
"ds_read_b128 a[4:7], v4 offset:256\n"
"ds_read_b128 a[8:11], v4 offset:512\n"
"v_cvt_pk_bf16_f32 v200, v188, v189\n"
"v_cvt_pk_bf16_f32 v201, v190, v191\n"
"v_exp_f32 v196, v196\n"
"v_mfma_f32_16x16x32_bf16 v[80:83], a[40:43], v[220:223], v[80:83]\n"
"ds_read_b128 a[12:15], v4 offset:768\n"
"v_readfirstlane_b32 s87, v29\n"
"v_exp_f32 v197, v197\n"
"v_exp_f32 v198, v198\n"
"v_mfma_f32_16x16x32_bf16 v[112:115], a[40:43], v[224:227], v[112:115]\n"
"s_waitcnt lgkmcnt(7)\n"
"ds_read_b64_tr_b16 a[16:17], v9 offset:13056\n"
"v_cvt_pk_bf16_f32 v202, v196, v197\n"
"v_exp_f32 v199, v199\n"
"v_exp_f32 v192, v192\n"
"v_exp_f32 v193, v193\n"
"v_mfma_f32_16x16x32_bf16 v[144:147], a[40:43], a[112:115], v[144:147]\n"
"s_mul_i32 s87, s87, 0x85ebca69\n"
"ds_write_b128 v13, a[232:235] offset:10240\n"
"v_mfma_f32_16x16x32_bf16 v[84:87], a[44:47], v[220:223], v[84:87]\n"
"s_add_u32 s87, s87, 0x165667b1\n"
"v_cvt_pk_bf16_f32 v203, v198, v199\n"
"v_cvt_pk_bf16_f32 v204, v192, v193\n"
"v_exp_f32 v194, v194\n"
"v_mfma_f32_16x16x32_bf16 v[116:119], a[44:47], v[224:227], v[116:119]\n"
"v_exp_f32 v195, v195\n"
"v_exp_f32 v152, v152\n"
"v_mfma_f32_16x16x32_bf16 v[148:151], a[44:47], a[112:115], v[148:151]\n"
"ds_read_b64_tr_b16 a[18:19], v11 offset:12800\n"
"ds_read_b64_tr_b16 a[20:21], v10 offset:13056\n"
"v_cvt_pk_bf16_f32 v205, v194, v195\n"
"v_mfma_f32_16x16x32_bf16 v[176:179], a[48:51], v[32:35], v[232:235]\n"
"ds_read_b64_tr_b16 a[22:23], v12 offset:12800\n"
"ds_read_b64_tr_b16 a[24:25], v9 offset:12288\n"
"v_mfma_f32_16x16x32_bf16 v[176:179], a[52:55], v[36:39], v[176:179]\n"
"s_waitcnt lgkmcnt(6)\n"
"ds_read_b64_tr_b16 a[26:27], v11 offset:12544\n"
"ds_read_b64_tr_b16 a[28:29], v10 offset:12288\n"
"v_mov_b32 v26, s86\n"
"v_mfma_f32_16x16x32_bf16 v[176:179], a[56:59], v[40:43], v[176:179]\n"
"ds_read_b64_tr_b16 a[30:31], v12 offset:12544\n"
"v_exp_f32 v153, v153\n"
"v_mfma_f32_16x16x32_bf16 v[176:179], a[60:63], v[44:47], v[176:179]\n"
"buffer_load_dwordx4 a[136:139], v19, s[48:51], s84 offen offset:2048 nt\n"
"ds_read_b64_tr_b16 a[32:33], v9 offset:12544\n"
"v_cvt_pk_bf16_f32 v206, v152, v153\n"
"v_mfma_f32_16x16x32_bf16 v[180:183], a[48:51], v[48:51], v[236:239]\n"
"ds_write_b128 v13, a[236:239] offset:11264\n"
"v_mfma_f32_16x16x32_bf16 v[180:183], a[52:55], v[52:55], v[180:183]\n"
"v_exp_f32 v154, v154\n"
"v_mfma_f32_16x16x32_bf16 v[180:183], a[56:59], v[56:59], v[180:183]\n"
"v_exp_f32 v155, v155\n"
"v_mfma_f32_16x16x32_bf16 v[180:183], a[60:63], v[60:63], v[180:183]\n"
"ds_read_b64_tr_b16 a[34:35], v11 offset:12288\n"
"ds_read_b64 a[116:117], v15 offset:38920\n"
"v_cvt_pk_bf16_f32 v207, v154, v155\n"
"v_mfma_f32_16x16x32_bf16 v[184:187], a[0:3], v[32:35], v[232:235]\n"
"v_exp_f32 v156, v156\n"
"v_mfma_f32_16x16x32_bf16 v[184:187], a[4:7], v[36:39], v[184:187]\n"
"v_exp_f32 v157, v157\n"
"v_mfma_f32_16x16x32_bf16 v[184:187], a[8:11], v[40:43], v[184:187]\n"
"ds_read_b64 a[118:119], v16 offset:38920\n"
"v_add_f32 v254, v254, v156\n"
"v_mfma_f32_16x16x32_bf16 v[184:187], a[12:15], v[44:47], v[184:187]\n"
"v_exp_f32 v158, v158\n"
"v_mfma_f32_16x16x32_bf16 v[188:191], a[0:3], v[48:51], v[236:239]\n"
"v_exp_f32 v159, v159\n"
"v_mfma_f32_16x16x32_bf16 v[188:191], a[4:7], v[52:55], v[188:191]\n"
"ds_read_b64_tr_b16 a[36:37], v10 offset:12544\n"
"v_add_f32 v254, v254, v157\n"
"v_mfma_f32_16x16x32_bf16 v[188:191], a[8:11], v[56:59], v[188:191]\n"
"ds_read_b64_tr_b16 a[38:39], v12 offset:12288\n"
"v_cvt_pk_bf16_f32 v156, v156, v157\n"
"v_mfma_f32_16x16x32_bf16 v[188:191], a[12:15], v[60:63], v[188:191]\n"
"buffer_load_dwordx4 a[140:143], v20, s[48:51], s84 offen offset:3072 nt\n"
"v_cvt_pk_bf16_f32 v157, v158, v159\n"
"v_mfma_f32_16x16x32_bf16 a[120:123], v[228:231], v[200:203], a[120:123]\n"
"ds_write_b64 v13, v[156:157] offset:38912\n"
"v_mfma_f32_16x16x32_bf16 a[124:127], v[228:231], v[204:207], a[124:127]\n"
"s_waitcnt lgkmcnt(6)\n"
"ds_read_b64_tr_b16 a[40:41], v9 offset:12800\n"
"ds_read_b64_tr_b16 a[42:43], v11 offset:13056\n"
"v_mfma_f32_16x16x32_bf16 v[88:91], a[16:19], v[200:203], v[88:91]\n"
"s_getpc_b64 s[98:99]\n"
"ds_read_b64_tr_b16 a[44:45], v10 offset:12800\n"
"ds_read_b64_tr_b16 a[46:47], v12 offset:13056\n"
"v_mfma_f32_16x16x32_bf16 v[120:123], a[16:19], v[204:207], v[120:123]\n"
"v_add_f32 v254, v254, v158\n"
"v_add_f32 v254, v254, v159\n"
"v_mfma_f32_16x16x32_bf16 v[92:95], a[20:23], v[200:203], v[92:95]\n"
"s_memrealtime s[24:25]\n"
"global_load_dword v250, v0, s[98:99] offset:1536\n"
"v_mov_b32 v27, s87\n"
"v_mfma_f32_16x16x32_bf16 v[124:127], a[20:23], v[204:207], v[124:127]\n"
"v_exp_f32 v160, v160\n"
"v_mfma_f32_16x16x32_bf16 v[96:99], a[24:27], v[200:203], v[96:99]\n"
"v_exp_f32 v161, v161\n"
"v_mfma_f32_16x16x32_bf16 v[128:131], a[24:27], v[204:207], v[128:131]\n"
"v_exp_f32 v162, v162\n"
"v_mfma_f32_16x16x32_bf16 v[100:103], a[28:31], v[200:203], v[100:103]\n"
"v_exp_f32 v163, v163\n"
"v_mfma_f32_16x16x32_bf16 v[132:135], a[28:31], v[204:207], v[132:135]\n"
"s_waitcnt lgkmcnt(0)\n"
"s_sub_u32 s25, s24, s5\n"
"s_cmp_lt_i32 s25, 0\n"
"s_cbranch_scc1 .Lmha_LIM5\n"
".Lmha_LIM5R:\n"
"v_cvt_pk_bf16_f32 v208, v160, v161\n"
"v_mfma_f32_16x16x32_bf16 v[104:107], a[32:35], v[200:203], v[104:107]\n"
"s_barrier\n"
"v_exp_f32 v168, v168\n"
"v_mfma_f32_16x16x32_bf16 v[136:139], a[32:35], v[204:207], v[136:139]\n"
"s_add_u32 s24, s24, s92\n"
"ds_read_b128 a[48:51], v1 offset:4096\n"
"ds_read_b128 a[52:55], v1 offset:4352\n"
"v_mfma_f32_16x16x32_bf16 v[108:111], a[36:39], v[200:203], v[108:111]\n"
"s_add_u32 s5, s24, 0x67\n"
"s_mov_b32 s75, 0x24000\n"
"s_add_u32 m0, s30, 0\n"
"ds_read_b128 a[56:59], v1 offset:4608\n"
"buffer_load_dwordx4 v17, s[44:47], s75 offen offset:0 nt lds\n"
"v_mfma_f32_16x16x32_bf16 v[140:143], a[36:39], v[204:207], v[140:143]\n"
"ds_read_b128 a[60:63], v1 offset:4864\n"
"ds_read_b128 a[0:3], v2 offset:4096\n"
"v_mfma_f32_16x16x32_bf16 v[80:83], a[40:43], v[200:203], v[80:83]\n"
"ds_read_b128 a[4:7], v2 offset:4352\n"
"ds_read_b128 a[8:11], v2 offset:4608\n"
"v_mfma_f32_16x16x32_bf16 v[112:115], a[40:43], v[204:207], v[112:115]\n"
"ds_read_b128 a[12:15], v2 offset:4864\n"
"v_cvt_pk_bf16_f32 v209, v162, v163\n"
"v_mfma_f32_16x16x32_bf16 v[144:147], a[40:43], a[116:119], v[144:147]\n"
"v_exp_f32 v169, v169\n"
"v_mfma_f32_16x16x32_bf16 v[84:87], a[44:47], v[200:203], v[84:87]\n"
"ds_read_b64_tr_b16 a[16:17], v5 offset:8448\n"
"v_cvt_pk_bf16_f32 v210, v168, v169\n"
"v_mfma_f32_16x16x32_bf16 v[116:119], a[44:47], v[204:207], v[116:119]\n"
"v_exp_f32 v170, v170\n"
"v_mfma_f32_16x16x32_bf16 v[148:151], a[44:47], a[116:119], v[148:151]\n"
"s_waitcnt lgkmcnt(8)\n"
"v_exp_f32 v171, v171\n"
"v_mfma_f32_16x16x32_bf16 v[192:195], a[48:51], v[64:67], v[240:243]\n"
"s_waitcnt lgkmcnt(7)\n"
"ds_read_b64_tr_b16 a[18:19], v7 offset:8192\n"
"v_cvt_pk_bf16_f32 v211, v170, v171\n"
"v_mfma_f32_16x16x32_bf16 v[192:195], a[52:55], v[68:71], v[192:195]\n"
"s_waitcnt lgkmcnt(7)\n"
"v_exp_f32 v164, v164\n"
"v_mfma_f32_16x16x32_bf16 v[192:195], a[56:59], v[72:75], v[192:195]\n"
"s_waitcnt lgkmcnt(6)\n"
"ds_read_b64_tr_b16 a[20:21], v6 offset:8448\n"
"ds_read_b64_tr_b16 a[22:23], v8 offset:8192\n"
"v_mfma_f32_16x16x32_bf16 v[192:195], a[60:63], v[76:79], v[192:195]\n"
"v_exp_f32 v165, v165\n"
"v_mfma_f32_16x16x32_bf16 v[196:199], a[48:51], v[32:35], v[232:235]\n"
"buffer_load_dwordx4 v17, s[44:47], s75 offen offset:1024 nt lds\n"
"ds_read_b64_tr_b16 a[24:25], v5 offset:8704\n"
"v_mfma_f32_16x16x32_bf16 v[196:199], a[52:55], v[36:39], v[196:199]\n"
"ds_read_b64_tr_b16 a[26:27], v7 offset:8960\n"
"v_cvt_pk_bf16_f32 v212, v164, v165\n"
"v_mfma_f32_16x16x32_bf16 v[196:199], a[56:59], v[40:43], v[196:199]\n"
"v_exp_f32 v166, v166\n"
"v_mfma_f32_16x16x32_bf16 v[196:199], a[60:63], v[44:47], v[196:199]\n"
"v_exp_f32 v167, v167\n"
"v_mfma_f32_16x16x32_bf16 v[152:155], a[48:51], v[48:51], v[236:239]\n"
"v_exp_f32 v172, v172\n"
"v_mfma_f32_16x16x32_bf16 v[152:155], a[52:55], v[52:55], v[152:155]\n"
"v_exp_f32 v173, v173\n"
"v_mfma_f32_16x16x32_bf16 v[152:155], a[56:59], v[56:59], v[152:155]\n"
"v_exp_f32 v174, v174\n"
"v_mfma_f32_16x16x32_bf16 v[152:155], a[60:63], v[60:63], v[152:155]\n"
"s_waitcnt lgkmcnt(4)\n"
"v_exp_f32 v175, v175\n"
"v_mfma_f32_16x16x32_bf16 v[156:159], a[0:3], v[32:35], v[232:235]\n"
"ds_read_b64_tr_b16 a[28:29], v6 offset:8704\n"
"v_cvt_pk_bf16_f32 v213, v166, v167\n"
"v_mfma_f32_16x16x32_bf16 v[156:159], a[4:7], v[36:39], v[156:159]\n"
"ds_read_b64_tr_b16 a[30:31], v8 offset:8960\n"
"v_cvt_pk_bf16_f32 v214, v172, v173\n"
"v_mfma_f32_16x16x32_bf16 v[156:159], a[8:11], v[40:43], v[156:159]\n"
"ds_read_b64_tr_b16 a[32:33], v5 offset:8960\n"
"v_cvt_pk_bf16_f32 v215, v174, v175\n"
"v_mfma_f32_16x16x32_bf16 v[156:159], a[12:15], v[44:47], v[156:159]\n"
"ds_read_b64_tr_b16 a[34:35], v7 offset:8704\n"
"ds_read_b64_tr_b16 a[36:37], v6 offset:8960\n"
"v_mfma_f32_16x16x32_bf16 v[160:163], a[0:3], v[48:51], v[236:239]\n"
"buffer_load_dwordx4 v18, s[44:47], s75 offen offset:2048 nt lds\n"
"ds_read_b64_tr_b16 a[38:39], v8 offset:8704\n"
"v_mfma_f32_16x16x32_bf16 v[160:163], a[4:7], v[52:55], v[160:163]\n"
"ds_read_b64_tr_b16 a[40:41], v5 offset:8192\n"
"ds_read_b64_tr_b16 a[42:43], v7 offset:8448\n"
"v_mfma_f32_16x16x32_bf16 v[160:163], a[8:11], v[56:59], v[160:163]\n"
"ds_read_b64 a[112:113], v13 offset:38912\n"
"ds_read_b64 a[114:115], v14 offset:38912\n"
"v_mfma_f32_16x16x32_bf16 v[160:163], a[12:15], v[60:63], v[160:163]\n"
"ds_read_b64_tr_b16 a[44:45], v6 offset:8192\n"
"ds_read_b64_tr_b16 a[46:47], v8 offset:8448\n"
"v_mfma_f32_16x16x32_bf16 a[120:123], v[228:231], v[208:211], a[120:123]\n"
"ds_read_b128 a[48:51], v3 offset:4096\n"
"ds_read_b128 a[52:55], v3 offset:4352\n"
"v_mfma_f32_16x16x32_bf16 a[124:127], v[228:231], v[212:215], a[124:127]\n"
"ds_read_b128 a[56:59], v3 offset:4608\n"
"ds_read_b128 a[60:63], v3 offset:4864\n"
"v_mfma_f32_16x16x32_bf16 v[88:91], a[16:19], v[208:211], v[88:91]\n"
"v_exp_f32 v176, v176\n"
"v_mfma_f32_16x16x32_bf16 v[120:123], a[16:19], v[212:215], v[120:123]\n"
"s_waitcnt lgkmcnt(15)\n"
"v_exp_f32 v177, v177\n"
"v_mfma_f32_16x16x32_bf16 v[92:95], a[20:23], v[208:211], v[92:95]\n"
"ds_read_b128 a[0:3], v4 offset:4096\n"
"v_cvt_pk_bf16_f32 v216, v176, v177\n"
"v_mfma_f32_16x16x32_bf16 v[124:127], a[20:23], v[212:215], v[124:127]\n"
"v_exp_f32 v178, v178\n"
"v_mfma_f32_16x16x32_bf16 v[96:99], a[24:27], v[208:211], v[96:99]\n"
"v_exp_f32 v179, v179\n"
"v_mfma_f32_16x16x32_bf16 v[128:131], a[24:27], v[212:215], v[128:131]\n"
"s_waitcnt lgkmcnt(11)\n"
"ds_read_b128 a[4:7], v4 offset:4352\n"
"v_cvt_pk_bf16_f32 v217, v178, v179\n"
"v_mfma_f32_16x16x32_bf16 v[100:103], a[28:31], v[208:211], v[100:103]\n"
"buffer_load_dwordx4 v18, s[44:47], s75 offen offset:3072 nt lds\n"
"ds_read_b128 a[8:11], v4 offset:4608\n"
"v_mfma_f32_16x16x32_bf16 v[132:135], a[28:31], v[212:215], v[132:135]\n"
"v_exp_f32 v184, v184\n"
"v_mfma_f32_16x16x32_bf16 v[104:107], a[32:35], v[208:211], v[104:107]\n"
"v_exp_f32 v185, v185\n"
"v_mfma_f32_16x16x32_bf16 v[136:139], a[32:35], v[212:215], v[136:139]\n"
"ds_read_b128 a[12:15], v4 offset:4864\n"
"v_cvt_pk_bf16_f32 v218, v184, v185\n"
"v_mfma_f32_16x16x32_bf16 v[108:111], a[36:39], v[208:211], v[108:111]\n"
"v_exp_f32 v186, v186\n"
"v_mfma_f32_16x16x32_bf16 v[140:143], a[36:39], v[212:215], v[140:143]\n"
"s_waitcnt lgkmcnt(4)\n"
"v_exp_f32 v187, v187\n"
"v_mfma_f32_16x16x32_bf16 v[80:83], a[40:43], v[208:211], v[80:83]\n"
"ds_read_b64_tr_b16 a[16:17], v9 offset:8960\n"
"v_cvt_pk_bf16_f32 v219, v186, v187\n"
"v_mfma_f32_16x16x32_bf16 v[112:115], a[40:43], v[212:215], v[112:115]\n"
"ds_read_b64_tr_b16 a[18:19], v11 offset:8704\n"
"ds_read_b64_tr_b16 a[20:21], v10 offset:8960\n"
"v_exp_f32 v180, v180\n"
"v_mfma_f32_16x16x32_bf16 v[144:147], a[40:43], a[112:115], v[144:147]\n"
"ds_read_b64_tr_b16 a[22:23], v12 offset:8704\n"
"v_exp_f32 v181, v181\n"
"v_mfma_f32_16x16x32_bf16 v[84:87], a[44:47], v[208:211], v[84:87]\n"
"ds_read_b64_tr_b16 a[24:25], v9 offset:8192\n"
"ds_read_b64_tr_b16 a[26:27], v11 offset:8448\n"
"v_cvt_pk_bf16_f32 v220, v180, v181\n"
"v_mfma_f32_16x16x32_bf16 v[116:119], a[44:47], v[212:215], v[116:119]\n"
"ds_read_b64_tr_b16 a[28:29], v10 offset:8192\n"
"v_exp_f32 v182, v182\n"
"v_mfma_f32_16x16x32_bf16 v[148:151], a[44:47], a[112:115], v[148:151]\n"
"s_add_u32 m0, s30, 0x3000\n"
"v_exp_f32 v183, v183\n"
"v_mfma_f32_16x16x32_bf16 v[164:167], a[48:51], v[32:35], v[232:235]\n"
"buffer_load_dwordx4 v19, s[48:51], s96 offen offset:0 nt lds\n"
"ds_read_b64_tr_b16 a[30:31], v12 offset:8448\n"
"v_mfma_f32_16x16x32_bf16 v[164:167], a[52:55], v[36:39], v[164:167]\n"
"v_exp_f32 v188, v188\n"
"v_mfma_f32_16x16x32_bf16 v[164:167], a[56:59], v[40:43], v[164:167]\n"
"v_exp_f32 v189, v189\n"
"v_mfma_f32_16x16x32_bf16 v[164:167], a[60:63], v[44:47], v[164:167]\n"
"v_exp_f32 v190, v190\n"
"v_mfma_f32_16x16x32_bf16 v[168:171], a[48:51], v[48:51], v[236:239]\n"
"v_exp_f32 v191, v191\n"
"v_mfma_f32_16x16x32_bf16 v[168:171], a[52:55], v[52:55], v[168:171]\n"
"ds_read_b64_tr_b16 a[32:33], v9 offset:8448\n"
"v_cvt_pk_bf16_f32 v221, v182, v183\n"
"v_mfma_f32_16x16x32_bf16 v[168:171], a[56:59], v[56:59], v[168:171]\n"
"ds_read_b64_tr_b16 a[34:35], v11 offset:8192\n"
"v_cvt_pk_bf16_f32 v222, v188, v189\n"
"v_mfma_f32_16x16x32_bf16 v[168:171], a[60:63], v[60:63], v[168:171]\n"
"s_waitcnt lgkmcnt(6)\n"
"ds_read_b64 a[116:117], v15 offset:38912\n"
"v_cvt_pk_bf16_f32 v223, v190, v191\n"
"v_mfma_f32_16x16x32_bf16 v[172:175], a[0:3], v[32:35], v[232:235]\n"
"v_exp_f32 v192, v192\n"
"v_mfma_f32_16x16x32_bf16 v[172:175], a[4:7], v[36:39], v[172:175]\n"
"v_exp_f32 v193, v193\n"
"v_mfma_f32_16x16x32_bf16 v[172:175], a[8:11], v[40:43], v[172:175]\n"
"ds_read_b64 a[118:119], v16 offset:38912\n"
"v_add_f32 v254, v254, v192\n"
"v_mfma_f32_16x16x32_bf16 v[172:175], a[12:15], v[44:47], v[172:175]\n"
"buffer_load_dwordx4 v20, s[48:51], s96 offen offset:1024 nt lds\n"
"v_add_f32 v254, v254, v193\n"
"v_mfma_f32_16x16x32_bf16 v[176:179], a[0:3], v[48:51], v[236:239]\n"
"v_exp_f32 v194, v194\n"
"v_mfma_f32_16x16x32_bf16 v[176:179], a[4:7], v[52:55], v[176:179]\n"
"v_exp_f32 v195, v195\n"
"v_mfma_f32_16x16x32_bf16 v[176:179], a[8:11], v[56:59], v[176:179]\n"
"ds_read_b64_tr_b16 a[36:37], v10 offset:8448\n"
"v_cvt_pk_bf16_f32 v192, v192, v193\n"
"v_mfma_f32_16x16x32_bf16 v[176:179], a[12:15], v[60:63], v[176:179]\n"
"ds_read_b64_tr_b16 a[38:39], v12 offset:8192\n"
"v_cvt_pk_bf16_f32 v193, v194, v195\n"
"v_mfma_f32_16x16x32_bf16 a[120:123], v[228:231], v[216:219], a[120:123]\n"
"ds_write_b64 v13, v[192:193] offset:38920\n"
"v_mfma_f32_16x16x32_bf16 a[124:127], v[228:231], v[220:223], a[124:127]\n"
"ds_read_b64_tr_b16 a[40:41], v9 offset:8704\n"
"ds_read_b64_tr_b16 a[42:43], v11 offset:8960\n"
"v_mfma_f32_16x16x32_bf16 v[88:91], a[16:19], v[216:219], v[88:91]\n"
"ds_read_b64_tr_b16 a[44:45], v10 offset:8704\n"
"ds_read_b64_tr_b16 a[46:47], v12 offset:8960\n"
"v_mfma_f32_16x16x32_bf16 v[120:123], a[16:19], v[220:223], v[120:123]\n"
"v_add_f32 v254, v254, v194\n"
"v_add_f32 v254, v254, v195\n"
"v_mfma_f32_16x16x32_bf16 v[92:95], a[20:23], v[216:219], v[92:95]\n"
"s_memrealtime s[24:25]\n"
"v_exp_f32 v196, v196\n"
"v_mfma_f32_16x16x32_bf16 v[124:127], a[20:23], v[220:223], v[124:127]\n"
"s_waitcnt lgkmcnt(0)\n"
"v_exp_f32 v197, v197\n"
"v_mfma_f32_16x16x32_bf16 v[96:99], a[24:27], v[216:219], v[96:99]\n"
"v_exp_f32 v198, v198\n"
"v_mfma_f32_16x16x32_bf16 v[128:131], a[24:27], v[220:223], v[128:131]\n"
"buffer_load_dwordx4 v19, s[48:51], s96 offen offset:2048 nt lds\n"
"v_cvt_pk_bf16_f32 v224, v196, v197\n"
"v_mfma_f32_16x16x32_bf16 v[100:103], a[28:31], v[216:219], v[100:103]\n"
"v_exp_f32 v199, v199\n"
"v_mfma_f32_16x16x32_bf16 v[132:135], a[28:31], v[220:223], v[132:135]\n"
"s_sub_u32 s25, s24, s5\n"
"s_cmp_lt_i32 s25, 0\n"
"s_cbranch_scc1 .Lmha_LIM6\n"
".Lmha_LIM6R:\n"
"v_exp_f32 v156, v156\n"
"v_mfma_f32_16x16x32_bf16 v[104:107], a[32:35], v[216:219], v[104:107]\n"
"s_barrier\n"
"v_exp_f32 v157, v157\n"
"v_mfma_f32_16x16x32_bf16 v[136:139], a[32:35], v[220:223], v[136:139]\n"
"s_add_u32 s24, s24, s92\n"
"ds_read_b128 a[48:51], v1 offset:16384\n"
"ds_read_b128 a[52:55], v1 offset:16640\n"
"v_mfma_f32_16x16x32_bf16 v[108:111], a[36:39], v[216:219], v[108:111]\n"
"s_add_u32 s5, s24, 0x67\n"
"ds_read_b128 a[56:59], v1 offset:16896\n"
"ds_read_b128 a[60:63], v1 offset:17152\n"
"v_mfma_f32_16x16x32_bf16 v[140:143], a[36:39], v[220:223], v[140:143]\n"
"ds_read_b128 a[0:3], v2 offset:16384\n"
"ds_read_b128 a[4:7], v2 offset:16640\n"
"v_mfma_f32_16x16x32_bf16 v[80:83], a[40:43], v[216:219], v[80:83]\n"
"ds_read_b128 a[8:11], v2 offset:16896\n"
"ds_read_b128 a[12:15], v2 offset:17152\n"
"v_mfma_f32_16x16x32_bf16 v[112:115], a[40:43], v[220:223], v[112:115]\n"
"ds_read_b64_tr_b16 a[16:17], v5 offset:20736\n"
"v_cvt_pk_bf16_f32 v225, v198, v199\n"
"v_mfma_f32_16x16x32_bf16 v[144:147], a[40:43], a[116:119], v[144:147]\n"
"ds_read_b64_tr_b16 a[18:19], v7 offset:20480\n"
"v_cvt_pk_bf16_f32 v226, v156, v157\n"
"v_mfma_f32_16x16x32_bf16 v[84:87], a[44:47], v[216:219], v[84:87]\n"
"v_exp_f32 v158, v158\n"
"v_mfma_f32_16x16x32_bf16 v[116:119], a[44:47], v[220:223], v[116:119]\n"
"v_exp_f32 v159, v159\n"
"v_mfma_f32_16x16x32_bf16 v[148:151], a[44:47], a[116:119], v[148:151]\n"
"s_waitcnt lgkmcnt(9)\n"
"buffer_load_dwordx4 v20, s[48:51], s96 offen offset:3072 nt lds\n"
"ds_read_b64_tr_b16 a[20:21], v6 offset:20736\n"
"v_mfma_f32_16x16x32_bf16 v[180:183], a[48:51], v[64:67], v[240:243]\n"
"s_waitcnt lgkmcnt(9)\n"
"ds_read_b64_tr_b16 a[22:23], v8 offset:20480\n"
"v_cvt_pk_bf16_f32 v227, v158, v159\n"
"v_mfma_f32_16x16x32_bf16 v[180:183], a[52:55], v[68:71], v[180:183]\n"
"s_waitcnt lgkmcnt(9)\n"
"v_exp_f32 v152, v152\n"
"v_mfma_f32_16x16x32_bf16 v[180:183], a[56:59], v[72:75], v[180:183]\n"
"s_waitcnt lgkmcnt(8)\n"
"v_exp_f32 v153, v153\n"
"v_mfma_f32_16x16x32_bf16 v[180:183], a[60:63], v[76:79], v[180:183]\n"
"ds_read_b64_tr_b16 a[24:25], v5 offset:20992\n"
"v_cvt_pk_bf16_f32 v200, v152, v153\n"
"v_mfma_f32_16x16x32_bf16 v[184:187], a[48:51], v[32:35], v[232:235]\n"
"v_exp_f32 v154, v154\n"
"v_mfma_f32_16x16x32_bf16 v[184:187], a[52:55], v[36:39], v[184:187]\n"
"v_exp_f32 v155, v155\n"
"v_mfma_f32_16x16x32_bf16 v[184:187], a[56:59], v[40:43], v[184:187]\n"
"ds_read_b64_tr_b16 a[26:27], v7 offset:21248\n"
"v_cvt_pk_bf16_f32 v201, v154, v155\n"
"v_mfma_f32_16x16x32_bf16 v[184:187], a[60:63], v[44:47], v[184:187]\n"
"v_exp_f32 v160, v160\n"
"v_mfma_f32_16x16x32_bf16 v[188:191], a[48:51], v[48:51], v[236:239]\n"
"v_exp_f32 v161, v161\n"
"v_mfma_f32_16x16x32_bf16 v[188:191], a[52:55], v[52:55], v[188:191]\n"
"v_exp_f32 v162, v162\n"
"v_mfma_f32_16x16x32_bf16 v[188:191], a[56:59], v[56:59], v[188:191]\n"
"v_exp_f32 v163, v163\n"
"v_mfma_f32_16x16x32_bf16 v[188:191], a[60:63], v[60:63], v[188:191]\n"
"s_waitcnt lgkmcnt(2)\n"
"s_mov_b32 s77, 0x28000\n"
"s_add_u32 m0, s30, 0x1000\n"
"s_nop 0\n"
"buffer_load_dwordx4 v17, s[44:47], s77 offen offset:0 nt lds\n"
"ds_read_b64_tr_b16 a[28:29], v6 offset:20992\n"
"v_mfma_f32_16x16x32_bf16 v[192:195], a[0:3], v[32:35], v[232:235]\n"
"ds_read_b64_tr_b16 a[30:31], v8 offset:21248\n"
"v_cvt_pk_bf16_f32 v202, v160, v161\n"
"v_mfma_f32_16x16x32_bf16 v[192:195], a[4:7], v[36:39], v[192:195]\n"
"ds_read_b64_tr_b16 a[32:33], v5 offset:21248\n"
"v_cvt_pk_bf16_f32 v203, v162, v163\n"
"v_mfma_f32_16x16x32_bf16 v[192:195], a[8:11], v[40:43], v[192:195]\n"
"ds_read_b64_tr_b16 a[34:35], v7 offset:20992\n"
"ds_read_b64_tr_b16 a[36:37], v6 offset:21248\n"
"v_mfma_f32_16x16x32_bf16 v[192:195], a[12:15], v[44:47], v[192:195]\n"
"ds_read_b64_tr_b16 a[38:39], v8 offset:20992\n"
"ds_read_b64_tr_b16 a[40:41], v5 offset:20480\n"
"v_mfma_f32_16x16x32_bf16 v[196:199], a[0:3], v[48:51], v[236:239]\n"
"ds_read_b64_tr_b16 a[42:43], v7 offset:20736\n"
"ds_read_b64 a[112:113], v13 offset:38920\n"
"v_mfma_f32_16x16x32_bf16 v[196:199], a[4:7], v[52:55], v[196:199]\n"
"ds_read_b64 a[114:115], v14 offset:38920\n"
"ds_read_b64_tr_b16 a[44:45], v6 offset:20480\n"
"v_mfma_f32_16x16x32_bf16 v[196:199], a[8:11], v[56:59], v[196:199]\n"
"ds_read_b64_tr_b16 a[46:47], v8 offset:20736\n"
"ds_read_b128 a[48:51], v3 offset:16384\n"
"v_mfma_f32_16x16x32_bf16 v[196:199], a[12:15], v[60:63], v[196:199]\n"
"v_exp_f32 v164, v164\n"
"v_mfma_f32_16x16x32_bf16 a[120:123], v[228:231], v[224:227], a[120:123]\n"
"ds_read_b128 a[52:55], v3 offset:16640\n"
"ds_read_b128 a[56:59], v3 offset:16896\n"
"v_mfma_f32_16x16x32_bf16 a[124:127], v[228:231], v[200:203], a[124:127]\n"
"v_exp_f32 v165, v165\n"
"v_mfma_f32_16x16x32_bf16 v[88:91], a[16:19], v[224:227], v[88:91]\n"
"buffer_load_dwordx4 v17, s[44:47], s77 offen offset:1024 nt lds\n"
"ds_read_b128 a[60:63], v3 offset:17152\n"
"v_mfma_f32_16x16x32_bf16 v[120:123], a[16:19], v[200:203], v[120:123]\n"
"ds_read_b128 a[0:3], v4 offset:16384\n"
"v_cvt_pk_bf16_f32 v204, v164, v165\n"
"v_mfma_f32_16x16x32_bf16 v[92:95], a[20:23], v[224:227], v[92:95]\n"
"v_exp_f32 v166, v166\n"
"v_mfma_f32_16x16x32_bf16 v[124:127], a[20:23], v[200:203], v[124:127]\n"
"s_waitcnt lgkmcnt(12)\n"
"v_exp_f32 v167, v167\n"
"v_mfma_f32_16x16x32_bf16 v[96:99], a[24:27], v[224:227], v[96:99]\n"
"ds_read_b128 a[4:7], v4 offset:16640\n"
"v_cvt_pk_bf16_f32 v205, v166, v167\n"
"v_mfma_f32_16x16x32_bf16 v[128:131], a[24:27], v[200:203], v[128:131]\n"
"v_exp_f32 v172, v172\n"
"v_mfma_f32_16x16x32_bf16 v[100:103], a[28:31], v[224:227], v[100:103]\n"
"v_exp_f32 v173, v173\n"
"v_mfma_f32_16x16x32_bf16 v[132:135], a[28:31], v[200:203], v[132:135]\n"
"ds_read_b128 a[8:11], v4 offset:16896\n"
"ds_read_b128 a[12:15], v4 offset:17152\n"
"v_mfma_f32_16x16x32_bf16 v[104:107], a[32:35], v[224:227], v[104:107]\n"
"ds_read_b64_tr_b16 a[16:17], v9 offset:21248\n"
"v_cvt_pk_bf16_f32 v206, v172, v173\n"
"v_mfma_f32_16x16x32_bf16 v[136:139], a[32:35], v[200:203], v[136:139]\n"
"s_waitcnt lgkmcnt(6)\n"
"v_exp_f32 v174, v174\n"
"v_mfma_f32_16x16x32_bf16 v[108:111], a[36:39], v[224:227], v[108:111]\n"
"v_exp_f32 v175, v175\n"
"v_mfma_f32_16x16x32_bf16 v[140:143], a[36:39], v[200:203], v[140:143]\n"
"ds_read_b64_tr_b16 a[18:19], v11 offset:20992\n"
"v_cvt_pk_bf16_f32 v207, v174, v175\n"
"v_mfma_f32_16x16x32_bf16 v[80:83], a[40:43], v[224:227], v[80:83]\n"
"buffer_load_dwordx4 v18, s[44:47], s77 offen offset:2048 nt lds\n"
"ds_read_b64_tr_b16 a[20:21], v10 offset:21248\n"
"v_mfma_f32_16x16x32_bf16 v[112:115], a[40:43], v[200:203], v[112:115]\n"
"ds_read_b64_tr_b16 a[22:23], v12 offset:20992\n"
"v_exp_f32 v168, v168\n"
"v_mfma_f32_16x16x32_bf16 v[144:147], a[40:43], a[112:115], v[144:147]\n"
"ds_read_b64_tr_b16 a[24:25], v9 offset:20480\n"
"v_exp_f32 v169, v169\n"
"v_mfma_f32_16x16x32_bf16 v[84:87], a[44:47], v[224:227], v[84:87]\n"
"ds_read_b64_tr_b16 a[26:27], v11 offset:20736\n"
"v_cvt_pk_bf16_f32 v208, v168, v169\n"
"v_mfma_f32_16x16x32_bf16 v[116:119], a[44:47], v[200:203], v[116:119]\n"
"v_exp_f32 v170, v170\n"
"v_mfma_f32_16x16x32_bf16 v[148:151], a[44:47], a[112:115], v[148:151]\n"
"v_exp_f32 v171, v171\n"
"v_mfma_f32_16x16x32_bf16 v[152:155], a[48:51], v[32:35], v[232:235]\n"
"v_exp_f32 v176, v176\n"
"v_mfma_f32_16x16x32_bf16 v[152:155], a[52:55], v[36:39], v[152:155]\n"
"v_exp_f32 v177, v177\n"
"v_mfma_f32_16x16x32_bf16 v[152:155], a[56:59], v[40:43], v[152:155]\n"
"s_waitcnt lgkmcnt(5)\n"
"v_exp_f32 v178, v178\n"
"v_mfma_f32_16x16x32_bf16 v[152:155], a[60:63], v[44:47], v[152:155]\n"
"v_exp_f32 v179, v179\n"
"v_mfma_f32_16x16x32_bf16 v[156:159], a[48:51], v[48:51], v[236:239]\n"
"ds_read_b64_tr_b16 a[28:29], v10 offset:20480\n"
"v_cvt_pk_bf16_f32 v209, v170, v171\n"
"v_mfma_f32_16x16x32_bf16 v[156:159], a[52:55], v[52:55], v[156:159]\n"
"ds_read_b64_tr_b16 a[30:31], v12 offset:20736\n"
"v_cvt_pk_bf16_f32 v210, v176, v177\n"
"v_mfma_f32_16x16x32_bf16 v[156:159], a[56:59], v[56:59], v[156:159]\n"
"buffer_load_dwordx4 v18, s[44:47], s77 offen offset:3072 nt lds\n"
"v_cvt_pk_bf16_f32 v211, v178, v179\n"
"v_mfma_f32_16x16x32_bf16 v[156:159], a[60:63], v[60:63], v[156:159]\n"
"v_exp_f32 v180, v180\n"
"v_mfma_f32_16x16x32_bf16 v[160:163], a[0:3], v[32:35], v[232:235]\n"
"v_exp_f32 v181, v181\n"
"v_mfma_f32_16x16x32_bf16 v[160:163], a[4:7], v[36:39], v[160:163]\n"
"ds_read_b64_tr_b16 a[32:33], v9 offset:20736\n"
"v_add_f32 v254, v254, v180\n"
"v_mfma_f32_16x16x32_bf16 v[160:163], a[8:11], v[40:43], v[160:163]\n"
"v_exp_f32 v182, v182\n"
"v_mfma_f32_16x16x32_bf16 v[160:163], a[12:15], v[44:47], v[160:163]\n"
"v_exp_f32 v183, v183\n"
"v_mfma_f32_16x16x32_bf16 v[164:167], a[0:3], v[48:51], v[236:239]\n"
"ds_read_b64_tr_b16 a[34:35], v11 offset:20480\n"
"v_add_f32 v254, v254, v181\n"
"v_mfma_f32_16x16x32_bf16 v[164:167], a[4:7], v[52:55], v[164:167]\n"
"ds_read_b64 a[116:117], v15 offset:38920\n"
"v_cvt_pk_bf16_f32 v180, v180, v181\n"
"v_mfma_f32_16x16x32_bf16 v[164:167], a[8:11], v[56:59], v[164:167]\n"
"ds_read_b64 a[118:119], v16 offset:38920\n"
"v_cvt_pk_bf16_f32 v181, v182, v183\n"
"v_mfma_f32_16x16x32_bf16 v[164:167], a[12:15], v[60:63], v[164:167]\n"
"ds_write_b64 v13, v[180:181] offset:38912\n"
"v_mfma_f32_16x16x32_bf16 a[120:123], v[228:231], v[204:207], a[120:123]\n"
"s_add_u32 m0, s30, 0x2000\n"
"v_add_f32 v254, v254, v182\n"
"v_add_f32 v254, v254, v183\n"
"v_mfma_f32_16x16x32_bf16 a[124:127], v[228:231], v[208:211], a[124:127]\n"
"s_waitcnt lgkmcnt(5)\n"
"ds_read_b64_tr_b16 a[36:37], v10 offset:20736\n"
"ds_read_b64_tr_b16 a[38:39], v12 offset:20480\n"
"v_mfma_f32_16x16x32_bf16 v[88:91], a[16:19], v[204:207], v[88:91]\n"
"ds_read_b64_tr_b16 a[40:41], v9 offset:20992\n"
"buffer_load_dwordx4 v19, s[48:51], s75 offen offset:0 nt lds\n"
"v_mfma_f32_16x16x32_bf16 v[120:123], a[16:19], v[208:211], v[120:123]\n"
"ds_read_b64_tr_b16 a[42:43], v11 offset:21248\n"
"ds_read_b64_tr_b16 a[44:45], v10 offset:20992\n"
"ds_read_b64_tr_b16 a[46:47], v12 offset:21248\n"
"v_mfma_f32_16x16x32_bf16 v[92:95], a[20:23], v[204:207], v[92:95]\n"
"s_memrealtime s[24:25]\n"
"v_exp_f32 v184, v184\n"
"v_mfma_f32_16x16x32_bf16 v[124:127], a[20:23], v[208:211], v[124:127]\n"
"v_exp_f32 v185, v185\n"
"v_mfma_f32_16x16x32_bf16 v[96:99], a[24:27], v[204:207], v[96:99]\n"
"v_exp_f32 v186, v186\n"
"v_mfma_f32_16x16x32_bf16 v[128:131], a[24:27], v[208:211], v[128:131]\n"
"v_exp_f32 v187, v187\n"
"v_mfma_f32_16x16x32_bf16 v[100:103], a[28:31], v[204:207], v[100:103]\n"
"v_cvt_pk_bf16_f32 v212, v184, v185\n"
"v_cvt_pk_bf16_f32 v213, v186, v187\n"
"v_mfma_f32_16x16x32_bf16 v[132:135], a[28:31], v[208:211], v[132:135]\n"
"s_waitcnt lgkmcnt(0)\n"
"s_sub_u32 s25, s24, s5\n"
"s_cmp_lt_i32 s25, 0\n"
"s_cbranch_scc1 .Lmha_LIM7\n"
".Lmha_LIM7R:\n"
"v_mfma_f32_16x16x32_bf16 v[104:107], a[32:35], v[204:207], v[104:107]\n"
"s_add_u32 s24, s24, s92\n"
"v_exp_f32 v192, v192\n"
"v_mfma_f32_16x16x32_bf16 v[136:139], a[32:35], v[208:211], v[136:139]\n"
"s_waitcnt vmcnt(22)\n"
"s_barrier\n"
"s_add_u32 s5, s24, 0x67\n"
"v_mfma_f32_16x16x32_bf16 v[108:111], a[36:39], v[204:207], v[108:111]\n"
"ds_read_b128 a[48:51], v1 offset:24576\n"
"ds_read_b128 a[52:55], v1 offset:24832\n"
"v_mfma_f32_16x16x32_bf16 v[140:143], a[36:39], v[208:211], v[140:143]\n"
"ds_read_b128 a[56:59], v1 offset:25088\n"
"buffer_load_dwordx4 v20, s[48:51], s75 offen offset:1024 nt lds\n"
"v_mfma_f32_16x16x32_bf16 v[80:83], a[40:43], v[204:207], v[80:83]\n"
"ds_read_b128 a[60:63], v1 offset:25344\n"
"ds_read_b128 a[0:3], v2 offset:24576\n"
"v_mfma_f32_16x16x32_bf16 v[112:115], a[40:43], v[208:211], v[112:115]\n"
"s_waitcnt vmcnt(18)\n"
"ds_write_b128 v13, a[128:131] offset:16384\n"
"v_mfma_f32_16x16x32_bf16 v[144:147], a[40:43], a[116:119], v[144:147]\n"
"v_exp_f32 v193, v193\n"
"v_mfma_f32_16x16x32_bf16 v[84:87], a[44:47], v[204:207], v[84:87]\n"
"v_exp_f32 v194, v194\n"
"v_mfma_f32_16x16x32_bf16 v[116:119], a[44:47], v[208:211], v[116:119]\n"
"ds_read_b128 a[4:7], v2 offset:24832\n"
"ds_read_b128 a[8:11], v2 offset:25088\n"
"v_mfma_f32_16x16x32_bf16 v[148:151], a[44:47], a[116:119], v[148:151]\n"
"s_waitcnt lgkmcnt(7)\n"
"ds_read_b128 a[12:15], v2 offset:25344\n"
"v_cvt_pk_bf16_f32 v214, v192, v193\n"
"v_mfma_f32_16x16x32_bf16 v[168:171], a[48:51], v[64:67], v[240:243]\n"
"s_waitcnt lgkmcnt(7)\n"
"ds_read_b64_tr_b16 a[16:17], v5 offset:28928\n"
"ds_read_b64_tr_b16 a[18:19], v7 offset:28672\n"
"v_mfma_f32_16x16x32_bf16 v[168:171], a[52:55], v[68:71], v[168:171]\n"
"s_waitcnt vmcnt(17) lgkmcnt(8)\n"
"ds_write_b128 v13, a[132:135] offset:17408\n"
"v_mfma_f32_16x16x32_bf16 v[168:171], a[56:59], v[72:75], v[168:171]\n"
"s_waitcnt lgkmcnt(8)\n"
"v_exp_f32 v195, v195\n"
"v_mfma_f32_16x16x32_bf16 v[168:171], a[60:63], v[76:79], v[168:171]\n"
"v_exp_f32 v188, v188\n"
"v_mfma_f32_16x16x32_bf16 v[172:175], a[48:51], v[32:35], v[232:235]\n"
"ds_read_b64_tr_b16 a[20:21], v6 offset:28928\n"
"ds_read_b64_tr_b16 a[22:23], v8 offset:28672\n"
"v_mfma_f32_16x16x32_bf16 v[172:175], a[52:55], v[36:39], v[172:175]\n"
"buffer_load_dwordx4 v19, s[48:51], s75 offen offset:2048 nt lds\n"
"ds_read_b64_tr_b16 a[24:25], v5 offset:29184\n"
"v_mfma_f32_16x16x32_bf16 v[172:175], a[56:59], v[40:43], v[172:175]\n"
"ds_read_b64_tr_b16 a[26:27], v7 offset:29440\n"
"v_cvt_pk_bf16_f32 v215, v194, v195\n"
"v_mfma_f32_16x16x32_bf16 v[172:175], a[60:63], v[44:47], v[172:175]\n"
"v_exp_f32 v189, v189\n"
"v_mfma_f32_16x16x32_bf16 v[176:179], a[48:51], v[48:51], v[236:239]\n"
"s_waitcnt vmcnt(17)\n"
"ds_write_b128 v13, a[136:139] offset:18432\n"
"v_mfma_f32_16x16x32_bf16 v[176:179], a[52:55], v[52:55], v[176:179]\n"
"v_exp_f32 v190, v190\n"
"v_mfma_f32_16x16x32_bf16 v[176:179], a[56:59], v[56:59], v[176:179]\n"
"v_exp_f32 v191, v191\n"
"v_mfma_f32_16x16x32_bf16 v[176:179], a[60:63], v[60:63], v[176:179]\n"
"s_waitcnt lgkmcnt(5)\n"
"v_exp_f32 v196, v196\n"
"v_mfma_f32_16x16x32_bf16 v[180:183], a[0:3], v[32:35], v[232:235]\n"
"v_exp_f32 v197, v197\n"
"v_mfma_f32_16x16x32_bf16 v[180:183], a[4:7], v[36:39], v[180:183]\n"
"v_exp_f32 v198, v198\n"
"v_mfma_f32_16x16x32_bf16 v[180:183], a[8:11], v[40:43], v[180:183]\n"
"v_exp_f32 v199, v199\n"
"v_mfma_f32_16x16x32_bf16 v[180:183], a[12:15], v[44:47], v[180:183]\n"
"s_waitcnt vmcnt(16)\n"
"ds_write_b128 v13, a[140:143] offset:19456\n"
"v_mfma_f32_16x16x32_bf16 v[184:187], a[0:3], v[48:51], v[236:239]\n"
"v_cvt_pk_bf16_f32 v216, v188, v189\n"
"v_cvt_pk_bf16_f32 v217, v190, v191\n"
"v_mfma_f32_16x16x32_bf16 v[184:187], a[4:7], v[52:55], v[184:187]\n"
"buffer_load_dwordx4 v20, s[48:51], s75 offen offset:3072 nt lds\n"
"v_cvt_pk_bf16_f32 v218, v196, v197\n"
"v_mfma_f32_16x16x32_bf16 v[184:187], a[8:11], v[56:59], v[184:187]\n"
"ds_read_b64_tr_b16 a[28:29], v6 offset:29184\n"
"v_cvt_pk_bf16_f32 v219, v198, v199\n"
"v_mfma_f32_16x16x32_bf16 v[184:187], a[12:15], v[60:63], v[184:187]\n"
"ds_read_b64_tr_b16 a[30:31], v8 offset:29440\n"
"ds_read_b64_tr_b16 a[32:33], v5 offset:29440\n"
"v_mfma_f32_16x16x32_bf16 a[120:123], v[228:231], v[212:215], a[120:123]\n"
"ds_read_b64_tr_b16 a[34:35], v7 offset:29184\n"
"ds_read_b64_tr_b16 a[36:37], v6 offset:29440\n"
"v_mfma_f32_16x16x32_bf16 a[124:127], v[228:231], v[216:219], a[124:127]\n"
"ds_read_b64_tr_b16 a[38:39], v8 offset:29184\n"
"ds_read_b64_tr_b16 a[40:41], v5 offset:28672\n"
"v_mfma_f32_16x16x32_bf16 v[88:91], a[16:19], v[212:215], v[88:91]\n"
"ds_read_b64_tr_b16 a[42:43], v7 offset:28928\n"
"ds_read_b64 a[112:113], v13 offset:38912\n"
"v_mfma_f32_16x16x32_bf16 v[120:123], a[16:19], v[216:219], v[120:123]\n"
"s_waitcnt lgkmcnt(10)\n"
"ds_read_b64 a[114:115], v14 offset:38912\n"
"ds_read_b64_tr_b16 a[44:45], v6 offset:28672\n"
"v_mfma_f32_16x16x32_bf16 v[92:95], a[20:23], v[212:215], v[92:95]\n"
"ds_read_b64_tr_b16 a[46:47], v8 offset:28928\n"
"ds_read_b128 a[48:51], v3 offset:24576\n"
"v_mfma_f32_16x16x32_bf16 v[124:127], a[20:23], v[216:219], v[124:127]\n"
"ds_read_b128 a[52:55], v3 offset:24832\n"
"ds_read_b128 a[56:59], v3 offset:25088\n"
"v_mfma_f32_16x16x32_bf16 v[96:99], a[24:27], v[212:215], v[96:99]\n"
"s_add_u32 m0, s30, 0x5000\n"
"ds_read_b128 a[60:63], v3 offset:25344\n"
"ds_read_b128 a[0:3], v4 offset:24576\n"
"v_mfma_f32_16x16x32_bf16 v[128:131], a[24:27], v[216:219], v[128:131]\n"
"s_waitcnt lgkmcnt(15)\n"
"v_exp_f32 v152, v152\n"
"v_mfma_f32_16x16x32_bf16 v[100:103], a[28:31], v[212:215], v[100:103]\n"
"s_mov_b32 s76, 0x2c000\n"
"ds_read_b128 a[4:7], v4 offset:24832\n"
"ds_read_b128 a[8:11], v4 offset:25088\n"
"v_mfma_f32_16x16x32_bf16 v[132:135], a[28:31], v[216:219], v[132:135]\n"
"s_waitcnt lgkmcnt(15)\n"
"buffer_load_dwordx4 v17, s[44:47], s76 offen offset:0 nt lds\n"
"ds_read_b128 a[12:15], v4 offset:25344\n"
"v_mfma_f32_16x16x32_bf16 v[104:107], a[32:35], v[212:215], v[104:107]\n"
"v_exp_f32 v153, v153\n"
"v_mfma_f32_16x16x32_bf16 v[136:139], a[32:35], v[216:219], v[136:139]\n"
"s_waitcnt lgkmcnt(14)\n"
"ds_read_b64_tr_b16 a[16:17], v9 offset:29440\n"
"v_cvt_pk_bf16_f32 v220, v152, v153\n"
"v_exp_f32 v154, v154\n"
"v_mfma_f32_16x16x32_bf16 v[108:111], a[36:39], v[212:215], v[108:111]\n"
"ds_read_b64_tr_b16 a[18:19], v11 offset:29184\n"
"v_exp_f32 v155, v155\n"
"v_exp_f32 v168, v168\n"
"v_mfma_f32_16x16x32_bf16 v[140:143], a[36:39], v[216:219], v[140:143]\n"
"s_waitcnt lgkmcnt(11)\n"
"ds_read_b64_tr_b16 a[20:21], v10 offset:29440\n"
"v_cvt_pk_bf16_f32 v221, v154, v155\n"
"v_exp_f32 v169, v169\n"
"v_mfma_f32_16x16x32_bf16 v[80:83], a[40:43], v[212:215], v[80:83]\n"
"ds_read_b64_tr_b16 a[22:23], v12 offset:29184\n"
"v_add_f32 v254, v254, v168\n"
"v_exp_f32 v170, v170\n"
"v_mfma_f32_16x16x32_bf16 v[112:115], a[40:43], v[216:219], v[112:115]\n"
"ds_read_b64_tr_b16 a[24:25], v9 offset:28672\n"
"v_add_f32 v254, v254, v169\n"
"v_exp_f32 v171, v171\n"
"v_exp_f32 v160, v160\n"
"v_exp_f32 v161, v161\n"
"v_mfma_f32_16x16x32_bf16 v[144:147], a[40:43], a[112:115], v[144:147]\n"
"s_waitcnt lgkmcnt(6)\n"
"ds_read_b64_tr_b16 a[26:27], v11 offset:28928\n"
"v_add_f32 v254, v254, v170\n"
"v_exp_f32 v162, v162\n"
"v_mfma_f32_16x16x32_bf16 v[84:87], a[44:47], v[212:215], v[84:87]\n"
"ds_read_b64_tr_b16 a[28:29], v10 offset:28672\n"
"v_exp_f32 v163, v163\n"
"v_add_f32 v254, v254, v171\n"
"v_mfma_f32_16x16x32_bf16 v[116:119], a[44:47], v[216:219], v[116:119]\n"
"ds_read_b64_tr_b16 a[30:31], v12 offset:28928\n"
"v_mov_b32 v255, v254\n"
"v_cvt_pk_bf16_f32 v222, v160, v161\n"
"v_mfma_f32_16x16x32_bf16 v[148:151], a[44:47], a[112:115], v[148:151]\n"
"ds_read_b64_tr_b16 a[32:33], v9 offset:28928\n"
"v_cvt_pk_bf16_f32 v223, v162, v163\n"
"v_permlane32_swap_b32 v254, v255\n"
"v_mfma_f32_16x16x32_bf16 v[188:191], a[48:51], v[32:35], v[232:235]\n"
"buffer_load_dwordx4 v17, s[44:47], s76 offen offset:1024 nt lds\n"
"v_add_f32 v254, v254, v255\n"
"v_cvt_pk_bf16_f32 v168, v168, v169\n"
"v_mfma_f32_16x16x32_bf16 v[188:191], a[52:55], v[36:39], v[188:191]\n"
"ds_read_b64_tr_b16 a[34:35], v11 offset:28672\n"
"v_exp_f32 v156, v156\n"
"v_mfma_f32_16x16x32_bf16 v[188:191], a[56:59], v[40:43], v[188:191]\n"
"v_exp_f32 v157, v157\n"
"v_mfma_f32_16x16x32_bf16 v[188:191], a[60:63], v[44:47], v[188:191]\n"
"v_exp_f32 v158, v158\n"
"v_mfma_f32_16x16x32_bf16 v[192:195], a[48:51], v[48:51], v[236:239]\n"
"v_exp_f32 v159, v159\n"
"v_mfma_f32_16x16x32_bf16 v[192:195], a[52:55], v[52:55], v[192:195]\n"
"v_exp_f32 v164, v164\n"
"v_mfma_f32_16x16x32_bf16 v[192:195], a[56:59], v[56:59], v[192:195]\n"
"v_exp_f32 v165, v165\n"
"v_mfma_f32_16x16x32_bf16 v[192:195], a[60:63], v[60:63], v[192:195]\n"
"v_exp_f32 v166, v166\n"
"v_mfma_f32_16x16x32_bf16 v[196:199], a[0:3], v[32:35], v[232:235]\n"
"v_exp_f32 v167, v167\n"
"v_mfma_f32_16x16x32_bf16 v[196:199], a[4:7], v[36:39], v[196:199]\n"
"ds_read_b64 a[116:117], v15 offset:38912\n"
"v_cvt_pk_bf16_f32 v224, v156, v157\n"
"v_mfma_f32_16x16x32_bf16 v[196:199], a[8:11], v[40:43], v[196:199]\n"
"s_waitcnt lgkmcnt(2)\n"
"ds_read_b64 a[118:119], v16 offset:38912\n"
"v_cvt_pk_bf16_f32 v225, v158, v159\n"
"v_mfma_f32_16x16x32_bf16 v[196:199], a[12:15], v[44:47], v[196:199]\n"
"ds_read_b64_tr_b16 a[36:37], v10 offset:28928\n"
"v_cvt_pk_bf16_f32 v226, v164, v165\n"
"v_mfma_f32_16x16x32_bf16 v[152:155], a[0:3], v[48:51], v[236:239]\n"
"buffer_load_dwordx4 v18, s[44:47], s76 offen offset:2048 nt lds\n"
"v_cvt_pk_bf16_f32 v227, v166, v167\n"
"v_mfma_f32_16x16x32_bf16 v[152:155], a[4:7], v[52:55], v[152:155]\n"
"ds_read_b64_tr_b16 a[38:39], v12 offset:28672\n"
"v_mov_b32 v255, v254\n"
"v_mfma_f32_16x16x32_bf16 v[152:155], a[8:11], v[56:59], v[152:155]\n"
"ds_read_b64_tr_b16 a[40:41], v9 offset:29184\n"
"v_cvt_pk_bf16_f32 v169, v170, v171\n"
"v_mfma_f32_16x16x32_bf16 v[152:155], a[12:15], v[60:63], v[152:155]\n"
"ds_read_b64_tr_b16 a[42:43], v11 offset:29440\n"
"v_permlane16_swap_b32 v254, v255\n"
"v_mfma_f32_16x16x32_bf16 a[120:123], v[228:231], v[220:223], a[120:123]\n"
"ds_read_b64_tr_b16 a[44:45], v10 offset:29184\n"
"v_add_f32 v254, v254, v255\n"
"v_mfma_f32_16x16x32_bf16 a[124:127], v[228:231], v[224:227], a[124:127]\n"
"ds_write_b64 v13, v[168:169] offset:38920\n"
"v_mfma_f32_16x16x32_bf16 v[88:91], a[16:19], v[220:223], v[88:91]\n"
"ds_write_b32 v13, v254 offset:39936\n"
"ds_read_b64_tr_b16 a[46:47], v12 offset:29440\n"
"v_mfma_f32_16x16x32_bf16 v[120:123], a[16:19], v[224:227], v[120:123]\n"
"v_exp_f32 v172, v172\n"
"v_mfma_f32_16x16x32_bf16 v[92:95], a[20:23], v[220:223], v[92:95]\n"
"s_memrealtime s[24:25]\n"
"v_exp_f32 v173, v173\n"
"v_mfma_f32_16x16x32_bf16 v[124:127], a[20:23], v[224:227], v[124:127]\n"
"v_exp_f32 v174, v174\n"
"v_mfma_f32_16x16x32_bf16 v[96:99], a[24:27], v[220:223], v[96:99]\n"
"v_exp_f32 v175, v175\n"
"v_mfma_f32_16x16x32_bf16 v[128:131], a[24:27], v[224:227], v[128:131]\n"
"v_cvt_pk_bf16_f32 v200, v172, v173\n"
"v_cvt_pk_bf16_f32 v201, v174, v175\n"
"v_mfma_f32_16x16x32_bf16 v[100:103], a[28:31], v[220:223], v[100:103]\n"
"buffer_load_dwordx4 v18, s[44:47], s76 offen offset:3072 nt lds\n"
"v_mfma_f32_16x16x32_bf16 v[132:135], a[28:31], v[224:227], v[132:135]\n"
"s_waitcnt lgkmcnt(0)\n"
"s_sub_u32 s25, s24, s5\n"
"s_cmp_lt_i32 s25, 0\n"
"s_cbranch_scc1 .Lmha_LIM8\n"
".Lmha_LIM8R:\n"
"v_mfma_f32_16x16x32_bf16 v[104:107], a[32:35], v[220:223], v[104:107]\n"
"s_add_u32 s24, s24, s92\n"
"v_exp_f32 v176, v176\n"
"v_mfma_f32_16x16x32_bf16 v[136:139], a[32:35], v[224:227], v[136:139]\n"
"s_barrier\n"
"v_exp_f32 v177, v177\n"
"v_mfma_f32_16x16x32_bf16 v[108:111], a[36:39], v[220:223], v[108:111]\n"
"s_add_u32 s5, s24, 0x6f\n"
"ds_read_b128 a[48:51], v21 offset:32768\n"
"ds_read_b128 a[52:55], v21 offset:33024\n"
"v_mfma_f32_16x16x32_bf16 v[140:143], a[36:39], v[224:227], v[140:143]\n"
"ds_read_b128 a[56:59], v21 offset:33280\n"
"ds_read_b128 a[60:63], v21 offset:33536\n"
"v_mfma_f32_16x16x32_bf16 v[80:83], a[40:43], v[220:223], v[80:83]\n"
"ds_read_b128 a[0:3], v1 offset:32768\n"
"ds_read_b128 a[4:7], v1 offset:33024\n"
"v_mfma_f32_16x16x32_bf16 v[112:115], a[40:43], v[224:227], v[112:115]\n"
"ds_read_b128 a[8:11], v1 offset:33280\n"
"ds_read_b128 a[12:15], v1 offset:33536\n"
"v_mfma_f32_16x16x32_bf16 v[144:147], a[40:43], a[116:119], v[144:147]\n"
"ds_read_b128 a[16:19], v2 offset:32768\n"
"ds_read_b128 a[20:23], v2 offset:33024\n"
"v_mfma_f32_16x16x32_bf16 v[84:87], a[44:47], v[220:223], v[84:87]\n"
"ds_read_b128 a[24:27], v2 offset:33280\n"
"ds_read_b128 a[28:31], v2 offset:33536\n"
"v_mfma_f32_16x16x32_bf16 v[116:119], a[44:47], v[224:227], v[116:119]\n"
"ds_read_b64_tr_b16 a[32:33], v5 offset:16640\n"
"v_cvt_pk_bf16_f32 v204, v176, v177\n"
"v_mfma_f32_16x16x32_bf16 v[148:151], a[44:47], a[116:119], v[148:151]\n"
"s_waitcnt lgkmcnt(12)\n"
"ds_read_b64_tr_b16 a[34:35], v7 offset:16384\n"
"v_add_u32 v21, s56, v22\n"
"v_mfma_f32_16x16x32_bf16 v[156:159], a[48:51], a[96:99], 0\n"
"s_waitcnt lgkmcnt(12)\n"
"s_add_u32 m0, s30, 0x6000\n"
"s_nop 0\n"
"buffer_load_dwordx4 v19, s[48:51], s77 offen offset:0 nt lds\n"
"ds_read_b64_tr_b16 a[36:37], v6 offset:16640\n"
"v_mfma_f32_16x16x32_bf16 v[156:159], a[52:55], a[100:103], v[156:159]\n"
"s_waitcnt lgkmcnt(12)\n"
"v_exp_f32 v178, v178\n"
"v_mfma_f32_16x16x32_bf16 v[156:159], a[56:59], a[104:107], v[156:159]\n"
"s_waitcnt lgkmcnt(11)\n"
"v_exp_f32 v179, v179\n"
"v_mfma_f32_16x16x32_bf16 v[156:159], a[60:63], a[108:111], v[156:159]\n"
"s_waitcnt lgkmcnt(10)\n"
"ds_read_b64_tr_b16 a[38:39], v8 offset:16384\n"
"v_cvt_pk_bf16_f32 v205, v178, v179\n"
"v_mfma_f32_16x16x32_bf16 v[160:163], a[0:3], a[64:67], 0\n"
"s_waitcnt lgkmcnt(10)\n"
"ds_read_b64_tr_b16 a[40:41], v5 offset:16896\n"
"v_bfe_u32 v21, v21, 7, 3\n"
"v_mfma_f32_16x16x32_bf16 v[160:163], a[4:7], a[68:71], v[160:163]\n"
"s_waitcnt lgkmcnt(10)\n"
"ds_read_b64_tr_b16 a[42:43], v7 offset:17152\n"
"v_max3_f32 v244, v156, v157, v158\n"
"v_mfma_f32_16x16x32_bf16 v[160:163], a[8:11], a[72:75], v[160:163]\n"
"s_waitcnt lgkmcnt(10)\n"
"ds_read_b64_tr_b16 a[44:45], v6 offset:16896\n"
"v_max_f32 v244, v244, v159\n"
"v_mfma_f32_16x16x32_bf16 v[160:163], a[12:15], a[76:79], v[160:163]\n"
"s_waitcnt lgkmcnt(9)\n"
"ds_read_b64_tr_b16 a[46:47], v8 offset:17152\n"
"v_mov_b32 v245, v244\n"
"v_mfma_f32_16x16x32_bf16 v[164:167], a[16:19], a[64:67], 0\n"
"v_exp_f32 v180, v180\n"
"v_mfma_f32_16x16x32_bf16 v[164:167], a[20:23], a[68:71], v[164:167]\n"
"s_waitcnt lgkmcnt(7)\n"
"ds_read_b64_tr_b16 a[48:49], v5 offset:17152\n"
"v_permlane32_swap_b32 v244, v245\n"
"v_mfma_f32_16x16x32_bf16 v[164:167], a[24:27], a[72:75], v[164:167]\n"
"ds_read_b64_tr_b16 a[50:51], v7 offset:16896\n"
"v_max_f32 v244, v244, v245\n"
"v_mfma_f32_16x16x32_bf16 v[164:167], a[28:31], a[76:79], v[164:167]\n"
"ds_read_b64_tr_b16 a[52:53], v6 offset:17152\n"
"ds_read_b64_tr_b16 a[54:55], v8 offset:16896\n"
"v_mov_b32 v245, v244\n"
"v_mfma_f32_16x16x32_bf16 v[168:171], a[0:3], a[80:83], 0\n"
"buffer_load_dwordx4 v20, s[48:51], s77 offen offset:1024 nt lds\n"
"ds_read_b64_tr_b16 a[56:57], v5 offset:16384\n"
"v_permlane16_swap_b32 v244, v245\n"
"v_mfma_f32_16x16x32_bf16 v[168:171], a[4:7], a[84:87], v[168:171]\n"
"s_getpc_b64 s[98:99]\n"
"ds_read_b64_tr_b16 a[58:59], v7 offset:16640\n"
"v_max_f32 v244, v244, v245\n"
"v_max3_f32 v246, v160, v161, v162\n"
"v_mfma_f32_16x16x32_bf16 v[168:171], a[8:11], a[88:91], v[168:171]\n"
"ds_read_b64 a[112:113], v13 offset:38920\n"
"v_mul_f32 v240, -1.0, v244\n"
"v_mul_f32 v241, -1.0, v244\n"
"v_mfma_f32_16x16x32_bf16 v[168:171], a[12:15], a[92:95], v[168:171]\n"
"ds_read_b64 a[114:115], v14 offset:38920\n"
"v_mul_f32 v242, -1.0, v244\n"
"v_mul_f32 v243, -1.0, v244\n"
"v_mfma_f32_16x16x32_bf16 v[172:175], a[16:19], a[80:83], 0\n"
"global_load_dword v250, v0, s[98:99] offset:1536\n"
"ds_read_b64_tr_b16 a[60:61], v6 offset:16384\n"
"v_max3_f32 v246, v246, v163, v164\n"
"v_mfma_f32_16x16x32_bf16 v[172:175], a[20:23], a[84:87], v[172:175]\n"
"ds_read_b64_tr_b16 a[62:63], v8 offset:16640\n"
"v_exp_f32 v181, v181\n"
"v_max3_f32 v246, v246, v165, v166\n"
"v_mfma_f32_16x16x32_bf16 v[172:175], a[24:27], a[88:91], v[172:175]\n"
"v_exp_f32 v182, v182\n"
"v_cvt_pk_bf16_f32 v202, v180, v181\n"
"v_max_f32 v246, v246, v167\n"
"v_mfma_f32_16x16x32_bf16 v[172:175], a[28:31], a[92:95], v[172:175]\n"
"v_exp_f32 v183, v183\n"
"v_mov_b32 v247, v246\n"
"v_mfma_f32_16x16x32_bf16 v[176:179], a[0:3], a[96:99], v[240:243]\n"
"ds_read_b128 a[0:3], v3 offset:32768\n"
"v_cvt_pk_bf16_f32 v203, v182, v183\n"
"v_permlane32_swap_b32 v246, v247\n"
"v_mfma_f32_16x16x32_bf16 v[176:179], a[4:7], a[100:103], v[176:179]\n"
"ds_read_b128 a[4:7], v3 offset:33024\n"
"v_exp_f32 v184, v184\n"
"v_max_f32 v246, v246, v247\n"
"v_mfma_f32_16x16x32_bf16 v[176:179], a[8:11], a[104:107], v[176:179]\n"
"ds_read_b128 a[8:11], v3 offset:33280\n"
"v_exp_f32 v185, v185\n"
"v_mov_b32 v247, v246\n"
"v_mfma_f32_16x16x32_bf16 v[176:179], a[12:15], a[108:111], v[176:179]\n"
"buffer_load_dwordx4 v19, s[48:51], s77 offen offset:2048 nt lds\n"
"v_exp_f32 v186, v186\n"
"v_cvt_pk_bf16_f32 v206, v184, v185\n"
"v_exp_f32 v187, v187\n"
"v_mfma_f32_16x16x32_bf16 a[120:123], v[228:231], v[200:203], a[120:123]\n"
"ds_read_b128 a[12:15], v3 offset:33536\n"
"v_cvt_pk_bf16_f32 v207, v186, v187\n"
"v_max3_f32 v248, v168, v169, v170\n"
"v_permlane16_swap_b32 v246, v247\n"
"v_mfma_f32_16x16x32_bf16 a[124:127], v[228:231], v[204:207], a[124:127]\n"
"s_waitcnt lgkmcnt(4)\n"
"ds_read_b128 a[16:19], v4 offset:32768\n"
"v_max3_f32 v248, v248, v171, v172\n"
"v_mfma_f32_16x16x32_bf16 v[88:91], a[32:35], v[200:203], v[88:91]\n"
"ds_read_b128 a[20:23], v4 offset:33024\n"
"v_max_f32 v246, v246, v247\n"
"v_mfma_f32_16x16x32_bf16 v[120:123], a[32:35], v[204:207], v[120:123]\n"
"ds_read_b128 a[24:27], v4 offset:33280\n"
"v_max3_f32 v248, v248, v173, v174\n"
"v_mfma_f32_16x16x32_bf16 v[92:95], a[36:39], v[200:203], v[92:95]\n"
"ds_read_b128 a[28:31], v4 offset:33536\n"
"v_mul_f32 v232, -1.0, v246\n"
"v_mfma_f32_16x16x32_bf16 v[124:127], a[36:39], v[204:207], v[124:127]\n"
"ds_read_b64_tr_b16 a[32:33], v9 offset:17152\n"
"v_mul_f32 v233, -1.0, v246\n"
"v_mfma_f32_16x16x32_bf16 v[96:99], a[40:43], v[200:203], v[96:99]\n"
"ds_read_b64_tr_b16 a[34:35], v11 offset:16896\n"
"v_mul_f32 v234, -1.0, v246\n"
"v_mfma_f32_16x16x32_bf16 v[128:131], a[40:43], v[204:207], v[128:131]\n"
"ds_read_b64_tr_b16 a[36:37], v10 offset:17152\n"
"v_mul_f32 v235, -1.0, v246\n"
"v_mfma_f32_16x16x32_bf16 v[100:103], a[44:47], v[200:203], v[100:103]\n"
"ds_read_b64_tr_b16 a[38:39], v12 offset:16896\n"
"v_max_f32 v248, v248, v175\n"
"v_mfma_f32_16x16x32_bf16 v[132:135], a[44:47], v[204:207], v[132:135]\n"
"ds_read_b64_tr_b16 a[40:41], v9 offset:16384\n"
"v_mov_b32 v249, v248\n"
"v_mfma_f32_16x16x32_bf16 v[104:107], a[48:51], v[200:203], v[104:107]\n"
"v_exp_f32 v188, v188\n"
"v_mfma_f32_16x16x32_bf16 v[136:139], a[48:51], v[204:207], v[136:139]\n"
"buffer_load_dwordx4 v20, s[48:51], s77 offen offset:3072 nt lds\n"
"v_permlane32_swap_b32 v248, v249\n"
"v_mfma_f32_16x16x32_bf16 v[108:111], a[52:55], v[200:203], v[108:111]\n"
"ds_read_b64_tr_b16 a[42:43], v11 offset:16640\n"
"v_max_f32 v248, v248, v249\n"
"v_exp_f32 v189, v189\n"
"v_mfma_f32_16x16x32_bf16 v[140:143], a[52:55], v[204:207], v[140:143]\n"
"ds_read_b64_tr_b16 a[44:45], v10 offset:16384\n"
"v_mov_b32 v249, v248\n"
"v_exp_f32 v190, v190\n"
"v_exp_f32 v191, v191\n"
"v_mfma_f32_16x16x32_bf16 v[80:83], a[56:59], v[200:203], v[80:83]\n"
"ds_read_b64_tr_b16 a[46:47], v12 offset:16640\n"
"v_permlane16_swap_b32 v248, v249\n"
"v_cvt_pk_bf16_f32 v208, v188, v189\n"
"v_mfma_f32_16x16x32_bf16 v[112:115], a[56:59], v[204:207], v[112:115]\n"
"ds_read_b64_tr_b16 a[48:49], v9 offset:16640\n"
"v_max_f32 v248, v248, v249\n"
"v_cvt_pk_bf16_f32 v209, v190, v191\n"
"v_exp_f32 v192, v192\n"
"v_exp_f32 v193, v193\n"
"v_mfma_f32_16x16x32_bf16 v[144:147], a[56:59], a[112:115], v[144:147]\n"
"ds_read_b64_tr_b16 a[50:51], v11 offset:16384\n"
"v_mul_f32 v236, -1.0, v248\n"
"v_mul_f32 v237, -1.0, v248\n"
"v_mfma_f32_16x16x32_bf16 v[84:87], a[60:63], v[200:203], v[84:87]\n"
"ds_read_b64 a[116:117], v15 offset:38920\n"
"v_mul_f32 v238, -1.0, v248\n"
"v_mul_f32 v239, -1.0, v248\n"
"v_mfma_f32_16x16x32_bf16 v[116:119], a[60:63], v[204:207], v[116:119]\n"
"ds_read_b64 a[118:119], v16 offset:38920\n"
"v_exp_f32 v194, v194\n"
"v_cvt_pk_bf16_f32 v212, v192, v193\n"
"v_mfma_f32_16x16x32_bf16 v[148:151], a[60:63], a[112:115], v[148:151]\n"
"s_waitcnt lgkmcnt(6)\n"
"ds_read_b64_tr_b16 a[52:53], v10 offset:16640\n"
"ds_read_b64_tr_b16 a[54:55], v12 offset:16384\n"
"v_exp_f32 v195, v195\n"
"v_mfma_f32_16x16x32_bf16 v[180:183], a[0:3], a[64:67], v[232:235]\n"
"ds_read_b64_tr_b16 a[56:57], v9 offset:16896\n"
"ds_read_b64_tr_b16 a[58:59], v11 offset:17152\n"
"v_cvt_pk_bf16_f32 v213, v194, v195\n"
"v_mfma_f32_16x16x32_bf16 v[180:183], a[4:7], a[68:71], v[180:183]\n"
"s_add_u32 m0, s30, 0x7000\n"
"ds_read_b64_tr_b16 a[60:61], v10 offset:16896\n"
"v_exp_f32 v196, v196\n"
"v_mfma_f32_16x16x32_bf16 v[180:183], a[8:11], a[72:75], v[180:183]\n"
"s_mov_b32 s78, 0x30000\n"
"v_exp_f32 v197, v197\n"
"v_mfma_f32_16x16x32_bf16 v[180:183], a[12:15], a[76:79], v[180:183]\n"
"buffer_load_dwordx4 v17, s[44:47], s78 offen offset:0 nt lds\n"
"v_cvt_pk_bf16_f32 v210, v196, v197\n"
"v_mfma_f32_16x16x32_bf16 v[184:187], a[0:3], a[80:83], v[236:239]\n"
"v_exp_f32 v198, v198\n"
"v_mfma_f32_16x16x32_bf16 v[184:187], a[4:7], a[84:87], v[184:187]\n"
"v_exp_f32 v199, v199\n"
"v_mfma_f32_16x16x32_bf16 v[184:187], a[8:11], a[88:91], v[184:187]\n"
"ds_read_b64_tr_b16 a[62:63], v12 offset:17152\n"
"v_cvt_pk_bf16_f32 v211, v198, v199\n"
"v_mfma_f32_16x16x32_bf16 v[184:187], a[12:15], a[92:95], v[184:187]\n"
"ds_read_b32 v244, v13 offset:39936\n"
"v_exp_f32 v152, v152\n"
"v_mfma_f32_16x16x32_bf16 v[188:191], a[16:19], a[64:67], v[232:235]\n"
"ds_read_b32 v245, v14 offset:39936\n"
"v_exp_f32 v153, v153\n"
"v_mfma_f32_16x16x32_bf16 v[188:191], a[20:23], a[68:71], v[188:191]\n"
"ds_read_b32 v246, v15 offset:39936\n"
"v_exp_f32 v154, v154\n"
"v_mfma_f32_16x16x32_bf16 v[188:191], a[24:27], a[72:75], v[188:191]\n"
"ds_read_b32 v247, v16 offset:39936\n"
"v_exp_f32 v155, v155\n"
"v_mfma_f32_16x16x32_bf16 v[188:191], a[28:31], a[76:79], v[188:191]\n"
"v_cvt_pk_bf16_f32 v214, v152, v153\n"
"v_cvt_pk_bf16_f32 v215, v154, v155\n"
"v_mfma_f32_16x16x32_bf16 v[192:195], a[16:19], a[80:83], v[236:239]\n"
"v_exp_f32 v176, v176\n"
"v_mfma_f32_16x16x32_bf16 v[192:195], a[20:23], a[84:87], v[192:195]\n"
"v_exp_f32 v177, v177\n"
"v_mfma_f32_16x16x32_bf16 v[192:195], a[24:27], a[88:91], v[192:195]\n"
"v_add_f32 v254, v176, v177\n"
"v_cvt_pk_bf16_f32 v176, v176, v177\n"
"v_mfma_f32_16x16x32_bf16 v[192:195], a[28:31], a[92:95], v[192:195]\n"
"s_waitcnt lgkmcnt(2)\n"
"buffer_load_dwordx4 v17, s[44:47], s78 offen offset:1024 nt lds\n"
"v_mfma_f32_16x16x32_bf16 a[120:123], v[228:231], v[208:211], a[120:123]\n"
"v_exp_f32 v178, v178\n"
"v_add_f32 v244, v244, v245\n"
"v_mfma_f32_16x16x32_bf16 a[124:127], v[228:231], v[212:215], a[124:127]\n"
"s_waitcnt lgkmcnt(0)\n"
"v_exp_f32 v179, v179\n"
"v_add_f32 v246, v246, v247\n"
"v_mfma_f32_16x16x32_bf16 v[88:91], a[32:35], v[208:211], v[88:91]\n"
"v_cvt_pk_bf16_f32 v177, v178, v179\n"
"v_accvgpr_read_b32 v252, a120\n"
"v_add_f32 v255, v244, v246\n"
"v_mfma_f32_16x16x32_bf16 v[120:123], a[32:35], v[212:215], v[120:123]\n"
"ds_write_b64 v13, v[176:177] offset:38912\n"
"v_mfma_f32_16x16x32_bf16 v[92:95], a[36:39], v[208:211], v[92:95]\n"
"s_memrealtime s[24:25]\n"
"v_accvgpr_read_b32 v253, a124\n"
"v_add_f32 v248, v252, v253\n"
"v_mfma_f32_16x16x32_bf16 v[124:127], a[36:39], v[212:215], v[124:127]\n"
"v_add_f32 v248, v248, v255\n"
"v_cmp_nge_f32 vcc, s97, v248\n"
"v_mfma_f32_16x16x32_bf16 v[96:99], a[40:43], v[208:211], v[96:99]\n"
"v_add_f32 v160, v160, v232\n"
"v_add_f32 v161, v161, v232\n"
"v_mfma_f32_16x16x32_bf16 v[128:131], a[40:43], v[212:215], v[128:131]\n"
"v_add_f32 v162, v162, v232\n"
"v_add_f32 v163, v163, v232\n"
"v_mfma_f32_16x16x32_bf16 v[100:103], a[44:47], v[208:211], v[100:103]\n"
"v_exp_f32 v160, v160\n"
"v_mfma_f32_16x16x32_bf16 v[132:135], a[44:47], v[212:215], v[132:135]\n"
"s_waitcnt lgkmcnt(0)\n"
"s_sub_u32 s25, s24, s5\n"
"s_cmp_lt_i32 s25, 0\n"
"s_cbranch_scc1 .Lmha_LIM9\n"
".Lmha_LIM9R:\n"
"v_mfma_f32_16x16x32_bf16 v[104:107], a[48:51], v[208:211], v[104:107]\n"
"buffer_load_dwordx4 v18, s[44:47], s78 offen offset:2048 nt lds\n"
"v_add_f32 v164, v164, v232\n"
"v_mfma_f32_16x16x32_bf16 v[136:139], a[48:51], v[212:215], v[136:139]\n"
"s_waitcnt vmcnt(20)\n"
"s_barrier\n"
"s_add_u32 s24, s24, s92\n"
"v_mfma_f32_16x16x32_bf16 v[108:111], a[52:55], v[208:211], v[108:111]\n"
"s_add_u32 s5, s24, 0x6b\n"
"ds_read_b128 a[0:3], v1 offset:0\n"
"ds_read_b128 a[4:7], v1 offset:256\n"
"v_mfma_f32_16x16x32_bf16 v[140:143], a[52:55], v[212:215], v[140:143]\n"
"ds_read_b128 a[8:11], v1 offset:512\n"
"ds_read_b128 a[12:15], v1 offset:768\n"
"v_mfma_f32_16x16x32_bf16 v[80:83], a[56:59], v[208:211], v[80:83]\n"
"ds_read_b128 a[16:19], v2 offset:0\n"
"ds_read_b128 a[20:23], v2 offset:256\n"
"v_mfma_f32_16x16x32_bf16 v[112:115], a[56:59], v[212:215], v[112:115]\n"
"ds_read_b128 a[24:27], v2 offset:512\n"
"ds_read_b128 a[28:31], v2 offset:768\n"
"v_exp_f32 v161, v161\n"
"v_mfma_f32_16x16x32_bf16 v[144:147], a[56:59], a[116:119], v[144:147]\n"
"ds_read_b64_tr_b16 a[32:33], v5 offset:12544\n"
"v_exp_f32 v162, v162\n"
"v_exp_f32 v163, v163\n"
"v_mfma_f32_16x16x32_bf16 v[84:87], a[60:63], v[208:211], v[84:87]\n"
"ds_read_b64_tr_b16 a[34:35], v7 offset:12288\n"
"v_cvt_pk_bf16_f32 v216, v160, v161\n"
"v_cvt_pk_bf16_f32 v217, v162, v163\n"
"v_mfma_f32_16x16x32_bf16 v[116:119], a[60:63], v[212:215], v[116:119]\n"
"ds_read_b64_tr_b16 a[36:37], v6 offset:12544\n"
"v_add_f32 v165, v165, v232\n"
"v_add_f32 v166, v166, v232\n"
"v_mfma_f32_16x16x32_bf16 v[148:151], a[60:63], a[116:119], v[148:151]\n"
"s_waitcnt lgkmcnt(10)\n"
"ds_read_b64_tr_b16 a[38:39], v8 offset:12288\n"
"v_add_f32 v167, v167, v232\n"
"v_add_f32 v168, v168, v236\n"
"v_mfma_f32_16x16x32_bf16 v[196:199], a[0:3], a[96:99], v[240:243]\n"
"s_waitcnt lgkmcnt(10)\n"
"ds_read_b64_tr_b16 a[40:41], v5 offset:12800\n"
"v_exp_f32 v164, v164\n"
"v_mfma_f32_16x16x32_bf16 v[196:199], a[4:7], a[100:103], v[196:199]\n"
"s_waitcnt lgkmcnt(10)\n"
"s_cbranch_vccnz .Lmha_SLOW0\n"
".Lmha_SLOWRET0:\n"
"ds_read_b64_tr_b16 a[42:43], v7 offset:13056\n"
"v_mfma_f32_16x16x32_bf16 v[196:199], a[8:11], a[104:107], v[196:199]\n"
"s_waitcnt lgkmcnt(10)\n"
"buffer_load_dwordx4 v18, s[44:47], s78 offen offset:3072 nt lds\n"
"v_add_f32 v169, v169, v236\n"
"v_add_f32 v170, v170, v236\n"
"v_mfma_f32_16x16x32_bf16 v[196:199], a[12:15], a[108:111], v[196:199]\n"
"ds_read_b64_tr_b16 a[44:45], v6 offset:12800\n"
"v_exp_f32 v165, v165\n"
"v_mfma_f32_16x16x32_bf16 v[152:155], a[0:3], a[64:67], v[232:235]\n"
"ds_read_b64_tr_b16 a[46:47], v8 offset:13056\n"
"v_exp_f32 v166, v166\n"
"v_mfma_f32_16x16x32_bf16 v[152:155], a[4:7], a[68:71], v[152:155]\n"
"v_exp_f32 v167, v167\n"
"v_mfma_f32_16x16x32_bf16 v[152:155], a[8:11], a[72:75], v[152:155]\n"
"ds_read_b64_tr_b16 a[48:49], v5 offset:13056\n"
"v_cvt_pk_bf16_f32 v218, v164, v165\n"
"v_cvt_pk_bf16_f32 v219, v166, v167\n"
"v_mfma_f32_16x16x32_bf16 v[152:155], a[12:15], a[76:79], v[152:155]\n"
"ds_read_b64_tr_b16 a[50:51], v7 offset:12800\n"
"v_add_f32 v171, v171, v236\n"
"v_add_f32 v172, v172, v236\n"
"v_mfma_f32_16x16x32_bf16 v[156:159], a[0:3], a[80:83], v[236:239]\n"
"ds_read_b64_tr_b16 a[52:53], v6 offset:13056\n"
"v_add_f32 v173, v173, v236\n"
"v_add_f32 v174, v174, v236\n"
"v_mfma_f32_16x16x32_bf16 v[156:159], a[4:7], a[84:87], v[156:159]\n"
"ds_read_b64_tr_b16 a[54:55], v8 offset:12800\n"
"v_add_f32 v175, v175, v236\n"
"v_mfma_f32_16x16x32_bf16 v[156:159], a[8:11], a[88:91], v[156:159]\n"
"ds_read_b64_tr_b16 a[56:57], v5 offset:12288\n"
"v_exp_f32 v168, v168\n"
"v_mfma_f32_16x16x32_bf16 v[156:159], a[12:15], a[92:95], v[156:159]\n"
"s_waitcnt lgkmcnt(7)\n"
"ds_read_b64_tr_b16 a[58:59], v7 offset:12544\n"
"v_exp_f32 v169, v169\n"
"v_mfma_f32_16x16x32_bf16 v[160:163], a[16:19], a[64:67], v[232:235]\n"
"ds_read_b64 a[112:113], v13 offset:38912\n"
"v_exp_f32 v170, v170\n"
"v_mfma_f32_16x16x32_bf16 v[160:163], a[20:23], a[68:71], v[160:163]\n"
"s_add_u32 m0, s30, 0x4000\n"
"ds_read_b64 a[114:115], v14 offset:38912\n"
"v_exp_f32 v171, v171\n"
"v_mfma_f32_16x16x32_bf16 v[160:163], a[24:27], a[72:75], v[160:163]\n"
"buffer_load_dwordx4 v19, s[48:51], s76 offen offset:0 nt lds\n"
"v_cvt_pk_bf16_f32 v220, v168, v169\n"
"v_cvt_pk_bf16_f32 v221, v170, v171\n"
"v_mfma_f32_16x16x32_bf16 v[160:163], a[28:31], a[76:79], v[160:163]\n"
"v_exp_f32 v172, v172\n"
"v_mfma_f32_16x16x32_bf16 v[164:167], a[16:19], a[80:83], v[236:239]\n"
"v_exp_f32 v173, v173\n"
"v_mfma_f32_16x16x32_bf16 v[164:167], a[20:23], a[84:87], v[164:167]\n"
"v_exp_f32 v174, v174\n"
"v_mfma_f32_16x16x32_bf16 v[164:167], a[24:27], a[88:91], v[164:167]\n"
"v_exp_f32 v175, v175\n"
"v_mfma_f32_16x16x32_bf16 v[164:167], a[28:31], a[92:95], v[164:167]\n"
"ds_read_b64_tr_b16 a[60:61], v6 offset:12288\n"
"v_cvt_pk_bf16_f32 v222, v172, v173\n"
"v_mfma_f32_16x16x32_bf16 a[200:203], v[228:231], v[216:219], 0\n"
"ds_read_b64_tr_b16 a[62:63], v8 offset:12544\n"
"v_cvt_pk_bf16_f32 v223, v174, v175\n"
"v_rcp_f32 v253, v253\n"
"ds_read_b128 a[0:3], v3 offset:0\n"
"v_mfma_f32_16x16x32_bf16 a[204:207], v[228:231], v[220:223], 0\n"
"ds_read_b128 a[4:7], v3 offset:256\n"
"v_mul_f32 v136, v253, v136\n"
"v_mfma_f32_16x16x32_bf16 a[136:139], a[32:35], v[216:219], 0\n"
"ds_read_b128 a[8:11], v3 offset:512\n"
"v_mul_f32 v137, v253, v137\n"
"v_mfma_f32_16x16x32_bf16 a[168:171], a[32:35], v[220:223], 0\n"
"ds_read_b128 a[12:15], v3 offset:768\n"
"v_mul_f32 v138, v253, v138\n"
"v_mfma_f32_16x16x32_bf16 a[140:143], a[36:39], v[216:219], 0\n"
"ds_read_b128 a[16:19], v4 offset:0\n"
"v_mul_f32 v139, v253, v139\n"
"v_mfma_f32_16x16x32_bf16 a[172:175], a[36:39], v[220:223], 0\n"
"ds_read_b128 a[20:23], v4 offset:256\n"
"v_cvt_pk_bf16_f32 v136, v136, v137\n"
"v_mfma_f32_16x16x32_bf16 a[144:147], a[40:43], v[216:219], 0\n"
"buffer_load_dwordx4 v20, s[48:51], s76 offen offset:1024 nt lds\n"
"v_mul_f32 v140, v253, v140\n"
"v_mfma_f32_16x16x32_bf16 a[176:179], a[40:43], v[220:223], 0\n"
"s_waitcnt lgkmcnt(8)\n"
"ds_read_b128 a[24:27], v4 offset:512\n"
"v_mul_f32 v141, v253, v141\n"
"v_mfma_f32_16x16x32_bf16 a[148:151], a[44:47], v[216:219], 0\n"
"ds_read_b128 a[28:31], v4 offset:768\n"
"v_mul_f32 v142, v253, v142\n"
"v_mfma_f32_16x16x32_bf16 a[180:183], a[44:47], v[220:223], 0\n"
"ds_read_b64_tr_b16 a[32:33], v9 offset:13056\n"
"v_mul_f32 v143, v253, v143\n"
"v_mfma_f32_16x16x32_bf16 a[152:155], a[48:51], v[216:219], 0\n"
"ds_read_b64_tr_b16 a[34:35], v11 offset:12800\n"
"v_cvt_pk_bf16_f32 v137, v138, v139\n"
"v_add_f32 v254, v254, v178\n"
"v_mfma_f32_16x16x32_bf16 a[184:187], a[48:51], v[220:223], 0\n"
"ds_read_b64_tr_b16 a[36:37], v10 offset:13056\n"
"v_cvt_pk_bf16_f32 v138, v140, v141\n"
"v_cvt_pk_bf16_f32 v139, v142, v143\n"
"v_rcp_f32 v252, v252\n"
"v_mfma_f32_16x16x32_bf16 a[156:159], a[52:55], v[216:219], 0\n"
"ds_read_b64_tr_b16 a[38:39], v12 offset:12800\n"
"v_mul_f32 v80, v252, v80\n"
"v_mul_f32 v81, v252, v81\n"
"v_mul_f32 v82, v252, v82\n"
"v_mul_f32 v83, v252, v83\n"
"v_mfma_f32_16x16x32_bf16 a[188:191], a[52:55], v[220:223], 0\n"
"ds_read_b64_tr_b16 a[40:41], v9 offset:12288\n"
"v_cvt_pk_bf16_f32 v80, v80, v81\n"
"v_mul_f32 v84, v252, v84\n"
"v_mul_f32 v85, v252, v85\n"
"v_mul_f32 v86, v252, v86\n"
"v_mfma_f32_16x16x32_bf16 a[128:131], a[56:59], v[216:219], 0\n"
"ds_read_b64_tr_b16 a[42:43], v11 offset:12544\n"
"v_mul_f32 v87, v252, v87\n"
"v_cvt_pk_bf16_f32 v81, v82, v83\n"
"v_add_f32 v254, v254, v179\n"
"v_exp_f32 v180, v180\n"
"v_mfma_f32_16x16x32_bf16 a[160:163], a[56:59], v[220:223], 0\n"
"ds_read_b64_tr_b16 a[44:45], v10 offset:12288\n"
"v_cvt_pk_bf16_f32 v82, v84, v85\n"
"v_cvt_pk_bf16_f32 v83, v86, v87\n"
"v_exp_f32 v181, v181\n"
"v_exp_f32 v182, v182\n"
"v_mfma_f32_16x16x32_bf16 a[192:195], a[56:59], a[112:115], 0\n"
"s_waitcnt lgkmcnt(6)\n"
"ds_read_b64_tr_b16 a[46:47], v12 offset:12544\n"
"v_exp_f32 v183, v183\n"
"v_cvt_pk_bf16_f32 v224, v180, v181\n"
"v_exp_f32 v188, v188\n"
"v_exp_f32 v189, v189\n"
"v_mfma_f32_16x16x32_bf16 a[132:135], a[60:63], v[216:219], 0\n"
"buffer_load_dwordx4 v19, s[48:51], s76 offen offset:2048 nt lds\n"
"v_cvt_pk_bf16_f32 v225, v182, v183\n"
"v_exp_f32 v190, v190\n"
"v_exp_f32 v191, v191\n"
"v_mfma_f32_16x16x32_bf16 a[164:167], a[60:63], v[220:223], 0\n"
"ds_read_b64_tr_b16 a[48:49], v9 offset:12544\n"
"v_cvt_pk_bf16_f32 v226, v188, v189\n"
"v_cvt_pk_bf16_f32 v227, v190, v191\n"
"v_exp_f32 v184, v184\n"
"v_mfma_f32_16x16x32_bf16 a[196:199], a[60:63], a[112:115], 0\n"
"ds_read_b64_tr_b16 a[50:51], v11 offset:12288\n"
"v_exp_f32 v185, v185\n"
"v_mul_f32 v88, v252, v88\n"
"v_mfma_f32_16x16x32_bf16 v[168:171], a[0:3], a[64:67], v[232:235]\n"
"ds_read_b64 a[116:117], v15 offset:38912\n"
"v_exp_f32 v186, v186\n"
"v_cvt_pk_bf16_f32 v200, v184, v185\n"
"v_mfma_f32_16x16x32_bf16 v[168:171], a[4:7], a[68:71], v[168:171]\n"
"ds_read_b64 a[118:119], v16 offset:38912\n"
"v_exp_f32 v187, v187\n"
"v_mfma_f32_16x16x32_bf16 v[168:171], a[8:11], a[72:75], v[168:171]\n"
"ds_read_b64_tr_b16 a[52:53], v10 offset:12544\n"
"v_exp_f32 v192, v192\n"
"v_mfma_f32_16x16x32_bf16 v[168:171], a[12:15], a[76:79], v[168:171]\n"
"ds_read_b64_tr_b16 a[54:55], v12 offset:12288\n"
"v_exp_f32 v193, v193\n"
"v_mfma_f32_16x16x32_bf16 v[172:175], a[0:3], a[80:83], v[236:239]\n"
"v_exp_f32 v194, v194\n"
"v_mfma_f32_16x16x32_bf16 v[172:175], a[4:7], a[84:87], v[172:175]\n"
"v_exp_f32 v195, v195\n"
"v_mfma_f32_16x16x32_bf16 v[172:175], a[8:11], a[88:91], v[172:175]\n"
"ds_read_b64_tr_b16 a[56:57], v9 offset:12800\n"
"v_cvt_pk_bf16_f32 v201, v186, v187\n"
"v_mfma_f32_16x16x32_bf16 v[172:175], a[12:15], a[92:95], v[172:175]\n"
"ds_read_b64_tr_b16 a[58:59], v11 offset:13056\n"
"v_cvt_pk_bf16_f32 v202, v192, v193\n"
"v_mfma_f32_16x16x32_bf16 v[176:179], a[16:19], a[64:67], v[232:235]\n"
"ds_read_b64_tr_b16 a[60:61], v10 offset:12800\n"
"v_cvt_pk_bf16_f32 v203, v194, v195\n"
"v_mfma_f32_16x16x32_bf16 v[176:179], a[20:23], a[68:71], v[176:179]\n"
"buffer_load_dwordx4 v20, s[48:51], s76 offen offset:3072 nt lds\n"
"ds_read_b64_tr_b16 a[62:63], v12 offset:13056\n"
"v_mfma_f32_16x16x32_bf16 v[176:179], a[24:27], a[72:75], v[176:179]\n"
"v_exp_f32 v196, v196\n"
"v_mfma_f32_16x16x32_bf16 v[176:179], a[28:31], a[76:79], v[176:179]\n"
"v_exp_f32 v197, v197\n"
"v_mfma_f32_16x16x32_bf16 v[180:183], a[16:19], a[80:83], v[236:239]\n"
"v_add_f32 v254, v254, v196\n"
"v_add_f32 v254, v254, v197\n"
"v_mfma_f32_16x16x32_bf16 v[180:183], a[20:23], a[84:87], v[180:183]\n"
"v_exp_f32 v198, v198\n"
"v_mfma_f32_16x16x32_bf16 v[180:183], a[24:27], a[88:91], v[180:183]\n"
"v_exp_f32 v199, v199\n"
"v_mfma_f32_16x16x32_bf16 v[180:183], a[28:31], a[92:95], v[180:183]\n"
"v_cvt_pk_bf16_f32 v196, v196, v197\n"
"v_cvt_pk_bf16_f32 v197, v198, v199\n"
"v_mfma_f32_16x16x32_bf16 a[200:203], v[228:231], v[224:227], a[200:203]\n"
"ds_write_b64 v13, v[196:197] offset:38920\n"
"v_mfma_f32_16x16x32_bf16 a[204:207], v[228:231], v[200:203], a[204:207]\n"
"s_waitcnt lgkmcnt(4)\n"
"v_mul_f32 v89, v252, v89\n"
"v_mul_f32 v90, v252, v90\n"
"v_mfma_f32_16x16x32_bf16 a[136:139], a[32:35], v[224:227], a[136:139]\n"
"s_add_u32 m0, s30, 0x8000\n"
"v_mul_f32 v91, v252, v91\n"
"v_cvt_pk_bf16_f32 v88, v88, v89\n"
"v_mfma_f32_16x16x32_bf16 a[168:171], a[32:35], v[200:203], a[168:171]\n"
"s_mov_b32 s84, 0x34000\n"
"v_mul_f32 v92, v252, v92\n"
"v_mul_f32 v93, v252, v93\n"
"v_mfma_f32_16x16x32_bf16 a[140:143], a[36:39], v[224:227], a[140:143]\n"
"s_memrealtime s[24:25]\n"
"v_mul_f32 v94, v252, v94\n"
"v_mul_f32 v95, v252, v95\n"
"v_mfma_f32_16x16x32_bf16 a[172:175], a[36:39], v[200:203], a[172:175]\n"
"buffer_load_dwordx4 v17, s[44:47], s84 offen offset:0 nt lds\n"
"v_cvt_pk_bf16_f32 v89, v90, v91\n"
"v_mfma_f32_16x16x32_bf16 a[144:147], a[40:43], v[224:227], a[144:147]\n"
"v_cvt_pk_bf16_f32 v90, v92, v93\n"
"v_cvt_pk_bf16_f32 v91, v94, v95\n"
"v_mfma_f32_16x16x32_bf16 a[176:179], a[40:43], v[200:203], a[176:179]\n"
"v_mul_f32 v96, v252, v96\n"
"v_mul_f32 v97, v252, v97\n"
"v_mfma_f32_16x16x32_bf16 a[148:151], a[44:47], v[224:227], a[148:151]\n"
"v_mul_f32 v98, v252, v98\n"
"v_mul_f32 v99, v252, v99\n"
"v_mfma_f32_16x16x32_bf16 a[180:183], a[44:47], v[200:203], a[180:183]\n"
"s_waitcnt lgkmcnt(0)\n"
"s_sub_u32 s25, s24, s5\n"
"s_cmp_lt_i32 s25, 0\n"
"s_cbranch_scc1 .Lmha_LIM10\n"
".Lmha_LIM10R:\n"
"v_cvt_pk_bf16_f32 v96, v96, v97\n"
"v_mfma_f32_16x16x32_bf16 a[152:155], a[48:51], v[224:227], a[152:155]\n"
"s_waitcnt vmcnt(18)\n"
"s_barrier\n"
"v_mul_f32 v100, v252, v100\n"
"v_mfma_f32_16x16x32_bf16 a[184:187], a[48:51], v[200:203], a[184:187]\n"
"s_add_u32 s24, s24, s92\n"
"ds_read_b128 a[0:3], v1 offset:4096\n"
"ds_read_b128 a[4:7], v1 offset:4352\n"
"v_mfma_f32_16x16x32_bf16 a[156:159], a[52:55], v[224:227], a[156:159]\n"
"s_add_u32 s5, s24, 0x6c\n"
"ds_read_b128 a[8:11], v1 offset:4608\n"
"ds_read_b128 a[12:15], v1 offset:4864\n"
"v_mfma_f32_16x16x32_bf16 a[188:191], a[52:55], v[200:203], a[188:191]\n"
"ds_read_b128 a[16:19], v2 offset:4096\n"
"v_mul_f32 v101, v252, v101\n"
"v_mfma_f32_16x16x32_bf16 a[128:131], a[56:59], v[224:227], a[128:131]\n"
"ds_read_b128 a[20:23], v2 offset:4352\n"
"v_mul_f32 v102, v252, v102\n"
"v_mfma_f32_16x16x32_bf16 a[160:163], a[56:59], v[200:203], a[160:163]\n"
"ds_read_b128 a[24:27], v2 offset:4608\n"
"v_mul_f32 v103, v252, v103\n"
"v_cvt_pk_bf16_f32 v97, v98, v99\n"
"v_add_f32 v254, v254, v198\n"
"v_exp_f32 v152, v152\n"
"v_mfma_f32_16x16x32_bf16 a[192:195], a[56:59], a[116:119], a[192:195]\n"
"ds_read_b128 a[28:31], v2 offset:4864\n"
"v_cvt_pk_bf16_f32 v98, v100, v101\n"
"v_cvt_pk_bf16_f32 v99, v102, v103\n"
"v_add_f32 v254, v254, v199\n"
"v_exp_f32 v153, v153\n"
"v_mfma_f32_16x16x32_bf16 a[132:135], a[60:63], v[224:227], a[132:135]\n"
"buffer_load_dwordx4 v17, s[44:47], s84 offen offset:1024 nt lds\n"
"ds_read_b64_tr_b16 a[32:33], v5 offset:8448\n"
"v_exp_f32 v154, v154\n"
"v_exp_f32 v155, v155\n"
"v_cvt_pk_bf16_f32 v204, v152, v153\n"
"v_mfma_f32_16x16x32_bf16 a[164:167], a[60:63], v[200:203], a[164:167]\n"
"ds_read_b64_tr_b16 a[34:35], v7 offset:8192\n"
"v_cvt_pk_bf16_f32 v205, v154, v155\n"
"v_mul_f32 v104, v252, v104\n"
"v_mul_f32 v105, v252, v105\n"
"v_mfma_f32_16x16x32_bf16 a[196:199], a[60:63], a[116:119], a[196:199]\n"
"s_waitcnt lgkmcnt(7)\n"
"ds_read_b64_tr_b16 a[36:37], v6 offset:8448\n"
"v_mul_f32 v106, v252, v106\n"
"v_mul_f32 v107, v252, v107\n"
"v_mfma_f32_16x16x32_bf16 v[184:187], a[0:3], a[96:99], v[240:243]\n"
"ds_read_b64_tr_b16 a[38:39], v8 offset:8192\n"
"v_cvt_pk_bf16_f32 v104, v104, v105\n"
"v_mul_f32 v108, v252, v108\n"
"v_mfma_f32_16x16x32_bf16 v[184:187], a[4:7], a[100:103], v[184:187]\n"
"ds_read_b64_tr_b16 a[40:41], v5 offset:8704\n"
"v_mul_f32 v109, v252, v109\n"
"v_mul_f32 v110, v252, v110\n"
"v_mfma_f32_16x16x32_bf16 v[184:187], a[8:11], a[104:107], v[184:187]\n"
"s_waitcnt lgkmcnt(6)\n"
"ds_read_b64_tr_b16 a[42:43], v7 offset:8960\n"
"v_mul_f32 v111, v252, v111\n"
"v_cvt_pk_bf16_f32 v105, v106, v107\n"
"v_mfma_f32_16x16x32_bf16 v[184:187], a[12:15], a[108:111], v[184:187]\n"
"ds_read_b64_tr_b16 a[44:45], v6 offset:8704\n"
"v_exp_f32 v160, v160\n"
"v_mfma_f32_16x16x32_bf16 v[188:191], a[0:3], a[64:67], v[232:235]\n"
"ds_read_b64_tr_b16 a[46:47], v8 offset:8960\n"
"v_exp_f32 v161, v161\n"
"v_mfma_f32_16x16x32_bf16 v[188:191], a[4:7], a[68:71], v[188:191]\n"
"ds_read_b64_tr_b16 a[48:49], v5 offset:8960\n"
"v_exp_f32 v162, v162\n"
"v_mfma_f32_16x16x32_bf16 v[188:191], a[8:11], a[72:75], v[188:191]\n"
"v_exp_f32 v163, v163\n"
"v_mfma_f32_16x16x32_bf16 v[188:191], a[12:15], a[76:79], v[188:191]\n"
"ds_read_b64_tr_b16 a[50:51], v7 offset:8704\n"
"v_cvt_pk_bf16_f32 v106, v108, v109\n"
"v_mfma_f32_16x16x32_bf16 v[192:195], a[0:3], a[80:83], v[236:239]\n"
"buffer_load_dwordx4 v18, s[44:47], s84 offen offset:2048 nt lds\n"
"v_cvt_pk_bf16_f32 v107, v110, v111\n"
"v_cvt_pk_bf16_f32 v206, v160, v161\n"
"v_mfma_f32_16x16x32_bf16 v[192:195], a[4:7], a[84:87], v[192:195]\n"
"ds_read_b64_tr_b16 a[52:53], v6 offset:8960\n"
"v_cvt_pk_bf16_f32 v207, v162, v163\n"
"v_mfma_f32_16x16x32_bf16 v[192:195], a[8:11], a[88:91], v[192:195]\n"
"ds_read_b64_tr_b16 a[54:55], v8 offset:8704\n"
"v_exp_f32 v156, v156\n"
"v_mfma_f32_16x16x32_bf16 v[192:195], a[12:15], a[92:95], v[192:195]\n"
"ds_read_b64_tr_b16 a[56:57], v5 offset:8192\n"
"v_exp_f32 v157, v157\n"
"v_mfma_f32_16x16x32_bf16 v[196:199], a[16:19], a[64:67], v[232:235]\n"
"ds_read_b64_tr_b16 a[58:59], v7 offset:8448\n"
"v_exp_f32 v158, v158\n"
"v_mfma_f32_16x16x32_bf16 v[196:199], a[20:23], a[68:71], v[196:199]\n"
"ds_read_b64 a[112:113], v13 offset:38920\n"
"v_exp_f32 v159, v159\n"
"v_mfma_f32_16x16x32_bf16 v[196:199], a[24:27], a[72:75], v[196:199]\n"
"s_waitcnt lgkmcnt(7)\n"
"ds_read_b64 a[114:115], v14 offset:38920\n"
"v_exp_f32 v164, v164\n"
"v_mfma_f32_16x16x32_bf16 v[196:199], a[28:31], a[76:79], v[196:199]\n"
"ds_read_b64_tr_b16 a[60:61], v6 offset:8192\n"
"v_exp_f32 v165, v165\n"
"v_mfma_f32_16x16x32_bf16 v[152:155], a[16:19], a[80:83], v[236:239]\n"
"ds_read_b64_tr_b16 a[62:63], v8 offset:8448\n"
"v_exp_f32 v166, v166\n"
"v_mfma_f32_16x16x32_bf16 v[152:155], a[20:23], a[84:87], v[152:155]\n"
"ds_read_b128 a[0:3], v3 offset:4096\n"
"v_exp_f32 v167, v167\n"
"v_mfma_f32_16x16x32_bf16 v[152:155], a[24:27], a[88:91], v[152:155]\n"
"ds_read_b128 a[4:7], v3 offset:4352\n"
"v_cvt_pk_bf16_f32 v208, v156, v157\n"
"v_cvt_pk_bf16_f32 v209, v158, v159\n"
"v_mfma_f32_16x16x32_bf16 v[152:155], a[28:31], a[92:95], v[152:155]\n"
"ds_read_b128 a[8:11], v3 offset:4608\n"
"v_cvt_pk_bf16_f32 v210, v164, v165\n"
"v_cvt_pk_bf16_f32 v211, v166, v167\n"
"v_mfma_f32_16x16x32_bf16 a[200:203], v[228:231], v[204:207], a[200:203]\n"
"buffer_load_dwordx4 v18, s[44:47], s84 offen offset:3072 nt lds\n"
"v_lshrrev_b32 v251, 7, v22\n"
"v_mfma_f32_16x16x32_bf16 a[204:207], v[228:231], v[208:211], a[204:207]\n"
"ds_read_b128 a[12:15], v3 offset:4864\n"
"v_lshlrev_b32 v251, 11, v251\n"
"v_mfma_f32_16x16x32_bf16 a[136:139], a[32:35], v[204:207], a[136:139]\n"
"buffer_load_dword v251, v251, s[52:55], s3 offen offset:0\n"
"v_mul_f32 v112, v253, v112\n"
"v_mfma_f32_16x16x32_bf16 a[168:171], a[32:35], v[208:211], a[168:171]\n"
"ds_read_b128 a[16:19], v4 offset:4096\n"
"v_mul_f32 v113, v253, v113\n"
"v_mfma_f32_16x16x32_bf16 a[140:143], a[36:39], v[204:207], a[140:143]\n"
"ds_read_b128 a[20:23], v4 offset:4352\n"
"v_mul_f32 v114, v253, v114\n"
"v_mfma_f32_16x16x32_bf16 a[172:175], a[36:39], v[208:211], a[172:175]\n"
"ds_read_b128 a[24:27], v4 offset:4608\n"
"v_mul_f32 v115, v253, v115\n"
"v_mfma_f32_16x16x32_bf16 a[144:147], a[40:43], v[204:207], a[144:147]\n"
"ds_read_b128 a[28:31], v4 offset:4864\n"
"v_cvt_pk_bf16_f32 v112, v112, v113\n"
"v_mfma_f32_16x16x32_bf16 a[176:179], a[40:43], v[208:211], a[176:179]\n"
"ds_read_b64_tr_b16 a[32:33], v9 offset:8960\n"
"v_mul_f32 v116, v253, v116\n"
"v_mfma_f32_16x16x32_bf16 a[148:151], a[44:47], v[204:207], a[148:151]\n"
"ds_read_b64_tr_b16 a[34:35], v11 offset:8704\n"
"v_mul_f32 v117, v253, v117\n"
"v_mfma_f32_16x16x32_bf16 a[180:183], a[44:47], v[208:211], a[180:183]\n"
"s_waitcnt lgkmcnt(8)\n"
"ds_read_b64_tr_b16 a[36:37], v10 offset:8960\n"
"v_mul_f32 v118, v253, v118\n"
"v_mfma_f32_16x16x32_bf16 a[152:155], a[48:51], v[204:207], a[152:155]\n"
"ds_read_b64_tr_b16 a[38:39], v12 offset:8704\n"
"v_mul_f32 v119, v253, v119\n"
"v_mfma_f32_16x16x32_bf16 a[184:187], a[48:51], v[208:211], a[184:187]\n"
"s_add_u32 m0, s30, 0\n"
"ds_read_b64_tr_b16 a[40:41], v9 offset:8192\n"
"v_cvt_pk_bf16_f32 v113, v114, v115\n"
"v_mul_f32 v120, v253, v120\n"
"v_mfma_f32_16x16x32_bf16 a[156:159], a[52:55], v[204:207], a[156:159]\n"
"buffer_load_dwordx4 v19, s[48:51], s78 offen offset:0 nt lds\n"
"v_cvt_pk_bf16_f32 v114, v116, v117\n"
"v_cvt_pk_bf16_f32 v115, v118, v119\n"
"v_mul_f32 v121, v253, v121\n"
"v_mul_f32 v122, v253, v122\n"
"v_mfma_f32_16x16x32_bf16 a[188:191], a[52:55], v[208:211], a[188:191]\n"
"ds_read_b64_tr_b16 a[42:43], v11 offset:8448\n"
"v_mul_f32 v123, v253, v123\n"
"v_cvt_pk_bf16_f32 v120, v120, v121\n"
"v_mul_f32 v124, v253, v124\n"
"v_mul_f32 v125, v253, v125\n"
"v_mfma_f32_16x16x32_bf16 a[128:131], a[56:59], v[204:207], a[128:131]\n"
"ds_read_b64_tr_b16 a[44:45], v10 offset:8192\n"
"v_mul_f32 v126, v253, v126\n"
"v_mul_f32 v127, v253, v127\n"
"v_cvt_pk_bf16_f32 v121, v122, v123\n"
"v_exp_f32 v168, v168\n"
"v_mfma_f32_16x16x32_bf16 a[160:163], a[56:59], v[208:211], a[160:163]\n"
"ds_read_b64_tr_b16 a[46:47], v12 offset:8448\n"
"v_cvt_pk_bf16_f32 v122, v124, v125\n"
"v_cvt_pk_bf16_f32 v123, v126, v127\n"
"v_exp_f32 v169, v169\n"
"v_exp_f32 v170, v170\n"
"v_mfma_f32_16x16x32_bf16 a[192:195], a[56:59], a[112:115], a[192:195]\n"
"ds_read_b64_tr_b16 a[48:49], v9 offset:8448\n"
"v_exp_f32 v171, v171\n"
"v_cvt_pk_bf16_f32 v212, v168, v169\n"
"v_exp_f32 v176, v176\n"
"v_mfma_f32_16x16x32_bf16 a[132:135], a[60:63], v[204:207], a[132:135]\n"
"ds_read_b64_tr_b16 a[50:51], v11 offset:8192\n"
"v_cvt_pk_bf16_f32 v213, v170, v171\n"
"v_exp_f32 v177, v177\n"
"v_exp_f32 v178, v178\n"
"v_mfma_f32_16x16x32_bf16 a[164:167], a[60:63], v[208:211], a[164:167]\n"
"ds_read_b64 a[116:117], v15 offset:38920\n"
"v_exp_f32 v179, v179\n"
"v_cvt_pk_bf16_f32 v214, v176, v177\n"
"v_mfma_f32_16x16x32_bf16 a[196:199], a[60:63], a[112:115], a[196:199]\n"
"ds_read_b64 a[118:119], v16 offset:38920\n"
"ds_read_b64_tr_b16 a[52:53], v10 offset:8448\n"
"v_cvt_pk_bf16_f32 v215, v178, v179\n"
"v_mfma_f32_16x16x32_bf16 v[156:159], a[0:3], a[64:67], v[232:235]\n"
"ds_read_b64_tr_b16 a[54:55], v12 offset:8192\n"
"v_exp_f32 v172, v172\n"
"v_mfma_f32_16x16x32_bf16 v[156:159], a[4:7], a[68:71], v[156:159]\n"
"s_waitcnt lgkmcnt(7)\n"
"ds_read_b64_tr_b16 a[56:57], v9 offset:8704\n"
"v_exp_f32 v173, v173\n"
"v_mfma_f32_16x16x32_bf16 v[156:159], a[8:11], a[72:75], v[156:159]\n"
"ds_read_b64_tr_b16 a[58:59], v11 offset:8960\n"
"v_exp_f32 v174, v174\n"
"v_mfma_f32_16x16x32_bf16 v[156:159], a[12:15], a[76:79], v[156:159]\n"
"v_exp_f32 v175, v175\n"
"v_mfma_f32_16x16x32_bf16 v[160:163], a[0:3], a[80:83], v[236:239]\n"
"buffer_load_dwordx4 v20, s[48:51], s78 offen offset:1024 nt lds\n"
"v_cvt_pk_bf16_f32 v216, v172, v173\n"
"v_mfma_f32_16x16x32_bf16 v[160:163], a[4:7], a[84:87], v[160:163]\n"
"v_exp_f32 v180, v180\n"
"v_mfma_f32_16x16x32_bf16 v[160:163], a[8:11], a[88:91], v[160:163]\n"
"v_exp_f32 v181, v181\n"
"v_mfma_f32_16x16x32_bf16 v[160:163], a[12:15], a[92:95], v[160:163]\n"
"v_exp_f32 v182, v182\n"
"v_mfma_f32_16x16x32_bf16 v[164:167], a[16:19], a[64:67], v[232:235]\n"
"v_exp_f32 v183, v183\n"
"v_mfma_f32_16x16x32_bf16 v[164:167], a[20:23], a[68:71], v[164:167]\n"
"ds_read_b64_tr_b16 a[60:61], v10 offset:8704\n"
"v_cvt_pk_bf16_f32 v217, v174, v175\n"
"v_mfma_f32_16x16x32_bf16 v[164:167], a[24:27], a[72:75], v[164:167]\n"
"ds_read_b64_tr_b16 a[62:63], v12 offset:8960\n"
"v_cvt_pk_bf16_f32 v218, v180, v181\n"
"v_mfma_f32_16x16x32_bf16 v[164:167], a[28:31], a[76:79], v[164:167]\n"
"v_cvt_pk_bf16_f32 v219, v182, v183\n"
"v_mul_f32 v128, v253, v128\n"
"v_mul_f32 v129, v253, v129\n"
"v_mfma_f32_16x16x32_bf16 v[168:171], a[16:19], a[80:83], v[236:239]\n"
"v_exp_f32 v184, v184\n"
"v_mul_f32 v130, v253, v130\n"
"v_mfma_f32_16x16x32_bf16 v[168:171], a[20:23], a[84:87], v[168:171]\n"
"v_exp_f32 v185, v185\n"
"v_add_f32 v254, v254, v184\n"
"v_mfma_f32_16x16x32_bf16 v[168:171], a[24:27], a[88:91], v[168:171]\n"
"v_exp_f32 v186, v186\n"
"v_add_f32 v254, v254, v185\n"
"v_mfma_f32_16x16x32_bf16 v[168:171], a[28:31], a[92:95], v[168:171]\n"
"v_exp_f32 v187, v187\n"
"v_mfma_f32_16x16x32_bf16 a[200:203], v[228:231], v[212:215], a[200:203]\n"
"buffer_load_dwordx4 v19, s[48:51], s78 offen offset:2048 nt lds\n"
"v_cvt_pk_bf16_f32 v184, v184, v185\n"
"v_mfma_f32_16x16x32_bf16 a[204:207], v[228:231], v[216:219], a[204:207]\n"
"v_cvt_pk_bf16_f32 v185, v186, v187\n"
"v_mul_f32 v131, v253, v131\n"
"v_mfma_f32_16x16x32_bf16 a[136:139], a[32:35], v[212:215], a[136:139]\n"
"ds_write_b64 v13, v[184:185] offset:38912\n"
"v_mfma_f32_16x16x32_bf16 a[168:171], a[32:35], v[216:219], a[168:171]\n"
"v_cvt_pk_bf16_f32 v128, v128, v129\n"
"v_mul_f32 v132, v253, v132\n"
"v_mfma_f32_16x16x32_bf16 a[140:143], a[36:39], v[212:215], a[140:143]\n"
"s_memrealtime s[24:25]\n"
"v_mul_f32 v133, v253, v133\n"
"v_mul_f32 v134, v253, v134\n"
"v_mfma_f32_16x16x32_bf16 a[172:175], a[36:39], v[216:219], a[172:175]\n"
"v_mul_f32 v135, v253, v135\n"
"v_cvt_pk_bf16_f32 v129, v130, v131\n"
"v_mfma_f32_16x16x32_bf16 a[144:147], a[40:43], v[212:215], a[144:147]\n"
"v_cvt_pk_bf16_f32 v130, v132, v133\n"
"v_cvt_pk_bf16_f32 v131, v134, v135\n"
"v_mfma_f32_16x16x32_bf16 a[176:179], a[40:43], v[216:219], a[176:179]\n"
"s_waitcnt lgkmcnt(0)\n"
"v_rcp_f32 v255, v255\n"
"v_mfma_f32_16x16x32_bf16 a[148:151], a[44:47], v[212:215], a[148:151]\n"
"v_mul_f32 v144, v255, v144\n"
"v_mul_f32 v145, v255, v145\n"
"v_mfma_f32_16x16x32_bf16 a[180:183], a[44:47], v[216:219], a[180:183]\n"
"s_sub_u32 s25, s24, s5\n"
"s_cmp_lt_i32 s25, 0\n"
"s_cbranch_scc1 .Lmha_LIM11\n"
".Lmha_LIM11R:\n"
"v_mul_f32 v146, v255, v146\n"
"v_mul_f32 v147, v255, v147\n"
"v_mfma_f32_16x16x32_bf16 a[152:155], a[48:51], v[212:215], a[152:155]\n"
"s_waitcnt vmcnt(16)\n"
"s_barrier\n"
"v_cvt_pk_bf16_f32 v144, v144, v145\n"
"v_mfma_f32_16x16x32_bf16 a[184:187], a[48:51], v[216:219], a[184:187]\n"
"s_add_u32 s24, s24, s92\n"
"ds_read_b128 a[0:3], v1 offset:20480\n"
"buffer_load_dwordx4 v20, s[48:51], s78 offen offset:3072 nt lds\n"
"v_mfma_f32_16x16x32_bf16 a[156:159], a[52:55], v[212:215], a[156:159]\n"
"s_add_u32 s5, s24, 0x6a\n"
"ds_read_b128 a[4:7], v1 offset:20736\n"
"ds_read_b128 a[8:11], v1 offset:20992\n"
"v_mfma_f32_16x16x32_bf16 a[188:191], a[52:55], v[216:219], a[188:191]\n"
"ds_read_b128 a[12:15], v1 offset:21248\n"
"v_mul_f32 v148, v255, v148\n"
"v_mfma_f32_16x16x32_bf16 a[128:131], a[56:59], v[212:215], a[128:131]\n"
"ds_read_b128 a[16:19], v2 offset:20480\n"
"v_mul_f32 v149, v255, v149\n"
"v_mfma_f32_16x16x32_bf16 a[160:163], a[56:59], v[216:219], a[160:163]\n"
"ds_read_b128 a[20:23], v2 offset:20736\n"
"v_mul_f32 v150, v255, v150\n"
"v_mul_f32 v151, v255, v151\n"
"v_mfma_f32_16x16x32_bf16 a[192:195], a[56:59], a[116:119], a[192:195]\n"
"ds_read_b128 a[24:27], v2 offset:20992\n"
"v_cvt_pk_bf16_f32 v145, v146, v147\n"
"v_add_f32 v254, v254, v186\n"
"v_exp_f32 v188, v188\n"
"v_mfma_f32_16x16x32_bf16 a[132:135], a[60:63], v[212:215], a[132:135]\n"
"ds_read_b128 a[28:31], v2 offset:21248\n"
"v_cvt_pk_bf16_f32 v146, v148, v149\n"
"v_cvt_pk_bf16_f32 v147, v150, v151\n"
"v_mfma_f32_16x16x32_bf16 a[164:167], a[60:63], v[216:219], a[164:167]\n"
"ds_read_b64_tr_b16 a[32:33], v5 offset:24832\n"
"ds_read_b64_tr_b16 a[34:35], v7 offset:24576\n"
"v_add_f32 v254, v254, v187\n"
"v_mfma_f32_16x16x32_bf16 a[196:199], a[60:63], a[116:119], a[196:199]\n"
"s_waitcnt lgkmcnt(9)\n"
"ds_read_b64_tr_b16 a[36:37], v6 offset:24832\n"
"ds_read_b64_tr_b16 a[38:39], v8 offset:24576\n"
"v_exp_f32 v189, v189\n"
"v_mfma_f32_16x16x32_bf16 v[172:175], a[0:3], a[96:99], v[240:243]\n"
"s_waitcnt lgkmcnt(10)\n"
"ds_read_b64_tr_b16 a[40:41], v5 offset:25088\n"
"v_exp_f32 v190, v190\n"
"v_mfma_f32_16x16x32_bf16 v[172:175], a[4:7], a[100:103], v[172:175]\n"
"s_waitcnt lgkmcnt(10)\n"
"ds_read_b64_tr_b16 a[42:43], v7 offset:25344\n"
"v_exp_f32 v191, v191\n"
"v_mfma_f32_16x16x32_bf16 v[172:175], a[8:11], a[104:107], v[172:175]\n"
"s_waitcnt lgkmcnt(10)\n"
"ds_read_b64_tr_b16 a[44:45], v6 offset:25088\n"
"v_cvt_pk_bf16_f32 v220, v188, v189\n"
"v_mfma_f32_16x16x32_bf16 v[172:175], a[12:15], a[108:111], v[172:175]\n"
"s_mov_b32 s96, 0x38000\n"
"s_add_u32 m0, s30, 0x1000\n"
"s_nop 0\n"
"buffer_load_dwordx4 v17, s[44:47], s96 offen offset:0 nt lds\n"
"v_cvt_pk_bf16_f32 v221, v190, v191\n"
"v_mfma_f32_16x16x32_bf16 v[176:179], a[0:3], a[64:67], v[232:235]\n"
"v_exp_f32 v196, v196\n"
"v_mfma_f32_16x16x32_bf16 v[176:179], a[4:7], a[68:71], v[176:179]\n"
"v_exp_f32 v197, v197\n"
"v_mfma_f32_16x16x32_bf16 v[176:179], a[8:11], a[72:75], v[176:179]\n"
"v_exp_f32 v198, v198\n"
"v_mfma_f32_16x16x32_bf16 v[176:179], a[12:15], a[76:79], v[176:179]\n"
"v_exp_f32 v199, v199\n"
"v_mfma_f32_16x16x32_bf16 v[180:183], a[0:3], a[80:83], v[236:239]\n"
"ds_read_b64_tr_b16 a[46:47], v8 offset:25344\n"
"v_cvt_pk_bf16_f32 v222, v196, v197\n"
"v_mfma_f32_16x16x32_bf16 v[180:183], a[4:7], a[84:87], v[180:183]\n"
"ds_read_b64_tr_b16 a[48:49], v5 offset:25344\n"
"v_cvt_pk_bf16_f32 v223, v198, v199\n"
"v_mfma_f32_16x16x32_bf16 v[180:183], a[8:11], a[88:91], v[180:183]\n"
"ds_read_b64_tr_b16 a[50:51], v7 offset:25088\n"
"v_exp_f32 v192, v192\n"
"v_mfma_f32_16x16x32_bf16 v[180:183], a[12:15], a[92:95], v[180:183]\n"
"s_waitcnt lgkmcnt(5)\n"
"ds_read_b64_tr_b16 a[52:53], v6 offset:25344\n"
"v_exp_f32 v193, v193\n"
"v_mfma_f32_16x16x32_bf16 v[184:187], a[16:19], a[64:67], v[232:235]\n"
"ds_read_b64_tr_b16 a[54:55], v8 offset:25088\n"
"v_exp_f32 v194, v194\n"
"v_mfma_f32_16x16x32_bf16 v[184:187], a[20:23], a[68:71], v[184:187]\n"
"ds_read_b64_tr_b16 a[56:57], v5 offset:24576\n"
"v_exp_f32 v195, v195\n"
"v_mfma_f32_16x16x32_bf16 v[184:187], a[24:27], a[72:75], v[184:187]\n"
"ds_read_b64_tr_b16 a[58:59], v7 offset:24832\n"
"v_exp_f32 v152, v152\n"
"v_mfma_f32_16x16x32_bf16 v[184:187], a[28:31], a[76:79], v[184:187]\n"
"buffer_load_dwordx4 v17, s[44:47], s96 offen offset:1024 nt lds\n"
"v_cvt_pk_bf16_f32 v224, v192, v193\n"
"v_cvt_pk_bf16_f32 v225, v194, v195\n"
"v_mfma_f32_16x16x32_bf16 v[188:191], a[16:19], a[80:83], v[236:239]\n"
"v_exp_f32 v153, v153\n"
"v_mfma_f32_16x16x32_bf16 v[188:191], a[20:23], a[84:87], v[188:191]\n"
"v_exp_f32 v154, v154\n"
"v_mfma_f32_16x16x32_bf16 v[188:191], a[24:27], a[88:91], v[188:191]\n"
"s_add_u32 m0, s30, 0x3000\n"
"v_exp_f32 v155, v155\n"
"v_mfma_f32_16x16x32_bf16 v[188:191], a[28:31], a[92:95], v[188:191]\n"
"buffer_load_dwordx4 v23, s[52:55], s13 offen offset:0 lds\n"
"v_cvt_pk_bf16_f32 v226, v152, v153\n"
"v_mfma_f32_16x16x32_bf16 a[200:203], v[228:231], v[220:223], a[200:203]\n"
"ds_read_b64 a[112:113], v13 offset:38912\n"
"v_cvt_pk_bf16_f32 v227, v154, v155\n"
"ds_read_b64 a[114:115], v14 offset:38912\n"
"ds_read_b64_tr_b16 a[60:61], v6 offset:24576\n"
"v_mfma_f32_16x16x32_bf16 a[204:207], v[228:231], v[224:227], a[204:207]\n"
"ds_read_b64_tr_b16 a[62:63], v8 offset:24832\n"
"ds_read_b128 a[0:3], v3 offset:20480\n"
"v_mfma_f32_16x16x32_bf16 a[136:139], a[32:35], v[220:223], a[136:139]\n"
"ds_read_b128 a[4:7], v3 offset:20736\n"
"ds_read_b128 a[8:11], v3 offset:20992\n"
"v_mfma_f32_16x16x32_bf16 a[168:171], a[32:35], v[224:227], a[168:171]\n"
"ds_read_b128 a[12:15], v3 offset:21248\n"
"v_xor_b32 v21, 3, v21\n"
"v_mfma_f32_16x16x32_bf16 a[140:143], a[36:39], v[220:223], a[140:143]\n"
"ds_read_b128 a[16:19], v4 offset:20480\n"
"v_min_u32 v21, 1, v21\n"
"v_mfma_f32_16x16x32_bf16 a[172:175], a[36:39], v[224:227], a[172:175]\n"
"s_waitcnt lgkmcnt(9)\n"
"ds_read_b128 a[20:23], v4 offset:20736\n"
"v_add_u32 v21, -1, v21\n"
"v_mfma_f32_16x16x32_bf16 a[144:147], a[40:43], v[220:223], a[144:147]\n"
"s_add_u32 m0, s30, 0x1000\n"
"ds_read_b128 a[24:27], v4 offset:20992\n"
"v_and_b32 v21, 0x5000080, v21\n"
"v_mfma_f32_16x16x32_bf16 a[176:179], a[40:43], v[224:227], a[176:179]\n"
"buffer_load_dwordx4 v18, s[44:47], s96 offen offset:2048 nt lds\n"
"ds_read_b128 a[28:31], v4 offset:21248\n"
"v_mfma_f32_16x16x32_bf16 a[148:151], a[44:47], v[220:223], a[148:151]\n"
"ds_read_b64_tr_b16 a[32:33], v9 offset:25344\n"
"v_add_u32 v21, v21, v22\n"
"v_mfma_f32_16x16x32_bf16 a[180:183], a[44:47], v[224:227], a[180:183]\n"
"s_add_u32 m0, s30, 0x3000\n"
"ds_read_b64_tr_b16 a[34:35], v11 offset:25088\n"
"v_bfe_u32 v143, v22, 8, 2\n"
"v_mfma_f32_16x16x32_bf16 a[152:155], a[48:51], v[220:223], a[152:155]\n"
"buffer_load_dwordx4 v24, s[52:55], s13 offen offset:1024 lds\n"
"ds_read_b64_tr_b16 a[36:37], v10 offset:25344\n"
"v_mfma_f32_16x16x32_bf16 a[184:187], a[48:51], v[224:227], a[184:187]\n"
"v_exp_f32 v156, v156\n"
"v_mfma_f32_16x16x32_bf16 a[156:159], a[52:55], v[220:223], a[156:159]\n"
"ds_read_b64_tr_b16 a[38:39], v12 offset:25088\n"
"ds_read_b64_tr_b16 a[40:41], v9 offset:24576\n"
"v_exp_f32 v157, v157\n"
"v_mfma_f32_16x16x32_bf16 a[188:191], a[52:55], v[224:227], a[188:191]\n"
"ds_read_b64_tr_b16 a[42:43], v11 offset:24832\n"
"v_exp_f32 v158, v158\n"
"v_cvt_pk_bf16_f32 v200, v156, v157\n"
"v_mfma_f32_16x16x32_bf16 a[128:131], a[56:59], v[220:223], a[128:131]\n"
"ds_read_b64_tr_b16 a[44:45], v10 offset:24576\n"
"ds_read_b64_tr_b16 a[46:47], v12 offset:24832\n"
"v_exp_f32 v159, v159\n"
"v_mfma_f32_16x16x32_bf16 a[160:163], a[56:59], v[224:227], a[160:163]\n"
"s_waitcnt lgkmcnt(10)\n"
"ds_read_b64_tr_b16 a[48:49], v9 offset:24832\n"
"v_cvt_pk_bf16_f32 v201, v158, v159\n"
"v_exp_f32 v164, v164\n"
"v_exp_f32 v165, v165\n"
"v_exp_f32 v166, v166\n"
"v_mfma_f32_16x16x32_bf16 a[192:195], a[56:59], a[112:115], a[192:195]\n"
"ds_read_b64_tr_b16 a[50:51], v11 offset:24576\n"
"v_exp_f32 v167, v167\n"
"v_cvt_pk_bf16_f32 v202, v164, v165\n"
"v_mfma_f32_16x16x32_bf16 a[132:135], a[60:63], v[220:223], a[132:135]\n"
"s_add_u32 m0, s30, 0x1000\n"
"ds_read_b64 a[116:117], v15 offset:38912\n"
"ds_read_b64 a[118:119], v16 offset:38912\n"
"v_cvt_pk_bf16_f32 v203, v166, v167\n"
"v_mfma_f32_16x16x32_bf16 a[164:167], a[60:63], v[224:227], a[164:167]\n"
"buffer_load_dwordx4 v18, s[44:47], s96 offen offset:3072 nt lds\n"
"ds_read_b64_tr_b16 a[52:53], v10 offset:24832\n"
"v_mfma_f32_16x16x32_bf16 a[196:199], a[60:63], a[112:115], a[196:199]\n"
"s_add_u32 m0, s30, 0x3000\n"
"ds_read_b64_tr_b16 a[54:55], v12 offset:24576\n"
"v_exp_f32 v160, v160\n"
"v_mfma_f32_16x16x32_bf16 v[192:195], a[0:3], a[64:67], v[232:235]\n"
"s_add_u32 s75, s13, 0x7800\n"
"ds_read_b64_tr_b16 a[56:57], v9 offset:25088\n"
"v_exp_f32 v161, v161\n"
"v_mfma_f32_16x16x32_bf16 v[192:195], a[4:7], a[68:71], v[192:195]\n"
"s_getpc_b64 s[98:99]\n"
"buffer_load_dwordx4 v23, s[52:55], s75 offen offset:2048 lds\n"
"ds_read_b64_tr_b16 a[58:59], v11 offset:25344\n"
"v_cvt_pk_bf16_f32 v204, v160, v161\n"
"v_mfma_f32_16x16x32_bf16 v[192:195], a[8:11], a[72:75], v[192:195]\n"
"v_exp_f32 v162, v162\n"
"v_mfma_f32_16x16x32_bf16 v[192:195], a[12:15], a[76:79], v[192:195]\n"
"v_exp_f32 v163, v163\n"
"v_mfma_f32_16x16x32_bf16 v[196:199], a[0:3], a[80:83], v[236:239]\n"
"global_load_dword v250, v0, s[98:99] offset:1536\n"
"v_cvt_pk_bf16_f32 v205, v162, v163\n"
"v_mfma_f32_16x16x32_bf16 v[196:199], a[4:7], a[84:87], v[196:199]\n"
"v_exp_f32 v168, v168\n"
"v_mfma_f32_16x16x32_bf16 v[196:199], a[8:11], a[88:91], v[196:199]\n"
"v_exp_f32 v169, v169\n"
"v_mfma_f32_16x16x32_bf16 v[196:199], a[12:15], a[92:95], v[196:199]\n"
"v_exp_f32 v170, v170\n"
"v_mfma_f32_16x16x32_bf16 v[152:155], a[16:19], a[64:67], v[232:235]\n"
"s_add_u32 m0, s30, 0x2000\n"
"v_exp_f32 v171, v171\n"
"v_mfma_f32_16x16x32_bf16 v[152:155], a[20:23], a[68:71], v[152:155]\n"
"s_waitcnt lgkmcnt(3)\n"
"ds_read_b64_tr_b16 a[60:61], v10 offset:25088\n"
"v_cvt_pk_bf16_f32 v206, v168, v169\n"
"v_mfma_f32_16x16x32_bf16 v[152:155], a[24:27], a[72:75], v[152:155]\n"
"buffer_load_dwordx4 v19, s[48:51], s84 offen offset:0 nt lds\n"
"v_cvt_pk_bf16_f32 v207, v170, v171\n"
"v_mfma_f32_16x16x32_bf16 v[152:155], a[28:31], a[76:79], v[152:155]\n"
"s_add_u32 m0, s30, 0x3000\n"
"v_exp_f32 v172, v172\n"
"v_mfma_f32_16x16x32_bf16 v[156:159], a[16:19], a[80:83], v[236:239]\n"
"buffer_load_dwordx4 v24, s[52:55], s75 offen offset:3072 lds\n"
"v_add_f32 v254, v254, v172\n"
"v_mfma_f32_16x16x32_bf16 v[156:159], a[20:23], a[84:87], v[156:159]\n"
"v_exp_f32 v173, v173\n"
"v_mfma_f32_16x16x32_bf16 v[156:159], a[24:27], a[88:91], v[156:159]\n"
"v_exp_f32 v174, v174\n"
"v_mfma_f32_16x16x32_bf16 v[156:159], a[28:31], a[92:95], v[156:159]\n"
"v_exp_f32 v175, v175\n"
"v_mfma_f32_16x16x32_bf16 a[200:203], v[228:231], v[200:203], a[200:203]\n"
"ds_read_b64_tr_b16 a[62:63], v12 offset:25344\n"
"v_add_f32 v254, v254, v173\n"
"v_mfma_f32_16x16x32_bf16 a[204:207], v[228:231], v[204:207], a[204:207]\n"
"v_cvt_pk_bf16_f32 v172, v172, v173\n"
"v_cvt_pk_bf16_f32 v173, v174, v175\n"
"v_mfma_f32_16x16x32_bf16 a[136:139], a[32:35], v[200:203], a[136:139]\n"
"ds_write_b64 v13, v[172:173] offset:38920\n"
"v_mfma_f32_16x16x32_bf16 a[168:171], a[32:35], v[204:207], a[168:171]\n"
"v_add_f32 v254, v254, v174\n"
"v_add_f32 v254, v254, v175\n"
"v_mfma_f32_16x16x32_bf16 a[140:143], a[36:39], v[200:203], a[140:143]\n"
"s_memrealtime s[24:25]\n"
"v_exp_f32 v176, v176\n"
"v_mfma_f32_16x16x32_bf16 a[172:175], a[36:39], v[204:207], a[172:175]\n"
"s_add_u32 m0, s30, 0x2000\n"
"v_exp_f32 v177, v177\n"
"v_mfma_f32_16x16x32_bf16 a[144:147], a[40:43], v[200:203], a[144:147]\n"
"buffer_load_dwordx4 v20, s[48:51], s84 offen offset:1024 nt lds\n"
"v_cvt_pk_bf16_f32 v208, v176, v177\n"
"v_mfma_f32_16x16x32_bf16 a[176:179], a[40:43], v[204:207], a[176:179]\n"
"v_exp_f32 v178, v178\n"
"v_mfma_f32_16x16x32_bf16 a[148:151], a[44:47], v[200:203], a[148:151]\n"
"v_exp_f32 v179, v179\n"
"v_mfma_f32_16x16x32_bf16 a[180:183], a[44:47], v[204:207], a[180:183]\n"
"s_waitcnt lgkmcnt(0)\n"
"s_sub_u32 s25, s24, s5\n"
"s_cmp_lt_i32 s25, 0\n"
"s_cbranch_scc1 .Lmha_LIM12\n"
".Lmha_LIM12R:\n"
"v_cvt_pk_bf16_f32 v209, v178, v179\n"
"v_mfma_f32_16x16x32_bf16 a[152:155], a[48:51], v[200:203], a[152:155]\n"
"s_waitcnt vmcnt(20)\n"
"s_barrier\n"
"s_add_u32 s24, s24, s92\n"
"v_mfma_f32_16x16x32_bf16 a[184:187], a[48:51], v[204:207], a[184:187]\n"
"s_add_u32 s5, s24, 0x5f\n"
"ds_read_b128 a[0:3], v1 offset:28672\n"
"ds_read_b128 a[4:7], v1 offset:28928\n"
"v_mfma_f32_16x16x32_bf16 a[156:159], a[52:55], v[200:203], a[156:159]\n"
"ds_read_b128 a[8:11], v1 offset:29184\n"
"ds_read_b128 a[12:15], v1 offset:29440\n"
"v_mfma_f32_16x16x32_bf16 a[188:191], a[52:55], v[204:207], a[188:191]\n"
"ds_read_b128 a[16:19], v2 offset:28672\n"
"ds_read_b128 a[20:23], v2 offset:28928\n"
"v_mfma_f32_16x16x32_bf16 a[128:131], a[56:59], v[200:203], a[128:131]\n"
"ds_read_b128 a[24:27], v2 offset:29184\n"
"ds_read_b128 a[28:31], v2 offset:29440\n"
"v_mfma_f32_16x16x32_bf16 a[160:163], a[56:59], v[204:207], a[160:163]\n"
"ds_read_b64_tr_b16 a[32:33], v5 offset:16640\n"
"v_lshlrev_b32 v143, 10, v143\n"
"v_mfma_f32_16x16x32_bf16 a[192:195], a[56:59], a[116:119], a[192:195]\n"
"ds_read_b64_tr_b16 a[34:35], v7 offset:16384\n"
"v_sub_u32 v142, v5, v143\n"
"v_mfma_f32_16x16x32_bf16 a[132:135], a[60:63], v[200:203], a[132:135]\n"
"ds_read_b64_tr_b16 a[36:37], v6 offset:16640\n"
"v_bfe_u32 v143, v22, 9, 1\n"
"v_mfma_f32_16x16x32_bf16 a[164:167], a[60:63], v[204:207], a[164:167]\n"
"buffer_load_dwordx4 v19, s[48:51], s84 offen offset:2048 nt lds\n"
"ds_read_b64_tr_b16 a[38:39], v8 offset:16384\n"
"v_mfma_f32_16x16x32_bf16 a[196:199], a[60:63], a[116:119], a[196:199]\n"
"s_waitcnt lgkmcnt(11)\n"
"ds_read_b64_tr_b16 a[40:41], v5 offset:16896\n"
"v_lshl_add_u32 v142, v143, 10, v142\n"
"v_mfma_f32_16x16x32_bf16 v[160:163], a[0:3], a[96:99], v[240:243]\n"
"s_waitcnt lgkmcnt(11)\n"
"ds_read_b64_tr_b16 a[42:43], v7 offset:17152\n"
"v_bfe_u32 v143, v22, 8, 1\n"
"v_mfma_f32_16x16x32_bf16 v[160:163], a[4:7], a[100:103], v[160:163]\n"
"s_waitcnt lgkmcnt(11)\n"
"v_exp_f32 v184, v184\n"
"v_mfma_f32_16x16x32_bf16 v[160:163], a[8:11], a[104:107], v[160:163]\n"
"s_waitcnt lgkmcnt(10)\n"
"v_exp_f32 v185, v185\n"
"v_mfma_f32_16x16x32_bf16 v[160:163], a[12:15], a[108:111], v[160:163]\n"
"v_exp_f32 v186, v186\n"
"v_mfma_f32_16x16x32_bf16 v[164:167], a[0:3], a[64:67], v[232:235]\n"
"v_exp_f32 v187, v187\n"
"v_mfma_f32_16x16x32_bf16 v[164:167], a[4:7], a[68:71], v[164:167]\n"
"ds_read_b64_tr_b16 a[44:45], v6 offset:16896\n"
"v_cvt_pk_bf16_f32 v210, v184, v185\n"
"v_mfma_f32_16x16x32_bf16 v[164:167], a[8:11], a[72:75], v[164:167]\n"
"ds_read_b64_tr_b16 a[46:47], v8 offset:17152\n"
"v_cvt_pk_bf16_f32 v211, v186, v187\n"
"v_mfma_f32_16x16x32_bf16 v[164:167], a[12:15], a[76:79], v[164:167]\n"
"v_exp_f32 v180, v180\n"
"v_mfma_f32_16x16x32_bf16 v[168:171], a[0:3], a[80:83], v[236:239]\n"
"v_exp_f32 v181, v181\n"
"v_mfma_f32_16x16x32_bf16 v[168:171], a[4:7], a[84:87], v[168:171]\n"
"buffer_load_dwordx4 v20, s[48:51], s84 offen offset:3072 nt lds\n"
"v_cvt_pk_bf16_f32 v212, v180, v181\n"
"v_mfma_f32_16x16x32_bf16 v[168:171], a[8:11], a[88:91], v[168:171]\n"
"v_exp_f32 v182, v182\n"
"v_mfma_f32_16x16x32_bf16 v[168:171], a[12:15], a[92:95], v[168:171]\n"
"s_waitcnt lgkmcnt(2)\n"
"v_exp_f32 v183, v183\n"
"v_mfma_f32_16x16x32_bf16 v[172:175], a[16:19], a[64:67], v[232:235]\n"
"v_exp_f32 v188, v188\n"
"v_mfma_f32_16x16x32_bf16 v[172:175], a[20:23], a[68:71], v[172:175]\n"
"v_exp_f32 v189, v189\n"
"v_mfma_f32_16x16x32_bf16 v[172:175], a[24:27], a[72:75], v[172:175]\n"
"v_exp_f32 v190, v190\n"
"v_mfma_f32_16x16x32_bf16 v[172:175], a[28:31], a[76:79], v[172:175]\n"
"v_exp_f32 v191, v191\n"
"v_mfma_f32_16x16x32_bf16 v[176:179], a[16:19], a[80:83], v[236:239]\n"
"ds_read_b64_tr_b16 a[48:49], v5 offset:17152\n"
"v_cvt_pk_bf16_f32 v213, v182, v183\n"
"v_mfma_f32_16x16x32_bf16 v[176:179], a[20:23], a[84:87], v[176:179]\n"
"ds_read_b64_tr_b16 a[50:51], v7 offset:16896\n"
"v_cvt_pk_bf16_f32 v214, v188, v189\n"
"v_mfma_f32_16x16x32_bf16 v[176:179], a[24:27], a[88:91], v[176:179]\n"
"ds_read_b64_tr_b16 a[52:53], v6 offset:17152\n"
"v_cvt_pk_bf16_f32 v215, v190, v191\n"
"v_mfma_f32_16x16x32_bf16 v[176:179], a[28:31], a[92:95], v[176:179]\n"
"s_add_u32 m0, s30, 0x5000\n"
"ds_read_b64_tr_b16 a[54:55], v8 offset:16896\n"
"ds_read_b64_tr_b16 a[56:57], v5 offset:16384\n"
"v_mfma_f32_16x16x32_bf16 a[200:203], v[228:231], v[208:211], a[200:203]\n"
"s_mov_b32 s77, 0x3c000\n"
"ds_read_b64_tr_b16 a[58:59], v7 offset:16640\n"
"ds_read_b64 a[112:113], v13 offset:38920\n"
"v_mfma_f32_16x16x32_bf16 a[204:207], v[228:231], v[212:215], a[204:207]\n"
"buffer_load_dwordx4 v17, s[44:47], s77 offen offset:0 nt lds\n"
"ds_read_b64 a[114:115], v14 offset:38920\n"
"v_mfma_f32_16x16x32_bf16 a[136:139], a[32:35], v[208:211], a[136:139]\n"
"ds_read_b64_tr_b16 a[60:61], v6 offset:16384\n"
"ds_read_b64_tr_b16 a[62:63], v8 offset:16640\n"
"v_mfma_f32_16x16x32_bf16 a[168:171], a[32:35], v[212:215], a[168:171]\n"
"ds_read_b128 a[0:3], v3 offset:28672\n"
"ds_read_b128 a[4:7], v3 offset:28928\n"
"v_mfma_f32_16x16x32_bf16 a[140:143], a[36:39], v[208:211], a[140:143]\n"
"ds_read_b128 a[8:11], v3 offset:29184\n"
"ds_read_b128 a[12:15], v3 offset:29440\n"
"v_mfma_f32_16x16x32_bf16 a[172:175], a[36:39], v[212:215], a[172:175]\n"
"ds_read_b128 a[16:19], v4 offset:28672\n"
"v_lshl_add_u32 v142, v143, 8, v142\n"
"v_mfma_f32_16x16x32_bf16 a[144:147], a[40:43], v[208:211], a[144:147]\n"
"ds_read_b128 a[20:23], v4 offset:28928\n"
"v_xor_b32 v143, 32, v142\n"
"v_mfma_f32_16x16x32_bf16 a[176:179], a[40:43], v[212:215], a[176:179]\n"
"s_waitcnt lgkmcnt(15)\n"
"ds_read_b128 a[24:27], v4 offset:29184\n"
"v_xor_b32 v140, 0x80, v23\n"
"v_mfma_f32_16x16x32_bf16 a[148:151], a[44:47], v[208:211], a[148:151]\n"
"ds_read_b128 a[28:31], v4 offset:29440\n"
"v_xor_b32 v141, 0x80, v24\n"
"v_mfma_f32_16x16x32_bf16 a[180:183], a[44:47], v[212:215], a[180:183]\n"
"s_waitcnt lgkmcnt(15)\n"
"v_exp_f32 v192, v192\n"
"v_mfma_f32_16x16x32_bf16 a[152:155], a[48:51], v[208:211], a[152:155]\n"
"v_exp_f32 v193, v193\n"
"v_mfma_f32_16x16x32_bf16 a[184:187], a[48:51], v[212:215], a[184:187]\n"
"s_waitcnt lgkmcnt(11)\n"
"ds_read_b64_tr_b16 a[32:33], v9 offset:17152\n"
"v_cvt_pk_bf16_f32 v216, v192, v193\n"
"v_mfma_f32_16x16x32_bf16 a[156:159], a[52:55], v[208:211], a[156:159]\n"
"v_exp_f32 v194, v194\n"
"v_mfma_f32_16x16x32_bf16 a[188:191], a[52:55], v[212:215], a[188:191]\n"
"buffer_load_dwordx4 v17, s[44:47], s77 offen offset:1024 nt lds\n"
"ds_read_b64_tr_b16 a[34:35], v11 offset:16896\n"
"v_mfma_f32_16x16x32_bf16 a[128:131], a[56:59], v[208:211], a[128:131]\n"
"ds_read_b64_tr_b16 a[36:37], v10 offset:17152\n"
"ds_read_b64_tr_b16 a[38:39], v12 offset:16896\n"
"v_exp_f32 v195, v195\n"
"v_mfma_f32_16x16x32_bf16 a[160:163], a[56:59], v[212:215], a[160:163]\n"
"s_waitcnt lgkmcnt(8)\n"
"ds_read_b64_tr_b16 a[40:41], v9 offset:16384\n"
"v_cvt_pk_bf16_f32 v217, v194, v195\n"
"v_exp_f32 v152, v152\n"
"v_exp_f32 v153, v153\n"
"v_exp_f32 v154, v154\n"
"v_mfma_f32_16x16x32_bf16 a[192:195], a[56:59], a[112:115], a[192:195]\n"
"ds_read_b64_tr_b16 a[42:43], v11 offset:16640\n"
"v_cvt_pk_bf16_f32 v218, v152, v153\n"
"v_exp_f32 v155, v155\n"
"v_mfma_f32_16x16x32_bf16 a[132:135], a[60:63], v[208:211], a[132:135]\n"
"ds_read_b64_tr_b16 a[44:45], v10 offset:16384\n"
"ds_read_b64_tr_b16 a[46:47], v12 offset:16640\n"
"v_cvt_pk_bf16_f32 v219, v154, v155\n"
"v_mfma_f32_16x16x32_bf16 a[164:167], a[60:63], v[212:215], a[164:167]\n"
"ds_read_b64_tr_b16 a[48:49], v9 offset:16640\n"
"v_exp_f32 v196, v196\n"
"v_mfma_f32_16x16x32_bf16 a[196:199], a[60:63], a[112:115], a[196:199]\n"
"ds_read_b64_tr_b16 a[50:51], v11 offset:16384\n"
"v_exp_f32 v197, v197\n"
"v_mfma_f32_16x16x32_bf16 v[180:183], a[0:3], a[64:67], v[232:235]\n"
"ds_read_b64 a[116:117], v15 offset:38920\n"
"v_exp_f32 v198, v198\n"
"v_mfma_f32_16x16x32_bf16 v[180:183], a[4:7], a[68:71], v[180:183]\n"
"v_exp_f32 v199, v199\n"
"v_mfma_f32_16x16x32_bf16 v[180:183], a[8:11], a[72:75], v[180:183]\n"
"v_exp_f32 v156, v156\n"
"v_mfma_f32_16x16x32_bf16 v[180:183], a[12:15], a[76:79], v[180:183]\n"
"v_exp_f32 v157, v157\n"
"v_mfma_f32_16x16x32_bf16 v[184:187], a[0:3], a[80:83], v[236:239]\n"
"v_exp_f32 v158, v158\n"
"v_mfma_f32_16x16x32_bf16 v[184:187], a[4:7], a[84:87], v[184:187]\n"
"v_exp_f32 v159, v159\n"
"v_mfma_f32_16x16x32_bf16 v[184:187], a[8:11], a[88:91], v[184:187]\n"
"ds_read_b64 a[118:119], v16 offset:38920\n"
"v_cvt_pk_bf16_f32 v220, v196, v197\n"
"v_mfma_f32_16x16x32_bf16 v[184:187], a[12:15], a[92:95], v[184:187]\n"
"s_waitcnt lgkmcnt(4)\n"
"ds_read_b64_tr_b16 a[52:53], v10 offset:16640\n"
"v_cvt_pk_bf16_f32 v221, v198, v199\n"
"v_mfma_f32_16x16x32_bf16 v[188:191], a[16:19], a[64:67], v[232:235]\n"
"s_lshl_b32 s38, s11, 10\n"
"ds_read_b64_tr_b16 a[54:55], v12 offset:16384\n"
"v_cvt_pk_bf16_f32 v222, v156, v157\n"
"v_mfma_f32_16x16x32_bf16 v[188:191], a[20:23], a[68:71], v[188:191]\n"
"ds_read_b64_tr_b16 a[56:57], v9 offset:16896\n"
"v_cvt_pk_bf16_f32 v223, v158, v159\n"
"v_mfma_f32_16x16x32_bf16 v[188:191], a[24:27], a[72:75], v[188:191]\n"
"v_exp_f32 v160, v160\n"
"v_mfma_f32_16x16x32_bf16 v[188:191], a[28:31], a[76:79], v[188:191]\n"
"v_exp_f32 v161, v161\n"
"v_mfma_f32_16x16x32_bf16 v[192:195], a[16:19], a[80:83], v[236:239]\n"
"ds_read_b64_tr_b16 a[58:59], v11 offset:17152\n"
"v_add_f32 v254, v254, v160\n"
"v_mfma_f32_16x16x32_bf16 v[192:195], a[20:23], a[84:87], v[192:195]\n"
"v_exp_f32 v162, v162\n"
"v_mfma_f32_16x16x32_bf16 v[192:195], a[24:27], a[88:91], v[192:195]\n"
"v_exp_f32 v163, v163\n"
"v_mfma_f32_16x16x32_bf16 v[192:195], a[28:31], a[92:95], v[192:195]\n"
"ds_read_b64_tr_b16 a[60:61], v10 offset:16896\n"
"v_add_f32 v254, v254, v161\n"
"v_mfma_f32_16x16x32_bf16 a[200:203], v[228:231], v[216:219], a[200:203]\n"
"buffer_load_dwordx4 v18, s[44:47], s77 offen offset:2048 nt lds\n"
"v_cvt_pk_bf16_f32 v160, v160, v161\n"
"v_mfma_f32_16x16x32_bf16 a[204:207], v[228:231], v[220:223], a[204:207]\n"
"ds_read_b64_tr_b16 a[62:63], v12 offset:17152\n"
"v_cvt_pk_bf16_f32 v161, v162, v163\n"
"v_mfma_f32_16x16x32_bf16 a[136:139], a[32:35], v[216:219], a[136:139]\n"
"ds_write_b64 v13, v[160:161] offset:38912\n"
"v_mfma_f32_16x16x32_bf16 a[168:171], a[32:35], v[220:223], a[168:171]\n"
"v_add_f32 v254, v254, v162\n"
"v_add_f32 v254, v254, v163\n"
"v_mfma_f32_16x16x32_bf16 a[140:143], a[36:39], v[216:219], a[140:143]\n"
"s_waitcnt vmcnt(11)\n"
"s_memrealtime s[24:25]\n"
"ds_read_b64_tr_b16 v[32:33], v142 offset:12288\n"
"v_add_u32 v244, s38, v13\n"
"v_mfma_f32_16x16x32_bf16 a[172:175], a[36:39], v[220:223], a[172:175]\n"
"ds_read_b64_tr_b16 v[36:37], v143 offset:12288\n"
"v_add_u32 v245, s38, v14\n"
"v_mfma_f32_16x16x32_bf16 a[144:147], a[40:43], v[216:219], a[144:147]\n"
"ds_read_b64_tr_b16 v[40:41], v142 offset:12800\n"
"v_add_u32 v246, s38, v15\n"
"v_mfma_f32_16x16x32_bf16 a[176:179], a[40:43], v[220:223], a[176:179]\n"
"ds_read_b64_tr_b16 v[44:45], v143 offset:12800\n"
"v_add_u32 v247, s38, v16\n"
"v_mfma_f32_16x16x32_bf16 a[148:151], a[44:47], v[216:219], a[148:151]\n"
"v_exp_f32 v164, v164\n"
"v_mfma_f32_16x16x32_bf16 a[180:183], a[44:47], v[220:223], a[180:183]\n"
"s_waitcnt lgkmcnt(0)\n"
"s_sub_u32 s25, s24, s5\n"
"s_cmp_lt_i32 s25, 0\n"
"s_cbranch_scc1 .Lmha_LIM13\n"
".Lmha_LIM13R:\n"
"v_mfma_f32_16x16x32_bf16 a[152:155], a[48:51], v[216:219], a[152:155]\n"
"s_barrier\n"
"v_exp_f32 v165, v165\n"
"v_mfma_f32_16x16x32_bf16 a[184:187], a[48:51], v[220:223], a[184:187]\n"
"s_add_u32 s24, s24, s92\n"
"ds_read_b128 a[0:3], v1 offset:32768\n"
"ds_read_b128 a[4:7], v1 offset:33024\n"
"v_mfma_f32_16x16x32_bf16 a[156:159], a[52:55], v[216:219], a[156:159]\n"
"s_add_u32 s5, s24, 0x62\n"
"ds_read_b128 a[8:11], v1 offset:33280\n"
"buffer_load_dwordx4 v18, s[44:47], s77 offen offset:3072 nt lds\n"
"v_mfma_f32_16x16x32_bf16 a[188:191], a[52:55], v[220:223], a[188:191]\n"
"ds_read_b128 a[12:15], v1 offset:33536\n"
"ds_read_b128 a[16:19], v2 offset:32768\n"
"v_mfma_f32_16x16x32_bf16 a[128:131], a[56:59], v[216:219], a[128:131]\n"
"ds_write_b64 v244, v[80:81] offset:28672\n"
"v_mfma_f32_16x16x32_bf16 a[160:163], a[56:59], v[220:223], a[160:163]\n"
"v_exp_f32 v166, v166\n"
"v_exp_f32 v167, v167\n"
"v_cvt_pk_bf16_f32 v224, v164, v165\n"
"v_exp_f32 v172, v172\n"
"v_exp_f32 v173, v173\n"
"v_mfma_f32_16x16x32_bf16 a[192:195], a[56:59], a[116:119], a[192:195]\n"
"ds_read_b128 a[20:23], v2 offset:33024\n"
"ds_read_b128 a[24:27], v2 offset:33280\n"
"v_cvt_pk_bf16_f32 v225, v166, v167\n"
"v_exp_f32 v174, v174\n"
"v_exp_f32 v175, v175\n"
"v_mfma_f32_16x16x32_bf16 a[132:135], a[60:63], v[216:219], a[132:135]\n"
"buffer_store_dwordx4 v[136:139], v21, s[56:59], s82 offen offset:1024 sc1\n"
"v_mfma_f32_16x16x32_bf16 a[164:167], a[60:63], v[220:223], a[164:167]\n"
"ds_write_b64 v244, v[82:83] offset:28680\n"
"v_mfma_f32_16x16x32_bf16 a[196:199], a[60:63], a[116:119], a[196:199]\n"
"s_waitcnt lgkmcnt(6)\n"
"v_cvt_pk_bf16_f32 v226, v172, v173\n"
"v_cvt_pk_bf16_f32 v227, v174, v175\n"
"v_exp_f32 v168, v168\n"
"v_mfma_f32_16x16x32_bf16 v[196:199], a[0:3], a[96:99], v[240:243]\n"
"s_waitcnt vmcnt(8)\n"
"ds_read_b128 a[28:31], v2 offset:33536\n"
"ds_read_b64_tr_b16 v[34:35], v142 offset:14336\n"
"v_exp_f32 v169, v169\n"
"v_mfma_f32_16x16x32_bf16 v[196:199], a[4:7], a[100:103], v[196:199]\n"
"ds_write_b64 v244, v[112:113] offset:24576\n"
"v_mfma_f32_16x16x32_bf16 v[196:199], a[8:11], a[104:107], v[196:199]\n"
"s_waitcnt lgkmcnt(6)\n"
"v_exp_f32 v170, v170\n"
"v_cvt_pk_bf16_f32 v200, v168, v169\n"
"v_mfma_f32_16x16x32_bf16 v[196:199], a[12:15], a[108:111], v[196:199]\n"
"s_add_u32 m0, s30, 0x4000\n"
"ds_read_b64_tr_b16 v[38:39], v143 offset:14336\n"
"ds_read_b64_tr_b16 v[42:43], v142 offset:14848\n"
"v_mfma_f32_16x16x32_bf16 v[152:155], a[0:3], a[64:67], v[232:235]\n"
"buffer_load_dwordx4 v19, s[48:51], s96 offen offset:0 nt lds\n"
"ds_read_b64_tr_b16 a[32:33], v5 offset:256\n"
"v_mfma_f32_16x16x32_bf16 v[152:155], a[4:7], a[68:71], v[152:155]\n"
"buffer_store_dwordx4 v[80:83], v21, s[56:59], s79 offen offset:0 sc1\n"
"v_mfma_f32_16x16x32_bf16 v[152:155], a[8:11], a[72:75], v[152:155]\n"
"ds_write_b64 v244, v[114:115] offset:24584\n"
"v_mfma_f32_16x16x32_bf16 v[152:155], a[12:15], a[76:79], v[152:155]\n"
"v_exp_f32 v171, v171\n"
"v_mfma_f32_16x16x32_bf16 v[156:159], a[0:3], a[80:83], v[236:239]\n"
"ds_write_b64 v245, v[88:89] offset:28672\n"
"v_mfma_f32_16x16x32_bf16 v[156:159], a[4:7], a[84:87], v[156:159]\n"
"v_exp_f32 v176, v176\n"
"v_cvt_pk_bf16_f32 v201, v170, v171\n"
"v_mfma_f32_16x16x32_bf16 v[156:159], a[8:11], a[88:91], v[156:159]\n"
"ds_read_b64_tr_b16 a[34:35], v7 offset:0\n"
"ds_read_b64_tr_b16 v[46:47], v143 offset:14848\n"
"v_mfma_f32_16x16x32_bf16 v[156:159], a[12:15], a[92:95], v[156:159]\n"
"ds_read_b64_tr_b16 a[36:37], v6 offset:256\n"
"ds_read_b64_tr_b16 a[38:39], v8 offset:0\n"
"v_mfma_f32_16x16x32_bf16 v[160:163], a[16:19], a[64:67], v[232:235]\n"
"s_waitcnt lgkmcnt(8)\n"
"ds_write_b64 v245, v[90:91] offset:28680\n"
"v_mfma_f32_16x16x32_bf16 v[160:163], a[20:23], a[68:71], v[160:163]\n"
"buffer_store_dwordx4 v[88:91], v21, s[56:59], s80 offen offset:0 sc1\n"
"v_mfma_f32_16x16x32_bf16 v[160:163], a[24:27], a[72:75], v[160:163]\n"
"ds_read_b64_tr_b16 a[40:41], v5 offset:512\n"
"ds_read_b64_tr_b16 a[42:43], v7 offset:768\n"
"v_mfma_f32_16x16x32_bf16 v[160:163], a[28:31], a[76:79], v[160:163]\n"
"ds_write_b64 v245, v[120:121] offset:24576\n"
"v_mfma_f32_16x16x32_bf16 v[164:167], a[16:19], a[80:83], v[236:239]\n"
"v_exp_f32 v177, v177\n"
"v_mfma_f32_16x16x32_bf16 v[164:167], a[20:23], a[84:87], v[164:167]\n"
"ds_read_b64_tr_b16 a[44:45], v6 offset:512\n"
"v_exp_f32 v178, v178\n"
"v_mfma_f32_16x16x32_bf16 v[164:167], a[24:27], a[88:91], v[164:167]\n"
"ds_read_b64_tr_b16 a[46:47], v8 offset:768\n"
"v_exp_f32 v179, v179\n"
"v_mfma_f32_16x16x32_bf16 v[164:167], a[28:31], a[92:95], v[164:167]\n"
"ds_read_b64_tr_b16 a[48:49], v5 offset:768\n"
"v_cvt_pk_bf16_f32 v202, v176, v177\n"
"v_cvt_pk_bf16_f32 v203, v178, v179\n"
"v_mfma_f32_16x16x32_bf16 a[200:203], v[228:231], v[224:227], a[200:203]\n"
"ds_read_b64_tr_b16 a[50:51], v7 offset:512\n"
"ds_read_b64_tr_b16 a[52:53], v6 offset:768\n"
"v_mfma_f32_16x16x32_bf16 a[204:207], v[228:231], v[200:203], a[204:207]\n"
"s_waitcnt lgkmcnt(8)\n"
"ds_write_b64 v245, v[122:123] offset:24584\n"
"v_mfma_f32_16x16x32_bf16 a[136:139], a[32:35], v[224:227], a[136:139]\n"
"buffer_store_dwordx4 v[96:99], v21, s[56:59], s81 offen offset:0 sc1\n"
"v_mfma_f32_16x16x32_bf16 a[168:171], a[32:35], v[200:203], a[168:171]\n"
"ds_read_b64_tr_b16 a[54:55], v8 offset:512\n"
"ds_read_b64_tr_b16 a[56:57], v5 offset:0\n"
"v_mfma_f32_16x16x32_bf16 a[140:143], a[36:39], v[224:227], a[140:143]\n"
"ds_write_b64 v246, v[96:97] offset:28672\n"
"v_mfma_f32_16x16x32_bf16 a[172:175], a[36:39], v[200:203], a[172:175]\n"
"s_waitcnt lgkmcnt(9)\n"
"ds_read_b64_tr_b16 a[58:59], v7 offset:256\n"
"v_mfma_f32_16x16x32_bf16 a[144:147], a[40:43], v[224:227], a[144:147]\n"
"ds_read_b64 a[112:113], v13 offset:38912\n"
"ds_read_b64 a[114:115], v14 offset:38912\n"
"v_mfma_f32_16x16x32_bf16 a[176:179], a[40:43], v[200:203], a[176:179]\n"
"s_waitcnt lgkmcnt(10)\n"
"ds_read_b64_tr_b16 a[60:61], v6 offset:0\n"
"buffer_load_dwordx4 v20, s[48:51], s96 offen offset:1024 nt lds\n"
"v_mfma_f32_16x16x32_bf16 a[148:151], a[44:47], v[224:227], a[148:151]\n"
"ds_read_b64_tr_b16 a[62:63], v8 offset:256\n"
"ds_read_b128 a[0:3], v3 offset:32768\n"
"v_mfma_f32_16x16x32_bf16 a[180:183], a[44:47], v[200:203], a[180:183]\n"
"s_waitcnt lgkmcnt(11)\n"
"ds_write_b64 v246, v[98:99] offset:28680\n"
"v_mfma_f32_16x16x32_bf16 a[152:155], a[48:51], v[224:227], a[152:155]\n"
"buffer_store_dwordx4 v[104:107], v21, s[56:59], s82 offen offset:0 sc1\n"
"v_mfma_f32_16x16x32_bf16 a[184:187], a[48:51], v[200:203], a[184:187]\n"
"s_waitcnt lgkmcnt(9)\n"
"ds_write_b64 v246, v[128:129] offset:24576\n"
"v_mfma_f32_16x16x32_bf16 a[156:159], a[52:55], v[224:227], a[156:159]\n"
"ds_read_b128 a[4:7], v3 offset:33024\n"
"ds_read_b128 a[8:11], v3 offset:33280\n"
"v_exp_f32 v180, v180\n"
"v_exp_f32 v181, v181\n"
"v_exp_f32 v182, v182\n"
"v_mfma_f32_16x16x32_bf16 a[188:191], a[52:55], v[200:203], a[188:191]\n"
"s_waitcnt lgkmcnt(9)\n"
"ds_read_b128 a[12:15], v3 offset:33536\n"
"ds_read_b128 a[16:19], v4 offset:32768\n"
"v_exp_f32 v183, v183\n"
"v_cvt_pk_bf16_f32 v204, v180, v181\n"
"v_exp_f32 v188, v188\n"
"v_mfma_f32_16x16x32_bf16 a[128:131], a[56:59], v[224:227], a[128:131]\n"
"ds_write_b64 v246, v[130:131] offset:24584\n"
"v_mfma_f32_16x16x32_bf16 a[160:163], a[56:59], v[200:203], a[160:163]\n"
"s_waitcnt lgkmcnt(7)\n"
"v_exp_f32 v189, v189\n"
"v_cvt_pk_bf16_f32 v205, v182, v183\n"
"v_exp_f32 v190, v190\n"
"v_exp_f32 v191, v191\n"
"v_exp_f32 v184, v184\n"
"v_mfma_f32_16x16x32_bf16 a[192:195], a[56:59], a[112:115], a[192:195]\n"
"ds_write_b64 v247, v[104:105] offset:28672\n"
"v_mfma_f32_16x16x32_bf16 a[132:135], a[60:63], v[224:227], a[132:135]\n"
"buffer_store_dwordx4 v[112:115], v21, s[56:59], s79 offen offset:1024 sc1\n"
"v_mfma_f32_16x16x32_bf16 a[164:167], a[60:63], v[200:203], a[164:167]\n"
"ds_read_b128 a[20:23], v4 offset:33024\n"
"ds_read_b128 a[24:27], v4 offset:33280\n"
"v_cvt_pk_bf16_f32 v206, v188, v189\n"
"v_cvt_pk_bf16_f32 v207, v190, v191\n"
"v_exp_f32 v185, v185\n"
"v_mfma_f32_16x16x32_bf16 a[196:199], a[60:63], a[112:115], a[196:199]\n"
"buffer_load_dwordx4 v19, s[48:51], s96 offen offset:2048 nt lds\n"
"ds_read_b128 a[28:31], v4 offset:33536\n"
"v_exp_f32 v186, v186\n"
"v_exp_f32 v187, v187\n"
"v_exp_f32 v192, v192\n"
"v_mfma_f32_16x16x32_bf16 v[168:171], a[0:3], a[64:67], v[232:235]\n"
"s_waitcnt lgkmcnt(4)\n"
"ds_write_b64 v247, v[106:107] offset:28680\n"
"v_mfma_f32_16x16x32_bf16 v[168:171], a[4:7], a[68:71], v[168:171]\n"
"v_exp_f32 v193, v193\n"
"v_exp_f32 v194, v194\n"
"v_exp_f32 v195, v195\n"
"v_cvt_pk_bf16_f32 v208, v184, v185\n"
"v_mfma_f32_16x16x32_bf16 v[168:171], a[8:11], a[72:75], v[168:171]\n"
"ds_read_b64_tr_b16 a[32:33], v9 offset:768\n"
"ds_read_b64_tr_b16 a[34:35], v11 offset:512\n"
"v_cvt_pk_bf16_f32 v209, v186, v187\n"
"v_cvt_pk_bf16_f32 v210, v192, v193\n"
"v_mfma_f32_16x16x32_bf16 v[168:171], a[12:15], a[76:79], v[168:171]\n"
"ds_write_b64 v247, v[136:137] offset:24576\n"
"v_mfma_f32_16x16x32_bf16 v[172:175], a[0:3], a[80:83], v[236:239]\n"
"v_exp_f32 v196, v196\n"
"v_cvt_pk_bf16_f32 v211, v194, v195\n"
"v_mfma_f32_16x16x32_bf16 v[172:175], a[4:7], a[84:87], v[172:175]\n"
"buffer_store_dwordx4 v[120:123], v21, s[56:59], s80 offen offset:1024 sc1\n"
"v_mfma_f32_16x16x32_bf16 v[172:175], a[8:11], a[88:91], v[172:175]\n"
"ds_read_b64_tr_b16 a[36:37], v10 offset:768\n"
"ds_read_b64_tr_b16 a[38:39], v12 offset:512\n"
"v_exp_f32 v197, v197\n"
"v_mfma_f32_16x16x32_bf16 v[172:175], a[12:15], a[92:95], v[172:175]\n"
"ds_write_b64 v247, v[138:139] offset:24584\n"
"v_mfma_f32_16x16x32_bf16 v[176:179], a[16:19], a[64:67], v[232:235]\n"
"s_waitcnt lgkmcnt(6)\n"
"v_add_f32 v254, v254, v196\n"
"v_add_f32 v254, v254, v197\n"
"v_mfma_f32_16x16x32_bf16 v[176:179], a[20:23], a[68:71], v[176:179]\n"
"ds_read_b64_tr_b16 a[40:41], v9 offset:0\n"
"ds_read_b64_tr_b16 a[42:43], v11 offset:256\n"
"v_cvt_pk_bf16_f32 v196, v196, v197\n"
"v_mfma_f32_16x16x32_bf16 v[176:179], a[24:27], a[72:75], v[176:179]\n"
"ds_read_b64_tr_b16 a[44:45], v10 offset:0\n"
"ds_read_b64_tr_b16 a[46:47], v12 offset:256\n"
"v_mfma_f32_16x16x32_bf16 v[176:179], a[28:31], a[76:79], v[176:179]\n"
"ds_read_b64_tr_b16 a[48:49], v9 offset:256\n"
"v_exp_f32 v198, v198\n"
"v_mfma_f32_16x16x32_bf16 v[180:183], a[16:19], a[80:83], v[236:239]\n"
"ds_read_b64_tr_b16 a[50:51], v11 offset:0\n"
"v_exp_f32 v199, v199\n"
"v_mfma_f32_16x16x32_bf16 v[180:183], a[20:23], a[84:87], v[180:183]\n"
"buffer_store_dwordx4 v[128:131], v21, s[56:59], s81 offen offset:1024 sc1\n"
"v_mfma_f32_16x16x32_bf16 v[180:183], a[24:27], a[88:91], v[180:183]\n"
"ds_read_b64 a[116:117], v15 offset:38912\n"
"ds_read_b64 a[118:119], v16 offset:38912\n"
"v_cvt_pk_bf16_f32 v197, v198, v199\n"
"v_mfma_f32_16x16x32_bf16 v[180:183], a[28:31], a[92:95], v[180:183]\n"
"ds_write_b64 v13, v[196:197] offset:38920\n"
"v_mfma_f32_16x16x32_bf16 a[200:203], v[228:231], v[204:207], a[200:203]\n"
"v_add_f32 v254, v254, v198\n"
"v_add_f32 v254, v254, v199\n"
"v_mfma_f32_16x16x32_bf16 a[204:207], v[228:231], v[208:211], a[204:207]\n"
"s_waitcnt lgkmcnt(9)\n"
"ds_read_b64_tr_b16 a[52:53], v10 offset:256\n"
"ds_read_b64_tr_b16 a[54:55], v12 offset:0\n"
"v_mfma_f32_16x16x32_bf16 a[136:139], a[32:35], v[204:207], a[136:139]\n"
"ds_read_b64_tr_b16 a[56:57], v9 offset:512\n"
"ds_read_b64_tr_b16 a[58:59], v11 offset:768\n"
"v_mfma_f32_16x16x32_bf16 a[168:171], a[32:35], v[208:211], a[168:171]\n"
"ds_read_b64_tr_b16 a[60:61], v10 offset:512\n"
"ds_read_b64_tr_b16 a[62:63], v12 offset:768\n"
"v_mfma_f32_16x16x32_bf16 a[140:143], a[36:39], v[204:207], a[140:143]\n"
"s_memrealtime s[24:25]\n"
"v_exp_f32 v152, v152\n"
"v_mfma_f32_16x16x32_bf16 a[172:175], a[36:39], v[208:211], a[172:175]\n"
"s_waitcnt lgkmcnt(0)\n"
"buffer_store_dwordx4 v[144:147], v21, s[56:59], s83 offen offset:0 sc1\n"
"v_mfma_f32_16x16x32_bf16 a[144:147], a[40:43], v[204:207], a[144:147]\n"
"v_exp_f32 v153, v153\n"
"buffer_load_dwordx4 v20, s[48:51], s96 offen offset:3072 nt lds\n"
"v_mfma_f32_16x16x32_bf16 a[176:179], a[40:43], v[208:211], a[176:179]\n"
"v_exp_f32 v154, v154\n"
"v_mfma_f32_16x16x32_bf16 a[148:151], a[44:47], v[204:207], a[148:151]\n"
"s_sub_u32 s25, s24, s5\n"
"s_cmp_lt_i32 s25, 0\n"
"s_cbranch_scc1 .Lmha_LIM14\n"
".Lmha_LIM14R:\n"
"v_exp_f32 v155, v155\n"
"v_mfma_f32_16x16x32_bf16 a[180:183], a[44:47], v[208:211], a[180:183]\n"
"s_add_u32 s24, s24, s92\n"
"v_cvt_pk_bf16_f32 v212, v152, v153\n"
"v_cvt_pk_bf16_f32 v213, v154, v155\n"
"v_mfma_f32_16x16x32_bf16 a[152:155], a[48:51], v[204:207], a[152:155]\n"
"s_waitcnt vmcnt(17)\n"
"s_barrier\n"
"s_add_u32 s5, s24, 0x5c\n"
"v_mfma_f32_16x16x32_bf16 a[184:187], a[48:51], v[208:211], a[184:187]\n"
"ds_read_b128 a[0:3], v1 offset:4096\n"
"ds_read_b128 a[4:7], v1 offset:4352\n"
"v_mfma_f32_16x16x32_bf16 a[156:159], a[52:55], v[204:207], a[156:159]\n"
"ds_read_b128 a[8:11], v1 offset:4608\n"
"ds_read_b128 a[12:15], v1 offset:4864\n"
"v_mfma_f32_16x16x32_bf16 a[188:191], a[52:55], v[208:211], a[188:191]\n"
"ds_read_b128 a[16:19], v2 offset:4096\n"
"ds_read_b128 a[20:23], v2 offset:4352\n"
"v_mfma_f32_16x16x32_bf16 a[128:131], a[56:59], v[204:207], a[128:131]\n"
"ds_read_b128 a[24:27], v2 offset:4608\n"
"ds_read_b128 a[28:31], v2 offset:4864\n"
"v_mfma_f32_16x16x32_bf16 a[160:163], a[56:59], v[208:211], a[160:163]\n"
"v_exp_f32 v160, v160\n"
"v_mfma_f32_16x16x32_bf16 a[192:195], a[56:59], a[116:119], a[192:195]\n"
"v_exp_f32 v161, v161\n"
"v_mfma_f32_16x16x32_bf16 a[132:135], a[60:63], v[204:207], a[132:135]\n"
"ds_read_b64_tr_b16 a[32:33], v5 offset:8448\n"
"v_cvt_pk_bf16_f32 v214, v160, v161\n"
"v_mfma_f32_16x16x32_bf16 a[164:167], a[60:63], v[208:211], a[164:167]\n"
"v_exp_f32 v162, v162\n"
"v_mfma_f32_16x16x32_bf16 a[196:199], a[60:63], a[116:119], a[196:199]\n"
"s_waitcnt lgkmcnt(8)\n"
"s_add_u32 m0, s30, 0\n"
"s_nop 0\n"
"buffer_load_dwordx4 v19, s[48:51], s77 offen offset:0 nt lds\n"
"ds_read_b64_tr_b16 a[34:35], v7 offset:8192\n"
"v_mfma_f32_16x16x32_bf16 v[184:187], a[0:3], a[96:99], v[240:243]\n"
"s_waitcnt lgkmcnt(8)\n"
"v_exp_f32 v163, v163\n"
"v_mfma_f32_16x16x32_bf16 v[184:187], a[4:7], a[100:103], v[184:187]\n"
"s_waitcnt lgkmcnt(7)\n"
"ds_read_b128 v[80:83], v13 offset:28672\n"
"v_cvt_pk_bf16_f32 v215, v162, v163\n"
"v_mfma_f32_16x16x32_bf16 v[184:187], a[8:11], a[104:107], v[184:187]\n"
"s_waitcnt lgkmcnt(7)\n"
"ds_read_b64_tr_b16 a[36:37], v6 offset:8448\n"
"ds_read_b64_tr_b16 a[38:39], v8 offset:8192\n"
"v_mfma_f32_16x16x32_bf16 v[184:187], a[12:15], a[108:111], v[184:187]\n"
"v_exp_f32 v156, v156\n"
"v_mfma_f32_16x16x32_bf16 v[188:191], a[0:3], a[64:67], v[232:235]\n"
"ds_read_b128 v[88:91], v13 offset:24576\n"
"ds_read_b64_tr_b16 a[40:41], v5 offset:8704\n"
"v_mfma_f32_16x16x32_bf16 v[188:191], a[4:7], a[68:71], v[188:191]\n"
"s_add_u32 m0, s30, 0x3000\n"
"ds_read_b128 v[96:99], v13 offset:29696\n"
"ds_read_b64_tr_b16 a[42:43], v7 offset:8960\n"
"v_mfma_f32_16x16x32_bf16 v[188:191], a[8:11], a[72:75], v[188:191]\n"
"buffer_load_dwordx4 v140, s[52:55], s13 offen offset:0 lds\n"
"ds_read_b64_tr_b16 a[44:45], v6 offset:8704\n"
"v_mfma_f32_16x16x32_bf16 v[188:191], a[12:15], a[76:79], v[188:191]\n"
"ds_read_b128 v[104:107], v13 offset:25600\n"
"ds_read_b64_tr_b16 a[46:47], v8 offset:8960\n"
"v_mfma_f32_16x16x32_bf16 v[192:195], a[0:3], a[80:83], v[236:239]\n"
"v_exp_f32 v157, v157\n"
"v_mfma_f32_16x16x32_bf16 v[192:195], a[4:7], a[84:87], v[192:195]\n"
"ds_read_b128 v[112:115], v13 offset:30720\n"
"v_cvt_pk_bf16_f32 v216, v156, v157\n"
"v_mfma_f32_16x16x32_bf16 v[192:195], a[8:11], a[88:91], v[192:195]\n"
"s_add_u32 m0, s30, 0\n"
"v_exp_f32 v158, v158\n"
"v_mfma_f32_16x16x32_bf16 v[192:195], a[12:15], a[92:95], v[192:195]\n"
"s_waitcnt lgkmcnt(11)\n"
"ds_read_b128 v[120:123], v13 offset:26624\n"
"ds_read_b64_tr_b16 a[48:49], v5 offset:8960\n"
"v_mfma_f32_16x16x32_bf16 v[196:199], a[16:19], a[64:67], v[232:235]\n"
"buffer_load_dwordx4 v20, s[48:51], s77 offen offset:1024 nt lds\n"
"ds_read_b64_tr_b16 a[50:51], v7 offset:8704\n"
"v_mfma_f32_16x16x32_bf16 v[196:199], a[20:23], a[68:71], v[196:199]\n"
"ds_read_b128 v[128:131], v13 offset:31744\n"
"ds_read_b64_tr_b16 a[52:53], v6 offset:8960\n"
"v_mfma_f32_16x16x32_bf16 v[196:199], a[24:27], a[72:75], v[196:199]\n"
"ds_read_b64_tr_b16 a[54:55], v8 offset:8704\n"
"v_exp_f32 v159, v159\n"
"v_mfma_f32_16x16x32_bf16 v[196:199], a[28:31], a[76:79], v[196:199]\n"
"ds_read_b128 v[136:139], v13 offset:27648\n"
"v_exp_f32 v164, v164\n"
"v_mfma_f32_16x16x32_bf16 v[152:155], a[16:19], a[80:83], v[236:239]\n"
"s_add_u32 m0, s30, 0x3000\n"
"ds_read_b64_tr_b16 a[56:57], v5 offset:8192\n"
"v_exp_f32 v165, v165\n"
"v_mfma_f32_16x16x32_bf16 v[152:155], a[20:23], a[84:87], v[152:155]\n"
"buffer_load_dwordx4 v141, s[52:55], s13 offen offset:1024 lds\n"
"v_cvt_pk_bf16_f32 v217, v158, v159\n"
"v_cvt_pk_bf16_f32 v218, v164, v165\n"
"v_mfma_f32_16x16x32_bf16 v[152:155], a[24:27], a[88:91], v[152:155]\n"
"ds_read_b64_tr_b16 a[58:59], v7 offset:8448\n"
"v_exp_f32 v166, v166\n"
"v_mfma_f32_16x16x32_bf16 v[152:155], a[28:31], a[92:95], v[152:155]\n"
"ds_read_b64 a[112:113], v13 offset:38920\n"
"v_exp_f32 v167, v167\n"
"v_mfma_f32_16x16x32_bf16 a[200:203], v[228:231], v[212:215], a[200:203]\n"
"ds_read_b64 a[114:115], v14 offset:38920\n"
"v_cvt_pk_bf16_f32 v219, v166, v167\n"
"ds_read_b64_tr_b16 a[60:61], v6 offset:8192\n"
"ds_read_b64_tr_b16 a[62:63], v8 offset:8448\n"
"v_mfma_f32_16x16x32_bf16 a[204:207], v[228:231], v[216:219], a[204:207]\n"
"ds_read_b128 a[0:3], v3 offset:4096\n"
"ds_read_b128 a[4:7], v3 offset:4352\n"
"v_mfma_f32_16x16x32_bf16 a[136:139], a[32:35], v[212:215], a[136:139]\n"
"s_add_u32 m0, s30, 0\n"
"ds_read_b128 a[8:11], v3 offset:4608\n"
"ds_read_b128 a[12:15], v3 offset:4864\n"
"v_mfma_f32_16x16x32_bf16 a[168:171], a[32:35], v[216:219], a[168:171]\n"
"s_waitcnt lgkmcnt(11)\n"
"v_exp_f32 v168, v168\n"
"v_mfma_f32_16x16x32_bf16 a[140:143], a[36:39], v[212:215], a[140:143]\n"
"buffer_load_dwordx4 v19, s[48:51], s77 offen offset:2048 nt lds\n"
"ds_read_b128 a[16:19], v4 offset:4096\n"
"v_mfma_f32_16x16x32_bf16 a[172:175], a[36:39], v[216:219], a[172:175]\n"
"v_exp_f32 v169, v169\n"
"v_mfma_f32_16x16x32_bf16 a[144:147], a[40:43], v[212:215], a[144:147]\n"
"ds_read_b128 a[20:23], v4 offset:4352\n"
"v_cvt_pk_bf16_f32 v220, v168, v169\n"
"v_mfma_f32_16x16x32_bf16 a[176:179], a[40:43], v[216:219], a[176:179]\n"
"s_add_u32 m0, s30, 0x3000\n"
"v_exp_f32 v170, v170\n"
"v_mfma_f32_16x16x32_bf16 a[148:151], a[44:47], v[212:215], a[148:151]\n"
"buffer_load_dwordx4 v140, s[52:55], s75 offen offset:2048 lds\n"
"ds_read_b128 a[24:27], v4 offset:4608\n"
"v_mfma_f32_16x16x32_bf16 a[180:183], a[44:47], v[216:219], a[180:183]\n"
"v_exp_f32 v171, v171\n"
"v_mfma_f32_16x16x32_bf16 a[152:155], a[48:51], v[212:215], a[152:155]\n"
"ds_read_b128 a[28:31], v4 offset:4864\n"
"v_cvt_pk_bf16_f32 v221, v170, v171\n"
"v_mfma_f32_16x16x32_bf16 a[184:187], a[48:51], v[216:219], a[184:187]\n"
"v_exp_f32 v176, v176\n"
"v_mfma_f32_16x16x32_bf16 a[156:159], a[52:55], v[212:215], a[156:159]\n"
"v_exp_f32 v177, v177\n"
"v_mfma_f32_16x16x32_bf16 a[188:191], a[52:55], v[216:219], a[188:191]\n"
"s_waitcnt lgkmcnt(4)\n"
"ds_read_b64_tr_b16 a[32:33], v9 offset:8960\n"
"v_cvt_pk_bf16_f32 v222, v176, v177\n"
"v_mfma_f32_16x16x32_bf16 a[128:131], a[56:59], v[212:215], a[128:131]\n"
"ds_read_b64_tr_b16 a[34:35], v11 offset:8704\n"
"v_exp_f32 v178, v178\n"
"v_mfma_f32_16x16x32_bf16 a[160:163], a[56:59], v[216:219], a[160:163]\n"
"ds_read_b64_tr_b16 a[36:37], v10 offset:8960\n"
"ds_read_b64_tr_b16 a[38:39], v12 offset:8704\n"
"v_exp_f32 v179, v179\n"
"v_mfma_f32_16x16x32_bf16 a[192:195], a[56:59], a[112:115], a[192:195]\n"
"s_add_u32 m0, s30, 0\n"
"ds_read_b64_tr_b16 a[40:41], v9 offset:8192\n"
"v_cvt_pk_bf16_f32 v223, v178, v179\n"
"v_exp_f32 v172, v172\n"
"v_mfma_f32_16x16x32_bf16 a[132:135], a[60:63], v[212:215], a[132:135]\n"
"buffer_load_dwordx4 v20, s[48:51], s77 offen offset:3072 nt lds\n"
"ds_read_b64_tr_b16 a[42:43], v11 offset:8448\n"
"v_mfma_f32_16x16x32_bf16 a[164:167], a[60:63], v[216:219], a[164:167]\n"
"s_add_u32 m0, s30, 0x3000\n"
"ds_read_b64_tr_b16 a[44:45], v10 offset:8192\n"
"ds_read_b64_tr_b16 a[46:47], v12 offset:8448\n"
"v_exp_f32 v173, v173\n"
"v_mfma_f32_16x16x32_bf16 a[196:199], a[60:63], a[112:115], a[196:199]\n"
"buffer_load_dwordx4 v141, s[52:55], s75 offen offset:3072 lds\n"
"ds_read_b64_tr_b16 a[48:49], v9 offset:8448\n"
"v_cvt_pk_bf16_f32 v224, v172, v173\n"
"v_mfma_f32_16x16x32_bf16 v[156:159], a[0:3], a[64:67], v[232:235]\n"
"ds_read_b64_tr_b16 a[50:51], v11 offset:8192\n"
"v_exp_f32 v174, v174\n"
"v_mfma_f32_16x16x32_bf16 v[156:159], a[4:7], a[68:71], v[156:159]\n"
"v_exp_f32 v175, v175\n"
"v_mfma_f32_16x16x32_bf16 v[156:159], a[8:11], a[72:75], v[156:159]\n"
"v_exp_f32 v180, v180\n"
"v_mfma_f32_16x16x32_bf16 v[156:159], a[12:15], a[76:79], v[156:159]\n"
"v_exp_f32 v181, v181\n"
"v_mfma_f32_16x16x32_bf16 v[160:163], a[0:3], a[80:83], v[236:239]\n"
"v_exp_f32 v182, v182\n"
"v_mfma_f32_16x16x32_bf16 v[160:163], a[4:7], a[84:87], v[160:163]\n"
"v_exp_f32 v183, v183\n"
"v_mfma_f32_16x16x32_bf16 v[160:163], a[8:11], a[88:91], v[160:163]\n"
"ds_read_b64 a[116:117], v15 offset:38920\n"
"v_cvt_pk_bf16_f32 v225, v174, v175\n"
"v_mfma_f32_16x16x32_bf16 v[160:163], a[12:15], a[92:95], v[160:163]\n"
"s_waitcnt lgkmcnt(5)\n"
"ds_read_b64 a[118:119], v16 offset:38920\n"
"v_cvt_pk_bf16_f32 v226, v180, v181\n"
"v_mfma_f32_16x16x32_bf16 v[164:167], a[16:19], a[64:67], v[232:235]\n"
"ds_read_b64_tr_b16 a[52:53], v10 offset:8448\n"
"v_cvt_pk_bf16_f32 v227, v182, v183\n"
"v_mfma_f32_16x16x32_bf16 v[164:167], a[20:23], a[68:71], v[164:167]\n"
"s_add_u32 m0, s30, 0x6000\n"
"v_exp_f32 v184, v184\n"
"v_mfma_f32_16x16x32_bf16 v[164:167], a[24:27], a[72:75], v[164:167]\n"
"buffer_load_dwordx4 v23, s[52:55], s15 offen offset:0 lds\n"
"v_add_f32 v254, v254, v184\n"
"v_mfma_f32_16x16x32_bf16 v[164:167], a[28:31], a[76:79], v[164:167]\n"
"v_exp_f32 v185, v185\n"
"v_mfma_f32_16x16x32_bf16 v[168:171], a[16:19], a[80:83], v[236:239]\n"
"v_exp_f32 v186, v186\n"
"v_mfma_f32_16x16x32_bf16 v[168:171], a[20:23], a[84:87], v[168:171]\n"
"v_exp_f32 v187, v187\n"
"v_mfma_f32_16x16x32_bf16 v[168:171], a[24:27], a[88:91], v[168:171]\n"
"ds_read_b64_tr_b16 a[54:55], v12 offset:8192\n"
"v_add_f32 v254, v254, v185\n"
"v_mfma_f32_16x16x32_bf16 v[168:171], a[28:31], a[92:95], v[168:171]\n"
"ds_read_b64_tr_b16 a[56:57], v9 offset:8704\n"
"v_cvt_pk_bf16_f32 v184, v184, v185\n"
"v_mfma_f32_16x16x32_bf16 a[200:203], v[228:231], v[220:223], a[200:203]\n"
"ds_read_b64_tr_b16 a[58:59], v11 offset:8960\n"
"v_cvt_pk_bf16_f32 v185, v186, v187\n"
"v_mfma_f32_16x16x32_bf16 a[204:207], v[228:231], v[224:227], a[204:207]\n"
"ds_write_b64 v13, v[184:185] offset:38912\n"
"v_mfma_f32_16x16x32_bf16 a[136:139], a[32:35], v[220:223], a[136:139]\n"
"ds_read_b64_tr_b16 a[60:61], v10 offset:8704\n"
"ds_read_b64_tr_b16 a[62:63], v12 offset:8960\n"
"v_mfma_f32_16x16x32_bf16 a[168:171], a[32:35], v[224:227], a[168:171]\n"
"v_add_f32 v254, v254, v186\n"
"v_add_f32 v254, v254, v187\n"
"v_mfma_f32_16x16x32_bf16 a[140:143], a[36:39], v[220:223], a[140:143]\n"
"s_memrealtime s[24:25]\n"
"v_exp_f32 v188, v188\n"
"v_mfma_f32_16x16x32_bf16 a[172:175], a[36:39], v[224:227], a[172:175]\n"
"buffer_load_dwordx4 v24, s[52:55], s15 offen offset:1024 lds\n"
"v_mfma_f32_16x16x32_bf16 a[144:147], a[40:43], v[220:223], a[144:147]\n"
"v_exp_f32 v189, v189\n"
"v_mfma_f32_16x16x32_bf16 a[176:179], a[40:43], v[224:227], a[176:179]\n"
"s_waitcnt lgkmcnt(0)\n"
"v_exp_f32 v190, v190\n"
"v_mfma_f32_16x16x32_bf16 a[148:151], a[44:47], v[220:223], a[148:151]\n"
"v_exp_f32 v191, v191\n"
"v_mfma_f32_16x16x32_bf16 a[180:183], a[44:47], v[224:227], a[180:183]\n"
"s_sub_u32 s25, s24, s5\n"
"s_cmp_lt_i32 s25, 0\n"
"s_cbranch_scc1 .Lmha_LIM15\n"
".Lmha_LIM15R:\n"
"v_cvt_pk_bf16_f32 v200, v188, v189\n"
"v_cvt_pk_bf16_f32 v201, v190, v191\n"
"v_mfma_f32_16x16x32_bf16 a[152:155], a[48:51], v[220:223], a[152:155]\n"
"s_waitcnt vmcnt(10)\n"
"s_barrier\n"
"v_mfma_f32_16x16x32_bf16 a[184:187], a[48:51], v[224:227], a[184:187]\n"
"s_add_u32 s76, s39, 0x80\n"
"ds_read_b128 a[0:3], v1 offset:20480\n"
"buffer_store_dwordx2 v[26:27], v25, s[64:67], s76 offen offset:0 sc1\n"
"v_mfma_f32_16x16x32_bf16 a[156:159], a[52:55], v[220:223], a[156:159]\n"
"ds_read_b128 a[4:7], v1 offset:20736\n"
"ds_read_b128 a[8:11], v1 offset:20992\n"
"v_mfma_f32_16x16x32_bf16 a[188:191], a[52:55], v[224:227], a[188:191]\n"
"ds_read_b128 a[12:15], v1 offset:21248\n"
"ds_read_b128 a[16:19], v2 offset:20480\n"
"v_mfma_f32_16x16x32_bf16 a[128:131], a[56:59], v[220:223], a[128:131]\n"
"ds_read_b128 a[20:23], v2 offset:20736\n"
"ds_read_b128 a[24:27], v2 offset:20992\n"
"v_mfma_f32_16x16x32_bf16 a[160:163], a[56:59], v[224:227], a[160:163]\n"
"s_add_u32 s78, s15, 0x7800\n"
"v_exp_f32 v196, v196\n"
"v_mfma_f32_16x16x32_bf16 a[192:195], a[56:59], a[116:119], a[192:195]\n"
"buffer_load_dwordx4 v23, s[52:55], s78 offen offset:2048 lds\n"
"ds_read_b128 a[28:31], v2 offset:21248\n"
"v_mfma_f32_16x16x32_bf16 a[132:135], a[60:63], v[220:223], a[132:135]\n"
"s_getpc_b64 s[98:99]\n"
"v_exp_f32 v197, v197\n"
"v_mfma_f32_16x16x32_bf16 a[164:167], a[60:63], v[224:227], a[164:167]\n"
"ds_read_b64_tr_b16 a[32:33], v5 offset:16640\n"
"v_cvt_pk_bf16_f32 v202, v196, v197\n"
"v_mfma_f32_16x16x32_bf16 a[196:199], a[60:63], a[116:119], a[196:199]\n"
"s_waitcnt lgkmcnt(8)\n"
"v_exp_f32 v198, v198\n"
"v_mfma_f32_16x16x32_bf16 v[172:175], a[0:3], a[96:99], v[240:243]\n"
"s_waitcnt lgkmcnt(7)\n"
"global_load_dword v18, v0, s[98:99] offset:1536\n"
"ds_read_b64_tr_b16 a[34:35], v7 offset:16384\n"
"v_mfma_f32_16x16x32_bf16 v[172:175], a[4:7], a[100:103], v[172:175]\n"
"s_waitcnt lgkmcnt(7)\n"
"v_exp_f32 v199, v199\n"
"v_mfma_f32_16x16x32_bf16 v[172:175], a[8:11], a[104:107], v[172:175]\n"
"s_waitcnt lgkmcnt(6)\n"
"ds_read_b64_tr_b16 a[36:37], v6 offset:16640\n"
"ds_read_b64_tr_b16 a[38:39], v8 offset:16384\n"
"v_mfma_f32_16x16x32_bf16 v[172:175], a[12:15], a[108:111], v[172:175]\n"
"ds_read_b64_tr_b16 a[40:41], v5 offset:16896\n"
"v_cvt_pk_bf16_f32 v203, v198, v199\n"
"v_mfma_f32_16x16x32_bf16 v[176:179], a[0:3], a[64:67], v[232:235]\n"
"v_exp_f32 v192, v192\n"
"v_mfma_f32_16x16x32_bf16 v[176:179], a[4:7], a[68:71], v[176:179]\n"
"v_exp_f32 v193, v193\n"
"v_mfma_f32_16x16x32_bf16 v[176:179], a[8:11], a[72:75], v[176:179]\n"
"ds_read_b64_tr_b16 a[42:43], v7 offset:17152\n"
"v_cvt_pk_bf16_f32 v204, v192, v193\n"
"v_mfma_f32_16x16x32_bf16 v[176:179], a[12:15], a[76:79], v[176:179]\n"
"buffer_load_dwordx4 v24, s[52:55], s78 offen offset:3072 lds\n"
"ds_read_b64_tr_b16 a[44:45], v6 offset:16896\n"
"v_mfma_f32_16x16x32_bf16 v[180:183], a[0:3], a[80:83], v[236:239]\n"
"v_exp_f32 v194, v194\n"
"v_mfma_f32_16x16x32_bf16 v[180:183], a[4:7], a[84:87], v[180:183]\n"
"v_exp_f32 v195, v195\n"
"v_mfma_f32_16x16x32_bf16 v[180:183], a[8:11], a[88:91], v[180:183]\n"
"v_exp_f32 v152, v152\n"
"v_mfma_f32_16x16x32_bf16 v[180:183], a[12:15], a[92:95], v[180:183]\n"
"s_waitcnt lgkmcnt(5)\n"
"v_exp_f32 v153, v153\n"
"v_mfma_f32_16x16x32_bf16 v[184:187], a[16:19], a[64:67], v[232:235]\n"
"v_exp_f32 v154, v154\n"
"v_mfma_f32_16x16x32_bf16 v[184:187], a[20:23], a[68:71], v[184:187]\n"
"v_exp_f32 v155, v155\n"
"v_mfma_f32_16x16x32_bf16 v[184:187], a[24:27], a[72:75], v[184:187]\n"
"ds_read_b64_tr_b16 a[46:47], v8 offset:17152\n"
"v_cvt_pk_bf16_f32 v205, v194, v195\n"
"v_mfma_f32_16x16x32_bf16 v[184:187], a[28:31], a[76:79], v[184:187]\n"
"ds_read_b64_tr_b16 a[48:49], v5 offset:17152\n"
"v_cvt_pk_bf16_f32 v206, v152, v153\n"
"v_mfma_f32_16x16x32_bf16 v[188:191], a[16:19], a[80:83], v[236:239]\n"
"ds_read_b64_tr_b16 a[50:51], v7 offset:16896\n"
"v_cvt_pk_bf16_f32 v207, v154, v155\n"
"v_mfma_f32_16x16x32_bf16 v[188:191], a[20:23], a[84:87], v[188:191]\n"
"s_add_u32 m0, s30, 0x1000\n"
"ds_read_b64_tr_b16 a[52:53], v6 offset:17152\n"
"ds_read_b64_tr_b16 a[54:55], v8 offset:16896\n"
"v_mfma_f32_16x16x32_bf16 v[188:191], a[24:27], a[88:91], v[188:191]\n"
"buffer_load_dwordx4 v23, s[52:55], s17 offen offset:0 lds\n"
"ds_read_b64_tr_b16 a[56:57], v5 offset:16384\n"
"v_mfma_f32_16x16x32_bf16 v[188:191], a[28:31], a[92:95], v[188:191]\n"
"ds_read_b64_tr_b16 a[58:59], v7 offset:16640\n"
"ds_read_b64 a[112:113], v13 offset:38912\n"
"v_mfma_f32_16x16x32_bf16 a[200:203], v[228:231], v[200:203], a[200:203]\n"
"ds_read_b64 a[114:115], v14 offset:38912\n"
"ds_read_b64_tr_b16 a[60:61], v6 offset:16384\n"
"v_mfma_f32_16x16x32_bf16 a[204:207], v[228:231], v[204:207], a[204:207]\n"
"ds_read_b64_tr_b16 a[62:63], v8 offset:16640\n"
"ds_read_b128 a[0:3], v3 offset:20480\n"
"v_mfma_f32_16x16x32_bf16 a[136:139], a[32:35], v[200:203], a[136:139]\n"
"buffer_load_dwordx4 v24, s[52:55], s17 offen offset:1024 lds\n"
"ds_read_b128 a[4:7], v3 offset:20736\n"
"v_mfma_f32_16x16x32_bf16 a[168:171], a[32:35], v[204:207], a[168:171]\n"
"s_waitcnt lgkmcnt(13)\n"
"ds_read_b128 a[8:11], v3 offset:20992\n"
"ds_read_b128 a[12:15], v3 offset:21248\n"
"v_mfma_f32_16x16x32_bf16 a[140:143], a[36:39], v[200:203], a[140:143]\n"
"s_add_u32 s84, s17, 0x7800\n"
"v_exp_f32 v156, v156\n"
"v_mfma_f32_16x16x32_bf16 a[172:175], a[36:39], v[204:207], a[172:175]\n"
"buffer_load_dwordx4 v23, s[52:55], s84 offen offset:2048 lds\n"
"ds_read_b128 a[16:19], v4 offset:20480\n"
"v_mfma_f32_16x16x32_bf16 a[144:147], a[40:43], v[200:203], a[144:147]\n"
"v_exp_f32 v157, v157\n"
"v_mfma_f32_16x16x32_bf16 a[176:179], a[40:43], v[204:207], a[176:179]\n"
"s_waitcnt lgkmcnt(13)\n"
"ds_read_b128 a[20:23], v4 offset:20736\n"
"v_cvt_pk_bf16_f32 v208, v156, v157\n"
"v_mfma_f32_16x16x32_bf16 a[148:151], a[44:47], v[200:203], a[148:151]\n"
"v_exp_f32 v158, v158\n"
"v_mfma_f32_16x16x32_bf16 a[180:183], a[44:47], v[204:207], a[180:183]\n"
"buffer_load_dwordx4 v24, s[52:55], s84 offen offset:3072 lds\n"
"ds_read_b128 a[24:27], v4 offset:20992\n"
"v_mfma_f32_16x16x32_bf16 a[152:155], a[48:51], v[200:203], a[152:155]\n"
"s_add_u32 m0, s30, 0x2000\n"
"v_exp_f32 v159, v159\n"
"v_mfma_f32_16x16x32_bf16 a[184:187], a[48:51], v[204:207], a[184:187]\n"
"s_waitcnt lgkmcnt(8)\n"
"ds_read_b128 a[28:31], v4 offset:21248\n"
"v_cvt_pk_bf16_f32 v209, v158, v159\n"
"v_exp_f32 v172, v172\n"
"v_mfma_f32_16x16x32_bf16 a[156:159], a[52:55], v[200:203], a[156:159]\n"
"buffer_load_dwordx4 v140, s[52:55], s15 offen offset:0 lds\n"
"v_lshrrev_b32 v17, 7, v22\n"
"v_exp_f32 v164, v164\n"
"v_exp_f32 v165, v165\n"
"v_mfma_f32_16x16x32_bf16 a[188:191], a[52:55], v[204:207], a[188:191]\n"
"ds_read_b64_tr_b16 a[32:33], v9 offset:17152\n"
"v_lshlrev_b32 v17, 11, v17\n"
"v_exp_f32 v166, v166\n"
"v_mfma_f32_16x16x32_bf16 a[128:131], a[56:59], v[200:203], a[128:131]\n"
"ds_read_b64_tr_b16 a[34:35], v11 offset:16896\n"
"v_add_f32 v254, v254, v172\n"
"v_exp_f32 v167, v167\n"
"v_mfma_f32_16x16x32_bf16 a[160:163], a[56:59], v[204:207], a[160:163]\n"
"buffer_load_dwordx4 v141, s[52:55], s15 offen offset:1024 lds\n"
"ds_read_b64_tr_b16 a[36:37], v10 offset:17152\n"
"v_cvt_pk_bf16_f32 v210, v164, v165\n"
"v_mfma_f32_16x16x32_bf16 a[192:195], a[56:59], a[112:115], a[192:195]\n"
"s_waitcnt lgkmcnt(5)\n"
"ds_read_b64_tr_b16 a[38:39], v12 offset:16896\n"
"v_cvt_pk_bf16_f32 v211, v166, v167\n"
"v_exp_f32 v173, v173\n"
"v_mfma_f32_16x16x32_bf16 a[132:135], a[60:63], v[200:203], a[132:135]\n"
"ds_read_b64_tr_b16 a[40:41], v9 offset:16384\n"
"v_add_f32 v254, v254, v173\n"
"v_exp_f32 v174, v174\n"
"v_mfma_f32_16x16x32_bf16 a[164:167], a[60:63], v[204:207], a[164:167]\n"
"ds_read_b64_tr_b16 a[42:43], v11 offset:16640\n"
"v_add_f32 v254, v254, v174\n"
"v_exp_f32 v160, v160\n"
"v_mfma_f32_16x16x32_bf16 a[196:199], a[60:63], a[112:115], a[196:199]\n"
"s_add_u32 s96, s3, 0x40000\n"
"buffer_load_dwordx4 v140, s[52:55], s78 offen offset:2048 lds\n"
"ds_read_b64_tr_b16 a[44:45], v10 offset:16384\n"
"v_cvt_pk_bf16_f32 v172, v172, v173\n"
"v_mfma_f32_16x16x32_bf16 v[192:195], a[0:3], a[64:67], v[232:235]\n"
"buffer_load_dword v17, v17, s[52:55], s96 offen offset:0\n"
"ds_read_b64_tr_b16 a[46:47], v12 offset:16640\n"
"v_mfma_f32_16x16x32_bf16 v[192:195], a[4:7], a[68:71], v[192:195]\n"
"ds_read_b64_tr_b16 a[48:49], v9 offset:16640\n"
"v_exp_f32 v161, v161\n"
"v_mfma_f32_16x16x32_bf16 v[192:195], a[8:11], a[72:75], v[192:195]\n"
"buffer_load_dwordx4 v141, s[52:55], s78 offen offset:3072 lds\n"
"ds_read_b64_tr_b16 a[50:51], v11 offset:16384\n"
"v_cvt_pk_bf16_f32 v212, v160, v161\n"
"v_mfma_f32_16x16x32_bf16 v[192:195], a[12:15], a[76:79], v[192:195]\n"
"ds_read_b64_tr_b16 a[52:53], v10 offset:16640\n"
"v_exp_f32 v162, v162\n"
"v_mfma_f32_16x16x32_bf16 v[196:199], a[0:3], a[80:83], v[236:239]\n"
"v_exp_f32 v163, v163\n"
"v_mfma_f32_16x16x32_bf16 v[196:199], a[4:7], a[84:87], v[196:199]\n"
"s_add_u32 m0, s30, 0x7000\n"
"v_exp_f32 v168, v168\n"
"v_mfma_f32_16x16x32_bf16 v[196:199], a[8:11], a[88:91], v[196:199]\n"
"buffer_load_dwordx4 v140, s[52:55], s17 offen offset:0 lds\n"
"v_cvt_pk_bf16_f32 v213, v162, v163\n"
"v_mfma_f32_16x16x32_bf16 v[196:199], a[12:15], a[92:95], v[196:199]\n"
"v_exp_f32 v169, v169\n"
"v_mfma_f32_16x16x32_bf16 v[152:155], a[16:19], a[64:67], v[232:235]\n"
"ds_read_b64_tr_b16 a[54:55], v12 offset:16384\n"
"v_exp_f32 v170, v170\n"
"v_mfma_f32_16x16x32_bf16 v[152:155], a[20:23], a[68:71], v[152:155]\n"
"s_waitcnt lgkmcnt(5)\n"
"buffer_load_dwordx4 v141, s[52:55], s17 offen offset:1024 lds\n"
"v_mfma_f32_16x16x32_bf16 v[152:155], a[24:27], a[72:75], v[152:155]\n"
"ds_read_b64 a[116:117], v15 offset:38912\n"
"v_exp_f32 v171, v171\n"
"v_exp_f32 v175, v175\n"
"v_cvt_pk_bf16_f32 v214, v168, v169\n"
"v_exp_f32 v176, v176\n"
"v_mfma_f32_16x16x32_bf16 v[152:155], a[28:31], a[76:79], v[152:155]\n"
"ds_read_b64 a[118:119], v16 offset:38912\n"
"v_cvt_pk_bf16_f32 v215, v170, v171\n"
"v_add_f32 v254, v254, v175\n"
"v_cvt_pk_bf16_f32 v173, v174, v175\n"
"v_exp_f32 v177, v177\n"
"v_mfma_f32_16x16x32_bf16 v[156:159], a[16:19], a[80:83], v[236:239]\n"
"buffer_load_dwordx4 v140, s[52:55], s84 offen offset:2048 lds\n"
"v_mov_b32 v255, v254\n"
"v_exp_f32 v178, v178\n"
"v_exp_f32 v179, v179\n"
"v_exp_f32 v184, v184\n"
"v_mfma_f32_16x16x32_bf16 v[156:159], a[20:23], a[84:87], v[156:159]\n"
"ds_read_b64_tr_b16 a[56:57], v9 offset:16896\n"
"v_permlane32_swap_b32 v254, v255\n"
"v_exp_f32 v185, v185\n"
"v_exp_f32 v186, v186\n"
"v_exp_f32 v187, v187\n"
"v_mfma_f32_16x16x32_bf16 v[156:159], a[24:27], a[88:91], v[156:159]\n"
"ds_read_b64_tr_b16 a[58:59], v11 offset:17152\n"
"v_add_f32 v254, v254, v255\n"
"v_cvt_pk_bf16_f32 v216, v176, v177\n"
"v_cvt_pk_bf16_f32 v217, v178, v179\n"
"v_cvt_pk_bf16_f32 v218, v184, v185\n"
"v_mfma_f32_16x16x32_bf16 v[156:159], a[28:31], a[92:95], v[156:159]\n"
"s_waitcnt vmcnt(16)\n"
"ds_read_b64_tr_b16 v[64:65], v142 offset:24576\n"
"v_mov_b32 v255, v254\n"
"v_cvt_pk_bf16_f32 v219, v186, v187\n"
"v_exp_f32 v180, v180\n"
"v_exp_f32 v181, v181\n"
"v_mfma_f32_16x16x32_bf16 a[200:203], v[228:231], v[208:211], a[200:203]\n"
"buffer_load_dwordx4 v141, s[52:55], s84 offen offset:3072 lds\n"
"v_permlane16_swap_b32 v254, v255\n"
"v_exp_f32 v182, v182\n"
"v_exp_f32 v183, v183\n"
"v_exp_f32 v188, v188\n"
"v_mfma_f32_16x16x32_bf16 a[204:207], v[228:231], v[212:215], a[204:207]\n"
"ds_read_b64_tr_b16 a[60:61], v10 offset:16896\n"
"v_add_f32 v254, v254, v255\n"
"v_exp_f32 v189, v189\n"
"v_mfma_f32_16x16x32_bf16 a[136:139], a[32:35], v[208:211], a[136:139]\n"
"s_add_u32 m0, s30, 0x8000\n"
"ds_write_b64 v13, v[172:173] offset:38920\n"
"v_mfma_f32_16x16x32_bf16 a[168:171], a[32:35], v[212:215], a[168:171]\n"
"buffer_load_dwordx4 v23, s[52:55], s19 offen offset:0 lds\n"
"ds_write_b32 v13, v254 offset:39936\n"
"v_cvt_pk_bf16_f32 v220, v180, v181\n"
"v_mfma_f32_16x16x32_bf16 a[140:143], a[36:39], v[208:211], a[140:143]\n"
"v_exp_f32 v190, v190\n"
"v_cvt_pk_bf16_f32 v221, v182, v183\n"
"v_cvt_pk_bf16_f32 v222, v188, v189\n"
"v_mfma_f32_16x16x32_bf16 a[172:175], a[36:39], v[212:215], a[172:175]\n"
"s_waitcnt vmcnt(14)\n"
"ds_read_b64_tr_b16 a[62:63], v12 offset:17152\n"
"ds_read_b64_tr_b16 v[66:67], v142 offset:26624\n"
"v_mfma_f32_16x16x32_bf16 a[144:147], a[40:43], v[208:211], a[144:147]\n"
"s_add_u32 s77, s19, 0x7800\n"
"buffer_load_dwordx4 v24, s[52:55], s19 offen offset:1024 lds\n"
"ds_read_b64_tr_b16 v[68:69], v143 offset:24576\n"
"v_mfma_f32_16x16x32_bf16 a[176:179], a[40:43], v[212:215], a[176:179]\n"
"s_waitcnt lgkmcnt(6)\n"
"ds_read_b64_tr_b16 v[70:71], v143 offset:26624\n"
"ds_read_b64_tr_b16 v[72:73], v142 offset:25088\n"
"v_mfma_f32_16x16x32_bf16 a[148:151], a[44:47], v[208:211], a[148:151]\n"
"s_and_b32 s38, s27, 8\n"
"ds_read_b64_tr_b16 v[74:75], v142 offset:27136\n"
"ds_read_b64_tr_b16 v[76:77], v143 offset:25088\n"
"v_bfe_u32 v30, v22, 4, 3\n"
"v_mfma_f32_16x16x32_bf16 a[180:183], a[44:47], v[212:215], a[180:183]\n"
"s_lshl_b32 s38, s38, 7\n"
"ds_read_b64_tr_b16 v[48:49], v142 offset:12288\n"
"ds_read_b64_tr_b16 v[78:79], v143 offset:27136\n"
"v_lshlrev_b32 v30, 7, v30\n"
"v_mfma_f32_16x16x32_bf16 a[152:155], a[48:51], v[208:211], a[152:155]\n"
"s_waitcnt lgkmcnt(9)\n"
"s_barrier\n"
"buffer_load_dwordx4 v23, s[52:55], s77 offen offset:2048 lds\n"
"ds_read_b64_tr_b16 v[50:51], v142 offset:14336\n"
"v_mfma_f32_16x16x32_bf16 a[184:187], a[48:51], v[212:215], a[184:187]\n"
"s_add_u32 s38, s38, s94\n"
"ds_read_b64_tr_b16 a[0:1], v5 offset:256\n"
"ds_read_b64_tr_b16 a[2:3], v7 offset:0\n"
"v_mfma_f32_16x16x32_bf16 a[156:159], a[52:55], v[208:211], a[156:159]\n"
"s_add_u32 s38, s38, 0x80\n"
"ds_read_b64_tr_b16 a[4:5], v6 offset:256\n"
"ds_read_b64_tr_b16 a[6:7], v8 offset:0\n"
"v_mfma_f32_16x16x32_bf16 a[188:191], a[52:55], v[212:215], a[188:191]\n"
"buffer_load_dwordx4 v24, s[52:55], s77 offen offset:3072 lds\n"
"ds_read_b64_tr_b16 v[52:53], v143 offset:12288\n"
"v_mfma_f32_16x16x32_bf16 a[128:131], a[56:59], v[208:211], a[128:131]\n"
"ds_read_b64_tr_b16 v[54:55], v143 offset:14336\n"
"ds_read_b64_tr_b16 a[8:9], v5 offset:512\n"
"v_exp_f32 v191, v191\n"
"v_exp_f32 v192, v192\n"
"v_exp_f32 v193, v193\n"
"v_mfma_f32_16x16x32_bf16 a[160:163], a[56:59], v[212:215], a[160:163]\n"
"s_add_u32 m0, s30, 0x5000\n"
"ds_read_b64_tr_b16 a[10:11], v7 offset:768\n"
"v_cvt_pk_bf16_f32 v223, v190, v191\n"
"v_exp_f32 v194, v194\n"
"v_exp_f32 v195, v195\n"
"v_exp_f32 v152, v152\n"
"v_mfma_f32_16x16x32_bf16 a[192:195], a[56:59], a[116:119], a[192:195]\n"
"s_waitcnt lgkmcnt(10)\n"
"ds_read_b64_tr_b16 v[56:57], v142 offset:12800\n"
"ds_read_b64_tr_b16 v[58:59], v142 offset:14848\n"
"v_exp_f32 v153, v153\n"
"v_exp_f32 v154, v154\n"
"v_exp_f32 v155, v155\n"
"v_mfma_f32_16x16x32_bf16 a[132:135], a[60:63], v[208:211], a[132:135]\n"
"buffer_load_dwordx4 v23, s[52:55], s21 offen offset:0 lds\n"
"ds_read_b64_tr_b16 a[12:13], v6 offset:512\n"
"v_cvt_pk_bf16_f32 v224, v192, v193\n"
"v_cvt_pk_bf16_f32 v225, v194, v195\n"
"v_cvt_pk_bf16_f32 v226, v152, v153\n"
"v_mfma_f32_16x16x32_bf16 a[164:167], a[60:63], v[212:215], a[164:167]\n"
"ds_read_b64_tr_b16 a[14:15], v8 offset:768\n"
"ds_read_b64_tr_b16 v[60:61], v143 offset:12800\n"
"v_cvt_pk_bf16_f32 v227, v154, v155\n"
"v_exp_f32 v196, v196\n"
"v_exp_f32 v197, v197\n"
"v_mfma_f32_16x16x32_bf16 a[196:199], a[60:63], a[116:119], a[196:199]\n"
"ds_read_b64_tr_b16 v[62:63], v143 offset:14848\n"
"ds_read_b64_tr_b16 a[16:17], v5 offset:768\n"
"v_exp_f32 v198, v198\n"
"v_mfma_f32_16x16x32_bf16 a[200:203], v[228:231], v[216:219], a[200:203]\n"
"buffer_load_dwordx4 v24, s[52:55], s21 offen offset:1024 lds\n"
"ds_read_b64_tr_b16 a[18:19], v7 offset:512\n"
"v_exp_f32 v199, v199\n"
"v_mfma_f32_16x16x32_bf16 a[204:207], v[228:231], v[220:223], a[204:207]\n"
"s_waitcnt lgkmcnt(8)\n"
"ds_read_b64_tr_b16 a[20:21], v6 offset:768\n"
"ds_read_b64_tr_b16 a[22:23], v8 offset:512\n"
"v_cvt_pk_bf16_f32 v200, v196, v197\n"
"v_mfma_f32_16x16x32_bf16 a[136:139], a[0:3], v[216:219], a[136:139]\n"
"s_add_u32 s75, s21, 0x7800\n"
"ds_read_b64_tr_b16 a[24:25], v5 offset:0\n"
"ds_read_b64_tr_b16 a[26:27], v7 offset:256\n"
"v_cvt_pk_bf16_f32 v201, v198, v199\n"
"v_mfma_f32_16x16x32_bf16 a[168:171], a[0:3], v[220:223], a[168:171]\n"
"buffer_load_dwordx4 v23, s[52:55], s75 offen offset:2048 lds\n"
"ds_read_b64 a[112:113], v13 offset:38920\n"
"v_mfma_f32_16x16x32_bf16 a[140:143], a[4:7], v[216:219], a[140:143]\n"
"buffer_load_dwordx2 v[28:29], v30, s[64:67], s38 offen offset:0 sc1\n"
"ds_read_b64 a[114:115], v14 offset:38920\n"
"v_mfma_f32_16x16x32_bf16 a[172:175], a[4:7], v[220:223], a[172:175]\n"
"ds_read_b64_tr_b16 a[28:29], v6 offset:0\n"
"ds_read_b64_tr_b16 a[30:31], v8 offset:256\n"
"v_mfma_f32_16x16x32_bf16 a[144:147], a[8:11], v[216:219], a[144:147]\n"
"s_waitcnt vmcnt(17)\n"
"ds_read_b64_tr_b16 v[84:85], v142 offset:4096\n"
"ds_read_b64_tr_b16 v[86:87], v142 offset:6144\n"
"v_mfma_f32_16x16x32_bf16 a[176:179], a[8:11], v[220:223], a[176:179]\n"
"s_waitcnt lgkmcnt(13)\n"
"buffer_load_dwordx4 v24, s[52:55], s75 offen offset:3072 lds\n"
"ds_read_b64_tr_b16 v[92:93], v143 offset:4096\n"
"v_mfma_f32_16x16x32_bf16 a[148:151], a[12:15], v[216:219], a[148:151]\n"
"s_add_u32 m0, s30, 0x3000\n"
"ds_read_b64_tr_b16 v[94:95], v143 offset:6144\n"
"ds_read_b64_tr_b16 v[100:101], v142 offset:4608\n"
"v_mfma_f32_16x16x32_bf16 a[180:183], a[12:15], v[220:223], a[180:183]\n"
"s_waitcnt lgkmcnt(13)\n"
"ds_read_b64_tr_b16 a[32:33], v9 offset:768\n"
"ds_read_b64_tr_b16 a[34:35], v11 offset:512\n"
"v_mfma_f32_16x16x32_bf16 a[152:155], a[16:19], v[216:219], a[152:155]\n"
"buffer_load_dwordx4 v140, s[52:55], s19 offen offset:0 lds\n"
"ds_read_b64_tr_b16 v[102:103], v142 offset:6656\n"
"v_mfma_f32_16x16x32_bf16 a[184:187], a[16:19], v[220:223], a[184:187]\n"
"s_waitcnt lgkmcnt(14)\n"
"ds_read_b64_tr_b16 v[108:109], v143 offset:4608\n"
"ds_read_b64_tr_b16 a[36:37], v10 offset:768\n"
"v_mfma_f32_16x16x32_bf16 a[156:159], a[20:23], v[216:219], a[156:159]\n"
"ds_read_b64_tr_b16 a[38:39], v12 offset:512\n"
"ds_read_b64_tr_b16 v[110:111], v143 offset:6656\n"
"v_mfma_f32_16x16x32_bf16 a[188:191], a[20:23], v[220:223], a[188:191]\n"
"s_waitcnt lgkmcnt(15)\n"
"buffer_load_dwordx4 v141, s[52:55], s19 offen offset:1024 lds\n"
"ds_read_b64_tr_b16 a[40:41], v9 offset:0\n"
"v_mfma_f32_16x16x32_bf16 a[128:131], a[24:27], v[216:219], a[128:131]\n"
"ds_read_b64_tr_b16 a[42:43], v11 offset:256\n"
"ds_read_b64_tr_b16 a[44:45], v10 offset:0\n"
"v_mfma_f32_16x16x32_bf16 a[160:163], a[24:27], v[220:223], a[160:163]\n"
"s_waitcnt lgkmcnt(15)\n"
"ds_read_b64_tr_b16 a[46:47], v12 offset:256\n"
"v_exp_f32 v156, v156\n"
"v_mfma_f32_16x16x32_bf16 a[192:195], a[24:27], a[112:115], a[192:195]\n"
"ds_read_b64_tr_b16 a[48:49], v9 offset:256\n"
"v_exp_f32 v157, v157\n"
"v_mfma_f32_16x16x32_bf16 a[132:135], a[28:31], v[216:219], a[132:135]\n"
"buffer_load_dwordx4 v140, s[52:55], s77 offen offset:2048 lds\n"
"ds_read_b64_tr_b16 a[50:51], v11 offset:0\n"
"v_cvt_pk_bf16_f32 v202, v156, v157\n"
"v_mfma_f32_16x16x32_bf16 a[164:167], a[28:31], v[220:223], a[164:167]\n"
"ds_read_b64_tr_b16 a[52:53], v10 offset:256\n"
"v_exp_f32 v158, v158\n"
"v_mfma_f32_16x16x32_bf16 a[196:199], a[28:31], a[112:115], a[196:199]\n"
"ds_read_b64_tr_b16 a[54:55], v12 offset:0\n"
"v_exp_f32 v159, v159\n"
"v_mfma_f32_16x16x32_bf16 a[200:203], v[228:231], v[224:227], a[200:203]\n"
"buffer_load_dwordx4 v141, s[52:55], s77 offen offset:3072 lds\n"
"v_cvt_pk_bf16_f32 v203, v158, v159\n"
"ds_read_b64_tr_b16 a[56:57], v9 offset:512\n"
"ds_read_b64_tr_b16 a[58:59], v11 offset:768\n"
"v_mfma_f32_16x16x32_bf16 a[204:207], v[228:231], v[200:203], a[204:207]\n"
"s_waitcnt lgkmcnt(10)\n"
"ds_read_b64 a[116:117], v15 offset:38920\n"
"ds_read_b64 a[118:119], v16 offset:38920\n"
"v_mfma_f32_16x16x32_bf16 a[136:139], a[32:35], v[224:227], a[136:139]\n"
"ds_read_b64_tr_b16 a[60:61], v10 offset:512\n"
"ds_read_b64_tr_b16 a[62:63], v12 offset:768\n"
"v_mfma_f32_16x16x32_bf16 a[168:171], a[32:35], v[200:203], a[168:171]\n"
"s_add_u32 m0, s30, 0x6000\n"
"ds_read_b32 v244, v13 offset:39936\n"
"ds_read_b32 v245, v14 offset:39936\n"
"v_mfma_f32_16x16x32_bf16 a[140:143], a[36:39], v[224:227], a[140:143]\n"
"buffer_load_dwordx4 v140, s[52:55], s21 offen offset:0 lds\n"
"ds_read_b32 v246, v15 offset:39936\n"
"v_mfma_f32_16x16x32_bf16 a[172:175], a[36:39], v[200:203], a[172:175]\n"
"s_waitcnt lgkmcnt(12)\n"
"ds_read_b32 v247, v16 offset:39936\n"
"v_accvgpr_read_b32 v252, a200\n"
"v_mfma_f32_16x16x32_bf16 a[144:147], a[40:43], v[224:227], a[144:147]\n"
"v_accvgpr_read_b32 v253, a204\n"
"v_add_f32 v248, v252, v253\n"
"v_mfma_f32_16x16x32_bf16 a[176:179], a[40:43], v[200:203], a[176:179]\n"
"buffer_load_dwordx4 v141, s[52:55], s21 offen offset:1024 lds\n"
"v_mfma_f32_16x16x32_bf16 a[148:151], a[44:47], v[224:227], a[148:151]\n"
"v_mfma_f32_16x16x32_bf16 a[180:183], a[44:47], v[200:203], a[180:183]\n"
"s_waitcnt lgkmcnt(10)\n"
"v_mfma_f32_16x16x32_bf16 a[152:155], a[48:51], v[224:227], a[152:155]\n"
"buffer_load_dwordx4 v140, s[52:55], s75 offen offset:2048 lds\n"
"v_mfma_f32_16x16x32_bf16 a[184:187], a[48:51], v[200:203], a[184:187]\n"
"v_mfma_f32_16x16x32_bf16 a[156:159], a[52:55], v[224:227], a[156:159]\n"
"s_waitcnt lgkmcnt(2)\n"
"v_add_f32 v244, v244, v245\n"
"v_mfma_f32_16x16x32_bf16 a[188:191], a[52:55], v[200:203], a[188:191]\n"
"s_waitcnt lgkmcnt(0)\n"
"v_add_f32 v246, v246, v247\n"
"v_add_f32 v255, v244, v246\n"
"v_mfma_f32_16x16x32_bf16 a[128:131], a[56:59], v[224:227], a[128:131]\n"
"buffer_load_dwordx4 v141, s[52:55], s75 offen offset:3072 lds\n"
"v_add_f32 v248, v248, v255\n"
"v_mfma_f32_16x16x32_bf16 a[160:163], a[56:59], v[200:203], a[160:163]\n"
"v_cmp_nge_f32 vcc, s97, v248\n"
"v_mfma_f32_16x16x32_bf16 a[192:195], a[56:59], a[116:119], a[192:195]\n"
"s_add_u32 s76, s13, 0x40000\n"
"s_add_u32 m0, s30, 0x1000\n"
"v_mfma_f32_16x16x32_bf16 a[132:135], a[60:63], v[224:227], a[132:135]\n"
"buffer_load_dwordx4 v23, s[52:55], s76 offen offset:0 lds\n"
"v_mfma_f32_16x16x32_bf16 a[164:167], a[60:63], v[200:203], a[164:167]\n"
"v_mfma_f32_16x16x32_bf16 a[196:199], a[60:63], a[116:119], a[196:199]\n"
"s_nop 0\n"
"s_nop 0\n"
"s_nop 0\n"
"v_mfma_f32_16x16x32_bf16 v[152:155], v[32:35], v[80:83], 0\n"
"s_nop 0\n"
"s_nop 0\n"
"s_nop 0\n"
"v_mfma_f32_16x16x32_bf16 a[208:211], v[36:39], v[80:83], 0\n"
"buffer_load_dwordx4 v24, s[52:55], s76 offen offset:1024 lds\n"
"v_mfma_f32_16x16x32_bf16 a[212:215], v[40:43], v[80:83], 0\n"
"s_cbranch_vccnz .Lmha_SLOW1\n"
".Lmha_SLOWRET1:\n"
"v_mfma_f32_16x16x32_bf16 a[216:219], v[44:47], v[80:83], 0\n"
"s_add_u32 s96, s13, 0x47800\n"
"v_rcp_f32 v252, v252\n"
"v_mfma_f32_16x16x32_bf16 v[188:191], v[48:51], v[80:83], 0\n"
"buffer_load_dwordx4 v23, s[52:55], s96 offen offset:2048 lds\n"
"v_accvgpr_read_b32 v244, a128\n"
"v_mfma_f32_16x16x32_bf16 a[220:223], v[52:55], v[80:83], 0\n"
"v_accvgpr_read_b32 v245, a129\n"
"v_mul_f32 v244, v252, v244\n"
"v_mfma_f32_16x16x32_bf16 a[224:227], v[56:59], v[80:83], 0\n"
"v_mul_f32 v245, v252, v245\n"
"v_accvgpr_read_b32 v246, a130\n"
"v_mfma_f32_16x16x32_bf16 a[228:231], v[60:63], v[80:83], 0\n"
"buffer_load_dwordx4 v24, s[52:55], s96 offen offset:3072 lds\n"
"v_accvgpr_read_b32 v247, a131\n"
"v_mfma_f32_16x16x32_bf16 v[156:159], v[32:35], v[88:91], 0\n"
"s_lshl_b32 s68, s11, 10\n"
"v_mul_f32 v246, v252, v246\n"
"v_mul_f32 v247, v252, v247\n"
"v_mfma_f32_16x16x32_bf16 a[232:235], v[36:39], v[88:91], 0\n"
"v_accvgpr_read_b32 v248, a132\n"
"v_accvgpr_read_b32 v249, a133\n"
"v_mfma_f32_16x16x32_bf16 a[236:239], v[40:43], v[88:91], 0\n"
"v_accvgpr_read_b32 v250, a134\n"
"v_accvgpr_read_b32 v251, a135\n"
"v_mfma_f32_16x16x32_bf16 a[240:243], v[44:47], v[88:91], 0\n"
"v_cvt_pk_bf16_f32 v244, v244, v245\n"
"v_mul_f32 v248, v252, v248\n"
"v_mfma_f32_16x16x32_bf16 v[192:195], v[48:51], v[88:91], 0\n"
"v_mul_f32 v249, v252, v249\n"
"v_mul_f32 v250, v252, v250\n"
"v_mfma_f32_16x16x32_bf16 a[244:247], v[52:55], v[88:91], 0\n"
"v_mul_f32 v251, v252, v251\n"
"v_cvt_pk_bf16_f32 v245, v246, v247\n"
"v_mfma_f32_16x16x32_bf16 a[248:251], v[56:59], v[88:91], 0\n"
"v_cvt_pk_bf16_f32 v246, v248, v249\n"
"v_cvt_pk_bf16_f32 v247, v250, v251\n"
"v_mfma_f32_16x16x32_bf16 v[224:227], v[60:63], v[88:91], 0\n"
"v_accvgpr_write_b32 a132, v244\n"
"v_accvgpr_write_b32 a133, v245\n"
"v_mfma_f32_16x16x32_bf16 v[160:163], v[32:35], v[96:99], 0\n"
"v_accvgpr_write_b32 a134, v246\n"
"v_accvgpr_write_b32 a135, v247\n"
"v_mfma_f32_16x16x32_bf16 a[252:255], v[36:39], v[96:99], 0\n"
"s_waitcnt vmcnt(13)\n"
"v_cmp_ne_u64 s[36:37], s[86:87], v[28:29]\n"
"v_accvgpr_read_b32 v244, a136\n"
"v_mfma_f32_16x16x32_bf16 a[0:3], v[40:43], v[96:99], 0\n"
"s_cmp_lg_u64 s[36:37], 0\n"
"s_cbranch_scc1 .Lmha_SPA\n"
".Lmha_SPAR:\n"
"v_accvgpr_read_b32 v245, a137\n"
"v_mul_f32 v244, v252, v244\n"
"v_mfma_f32_16x16x32_bf16 a[4:7], v[44:47], v[96:99], 0\n"
"ds_read_b64_tr_b16 a[136:137], v142 offset:32768\n"
"v_mul_f32 v245, v252, v245\n"
"v_mfma_f32_16x16x32_bf16 v[196:199], v[48:51], v[96:99], 0\n"
"v_accvgpr_read_b32 v246, a138\n"
"v_accvgpr_read_b32 v247, a139\n"
"v_mfma_f32_16x16x32_bf16 v[228:231], v[52:55], v[96:99], 0\n"
"ds_read_b64_tr_b16 a[138:139], v142 offset:34816\n"
"v_mul_f32 v246, v252, v246\n"
"v_mfma_f32_16x16x32_bf16 v[232:235], v[56:59], v[96:99], 0\n"
"v_mul_f32 v247, v252, v247\n"
"v_accvgpr_read_b32 v248, a140\n"
"buffer_load_dwordx4 v[116:119], v21, s[56:59], s14 offen offset:0 sc1\n"
"v_mfma_f32_16x16x32_bf16 v[236:239], v[60:63], v[96:99], 0\n"
"v_accvgpr_read_b32 v249, a141\n"
"v_accvgpr_read_b32 v250, a142\n"
"buffer_load_dwordx4 v[124:127], v21, s[56:59], s16 offen offset:0 sc1\n"
"v_mfma_f32_16x16x32_bf16 v[164:167], v[32:35], v[104:107], 0\n"
"v_accvgpr_read_b32 v251, a143\n"
"v_cvt_pk_bf16_f32 v244, v244, v245\n"
"v_mfma_f32_16x16x32_bf16 a[8:11], v[36:39], v[104:107], 0\n"
"v_mul_f32 v248, v252, v248\n"
"v_mul_f32 v249, v252, v249\n"
"v_mfma_f32_16x16x32_bf16 a[12:15], v[40:43], v[104:107], 0\n"
"v_mul_f32 v250, v252, v250\n"
"v_mul_f32 v251, v252, v251\n"
"v_mfma_f32_16x16x32_bf16 a[16:19], v[44:47], v[104:107], 0\n"
"v_cvt_pk_bf16_f32 v245, v246, v247\n"
"v_cvt_pk_bf16_f32 v246, v248, v249\n"
"v_mfma_f32_16x16x32_bf16 v[200:203], v[48:51], v[104:107], 0\n"
"v_cvt_pk_bf16_f32 v247, v250, v251\n"
"v_accvgpr_write_b32 a140, v244\n"
"v_mfma_f32_16x16x32_bf16 v[240:243], v[52:55], v[104:107], 0\n"
"v_accvgpr_write_b32 a141, v245\n"
"v_accvgpr_write_b32 a142, v246\n"
"v_mfma_f32_16x16x32_bf16 v[132:135], v[56:59], v[104:107], 0\n"
"v_accvgpr_write_b32 a143, v247\n"
"v_add_u32 v1, s68, v13\n"
"v_mfma_f32_16x16x32_bf16 v[148:151], v[60:63], v[104:107], 0\n"
"v_add_u32 v2, s68, v14\n"
"v_add_u32 v3, s68, v15\n"
"v_mfma_f32_16x16x32_bf16 v[168:171], v[32:35], v[112:115], 0\n"
"v_add_u32 v4, s68, v16\n"
"v_accvgpr_read_b32 v244, a144\n"
"v_mfma_f32_16x16x32_bf16 a[20:23], v[36:39], v[112:115], 0\n"
"buffer_load_dwordx4 v[80:83], v21, s[56:59], s14 offen offset:1024 sc1\n"
"v_accvgpr_read_b32 v245, a145\n"
"v_mfma_f32_16x16x32_bf16 a[24:27], v[40:43], v[112:115], 0\n"
"ds_read_b64_tr_b16 a[144:145], v143 offset:32768\n"
"v_mul_f32 v244, v252, v244\n"
"v_mfma_f32_16x16x32_bf16 a[28:31], v[44:47], v[112:115], 0\n"
"v_mul_f32 v245, v252, v245\n"
"v_accvgpr_read_b32 v246, a146\n"
"v_mfma_f32_16x16x32_bf16 v[204:207], v[48:51], v[112:115], 0\n"
"buffer_load_dwordx4 v[88:91], v21, s[56:59], s16 offen offset:1024 sc1\n"
"v_accvgpr_read_b32 v247, a147\n"
"v_mfma_f32_16x16x32_bf16 a[32:35], v[52:55], v[112:115], 0\n"
"ds_read_b64_tr_b16 a[146:147], v143 offset:34816\n"
"v_mul_f32 v246, v252, v246\n"
"v_mfma_f32_16x16x32_bf16 a[36:39], v[56:59], v[112:115], 0\n"
"v_mul_f32 v247, v252, v247\n"
"v_accvgpr_read_b32 v248, a148\n"
"v_mfma_f32_16x16x32_bf16 a[40:43], v[60:63], v[112:115], 0\n"
"buffer_load_dwordx4 v[96:99], v21, s[56:59], s14 offen offset:2048 sc1\n"
"v_accvgpr_read_b32 v249, a149\n"
"v_mfma_f32_16x16x32_bf16 v[172:175], v[32:35], v[120:123], 0\n"
"v_accvgpr_read_b32 v250, a150\n"
"v_accvgpr_read_b32 v251, a151\n"
"v_mfma_f32_16x16x32_bf16 a[44:47], v[36:39], v[120:123], 0\n"
"v_cvt_pk_bf16_f32 v244, v244, v245\n"
"v_mul_f32 v248, v252, v248\n"
"v_mfma_f32_16x16x32_bf16 a[48:51], v[40:43], v[120:123], 0\n"
"buffer_load_dwordx4 v[104:107], v21, s[56:59], s16 offen offset:2048 sc1\n"
"v_mul_f32 v249, v252, v249\n"
"v_mfma_f32_16x16x32_bf16 a[52:55], v[44:47], v[120:123], 0\n"
"v_mul_f32 v250, v252, v250\n"
"v_mul_f32 v251, v252, v251\n"
"v_mfma_f32_16x16x32_bf16 v[208:211], v[48:51], v[120:123], 0\n"
"v_cvt_pk_bf16_f32 v245, v246, v247\n"
"v_cvt_pk_bf16_f32 v246, v248, v249\n"
"v_mfma_f32_16x16x32_bf16 a[56:59], v[52:55], v[120:123], 0\n"
"buffer_load_dwordx4 v[112:115], v21, s[56:59], s14 offen offset:3072 sc1\n"
"v_cvt_pk_bf16_f32 v247, v250, v251\n"
"v_mfma_f32_16x16x32_bf16 a[60:63], v[56:59], v[120:123], 0\n"
"s_barrier\n"
"v_accvgpr_write_b32 a148, v244\n"
"v_accvgpr_write_b32 a149, v245\n"
"v_mfma_f32_16x16x32_bf16 a[112:115], v[60:63], v[120:123], 0\n"
"v_accvgpr_write_b32 a150, v246\n"
"v_accvgpr_write_b32 a151, v247\n"
"v_mfma_f32_16x16x32_bf16 v[176:179], v[32:35], v[128:131], 0\n"
"buffer_load_dwordx4 v[120:123], v21, s[56:59], s16 offen offset:3072 sc1\n"
"v_accvgpr_read_b32 v244, a152\n"
"v_mfma_f32_16x16x32_bf16 a[116:119], v[36:39], v[128:131], 0\n"
"v_accvgpr_read_b32 v245, a153\n"
"v_mul_f32 v244, v252, v244\n"
"v_mfma_f32_16x16x32_bf16 a[120:123], v[40:43], v[128:131], 0\n"
"ds_read_b64_tr_b16 a[152:153], v142 offset:33280\n"
"v_mul_f32 v245, v252, v245\n"
"v_mfma_f32_16x16x32_bf16 a[124:127], v[44:47], v[128:131], 0\n"
"v_accvgpr_read_b32 v246, a154\n"
"v_accvgpr_read_b32 v247, a155\n"
"v_mfma_f32_16x16x32_bf16 v[212:215], v[48:51], v[128:131], 0\n"
"ds_read_b64_tr_b16 a[154:155], v142 offset:35328\n"
"v_mul_f32 v246, v252, v246\n"
"v_mfma_f32_16x16x32_bf16 a[200:203], v[52:55], v[128:131], 0\n"
"s_add_u32 s78, s79, 0x9000\n"
"v_mul_f32 v247, v252, v247\n"
"v_accvgpr_read_b32 v248, a156\n"
"v_mfma_f32_16x16x32_bf16 a[204:207], v[56:59], v[128:131], 0\n"
"buffer_store_dwordx4 a[132:135], v21, s[56:59], s78 offen offset:0 sc1\n"
"v_mfma_f32_16x16x32_bf16 a[64:67], v[60:63], v[128:131], 0\n"
"v_accvgpr_read_b32 v249, a157\n"
"v_accvgpr_read_b32 v250, a158\n"
"v_mfma_f32_16x16x32_bf16 v[180:183], v[32:35], v[136:139], 0\n"
"v_accvgpr_read_b32 v251, a159\n"
"v_cvt_pk_bf16_f32 v244, v244, v245\n"
"v_mfma_f32_16x16x32_bf16 a[68:71], v[36:39], v[136:139], 0\n"
"v_mul_f32 v248, v252, v248\n"
"v_mul_f32 v249, v252, v249\n"
"v_mfma_f32_16x16x32_bf16 a[72:75], v[40:43], v[136:139], 0\n"
"ds_write_b128 v1, a[132:135] offset:0\n"
"v_mfma_f32_16x16x32_bf16 a[76:79], v[44:47], v[136:139], 0\n"
"v_mul_f32 v250, v252, v250\n"
"v_mul_f32 v251, v252, v251\n"
"v_mfma_f32_16x16x32_bf16 v[216:219], v[48:51], v[136:139], 0\n"
"v_cvt_pk_bf16_f32 v245, v246, v247\n"
"v_cvt_pk_bf16_f32 v246, v248, v249\n"
"v_mfma_f32_16x16x32_bf16 a[80:83], v[52:55], v[136:139], 0\n"
"v_cvt_pk_bf16_f32 v247, v250, v251\n"
"v_accvgpr_write_b32 a156, v244\n"
"v_mfma_f32_16x16x32_bf16 a[84:87], v[56:59], v[136:139], 0\n"
"v_accvgpr_write_b32 a157, v245\n"
"v_accvgpr_write_b32 a158, v246\n"
"v_mfma_f32_16x16x32_bf16 a[88:91], v[60:63], v[136:139], 0\n"
"v_accvgpr_write_b32 a159, v247\n"
"v_accvgpr_read_b32 v244, a160\n"
"v_mfma_f32_16x16x32_bf16 v[184:187], v[32:35], v[144:147], 0\n"
"ds_read_b64_tr_b16 v[32:33], v142 offset:8192\n"
"ds_read_b64_tr_b16 v[34:35], v142 offset:10240\n"
"v_mfma_f32_16x16x32_bf16 a[92:95], v[36:39], v[144:147], 0\n"
"s_add_u32 s84, s14, 0x1000\n"
"ds_read_b64_tr_b16 v[36:37], v143 offset:8192\n"
"ds_read_b64_tr_b16 v[38:39], v143 offset:10240\n"
"v_mfma_f32_16x16x32_bf16 a[96:99], v[40:43], v[144:147], 0\n"
"buffer_load_dwordx4 v[128:131], v21, s[56:59], s84 offen offset:0 sc1\n"
"ds_read_b64_tr_b16 v[40:41], v142 offset:8704\n"
"v_mfma_f32_16x16x32_bf16 a[100:103], v[44:47], v[144:147], 0\n"
"ds_read_b64_tr_b16 v[42:43], v142 offset:10752\n"
"ds_read_b64_tr_b16 v[44:45], v143 offset:8704\n"
"v_mfma_f32_16x16x32_bf16 v[220:223], v[48:51], v[144:147], 0\n"
"s_add_u32 s77, s16, 0x1000\n"
"ds_read_b64_tr_b16 v[48:49], v142 offset:28672\n"
"ds_read_b64_tr_b16 v[50:51], v142 offset:30720\n"
"v_mfma_f32_16x16x32_bf16 a[104:107], v[52:55], v[144:147], 0\n"
"buffer_load_dwordx4 v[136:139], v21, s[56:59], s77 offen offset:0 sc1\n"
"ds_read_b64_tr_b16 v[52:53], v143 offset:28672\n"
"v_mfma_f32_16x16x32_bf16 a[108:111], v[56:59], v[144:147], 0\n"
"ds_read_b64_tr_b16 v[54:55], v143 offset:30720\n"
"ds_read_b64_tr_b16 v[56:57], v142 offset:29184\n"
"v_mfma_f32_16x16x32_bf16 a[128:131], v[60:63], v[144:147], 0\n"
"s_waitcnt vmcnt(10)\n"
"ds_read_b64_tr_b16 v[58:59], v142 offset:31232\n"
"ds_read_b64_tr_b16 v[46:47], v143 offset:10752\n"
"v_mfma_f32_16x16x32_bf16 v[152:155], v[64:67], v[116:119], v[152:155]\n"
"s_waitcnt vmcnt(9)\n"
"buffer_load_dwordx4 v[144:147], v21, s[56:59], s84 offen offset:1024 sc1\n"
"ds_read_b64_tr_b16 v[60:61], v143 offset:29184\n"
"v_mfma_f32_16x16x32_bf16 v[152:155], v[84:87], v[124:127], v[152:155]\n"
"ds_read_b64_tr_b16 v[62:63], v143 offset:31232\n"
"v_accvgpr_read_b32 v245, a161\n"
"v_mfma_f32_16x16x32_bf16 a[208:211], v[68:71], v[116:119], a[208:211]\n"
"s_add_u32 m0, s30, 0x4000\n"
"v_rcp_f32 v253, v253\n"
"v_mfma_f32_16x16x32_bf16 a[208:211], v[92:95], v[124:127], a[208:211]\n"
"buffer_load_dwordx4 v23, s[52:55], s23 offen offset:0 lds\n"
"v_mul_f32 v244, v253, v244\n"
"v_mfma_f32_16x16x32_bf16 a[212:215], v[72:75], v[116:119], a[212:215]\n"
"ds_read_b64_tr_b16 a[160:161], v143 offset:33280\n"
"v_mul_f32 v245, v253, v245\n"
"v_mfma_f32_16x16x32_bf16 a[212:215], v[100:103], v[124:127], a[212:215]\n"
"s_add_u32 s75, s80, 0x9000\n"
"v_accvgpr_read_b32 v246, a162\n"
"v_accvgpr_read_b32 v247, a163\n"
"v_mfma_f32_16x16x32_bf16 a[216:219], v[76:79], v[116:119], a[216:219]\n"
"buffer_store_dwordx4 a[140:143], v21, s[56:59], s75 offen offset:0 sc1\n"
"v_mfma_f32_16x16x32_bf16 a[216:219], v[108:111], v[124:127], a[216:219]\n"
"s_waitcnt lgkmcnt(8)\n"
"ds_read_b64_tr_b16 a[162:163], v143 offset:35328\n"
"v_mul_f32 v246, v253, v246\n"
"v_mfma_f32_16x16x32_bf16 v[188:191], v[32:35], v[116:119], v[188:191]\n"
"v_mul_f32 v247, v253, v247\n"
"v_accvgpr_read_b32 v248, a164\n"
"v_accvgpr_read_b32 v249, a165\n"
"v_mfma_f32_16x16x32_bf16 v[188:191], v[48:51], v[124:127], v[188:191]\n"
"v_accvgpr_read_b32 v250, a166\n"
"v_accvgpr_read_b32 v251, a167\n"
"v_mfma_f32_16x16x32_bf16 a[220:223], v[36:39], v[116:119], a[220:223]\n"
"s_waitcnt lgkmcnt(4)\n"
"ds_write_b128 v2, a[140:143] offset:0\n"
"v_mfma_f32_16x16x32_bf16 a[220:223], v[52:55], v[124:127], a[220:223]\n"
"v_cvt_pk_bf16_f32 v244, v244, v245\n"
"v_mul_f32 v248, v253, v248\n"
"v_mfma_f32_16x16x32_bf16 a[224:227], v[40:43], v[116:119], a[224:227]\n"
"v_mul_f32 v249, v253, v249\n"
"v_mul_f32 v250, v253, v250\n"
"v_mfma_f32_16x16x32_bf16 a[224:227], v[56:59], v[124:127], a[224:227]\n"
"v_mul_f32 v251, v253, v251\n"
"v_cvt_pk_bf16_f32 v245, v246, v247\n"
"v_mfma_f32_16x16x32_bf16 a[228:231], v[44:47], v[116:119], a[228:231]\n"
"s_waitcnt lgkmcnt(2)\n"
"v_cvt_pk_bf16_f32 v246, v248, v249\n"
"v_cvt_pk_bf16_f32 v247, v250, v251\n"
"v_mfma_f32_16x16x32_bf16 a[228:231], v[60:63], v[124:127], a[228:231]\n"
"s_waitcnt vmcnt(11)\n"
"v_accvgpr_write_b32 a164, v244\n"
"v_accvgpr_write_b32 a165, v245\n"
"v_mfma_f32_16x16x32_bf16 v[156:159], v[64:67], v[80:83], v[156:159]\n"
"s_waitcnt vmcnt(10)\n"
"v_accvgpr_write_b32 a166, v246\n"
"v_accvgpr_write_b32 a167, v247\n"
"v_mfma_f32_16x16x32_bf16 v[156:159], v[84:87], v[88:91], v[156:159]\n"
"v_accvgpr_read_b32 v244, a168\n"
"v_accvgpr_read_b32 v245, a169\n"
"v_mfma_f32_16x16x32_bf16 a[232:235], v[68:71], v[80:83], a[232:235]\n"
"buffer_load_dwordx4 v24, s[52:55], s23 offen offset:1024 lds\n"
"v_mul_f32 v244, v253, v244\n"
"v_mfma_f32_16x16x32_bf16 a[232:235], v[92:95], v[88:91], a[232:235]\n"
"ds_read_b64_tr_b16 a[168:169], v142 offset:20480\n"
"v_mul_f32 v245, v253, v245\n"
"v_mfma_f32_16x16x32_bf16 a[236:239], v[72:75], v[80:83], a[236:239]\n"
"s_add_u32 s76, s23, 0x7800\n"
"v_accvgpr_read_b32 v246, a170\n"
"v_accvgpr_read_b32 v247, a171\n"
"v_accvgpr_read_b32 v248, a172\n"
"v_mfma_f32_16x16x32_bf16 a[236:239], v[100:103], v[88:91], a[236:239]\n"
"buffer_load_dwordx4 v23, s[52:55], s76 offen offset:2048 lds\n"
"v_mul_f32 v246, v253, v246\n"
"v_mfma_f32_16x16x32_bf16 a[240:243], v[76:79], v[80:83], a[240:243]\n"
"ds_read_b64_tr_b16 a[170:171], v142 offset:22528\n"
"v_mul_f32 v247, v253, v247\n"
"v_mfma_f32_16x16x32_bf16 a[240:243], v[108:111], v[88:91], a[240:243]\n"
"v_accvgpr_read_b32 v249, a173\n"
"v_accvgpr_read_b32 v250, a174\n"
"v_accvgpr_read_b32 v251, a175\n"
"v_mfma_f32_16x16x32_bf16 v[192:195], v[32:35], v[80:83], v[192:195]\n"
"buffer_load_dwordx4 v24, s[52:55], s76 offen offset:3072 lds\n"
"v_cvt_pk_bf16_f32 v244, v244, v245\n"
"v_mfma_f32_16x16x32_bf16 v[192:195], v[48:51], v[88:91], v[192:195]\n"
"v_mul_f32 v248, v253, v248\n"
"v_mul_f32 v249, v253, v249\n"
"v_mfma_f32_16x16x32_bf16 a[244:247], v[36:39], v[80:83], a[244:247]\n"
"v_mul_f32 v250, v253, v250\n"
"v_mul_f32 v251, v253, v251\n"
"v_mfma_f32_16x16x32_bf16 a[244:247], v[52:55], v[88:91], a[244:247]\n"
"buffer_load_dwordx4 v[116:119], v21, s[56:59], s77 offen offset:1024 sc1\n"
"v_cvt_pk_bf16_f32 v245, v246, v247\n"
"v_mfma_f32_16x16x32_bf16 a[248:251], v[40:43], v[80:83], a[248:251]\n"
"v_cvt_pk_bf16_f32 v246, v248, v249\n"
"v_cvt_pk_bf16_f32 v247, v250, v251\n"
"v_mfma_f32_16x16x32_bf16 a[248:251], v[56:59], v[88:91], a[248:251]\n"
"s_add_u32 s96, s81, 0x9000\n"
"v_accvgpr_write_b32 a172, v244\n"
"v_accvgpr_write_b32 a173, v245\n"
"v_mfma_f32_16x16x32_bf16 v[224:227], v[44:47], v[80:83], v[224:227]\n"
"buffer_store_dwordx4 a[148:151], v21, s[56:59], s96 offen offset:0 sc1\n"
"v_mfma_f32_16x16x32_bf16 v[224:227], v[60:63], v[88:91], v[224:227]\n"
"s_waitcnt vmcnt(14)\n"
"v_accvgpr_write_b32 a174, v246\n"
"v_accvgpr_write_b32 a175, v247\n"
"v_mfma_f32_16x16x32_bf16 v[160:163], v[64:67], v[96:99], v[160:163]\n"
"s_waitcnt vmcnt(13)\n"
"v_accvgpr_read_b32 v244, a176\n"
"v_accvgpr_read_b32 v245, a177\n"
"v_mfma_f32_16x16x32_bf16 v[160:163], v[84:87], v[104:107], v[160:163]\n"
"v_mul_f32 v244, v253, v244\n"
"v_mul_f32 v245, v253, v245\n"
"v_accvgpr_read_b32 v246, a178\n"
"v_mfma_f32_16x16x32_bf16 a[252:255], v[68:71], v[96:99], a[252:255]\n"
"ds_write_b128 v3, a[148:151] offset:0\n"
"v_mfma_f32_16x16x32_bf16 a[252:255], v[92:95], v[104:107], a[252:255]\n"
"v_accvgpr_read_b32 v247, a179\n"
"v_mul_f32 v246, v253, v246\n"
"v_accvgpr_read_b32 v248, a180\n"
"v_mfma_f32_16x16x32_bf16 a[0:3], v[72:75], v[96:99], a[0:3]\n"
"v_mul_f32 v247, v253, v247\n"
"v_accvgpr_read_b32 v249, a181\n"
"v_mfma_f32_16x16x32_bf16 a[0:3], v[100:103], v[104:107], a[0:3]\n"
"v_accvgpr_read_b32 v250, a182\n"
"v_accvgpr_read_b32 v251, a183\n"
"v_mfma_f32_16x16x32_bf16 a[4:7], v[76:79], v[96:99], a[4:7]\n"
"v_cvt_pk_bf16_f32 v244, v244, v245\n"
"v_mul_f32 v248, v253, v248\n"
"v_mfma_f32_16x16x32_bf16 a[4:7], v[108:111], v[104:107], a[4:7]\n"
"v_mul_f32 v249, v253, v249\n"
"v_mul_f32 v250, v253, v250\n"
"v_mfma_f32_16x16x32_bf16 v[196:199], v[32:35], v[96:99], v[196:199]\n"
"v_mul_f32 v251, v253, v251\n"
"v_cvt_pk_bf16_f32 v245, v246, v247\n"
"v_mfma_f32_16x16x32_bf16 v[196:199], v[48:51], v[104:107], v[196:199]\n"
"v_cvt_pk_bf16_f32 v246, v248, v249\n"
"v_cvt_pk_bf16_f32 v247, v250, v251\n"
"v_mfma_f32_16x16x32_bf16 v[228:231], v[36:39], v[96:99], v[228:231]\n"
"buffer_load_dwordx4 v[124:127], v21, s[56:59], s84 offen offset:2048 sc1\n"
"v_accvgpr_write_b32 a180, v244\n"
"v_mfma_f32_16x16x32_bf16 v[228:231], v[52:55], v[104:107], v[228:231]\n"
"v_accvgpr_write_b32 a181, v245\n"
"v_accvgpr_write_b32 a182, v246\n"
"v_mfma_f32_16x16x32_bf16 v[232:235], v[40:43], v[96:99], v[232:235]\n"
"v_accvgpr_write_b32 a183, v247\n"
"v_accvgpr_read_b32 v244, a184\n"
"v_mfma_f32_16x16x32_bf16 v[232:235], v[56:59], v[104:107], v[232:235]\n"
"v_accvgpr_read_b32 v245, a185\n"
"v_mul_f32 v244, v253, v244\n"
"v_mfma_f32_16x16x32_bf16 v[236:239], v[44:47], v[96:99], v[236:239]\n"
"buffer_load_dwordx4 v[80:83], v21, s[56:59], s77 offen offset:2048 sc1\n"
"v_mul_f32 v245, v253, v245\n"
"v_accvgpr_read_b32 v246, a186\n"
"v_mfma_f32_16x16x32_bf16 v[236:239], v[60:63], v[104:107], v[236:239]\n"
"s_waitcnt vmcnt(14)\n"
"v_accvgpr_read_b32 v247, a187\n"
"v_mul_f32 v246, v253, v246\n"
"v_accvgpr_read_b32 v248, a188\n"
"v_mfma_f32_16x16x32_bf16 v[164:167], v[64:67], v[112:115], v[164:167]\n"
"s_waitcnt vmcnt(13)\n"
"v_mul_f32 v247, v253, v247\n"
"v_accvgpr_read_b32 v249, a189\n"
"v_mfma_f32_16x16x32_bf16 v[164:167], v[84:87], v[120:123], v[164:167]\n"
"buffer_load_dwordx4 v[88:91], v21, s[56:59], s84 offen offset:3072 sc1\n"
"v_accvgpr_read_b32 v250, a190\n"
"v_mfma_f32_16x16x32_bf16 a[8:11], v[68:71], v[112:115], a[8:11]\n"
"v_accvgpr_read_b32 v251, a191\n"
"v_cvt_pk_bf16_f32 v244, v244, v245\n"
"v_mfma_f32_16x16x32_bf16 a[8:11], v[92:95], v[120:123], a[8:11]\n"
"v_mul_f32 v248, v253, v248\n"
"v_mul_f32 v249, v253, v249\n"
"v_mfma_f32_16x16x32_bf16 a[12:15], v[72:75], v[112:115], a[12:15]\n"
"buffer_load_dwordx4 v[96:99], v21, s[56:59], s77 offen offset:3072 sc1\n"
"v_mul_f32 v250, v253, v250\n"
"v_mfma_f32_16x16x32_bf16 a[12:15], v[100:103], v[120:123], a[12:15]\n"
"s_add_u32 s78, s82, 0x9000\n"
"v_mul_f32 v251, v253, v251\n"
"v_cvt_pk_bf16_f32 v245, v246, v247\n"
"v_mfma_f32_16x16x32_bf16 a[16:19], v[76:79], v[112:115], a[16:19]\n"
"buffer_store_dwordx4 a[156:159], v21, s[56:59], s78 offen offset:0 sc1\n"
"v_mfma_f32_16x16x32_bf16 a[16:19], v[108:111], v[120:123], a[16:19]\n"
"v_cvt_pk_bf16_f32 v246, v248, v249\n"
"v_cvt_pk_bf16_f32 v247, v250, v251\n"
"v_mfma_f32_16x16x32_bf16 v[200:203], v[32:35], v[112:115], v[200:203]\n"
"v_accvgpr_write_b32 a188, v244\n"
"v_accvgpr_write_b32 a189, v245\n"
"v_mfma_f32_16x16x32_bf16 v[200:203], v[48:51], v[120:123], v[200:203]\n"
"v_accvgpr_write_b32 a190, v246\n"
"v_accvgpr_write_b32 a191, v247\n"
"v_mfma_f32_16x16x32_bf16 v[240:243], v[36:39], v[112:115], v[240:243]\n"
"ds_write_b128 v4, a[156:159] offset:0\n"
"v_mfma_f32_16x16x32_bf16 v[240:243], v[52:55], v[120:123], v[240:243]\n"
"v_rcp_f32 v255, v255\n"
"v_mfma_f32_16x16x32_bf16 v[132:135], v[40:43], v[112:115], v[132:135]\n"
"v_accvgpr_read_b32 v244, a192\n"
"v_accvgpr_read_b32 v245, a193\n"
"v_mfma_f32_16x16x32_bf16 v[132:135], v[56:59], v[120:123], v[132:135]\n"
"v_mul_f32 v244, v255, v244\n"
"v_mul_f32 v245, v255, v245\n"
"v_mfma_f32_16x16x32_bf16 v[148:151], v[44:47], v[112:115], v[148:151]\n"
"v_accvgpr_read_b32 v246, a194\n"
"v_accvgpr_read_b32 v247, a195\n"
"v_mfma_f32_16x16x32_bf16 v[148:151], v[60:63], v[120:123], v[148:151]\n"
"s_waitcnt vmcnt(14)\n"
"v_mul_f32 v246, v255, v246\n"
"v_mul_f32 v247, v255, v247\n"
"v_mfma_f32_16x16x32_bf16 v[168:171], v[64:67], v[128:131], v[168:171]\n"
"s_waitcnt vmcnt(13)\n"
"v_accvgpr_read_b32 v248, a196\n"
"v_accvgpr_read_b32 v249, a197\n"
"v_mfma_f32_16x16x32_bf16 v[168:171], v[84:87], v[136:139], v[168:171]\n"
"s_add_u32 s75, s14, 0x2000\n"
"v_accvgpr_read_b32 v250, a198\n"
"v_accvgpr_read_b32 v251, a199\n"
"v_mfma_f32_16x16x32_bf16 a[20:23], v[68:71], v[128:131], a[20:23]\n"
"buffer_load_dwordx4 v[104:107], v21, s[56:59], s75 offen offset:0 sc1\n"
"v_cvt_pk_bf16_f32 v244, v244, v245\n"
"v_mfma_f32_16x16x32_bf16 a[20:23], v[92:95], v[136:139], a[20:23]\n"
"v_mul_f32 v248, v255, v248\n"
"v_mul_f32 v249, v255, v249\n"
"v_mfma_f32_16x16x32_bf16 a[24:27], v[72:75], v[128:131], a[24:27]\n"
"v_mul_f32 v250, v255, v250\n"
"v_mul_f32 v251, v255, v251\n"
"v_mfma_f32_16x16x32_bf16 a[24:27], v[100:103], v[136:139], a[24:27]\n"
"s_add_u32 s76, s16, 0x2000\n"
"v_cvt_pk_bf16_f32 v245, v246, v247\n"
"v_cvt_pk_bf16_f32 v246, v248, v249\n"
"v_mfma_f32_16x16x32_bf16 a[28:31], v[76:79], v[128:131], a[28:31]\n"
"buffer_load_dwordx4 v[112:115], v21, s[56:59], s76 offen offset:0 sc1\n"
"v_cvt_pk_bf16_f32 v247, v250, v251\n"
"v_mfma_f32_16x16x32_bf16 a[28:31], v[108:111], v[136:139], a[28:31]\n"
"v_accvgpr_write_b32 a196, v244\n"
"v_accvgpr_write_b32 a197, v245\n"
"v_mfma_f32_16x16x32_bf16 v[204:207], v[32:35], v[128:131], v[204:207]\n"
"v_accvgpr_write_b32 a198, v246\n"
"v_accvgpr_write_b32 a199, v247\n"
"v_mfma_f32_16x16x32_bf16 v[204:207], v[48:51], v[136:139], v[204:207]\n"
"buffer_load_dwordx4 v[120:123], v21, s[56:59], s18 offen offset:0 sc1\n"
"v_mfma_f32_16x16x32_bf16 a[32:35], v[36:39], v[128:131], a[32:35]\n"
"v_mfma_f32_16x16x32_bf16 a[32:35], v[52:55], v[136:139], a[32:35]\n"
"v_mfma_f32_16x16x32_bf16 a[36:39], v[40:43], v[128:131], a[36:39]\n"
"buffer_load_dwordx4 a[176:179], v21, s[56:59], s20 offen offset:0 sc1\n"
"v_mfma_f32_16x16x32_bf16 a[36:39], v[56:59], v[136:139], a[36:39]\n"
"s_add_u32 s96, s79, 0x9000\n"
"v_mfma_f32_16x16x32_bf16 a[40:43], v[44:47], v[128:131], a[40:43]\n"
"buffer_store_dwordx4 a[164:167], v21, s[56:59], s96 offen offset:1024 sc1\n"
"v_mfma_f32_16x16x32_bf16 a[40:43], v[60:63], v[136:139], a[40:43]\n"
"s_waitcnt vmcnt(17)\n"
"v_mfma_f32_16x16x32_bf16 v[172:175], v[64:67], v[144:147], v[172:175]\n"
"s_waitcnt vmcnt(11)\n"
"v_mfma_f32_16x16x32_bf16 v[172:175], v[84:87], v[116:119], v[172:175]\n"
"v_mfma_f32_16x16x32_bf16 a[44:47], v[68:71], v[144:147], a[44:47]\n"
"ds_write_b128 v1, a[164:167] offset:36864\n"
"v_mfma_f32_16x16x32_bf16 a[44:47], v[92:95], v[116:119], a[44:47]\n"
"v_mfma_f32_16x16x32_bf16 a[48:51], v[72:75], v[144:147], a[48:51]\n"
"v_mfma_f32_16x16x32_bf16 a[48:51], v[100:103], v[116:119], a[48:51]\n"
"v_mfma_f32_16x16x32_bf16 a[52:55], v[76:79], v[144:147], a[52:55]\n"
"v_mfma_f32_16x16x32_bf16 a[52:55], v[108:111], v[116:119], a[52:55]\n"
"v_mfma_f32_16x16x32_bf16 v[208:211], v[32:35], v[144:147], v[208:211]\n"
"v_mfma_f32_16x16x32_bf16 v[208:211], v[48:51], v[116:119], v[208:211]\n"
"v_mfma_f32_16x16x32_bf16 a[56:59], v[36:39], v[144:147], a[56:59]\n"
"buffer_load_dwordx4 v[128:131], v21, s[56:59], s18 offen offset:1024 sc1\n"
"v_mfma_f32_16x16x32_bf16 a[56:59], v[52:55], v[116:119], a[56:59]\n"
"v_mfma_f32_16x16x32_bf16 a[60:63], v[40:43], v[144:147], a[60:63]\n"
"v_mfma_f32_16x16x32_bf16 a[60:63], v[56:59], v[116:119], a[60:63]\n"
"v_mfma_f32_16x16x32_bf16 a[112:115], v[44:47], v[144:147], a[112:115]\n"
"buffer_load_dwordx4 v[136:139], v21, s[56:59], s20 offen offset:1024 sc1\n"
"v_mfma_f32_16x16x32_bf16 a[112:115], v[60:63], v[116:119], a[112:115]\n"
"s_waitcnt vmcnt(11)\n"
"v_mfma_f32_16x16x32_bf16 v[176:179], v[64:67], v[124:127], v[176:179]\n"
"s_waitcnt vmcnt(10)\n"
"v_mfma_f32_16x16x32_bf16 v[176:179], v[84:87], v[80:83], v[176:179]\n"
"buffer_load_dwordx4 a[184:187], v21, s[56:59], s18 offen offset:2048 sc1\n"
"v_mfma_f32_16x16x32_bf16 a[116:119], v[68:71], v[124:127], a[116:119]\n"
"v_mfma_f32_16x16x32_bf16 a[116:119], v[92:95], v[80:83], a[116:119]\n"
"v_mfma_f32_16x16x32_bf16 a[120:123], v[72:75], v[124:127], a[120:123]\n"
"v_mfma_f32_16x16x32_bf16 a[120:123], v[100:103], v[80:83], a[120:123]\n"
"s_add_u32 s84, s80, 0x9000\n"
"v_mfma_f32_16x16x32_bf16 a[124:127], v[76:79], v[124:127], a[124:127]\n"
"buffer_store_dwordx4 a[172:175], v21, s[56:59], s84 offen offset:1024 sc1\n"
"v_mfma_f32_16x16x32_bf16 a[124:127], v[108:111], v[80:83], a[124:127]\n"
"v_mfma_f32_16x16x32_bf16 v[212:215], v[32:35], v[124:127], v[212:215]\n"
"v_mfma_f32_16x16x32_bf16 v[212:215], v[48:51], v[80:83], v[212:215]\n"
"v_mfma_f32_16x16x32_bf16 a[200:203], v[36:39], v[124:127], a[200:203]\n"
"ds_write_b128 v2, a[172:175] offset:36864\n"
"v_mfma_f32_16x16x32_bf16 a[200:203], v[52:55], v[80:83], a[200:203]\n"
"v_mfma_f32_16x16x32_bf16 a[204:207], v[40:43], v[124:127], a[204:207]\n"
"v_mfma_f32_16x16x32_bf16 a[204:207], v[56:59], v[80:83], a[204:207]\n"
"s_getpc_b64 s[98:99]\n"
"v_mfma_f32_16x16x32_bf16 a[64:67], v[44:47], v[124:127], a[64:67]\n"
"v_mfma_f32_16x16x32_bf16 a[64:67], v[60:63], v[80:83], a[64:67]\n"
"s_waitcnt vmcnt(11)\n"
"v_mfma_f32_16x16x32_bf16 v[180:183], v[64:67], v[88:91], v[180:183]\n"
"s_waitcnt vmcnt(10)\n"
"global_load_dword v18, v0, s[98:99] offset:1536\n"
"v_mfma_f32_16x16x32_bf16 v[180:183], v[84:87], v[96:99], v[180:183]\n"
"v_mfma_f32_16x16x32_bf16 a[68:71], v[68:71], v[88:91], a[68:71]\n"
"buffer_load_dwordx4 v[144:147], v21, s[56:59], s20 offen offset:2048 sc1\n"
"v_mfma_f32_16x16x32_bf16 a[68:71], v[92:95], v[96:99], a[68:71]\n"
"v_mfma_f32_16x16x32_bf16 a[72:75], v[72:75], v[88:91], a[72:75]\n"
"v_mfma_f32_16x16x32_bf16 a[72:75], v[100:103], v[96:99], a[72:75]\n"
"v_mfma_f32_16x16x32_bf16 a[76:79], v[76:79], v[88:91], a[76:79]\n"
"buffer_load_dwordx4 v[116:119], v21, s[56:59], s18 offen offset:3072 sc1\n"
"v_mfma_f32_16x16x32_bf16 a[76:79], v[108:111], v[96:99], a[76:79]\n"
"v_mfma_f32_16x16x32_bf16 v[216:219], v[32:35], v[88:91], v[216:219]\n"
"v_mfma_f32_16x16x32_bf16 v[216:219], v[48:51], v[96:99], v[216:219]\n"
"buffer_load_dwordx4 a[192:195], v21, s[56:59], s20 offen offset:3072 sc1\n"
"v_mfma_f32_16x16x32_bf16 a[80:83], v[36:39], v[88:91], a[80:83]\n"
"v_mfma_f32_16x16x32_bf16 a[80:83], v[52:55], v[96:99], a[80:83]\n"
"v_mfma_f32_16x16x32_bf16 a[84:87], v[40:43], v[88:91], a[84:87]\n"
"v_mfma_f32_16x16x32_bf16 a[84:87], v[56:59], v[96:99], a[84:87]\n"
"s_add_u32 s77, s81, 0x9000\n"
"v_mfma_f32_16x16x32_bf16 a[88:91], v[44:47], v[88:91], a[88:91]\n"
"buffer_store_dwordx4 a[180:183], v21, s[56:59], s77 offen offset:1024 sc1\n"
"v_mfma_f32_16x16x32_bf16 a[88:91], v[60:63], v[96:99], a[88:91]\n"
"s_waitcnt vmcnt(13)\n"
"v_mfma_f32_16x16x32_bf16 v[184:187], v[64:67], v[104:107], v[184:187]\n"
"s_waitcnt vmcnt(12)\n"
"ds_read_b64_tr_b16 v[64:65], v143 offset:20480\n"
"ds_read_b64_tr_b16 v[66:67], v143 offset:22528\n"
"v_mfma_f32_16x16x32_bf16 v[184:187], v[84:87], v[112:115], v[184:187]\n"
"ds_read_b64_tr_b16 v[84:85], v142 offset:20992\n"
"ds_read_b64_tr_b16 v[86:87], v142 offset:23040\n"
"v_mfma_f32_16x16x32_bf16 a[92:95], v[68:71], v[104:107], a[92:95]\n"
"ds_write_b128 v3, a[180:183] offset:36864\n"
"v_mfma_f32_16x16x32_bf16 a[92:95], v[92:95], v[112:115], a[92:95]\n"
"v_mfma_f32_16x16x32_bf16 a[96:99], v[72:75], v[104:107], a[96:99]\n"
"v_mfma_f32_16x16x32_bf16 a[96:99], v[100:103], v[112:115], a[96:99]\n"
"ds_read_b64_tr_b16 v[92:93], v142 offset:12288\n"
"ds_read_b64_tr_b16 v[94:95], v142 offset:14336\n"
"v_mfma_f32_16x16x32_bf16 a[100:103], v[76:79], v[104:107], a[100:103]\n"
"ds_read_b64_tr_b16 v[72:73], v143 offset:12288\n"
"ds_read_b64_tr_b16 v[68:69], v143 offset:20992\n"
"v_mfma_f32_16x16x32_bf16 a[100:103], v[108:111], v[112:115], a[100:103]\n"
"ds_read_b64_tr_b16 v[70:71], v143 offset:23040\n"
"ds_read_b64_tr_b16 v[74:75], v143 offset:14336\n"
"v_mfma_f32_16x16x32_bf16 v[220:223], v[32:35], v[104:107], v[220:223]\n"
"ds_read_b64_tr_b16 v[108:109], v142 offset:24576\n"
"ds_read_b64_tr_b16 v[110:111], v142 offset:26624\n"
"v_mfma_f32_16x16x32_bf16 v[220:223], v[48:51], v[112:115], v[220:223]\n"
"s_add_u32 s78, s18, 0x1000\n"
"ds_read_b64_tr_b16 v[100:101], v142 offset:12800\n"
"ds_read_b64_tr_b16 v[102:103], v142 offset:14848\n"
"v_mfma_f32_16x16x32_bf16 a[104:107], v[36:39], v[104:107], a[104:107]\n"
"buffer_load_dwordx4 v[124:127], v21, s[56:59], s78 offen offset:0 sc1\n"
"ds_read_b64_tr_b16 v[32:33], v143 offset:24576\n"
"v_mfma_f32_16x16x32_bf16 a[104:107], v[52:55], v[112:115], a[104:107]\n"
"ds_read_b64_tr_b16 v[34:35], v143 offset:26624\n"
"ds_read_b64_tr_b16 v[52:53], v142 offset:4096\n"
"v_mfma_f32_16x16x32_bf16 a[108:111], v[40:43], v[104:107], a[108:111]\n"
"ds_read_b64_tr_b16 v[76:77], v143 offset:12800\n"
"ds_read_b64_tr_b16 v[54:55], v142 offset:6144\n"
"v_mfma_f32_16x16x32_bf16 a[108:111], v[56:59], v[112:115], a[108:111]\n"
"s_add_u32 s75, s20, 0x1000\n"
"ds_read_b64_tr_b16 v[78:79], v143 offset:14848\n"
"ds_read_b64_tr_b16 v[48:49], v142 offset:25088\n"
"v_mfma_f32_16x16x32_bf16 a[128:131], v[44:47], v[104:107], a[128:131]\n"
"buffer_load_dwordx4 v[80:83], v21, s[56:59], s75 offen offset:0 sc1\n"
"ds_read_b64_tr_b16 v[50:51], v142 offset:27136\n"
"v_mfma_f32_16x16x32_bf16 a[128:131], v[60:63], v[112:115], a[128:131]\n"
"s_waitcnt vmcnt(13)\n"
"ds_read_b64_tr_b16 v[40:41], v143 offset:4096\n"
"ds_read_b64_tr_b16 v[36:37], v143 offset:25088\n"
"v_mfma_f32_16x16x32_bf16 v[152:155], a[136:139], v[120:123], v[152:155]\n"
"s_waitcnt vmcnt(12) lgkmcnt(15)\n"
"ds_read_b64_tr_b16 v[38:39], v143 offset:27136\n"
"ds_read_b64_tr_b16 v[42:43], v143 offset:6144\n"
"v_mfma_f32_16x16x32_bf16 v[152:155], a[168:171], a[176:179], v[152:155]\n"
"buffer_load_dwordx4 v[88:91], v21, s[56:59], s78 offen offset:1024 sc1\n"
"ds_read_b64_tr_b16 v[60:61], v142 offset:16384\n"
"v_mfma_f32_16x16x32_bf16 a[208:211], a[144:147], v[120:123], a[208:211]\n"
"ds_read_b64_tr_b16 v[62:63], v142 offset:18432\n"
"ds_read_b64_tr_b16 v[56:57], v142 offset:4608\n"
"v_mfma_f32_16x16x32_bf16 a[208:211], v[64:67], a[176:179], a[208:211]\n"
"ds_read_b64_tr_b16 v[58:59], v142 offset:6656\n"
"ds_read_b64_tr_b16 v[44:45], v143 offset:4608\n"
"v_mfma_f32_16x16x32_bf16 a[212:215], a[152:155], v[120:123], a[212:215]\n"
"ds_read_b64_tr_b16 v[46:47], v143 offset:6656\n"
"v_mfma_f32_16x16x32_bf16 a[212:215], v[84:87], a[176:179], a[212:215]\n"
"s_add_u32 s76, s82, 0x9000\n"
"v_mfma_f32_16x16x32_bf16 a[216:219], a[160:163], v[120:123], a[216:219]\n"
"buffer_store_dwordx4 a[188:191], v21, s[56:59], s76 offen offset:1024 sc1\n"
"v_mfma_f32_16x16x32_bf16 a[216:219], v[68:71], a[176:179], a[216:219]\n"
"v_mfma_f32_16x16x32_bf16 v[188:191], v[92:95], v[120:123], v[188:191]\n"
"s_waitcnt lgkmcnt(10)\n"
"v_mfma_f32_16x16x32_bf16 v[188:191], v[108:111], a[176:179], v[188:191]\n"
"v_mfma_f32_16x16x32_bf16 a[220:223], v[72:75], v[120:123], a[220:223]\n"
"ds_write_b128 v4, a[188:191] offset:36864\n"
"v_mfma_f32_16x16x32_bf16 a[220:223], v[32:35], a[176:179], a[220:223]\n"
"v_mfma_f32_16x16x32_bf16 a[224:227], v[100:103], v[120:123], a[224:227]\n"
"v_mfma_f32_16x16x32_bf16 a[224:227], v[48:51], a[176:179], a[224:227]\n"
"v_mfma_f32_16x16x32_bf16 a[228:231], v[76:79], v[120:123], a[228:231]\n"
"s_waitcnt lgkmcnt(1)\n"
"v_mfma_f32_16x16x32_bf16 a[228:231], v[36:39], a[176:179], a[228:231]\n"
"s_waitcnt vmcnt(12)\n"
"v_mfma_f32_16x16x32_bf16 v[156:159], a[136:139], v[128:131], v[156:159]\n"
"s_waitcnt vmcnt(11)\n"
"v_mfma_f32_16x16x32_bf16 v[156:159], a[168:171], v[136:139], v[156:159]\n"
"v_mfma_f32_16x16x32_bf16 a[232:235], a[144:147], v[128:131], a[232:235]\n"
"buffer_load_dwordx4 v[96:99], v21, s[56:59], s75 offen offset:1024 sc1\n"
"v_mfma_f32_16x16x32_bf16 a[232:235], v[64:67], v[136:139], a[232:235]\n"
"v_mfma_f32_16x16x32_bf16 a[236:239], a[152:155], v[128:131], a[236:239]\n"
"v_mfma_f32_16x16x32_bf16 a[236:239], v[84:87], v[136:139], a[236:239]\n"
"v_mfma_f32_16x16x32_bf16 a[240:243], a[160:163], v[128:131], a[240:243]\n"
"buffer_load_dwordx4 v[104:107], v21, s[56:59], s78 offen offset:2048 sc1\n"
"v_mfma_f32_16x16x32_bf16 a[240:243], v[68:71], v[136:139], a[240:243]\n"
"v_mfma_f32_16x16x32_bf16 v[192:195], v[92:95], v[128:131], v[192:195]\n"
"v_mfma_f32_16x16x32_bf16 v[192:195], v[108:111], v[136:139], v[192:195]\n"
"buffer_load_dwordx4 v[112:115], v21, s[56:59], s75 offen offset:2048 sc1\n"
"v_mfma_f32_16x16x32_bf16 a[244:247], v[72:75], v[128:131], a[244:247]\n"
"v_mfma_f32_16x16x32_bf16 a[244:247], v[32:35], v[136:139], a[244:247]\n"
"v_mfma_f32_16x16x32_bf16 a[248:251], v[100:103], v[128:131], a[248:251]\n"
"v_mfma_f32_16x16x32_bf16 a[248:251], v[48:51], v[136:139], a[248:251]\n"
"s_add_u32 s96, s83, 0x9000\n"
"v_mfma_f32_16x16x32_bf16 v[224:227], v[76:79], v[128:131], v[224:227]\n"
"buffer_store_dwordx4 a[196:199], v21, s[56:59], s96 offen offset:0 sc1\n"
"v_mfma_f32_16x16x32_bf16 v[224:227], v[36:39], v[136:139], v[224:227]\n"
"s_waitcnt vmcnt(14)\n"
"v_mfma_f32_16x16x32_bf16 v[160:163], a[136:139], a[184:187], v[160:163]\n"
"s_waitcnt vmcnt(11)\n"
"v_mfma_f32_16x16x32_bf16 v[160:163], a[168:171], v[144:147], v[160:163]\n"
"v_mfma_f32_16x16x32_bf16 a[252:255], a[144:147], a[184:187], a[252:255]\n"
"v_mfma_f32_16x16x32_bf16 a[252:255], v[64:67], v[144:147], a[252:255]\n"
"v_mfma_f32_16x16x32_bf16 a[0:3], a[152:155], a[184:187], a[0:3]\n"
"v_mfma_f32_16x16x32_bf16 a[0:3], v[84:87], v[144:147], a[0:3]\n"
"v_mfma_f32_16x16x32_bf16 a[4:7], a[160:163], a[184:187], a[4:7]\n"
"v_mfma_f32_16x16x32_bf16 a[4:7], v[68:71], v[144:147], a[4:7]\n"
"v_mfma_f32_16x16x32_bf16 v[196:199], v[92:95], a[184:187], v[196:199]\n"
"v_mfma_f32_16x16x32_bf16 v[196:199], v[108:111], v[144:147], v[196:199]\n"
"s_add_u32 s84, s13, 0x40000\n"
"s_add_u32 m0, s30, 0x1000\n"
"v_mfma_f32_16x16x32_bf16 v[228:231], v[72:75], a[184:187], v[228:231]\n"
"buffer_load_dwordx4 v140, s[52:55], s84 offen offset:0 lds\n"
"v_mfma_f32_16x16x32_bf16 v[228:231], v[32:35], v[144:147], v[228:231]\n"
"v_mfma_f32_16x16x32_bf16 v[232:235], v[100:103], a[184:187], v[232:235]\n"
"v_mfma_f32_16x16x32_bf16 v[232:235], v[48:51], v[144:147], v[232:235]\n"
"v_mfma_f32_16x16x32_bf16 v[236:239], v[76:79], a[184:187], v[236:239]\n"
"buffer_load_dwordx4 v141, s[52:55], s84 offen offset:1024 lds\n"
"v_mfma_f32_16x16x32_bf16 v[236:239], v[36:39], v[144:147], v[236:239]\n"
"s_waitcnt vmcnt(12)\n"
"v_mfma_f32_16x16x32_bf16 v[164:167], a[136:139], v[116:119], v[164:167]\n"
"s_waitcnt vmcnt(11)\n"
"s_add_u32 s77, s13, 0x47800\n"
"v_mfma_f32_16x16x32_bf16 v[164:167], a[168:171], a[192:195], v[164:167]\n"
"buffer_load_dwordx4 v140, s[52:55], s77 offen offset:2048 lds\n"
"v_mfma_f32_16x16x32_bf16 a[8:11], a[144:147], v[116:119], a[8:11]\n"
"s_waitcnt lgkmcnt(0)\n"
"s_barrier\n"
"v_mfma_f32_16x16x32_bf16 a[8:11], v[64:67], a[192:195], a[8:11]\n"
"v_mfma_f32_16x16x32_bf16 a[12:15], a[152:155], v[116:119], a[12:15]\n"
"v_mfma_f32_16x16x32_bf16 a[12:15], v[84:87], a[192:195], a[12:15]\n"
"buffer_load_dwordx4 v141, s[52:55], s77 offen offset:3072 lds\n"
"v_mfma_f32_16x16x32_bf16 a[16:19], a[160:163], v[116:119], a[16:19]\n"
"ds_read_b128 a[132:135], v13 offset:0\n"
"v_mfma_f32_16x16x32_bf16 a[16:19], v[68:71], a[192:195], a[16:19]\n"
"v_mfma_f32_16x16x32_bf16 v[200:203], v[92:95], v[116:119], v[200:203]\n"
"buffer_load_dwordx4 v[120:123], v21, s[56:59], s78 offen offset:3072 sc1\n"
"ds_read_b128 a[140:143], v13 offset:36864\n"
"v_mfma_f32_16x16x32_bf16 v[200:203], v[108:111], a[192:195], v[200:203]\n"
"v_mfma_f32_16x16x32_bf16 v[240:243], v[72:75], v[116:119], v[240:243]\n"
"s_add_u32 m0, s30, 0x2000\n"
"ds_read_b128 a[148:151], v13 offset:1024\n"
"v_mfma_f32_16x16x32_bf16 v[240:243], v[32:35], a[192:195], v[240:243]\n"
"buffer_load_dwordx4 v140, s[52:55], s23 offen offset:0 lds\n"
"v_mfma_f32_16x16x32_bf16 v[132:135], v[100:103], v[116:119], v[132:135]\n"
"ds_read_b128 a[156:159], v13 offset:37888\n"
"v_mfma_f32_16x16x32_bf16 v[132:135], v[48:51], a[192:195], v[132:135]\n"
"v_mfma_f32_16x16x32_bf16 v[148:151], v[76:79], v[116:119], v[148:151]\n"
"ds_read_b128 a[164:167], v13 offset:2048\n"
"v_mfma_f32_16x16x32_bf16 v[148:151], v[36:39], a[192:195], v[148:151]\n"
"s_waitcnt vmcnt(13)\n"
"buffer_load_dwordx4 v141, s[52:55], s23 offen offset:1024 lds\n"
"buffer_load_dwordx4 a[176:179], v21, s[56:59], s75 offen offset:3072 sc1\n"
"v_mfma_f32_16x16x32_bf16 v[168:171], a[136:139], v[124:127], v[168:171]\n"
"s_waitcnt vmcnt(14)\n"
"ds_read_b128 a[172:175], v13 offset:38912\n"
"v_mfma_f32_16x16x32_bf16 v[168:171], a[168:171], v[80:83], v[168:171]\n"
"s_add_u32 s76, s23, 0x7800\n"
"v_mfma_f32_16x16x32_bf16 a[20:23], a[144:147], v[124:127], a[20:23]\n"
"buffer_load_dwordx4 v140, s[52:55], s76 offen offset:2048 lds\n"
"ds_read_b128 a[180:183], v13 offset:3072\n"
"v_mfma_f32_16x16x32_bf16 a[20:23], v[64:67], v[80:83], a[20:23]\n"
"v_mfma_f32_16x16x32_bf16 a[24:27], a[152:155], v[124:127], a[24:27]\n"
"ds_read_b128 a[188:191], v13 offset:39936\n"
"v_mfma_f32_16x16x32_bf16 a[24:27], v[84:87], v[80:83], a[24:27]\n"
"buffer_load_dwordx4 v141, s[52:55], s76 offen offset:3072 lds\n"
"v_mfma_f32_16x16x32_bf16 a[28:31], a[160:163], v[124:127], a[28:31]\n"
"v_mfma_f32_16x16x32_bf16 a[28:31], v[68:71], v[80:83], a[28:31]\n"
"v_mfma_f32_16x16x32_bf16 v[204:207], v[92:95], v[124:127], v[204:207]\n"
"v_mfma_f32_16x16x32_bf16 v[204:207], v[108:111], v[80:83], v[204:207]\n"
"v_mfma_f32_16x16x32_bf16 a[32:35], v[72:75], v[124:127], a[32:35]\n"
"v_mfma_f32_16x16x32_bf16 a[32:35], v[32:35], v[80:83], a[32:35]\n"
"v_mfma_f32_16x16x32_bf16 a[36:39], v[100:103], v[124:127], a[36:39]\n"
"s_add_u32 s96, s18, 0x2000\n"
"v_mfma_f32_16x16x32_bf16 a[36:39], v[48:51], v[80:83], a[36:39]\n"
"buffer_load_dwordx4 v[128:131], v21, s[56:59], s96 offen offset:0 sc1\n"
"v_mfma_f32_16x16x32_bf16 a[40:43], v[76:79], v[124:127], a[40:43]\n"
"s_add_u32 s84, s20, 0x2000\n"
"v_mfma_f32_16x16x32_bf16 a[40:43], v[36:39], v[80:83], a[40:43]\n"
"s_waitcnt vmcnt(16)\n"
"buffer_load_dwordx4 v[136:139], v21, s[56:59], s84 offen offset:0 sc1\n"
"v_mfma_f32_16x16x32_bf16 v[172:175], a[136:139], v[88:91], v[172:175]\n"
"s_waitcnt vmcnt(15)\n"
"v_mfma_f32_16x16x32_bf16 v[172:175], a[168:171], v[96:99], v[172:175]\n"
"v_mfma_f32_16x16x32_bf16 a[44:47], a[144:147], v[88:91], a[44:47]\n"
"v_mfma_f32_16x16x32_bf16 a[44:47], v[64:67], v[96:99], a[44:47]\n"
"v_mfma_f32_16x16x32_bf16 a[48:51], a[152:155], v[88:91], a[48:51]\n"
"v_mfma_f32_16x16x32_bf16 a[48:51], v[84:87], v[96:99], a[48:51]\n"
"buffer_load_dwordx4 a[184:187], v21, s[56:59], s22 offen offset:0 sc1\n"
"v_mfma_f32_16x16x32_bf16 a[52:55], a[160:163], v[88:91], a[52:55]\n"
"v_mfma_f32_16x16x32_bf16 a[52:55], v[68:71], v[96:99], a[52:55]\n"
"v_mfma_f32_16x16x32_bf16 v[208:211], v[92:95], v[88:91], v[208:211]\n"
"v_mfma_f32_16x16x32_bf16 v[208:211], v[108:111], v[96:99], v[208:211]\n"
"buffer_load_dwordx4 v[144:147], v21, s[56:59], s22 offen offset:1024 sc1\n"
"v_mfma_f32_16x16x32_bf16 a[56:59], v[72:75], v[88:91], a[56:59]\n"
"v_mfma_f32_16x16x32_bf16 a[56:59], v[32:35], v[96:99], a[56:59]\n"
"v_mfma_f32_16x16x32_bf16 a[60:63], v[100:103], v[88:91], a[60:63]\n"
"v_mfma_f32_16x16x32_bf16 a[60:63], v[48:51], v[96:99], a[60:63]\n"
"buffer_load_dwordx4 v[116:119], v21, s[56:59], s22 offen offset:2048 sc1\n"
"v_mfma_f32_16x16x32_bf16 a[112:115], v[76:79], v[88:91], a[112:115]\n"
"v_mfma_f32_16x16x32_bf16 a[112:115], v[36:39], v[96:99], a[112:115]\n"
"s_waitcnt vmcnt(17)\n"
"v_mfma_f32_16x16x32_bf16 v[176:179], a[136:139], v[104:107], v[176:179]\n"
"s_waitcnt vmcnt(16)\n"
"v_mfma_f32_16x16x32_bf16 v[176:179], a[168:171], v[112:115], v[176:179]\n"
"buffer_load_dwordx4 a[192:195], v21, s[56:59], s22 offen offset:3072 sc1\n"
"v_mfma_f32_16x16x32_bf16 a[116:119], a[144:147], v[104:107], a[116:119]\n"
"v_mfma_f32_16x16x32_bf16 a[116:119], v[64:67], v[112:115], a[116:119]\n"
"v_mfma_f32_16x16x32_bf16 a[120:123], a[152:155], v[104:107], a[120:123]\n"
"s_add_u32 s77, s22, 0x1000\n"
"v_mfma_f32_16x16x32_bf16 a[120:123], v[84:87], v[112:115], a[120:123]\n"
"buffer_load_dwordx4 v[124:127], v21, s[56:59], s77 offen offset:0 sc1\n"
"v_mfma_f32_16x16x32_bf16 a[124:127], a[160:163], v[104:107], a[124:127]\n"
"v_mfma_f32_16x16x32_bf16 a[124:127], v[68:71], v[112:115], a[124:127]\n"
"v_mfma_f32_16x16x32_bf16 v[212:215], v[92:95], v[104:107], v[212:215]\n"
"s_waitcnt vmcnt(17)\n"
"s_barrier\n"
"v_mfma_f32_16x16x32_bf16 v[212:215], v[108:111], v[112:115], v[212:215]\n"
"s_add_u32 s78, s39, 0x880\n"
"buffer_load_dwordx4 v[80:83], v21, s[56:59], s77 offen offset:1024 sc1\n"
"v_mfma_f32_16x16x32_bf16 a[200:203], v[72:75], v[104:107], a[200:203]\n"
"buffer_store_dwordx2 v[26:27], v25, s[64:67], s78 offen offset:0 sc1\n"
"v_mfma_f32_16x16x32_bf16 a[200:203], v[32:35], v[112:115], a[200:203]\n"
"v_mfma_f32_16x16x32_bf16 a[204:207], v[100:103], v[104:107], a[204:207]\n"
"v_mfma_f32_16x16x32_bf16 a[204:207], v[48:51], v[112:115], a[204:207]\n"
"buffer_load_dwordx4 v[88:91], v21, s[56:59], s77 offen offset:2048 sc1\n"
"v_mfma_f32_16x16x32_bf16 a[64:67], v[76:79], v[104:107], a[64:67]\n"
"v_mfma_f32_16x16x32_bf16 a[64:67], v[36:39], v[112:115], a[64:67]\n"
"s_waitcnt vmcnt(15)\n"
"v_mfma_f32_16x16x32_bf16 v[180:183], a[136:139], v[120:123], v[180:183]\n"
"s_waitcnt vmcnt(12)\n"
"s_add_u32 m0, s30, 0\n"
"v_mfma_f32_16x16x32_bf16 v[180:183], a[168:171], a[176:179], v[180:183]\n"
"s_waitcnt lgkmcnt(0)\n"
"buffer_load_dwordx4 v23, s[52:55], s89 offen offset:0 lds\n"
"v_mfma_f32_16x16x32_bf16 a[68:71], a[144:147], v[120:123], a[68:71]\n"
"v_mfma_f32_16x16x32_bf16 a[68:71], v[64:67], a[176:179], a[68:71]\n"
"v_mfma_f32_16x16x32_bf16 a[72:75], a[152:155], v[120:123], a[72:75]\n"
"v_mfma_f32_16x16x32_bf16 a[72:75], v[84:87], a[176:179], a[72:75]\n"
"buffer_load_dwordx4 v24, s[52:55], s89 offen offset:1024 lds\n"
"v_mfma_f32_16x16x32_bf16 a[76:79], a[160:163], v[120:123], a[76:79]\n"
"v_mfma_f32_16x16x32_bf16 a[76:79], v[68:71], a[176:179], a[76:79]\n"
"v_mfma_f32_16x16x32_bf16 v[216:219], v[92:95], v[120:123], v[216:219]\n"
"s_add_u32 s75, s89, 0x7800\n"
"v_mfma_f32_16x16x32_bf16 v[216:219], v[108:111], a[176:179], v[216:219]\n"
"buffer_load_dwordx4 v23, s[52:55], s75 offen offset:2048 lds\n"
"v_mfma_f32_16x16x32_bf16 a[80:83], v[72:75], v[120:123], a[80:83]\n"
"v_mfma_f32_16x16x32_bf16 a[80:83], v[32:35], a[176:179], a[80:83]\n"
"v_mfma_f32_16x16x32_bf16 a[84:87], v[100:103], v[120:123], a[84:87]\n"
"v_mfma_f32_16x16x32_bf16 a[84:87], v[48:51], a[176:179], a[84:87]\n"
"buffer_load_dwordx4 v24, s[52:55], s75 offen offset:3072 lds\n"
"v_mfma_f32_16x16x32_bf16 a[88:91], v[76:79], v[120:123], a[88:91]\n"
"v_mfma_f32_16x16x32_bf16 a[88:91], v[36:39], a[176:179], a[88:91]\n"
"s_waitcnt vmcnt(13)\n"
"s_add_u32 m0, s30, 0x3000\n"
"v_mfma_f32_16x16x32_bf16 v[184:187], a[136:139], v[128:131], v[184:187]\n"
"s_waitcnt vmcnt(12)\n"
"ds_read_b64_tr_b16 a[136:137], v143 offset:16384\n"
"ds_read_b64_tr_b16 a[138:139], v143 offset:18432\n"
"v_mfma_f32_16x16x32_bf16 v[184:187], a[168:171], v[136:139], v[184:187]\n"
"buffer_load_dwordx4 v23, s[52:55], s91 offen offset:0 lds\n"
"ds_read_b64_tr_b16 a[168:169], v142 offset:16896\n"
"v_mfma_f32_16x16x32_bf16 a[92:95], a[144:147], v[128:131], a[92:95]\n"
"ds_read_b64_tr_b16 a[170:171], v142 offset:18944\n"
"ds_read_b64_tr_b16 a[144:145], v143 offset:16896\n"
"v_mfma_f32_16x16x32_bf16 a[92:95], v[64:67], v[136:139], a[92:95]\n"
"ds_read_b64_tr_b16 v[64:65], v142 offset:4096\n"
"ds_read_b64_tr_b16 v[66:67], v142 offset:6144\n"
"v_mfma_f32_16x16x32_bf16 a[96:99], a[152:155], v[128:131], a[96:99]\n"
"ds_read_b64_tr_b16 a[152:153], v143 offset:4096\n"
"ds_read_b64_tr_b16 a[146:147], v143 offset:18944\n"
"v_mfma_f32_16x16x32_bf16 a[96:99], v[84:87], v[136:139], a[96:99]\n"
"buffer_load_dwordx4 v24, s[52:55], s91 offen offset:1024 lds\n"
"ds_read_b64_tr_b16 a[154:155], v143 offset:6144\n"
"v_mfma_f32_16x16x32_bf16 a[100:103], a[160:163], v[128:131], a[100:103]\n"
"ds_read_b64_tr_b16 v[84:85], v142 offset:4608\n"
"ds_read_b64_tr_b16 v[86:87], v142 offset:6656\n"
"v_mfma_f32_16x16x32_bf16 a[100:103], v[68:71], v[136:139], a[100:103]\n"
"ds_read_b64_tr_b16 v[68:69], v142 offset:8192\n"
"ds_read_b64_tr_b16 v[70:71], v142 offset:10240\n"
"v_mfma_f32_16x16x32_bf16 v[220:223], v[92:95], v[128:131], v[220:223]\n"
"s_add_u32 s76, s91, 0x7800\n"
"ds_read_b64_tr_b16 v[92:93], v143 offset:8192\n"
"ds_read_b64_tr_b16 v[94:95], v143 offset:10240\n"
"v_mfma_f32_16x16x32_bf16 v[220:223], v[108:111], v[136:139], v[220:223]\n"
"buffer_load_dwordx4 v23, s[52:55], s76 offen offset:2048 lds\n"
"ds_read_b64_tr_b16 a[160:161], v143 offset:4608\n"
"v_mfma_f32_16x16x32_bf16 a[104:107], v[72:75], v[128:131], a[104:107]\n"
"ds_read_b64_tr_b16 a[162:163], v143 offset:6656\n"
"ds_read_b64_tr_b16 v[108:109], v142 offset:8704\n"
"v_mfma_f32_16x16x32_bf16 a[104:107], v[32:35], v[136:139], a[104:107]\n"
"ds_read_b64_tr_b16 v[110:111], v142 offset:10752\n"
"ds_read_b64_tr_b16 v[72:73], v143 offset:8704\n"
"v_mfma_f32_16x16x32_bf16 a[108:111], v[100:103], v[128:131], a[108:111]\n"
"ds_read_b64_tr_b16 v[74:75], v143 offset:10752\n"
"v_mfma_f32_16x16x32_bf16 a[108:111], v[48:51], v[136:139], a[108:111]\n"
"buffer_load_dwordx4 v24, s[52:55], s76 offen offset:3072 lds\n"
"v_mfma_f32_16x16x32_bf16 a[128:131], v[76:79], v[128:131], a[128:131]\n"
"v_mfma_f32_16x16x32_bf16 a[128:131], v[36:39], v[136:139], a[128:131]\n"
"v_mfma_f32_16x16x32_bf16 v[152:155], v[52:55], a[132:135], v[152:155]\n"
"s_waitcnt vmcnt(15)\n"
"s_add_u32 m0, s30, 0x5000\n"
"v_mfma_f32_16x16x32_bf16 v[152:155], v[60:63], a[184:187], v[152:155]\n"
"buffer_load_dwordx4 v140, s[52:55], s89 offen offset:0 lds\n"
"v_mfma_f32_16x16x32_bf16 a[208:211], v[40:43], a[132:135], a[208:211]\n"
"s_waitcnt lgkmcnt(8)\n"
"v_mfma_f32_16x16x32_bf16 a[208:211], a[136:139], a[184:187], a[208:211]\n"
"v_mfma_f32_16x16x32_bf16 a[212:215], v[56:59], a[132:135], a[212:215]\n"
"v_mfma_f32_16x16x32_bf16 a[212:215], a[168:171], a[184:187], a[212:215]\n"
"buffer_load_dwordx4 v141, s[52:55], s89 offen offset:1024 lds\n"
"v_mfma_f32_16x16x32_bf16 a[216:219], v[44:47], a[132:135], a[216:219]\n"
"v_mfma_f32_16x16x32_bf16 a[216:219], a[144:147], a[184:187], a[216:219]\n"
"v_mfma_f32_16x16x32_bf16 v[188:191], v[64:67], a[132:135], v[188:191]\n"
"v_mfma_f32_16x16x32_bf16 v[188:191], v[68:71], a[184:187], v[188:191]\n"
"buffer_load_dwordx4 v140, s[52:55], s75 offen offset:2048 lds\n"
"v_mfma_f32_16x16x32_bf16 a[220:223], a[152:155], a[132:135], a[220:223]\n"
"s_waitcnt lgkmcnt(0)\n"
"v_mfma_f32_16x16x32_bf16 a[220:223], v[92:95], a[184:187], a[220:223]\n"
"v_mfma_f32_16x16x32_bf16 a[224:227], v[84:87], a[132:135], a[224:227]\n"
"v_mfma_f32_16x16x32_bf16 a[224:227], v[108:111], a[184:187], a[224:227]\n"
"buffer_load_dwordx4 v141, s[52:55], s75 offen offset:3072 lds\n"
"v_mfma_f32_16x16x32_bf16 a[228:231], a[160:163], a[132:135], a[228:231]\n"
"v_mfma_f32_16x16x32_bf16 a[228:231], v[72:75], a[184:187], a[228:231]\n"
"v_mfma_f32_16x16x32_bf16 v[156:159], v[52:55], a[140:143], v[156:159]\n"
"s_waitcnt vmcnt(18)\n"
"s_add_u32 m0, s30, 0x1000\n"
"v_mfma_f32_16x16x32_bf16 v[156:159], v[60:63], v[144:147], v[156:159]\n"
"buffer_load_dwordx4 v140, s[52:55], s91 offen offset:0 lds\n"
"v_mfma_f32_16x16x32_bf16 a[232:235], v[40:43], a[140:143], a[232:235]\n"
"v_mfma_f32_16x16x32_bf16 a[232:235], a[136:139], v[144:147], a[232:235]\n"
"v_mfma_f32_16x16x32_bf16 a[236:239], v[56:59], a[140:143], a[236:239]\n"
"v_mfma_f32_16x16x32_bf16 a[236:239], a[168:171], v[144:147], a[236:239]\n"
"buffer_load_dwordx4 v141, s[52:55], s91 offen offset:1024 lds\n"
"v_mfma_f32_16x16x32_bf16 a[240:243], v[44:47], a[140:143], a[240:243]\n"
"s_waitcnt vmcnt(12)\n"
"ds_read_b64_tr_b16 v[32:33], v142 offset:0\n"
"v_mfma_f32_16x16x32_bf16 a[240:243], a[144:147], v[144:147], a[240:243]\n"
"ds_read_b64_tr_b16 v[100:101], v143 offset:0\n"
"v_mfma_f32_16x16x32_bf16 v[192:195], v[64:67], a[140:143], v[192:195]\n"
"ds_read_b64_tr_b16 v[48:49], v142 offset:512\n"
"v_mfma_f32_16x16x32_bf16 v[192:195], v[68:71], v[144:147], v[192:195]\n"
"buffer_load_dwordx4 v140, s[52:55], s76 offen offset:2048 lds\n"
"ds_read_b64_tr_b16 v[76:77], v143 offset:512\n"
"v_mfma_f32_16x16x32_bf16 a[244:247], a[152:155], a[140:143], a[244:247]\n"
"v_mfma_f32_16x16x32_bf16 a[244:247], v[92:95], v[144:147], a[244:247]\n"
"v_mfma_f32_16x16x32_bf16 a[248:251], v[84:87], a[140:143], a[248:251]\n"
"v_mfma_f32_16x16x32_bf16 a[248:251], v[108:111], v[144:147], a[248:251]\n"
"buffer_load_dwordx4 v141, s[52:55], s76 offen offset:3072 lds\n"
"v_mfma_f32_16x16x32_bf16 v[224:227], a[160:163], a[140:143], v[224:227]\n"
"s_waitcnt vmcnt(12)\n"
"ds_read_b64_tr_b16 v[34:35], v142 offset:2048\n"
"ds_read_b64_tr_b16 v[102:103], v143 offset:2048\n"
"v_mfma_f32_16x16x32_bf16 v[224:227], v[72:75], v[144:147], v[224:227]\n"
"ds_read_b64_tr_b16 v[50:51], v142 offset:2560\n"
"ds_read_b64_tr_b16 v[78:79], v143 offset:2560\n"
"v_mfma_f32_16x16x32_bf16 v[160:163], v[52:55], a[148:151], v[160:163]\n"
"v_mfma_f32_16x16x32_bf16 v[160:163], v[60:63], v[116:119], v[160:163]\n"
"buffer_load_dwordx4 v[96:99], v21, s[56:59], s77 offen offset:3072 sc1\n"
"v_mfma_f32_16x16x32_bf16 a[252:255], v[40:43], a[148:151], a[252:255]\n"
"v_mfma_f32_16x16x32_bf16 a[252:255], a[136:139], v[116:119], a[252:255]\n"
"v_mfma_f32_16x16x32_bf16 a[0:3], v[56:59], a[148:151], a[0:3]\n"
"s_add_u32 s96, s22, 0x2000\n"
"v_mfma_f32_16x16x32_bf16 a[0:3], a[168:171], v[116:119], a[0:3]\n"
"buffer_load_dwordx4 v[104:107], v21, s[56:59], s96 offen offset:0 sc1\n"
"v_mfma_f32_16x16x32_bf16 a[4:7], v[44:47], a[148:151], a[4:7]\n"
"v_mfma_f32_16x16x32_bf16 a[4:7], a[144:147], v[116:119], a[4:7]\n"
"s_waitcnt vmcnt(12)\n"
"ds_read_b64_tr_b16 v[36:37], v142 offset:12288\n"
"v_mfma_f32_16x16x32_bf16 v[196:199], v[64:67], a[148:151], v[196:199]\n"
"v_mfma_f32_16x16x32_bf16 v[196:199], v[68:71], v[116:119], v[196:199]\n"
"buffer_load_dwordx4 v[112:115], v21, s[56:59], s88 offen offset:0 sc1\n"
"v_mfma_f32_16x16x32_bf16 v[228:231], a[152:155], a[148:151], v[228:231]\n"
"v_mfma_f32_16x16x32_bf16 v[228:231], v[92:95], v[116:119], v[228:231]\n"
"v_mfma_f32_16x16x32_bf16 v[232:235], v[84:87], a[148:151], v[232:235]\n"
"v_mfma_f32_16x16x32_bf16 v[232:235], v[108:111], v[116:119], v[232:235]\n"
"buffer_load_dwordx4 v[120:123], v21, s[56:59], s90 offen offset:0 sc1\n"
"v_mfma_f32_16x16x32_bf16 v[236:239], a[160:163], a[148:151], v[236:239]\n"
"v_mfma_f32_16x16x32_bf16 v[236:239], v[72:75], v[116:119], v[236:239]\n"
"s_waitcnt vmcnt(12)\n"
"ds_read_b64_tr_b16 v[38:39], v142 offset:14336\n"
"v_mfma_f32_16x16x32_bf16 v[164:167], v[52:55], a[156:159], v[164:167]\n"
"v_mfma_f32_16x16x32_bf16 v[164:167], v[60:63], a[192:195], v[164:167]\n"
"buffer_load_dwordx4 a[176:179], v21, s[56:59], s88 offen offset:1024 sc1\n"
"v_mfma_f32_16x16x32_bf16 a[8:11], v[40:43], a[156:159], a[8:11]\n"
"v_mfma_f32_16x16x32_bf16 a[8:11], a[136:139], a[192:195], a[8:11]\n"
"v_mfma_f32_16x16x32_bf16 a[12:15], v[56:59], a[156:159], a[12:15]\n"
"v_mfma_f32_16x16x32_bf16 a[12:15], a[168:171], a[192:195], a[12:15]\n"
"buffer_load_dwordx4 v[128:131], v21, s[56:59], s90 offen offset:1024 sc1\n"
"v_mfma_f32_16x16x32_bf16 a[16:19], v[44:47], a[156:159], a[16:19]\n"
"v_mfma_f32_16x16x32_bf16 a[16:19], a[144:147], a[192:195], a[16:19]\n"
"v_mfma_f32_16x16x32_bf16 v[200:203], v[64:67], a[156:159], v[200:203]\n"
"v_mfma_f32_16x16x32_bf16 v[200:203], v[68:71], a[192:195], v[200:203]\n"
"buffer_load_dwordx4 v[136:139], v21, s[56:59], s88 offen offset:2048 sc1\n"
"v_mfma_f32_16x16x32_bf16 v[240:243], a[152:155], a[156:159], v[240:243]\n"
"v_mfma_f32_16x16x32_bf16 v[240:243], v[92:95], a[192:195], v[240:243]\n"
"v_mfma_f32_16x16x32_bf16 v[132:135], v[84:87], a[156:159], v[132:135]\n"
"v_mfma_f32_16x16x32_bf16 v[132:135], v[108:111], a[192:195], v[132:135]\n"
"buffer_load_dwordx4 a[132:135], v21, s[56:59], s90 offen offset:2048 sc1\n"
"v_mfma_f32_16x16x32_bf16 v[148:151], a[160:163], a[156:159], v[148:151]\n"
"v_mfma_f32_16x16x32_bf16 v[148:151], v[72:75], a[192:195], v[148:151]\n"
"v_mfma_f32_16x16x32_bf16 v[168:171], v[52:55], a[164:167], v[168:171]\n"
"v_mfma_f32_16x16x32_bf16 v[168:171], v[60:63], v[124:127], v[168:171]\n"
"buffer_load_dwordx4 a[184:187], v21, s[56:59], s88 offen offset:3072 sc1\n"
"v_mfma_f32_16x16x32_bf16 a[20:23], v[40:43], a[164:167], a[20:23]\n"
"v_mfma_f32_16x16x32_bf16 a[20:23], a[136:139], v[124:127], a[20:23]\n"
"v_mfma_f32_16x16x32_bf16 a[24:27], v[56:59], a[164:167], a[24:27]\n"
"v_mfma_f32_16x16x32_bf16 a[24:27], a[168:171], v[124:127], a[24:27]\n"
"buffer_load_dwordx4 a[140:143], v21, s[56:59], s90 offen offset:3072 sc1\n"
"v_mfma_f32_16x16x32_bf16 a[28:31], v[44:47], a[164:167], a[28:31]\n"
"v_mfma_f32_16x16x32_bf16 a[28:31], a[144:147], v[124:127], a[28:31]\n"
"v_mfma_f32_16x16x32_bf16 v[204:207], v[64:67], a[164:167], v[204:207]\n"
"s_add_u32 s84, s88, 0x1000\n"
"v_mfma_f32_16x16x32_bf16 v[204:207], v[68:71], v[124:127], v[204:207]\n"
"buffer_load_dwordx4 v[144:147], v21, s[56:59], s84 offen offset:0 sc1\n"
"v_mfma_f32_16x16x32_bf16 a[32:35], a[152:155], a[164:167], a[32:35]\n"
"v_mfma_f32_16x16x32_bf16 a[32:35], v[92:95], v[124:127], a[32:35]\n"
"v_mfma_f32_16x16x32_bf16 a[36:39], v[84:87], a[164:167], a[36:39]\n"
"s_add_u32 s78, s90, 0x1000\n"
"v_mfma_f32_16x16x32_bf16 a[36:39], v[108:111], v[124:127], a[36:39]\n"
"buffer_load_dwordx4 a[148:151], v21, s[56:59], s78 offen offset:0 sc1\n"
"v_mfma_f32_16x16x32_bf16 a[40:43], a[160:163], a[164:167], a[40:43]\n"
"v_mfma_f32_16x16x32_bf16 a[40:43], v[72:75], v[124:127], a[40:43]\n"
"v_mfma_f32_16x16x32_bf16 v[172:175], v[52:55], a[172:175], v[172:175]\n"
"v_mfma_f32_16x16x32_bf16 v[172:175], v[60:63], v[80:83], v[172:175]\n"
"buffer_load_dwordx4 v[116:119], v21, s[56:59], s84 offen offset:1024 sc1\n"
"v_mfma_f32_16x16x32_bf16 a[44:47], v[40:43], a[172:175], a[44:47]\n"
"v_mfma_f32_16x16x32_bf16 a[44:47], a[136:139], v[80:83], a[44:47]\n"
"v_mfma_f32_16x16x32_bf16 a[48:51], v[56:59], a[172:175], a[48:51]\n"
"v_mfma_f32_16x16x32_bf16 a[48:51], a[168:171], v[80:83], a[48:51]\n"
"buffer_load_dwordx4 a[156:159], v21, s[56:59], s78 offen offset:1024 sc1\n"
"v_mfma_f32_16x16x32_bf16 a[52:55], v[44:47], a[172:175], a[52:55]\n"
"v_mfma_f32_16x16x32_bf16 a[52:55], a[144:147], v[80:83], a[52:55]\n"
"v_mfma_f32_16x16x32_bf16 v[208:211], v[64:67], a[172:175], v[208:211]\n"
"v_mfma_f32_16x16x32_bf16 v[208:211], v[68:71], v[80:83], v[208:211]\n"
"buffer_load_dwordx4 a[192:195], v21, s[56:59], s84 offen offset:2048 sc1\n"
"v_mfma_f32_16x16x32_bf16 a[56:59], a[152:155], a[172:175], a[56:59]\n"
"v_mfma_f32_16x16x32_bf16 a[56:59], v[92:95], v[80:83], a[56:59]\n"
"v_mfma_f32_16x16x32_bf16 a[60:63], v[84:87], a[172:175], a[60:63]\n"
"v_mfma_f32_16x16x32_bf16 a[60:63], v[108:111], v[80:83], a[60:63]\n"
"buffer_load_dwordx4 a[164:167], v21, s[56:59], s78 offen offset:2048 sc1\n"
"v_mfma_f32_16x16x32_bf16 a[112:115], a[160:163], a[172:175], a[112:115]\n"
"v_mfma_f32_16x16x32_bf16 a[112:115], v[72:75], v[80:83], a[112:115]\n"
"v_mfma_f32_16x16x32_bf16 v[176:179], v[52:55], a[180:183], v[176:179]\n"
"s_add_u32 s75, s15, 0x40000\n"
"s_add_u32 m0, s30, 0\n"
"v_mfma_f32_16x16x32_bf16 v[176:179], v[60:63], v[88:91], v[176:179]\n"
"s_waitcnt lgkmcnt(0)\n"
"buffer_load_dwordx4 v23, s[52:55], s75 offen offset:0 lds\n"
"v_mfma_f32_16x16x32_bf16 a[116:119], v[40:43], a[180:183], a[116:119]\n"
"v_mfma_f32_16x16x32_bf16 a[116:119], a[136:139], v[88:91], a[116:119]\n"
"v_mfma_f32_16x16x32_bf16 a[120:123], v[56:59], a[180:183], a[120:123]\n"
"v_mfma_f32_16x16x32_bf16 a[120:123], a[168:171], v[88:91], a[120:123]\n"
"buffer_load_dwordx4 v24, s[52:55], s75 offen offset:1024 lds\n"
"v_mfma_f32_16x16x32_bf16 a[124:127], v[44:47], a[180:183], a[124:127]\n"
"v_mfma_f32_16x16x32_bf16 a[124:127], a[144:147], v[88:91], a[124:127]\n"
"v_mfma_f32_16x16x32_bf16 v[212:215], v[64:67], a[180:183], v[212:215]\n"
"s_add_u32 s76, s15, 0x47800\n"
"v_mfma_f32_16x16x32_bf16 v[212:215], v[68:71], v[88:91], v[212:215]\n"
"buffer_load_dwordx4 v23, s[52:55], s76 offen offset:2048 lds\n"
"v_mfma_f32_16x16x32_bf16 a[200:203], a[152:155], a[180:183], a[200:203]\n"
"v_mfma_f32_16x16x32_bf16 a[200:203], v[92:95], v[88:91], a[200:203]\n"
"v_mfma_f32_16x16x32_bf16 a[204:207], v[84:87], a[180:183], a[204:207]\n"
"v_mfma_f32_16x16x32_bf16 a[204:207], v[108:111], v[88:91], a[204:207]\n"
"buffer_load_dwordx4 v24, s[52:55], s76 offen offset:3072 lds\n"
"v_mfma_f32_16x16x32_bf16 a[64:67], a[160:163], a[180:183], a[64:67]\n"
"v_mfma_f32_16x16x32_bf16 a[64:67], v[72:75], v[88:91], a[64:67]\n"
"v_mfma_f32_16x16x32_bf16 v[180:183], v[52:55], a[188:191], v[180:183]\n"
"s_waitcnt vmcnt(19)\n"
"s_add_u32 s77, s17, 0x40000\n"
"s_add_u32 m0, s30, 0x2000\n"
"v_mfma_f32_16x16x32_bf16 v[180:183], v[60:63], v[96:99], v[180:183]\n"
"buffer_load_dwordx4 v23, s[52:55], s77 offen offset:0 lds\n"
"v_mfma_f32_16x16x32_bf16 a[68:71], v[40:43], a[188:191], a[68:71]\n"
"v_mfma_f32_16x16x32_bf16 a[68:71], a[136:139], v[96:99], a[68:71]\n"
"v_mfma_f32_16x16x32_bf16 a[72:75], v[56:59], a[188:191], a[72:75]\n"
"v_mfma_f32_16x16x32_bf16 a[72:75], a[168:171], v[96:99], a[72:75]\n"
"buffer_load_dwordx4 v24, s[52:55], s77 offen offset:1024 lds\n"
"v_mfma_f32_16x16x32_bf16 a[76:79], v[44:47], a[188:191], a[76:79]\n"
"v_mfma_f32_16x16x32_bf16 a[76:79], a[144:147], v[96:99], a[76:79]\n"
"v_mfma_f32_16x16x32_bf16 v[216:219], v[64:67], a[188:191], v[216:219]\n"
"s_add_u32 s96, s17, 0x47800\n"
"v_mfma_f32_16x16x32_bf16 v[216:219], v[68:71], v[96:99], v[216:219]\n"
"buffer_load_dwordx4 v23, s[52:55], s96 offen offset:2048 lds\n"
"v_mfma_f32_16x16x32_bf16 a[80:83], a[152:155], a[188:191], a[80:83]\n"
"v_mfma_f32_16x16x32_bf16 a[80:83], v[92:95], v[96:99], a[80:83]\n"
"v_mfma_f32_16x16x32_bf16 a[84:87], v[84:87], a[188:191], a[84:87]\n"
"v_mfma_f32_16x16x32_bf16 a[84:87], v[108:111], v[96:99], a[84:87]\n"
"buffer_load_dwordx4 v24, s[52:55], s96 offen offset:3072 lds\n"
"v_mfma_f32_16x16x32_bf16 a[88:91], a[160:163], a[188:191], a[88:91]\n"
"v_mfma_f32_16x16x32_bf16 a[88:91], v[72:75], v[96:99], a[88:91]\n"
"s_add_u32 m0, s30, 0x4000\n"
"v_mfma_f32_16x16x32_bf16 v[184:187], v[52:55], a[196:199], v[184:187]\n"
"s_waitcnt vmcnt(22)\n"
"ds_read_b64_tr_b16 v[52:53], v143 offset:12288\n"
"ds_read_b64_tr_b16 v[54:55], v143 offset:14336\n"
"v_mfma_f32_16x16x32_bf16 v[184:187], v[60:63], v[104:107], v[184:187]\n"
"buffer_load_dwordx4 v140, s[52:55], s75 offen offset:0 lds\n"
"ds_read_b64_tr_b16 v[60:61], v142 offset:12800\n"
"v_mfma_f32_16x16x32_bf16 a[92:95], v[40:43], a[196:199], a[92:95]\n"
"ds_read_b64_tr_b16 v[62:63], v142 offset:14848\n"
"ds_read_b64_tr_b16 v[40:41], v143 offset:12800\n"
"v_mfma_f32_16x16x32_bf16 a[92:95], a[136:139], v[104:107], a[92:95]\n"
"ds_read_b64_tr_b16 a[136:137], v142 offset:20480\n"
"ds_read_b64_tr_b16 a[138:139], v142 offset:22528\n"
"v_mfma_f32_16x16x32_bf16 a[96:99], v[56:59], a[196:199], a[96:99]\n"
"ds_read_b64_tr_b16 v[56:57], v143 offset:20480\n"
"ds_read_b64_tr_b16 v[42:43], v143 offset:14848\n"
"v_mfma_f32_16x16x32_bf16 a[96:99], a[168:171], v[104:107], a[96:99]\n"
"buffer_load_dwordx4 v141, s[52:55], s75 offen offset:1024 lds\n"
"ds_read_b64_tr_b16 v[58:59], v143 offset:22528\n"
"v_mfma_f32_16x16x32_bf16 a[100:103], v[44:47], a[196:199], a[100:103]\n"
"ds_read_b64_tr_b16 a[168:169], v142 offset:20992\n"
"ds_read_b64_tr_b16 a[170:171], v142 offset:23040\n"
"v_mfma_f32_16x16x32_bf16 a[100:103], a[144:147], v[104:107], a[100:103]\n"
"ds_read_b64_tr_b16 a[144:145], v142 offset:4096\n"
"ds_read_b64_tr_b16 a[146:147], v142 offset:6144\n"
"v_mfma_f32_16x16x32_bf16 v[220:223], v[64:67], a[196:199], v[220:223]\n"
"ds_read_b64_tr_b16 v[64:65], v143 offset:4096\n"
"ds_read_b64_tr_b16 v[66:67], v143 offset:6144\n"
"v_mfma_f32_16x16x32_bf16 v[220:223], v[68:71], v[104:107], v[220:223]\n"
"buffer_load_dwordx4 v140, s[52:55], s76 offen offset:2048 lds\n"
"ds_read_b64_tr_b16 v[44:45], v143 offset:20992\n"
"v_mfma_f32_16x16x32_bf16 a[104:107], a[152:155], a[196:199], a[104:107]\n"
"ds_read_b64_tr_b16 v[46:47], v143 offset:23040\n"
"ds_read_b64_tr_b16 v[68:69], v142 offset:4608\n"
"v_mfma_f32_16x16x32_bf16 a[104:107], v[92:95], v[104:107], a[104:107]\n"
"ds_read_b64_tr_b16 v[70:71], v142 offset:6656\n"
"ds_read_b64_tr_b16 a[152:153], v143 offset:4608\n"
"v_mfma_f32_16x16x32_bf16 a[108:111], v[84:87], a[196:199], a[108:111]\n"
"ds_read_b64_tr_b16 a[154:155], v143 offset:6656\n"
"v_mfma_f32_16x16x32_bf16 a[108:111], v[108:111], v[104:107], a[108:111]\n"
"buffer_load_dwordx4 v141, s[52:55], s76 offen offset:3072 lds\n"
"v_mfma_f32_16x16x32_bf16 a[128:131], a[160:163], a[196:199], a[128:131]\n"
"s_and_b32 s38, s27, 0\n"
"v_bfe_u32 v30, v22, 4, 4\n"
"v_lshlrev_b32 v30, 7, v30\n"
"v_mfma_f32_16x16x32_bf16 a[128:131], v[72:75], v[104:107], a[128:131]\n"
"s_waitcnt vmcnt(25)\n"
"s_lshl_b32 s38, s38, 7\n"
"v_mfma_f32_16x16x32_bf16 v[152:155], v[32:35], v[112:115], v[152:155]\n"
"s_waitcnt vmcnt(24)\n"
"s_add_u32 s38, s38, s94\n"
"v_mfma_f32_16x16x32_bf16 v[152:155], v[36:39], v[120:123], v[152:155]\n"
"s_add_u32 s38, s38, 0x880\n"
"buffer_load_dwordx4 v[124:127], v21, s[56:59], s84 offen offset:3072 sc1\n"
"v_mfma_f32_16x16x32_bf16 a[208:211], v[100:103], v[112:115], a[208:211]\n"
"s_waitcnt lgkmcnt(8)\n"
"v_mfma_f32_16x16x32_bf16 a[208:211], v[52:55], v[120:123], a[208:211]\n"
"v_mfma_f32_16x16x32_bf16 a[212:215], v[48:51], v[112:115], a[212:215]\n"
"s_add_u32 m0, s30, 0x6000\n"
"v_mfma_f32_16x16x32_bf16 a[212:215], v[60:63], v[120:123], a[212:215]\n"
"buffer_load_dwordx4 v140, s[52:55], s77 offen offset:0 lds\n"
"v_mfma_f32_16x16x32_bf16 a[216:219], v[76:79], v[112:115], a[216:219]\n"
"s_waitcnt vmcnt(12)\n"
"ds_read_b64_tr_b16 v[92:93], v142 offset:0\n"
"v_mfma_f32_16x16x32_bf16 a[216:219], v[40:43], v[120:123], a[216:219]\n"
"ds_read_b64_tr_b16 v[84:85], v143 offset:0\n"
"v_mfma_f32_16x16x32_bf16 v[188:191], a[136:139], v[112:115], v[188:191]\n"
"ds_read_b64_tr_b16 v[108:109], v142 offset:512\n"
"v_mfma_f32_16x16x32_bf16 v[188:191], a[144:147], v[120:123], v[188:191]\n"
"buffer_load_dwordx4 v141, s[52:55], s77 offen offset:1024 lds\n"
"ds_read_b64_tr_b16 a[160:161], v143 offset:512\n"
"v_mfma_f32_16x16x32_bf16 a[220:223], v[56:59], v[112:115], a[220:223]\n"
"s_waitcnt lgkmcnt(4)\n"
"v_mfma_f32_16x16x32_bf16 a[220:223], v[64:67], v[120:123], a[220:223]\n"
"v_mfma_f32_16x16x32_bf16 a[224:227], a[168:171], v[112:115], a[224:227]\n"
"v_mfma_f32_16x16x32_bf16 a[224:227], v[68:71], v[120:123], a[224:227]\n"
"buffer_load_dwordx4 v140, s[52:55], s96 offen offset:2048 lds\n"
"v_mfma_f32_16x16x32_bf16 a[228:231], v[44:47], v[112:115], a[228:231]\n"
"s_waitcnt vmcnt(12)\n"
"buffer_load_dwordx2 v[28:29], v30, s[64:67], s38 offen offset:0 sc1\n"
"ds_read_b64_tr_b16 v[94:95], v142 offset:2048\n"
"v_mfma_f32_16x16x32_bf16 a[228:231], a[152:155], v[120:123], a[228:231]\n"
"ds_read_b64_tr_b16 v[86:87], v143 offset:2048\n"
"ds_read_b64_tr_b16 v[110:111], v142 offset:2560\n"
"v_mfma_f32_16x16x32_bf16 v[156:159], v[32:35], a[176:179], v[156:159]\n"
"ds_read_b64_tr_b16 a[162:163], v143 offset:2560\n"
"v_mfma_f32_16x16x32_bf16 v[156:159], v[36:39], v[128:131], v[156:159]\n"
"buffer_load_dwordx4 v141, s[52:55], s96 offen offset:3072 lds\n"
"v_mfma_f32_16x16x32_bf16 a[232:235], v[100:103], a[176:179], a[232:235]\n"
"v_mfma_f32_16x16x32_bf16 a[232:235], v[52:55], v[128:131], a[232:235]\n"
"v_mfma_f32_16x16x32_bf16 a[236:239], v[48:51], a[176:179], a[236:239]\n"
"v_mfma_f32_16x16x32_bf16 a[236:239], v[60:63], v[128:131], a[236:239]\n"
"buffer_load_dwordx4 a[172:175], v21, s[56:59], s78 offen offset:3072 sc1\n"
"v_mfma_f32_16x16x32_bf16 a[240:243], v[76:79], a[176:179], a[240:243]\n"
"s_waitcnt vmcnt(13)\n"
"ds_read_b64_tr_b16 v[72:73], v142 offset:8192\n"
"v_mfma_f32_16x16x32_bf16 a[240:243], v[40:43], v[128:131], a[240:243]\n"
"v_mfma_f32_16x16x32_bf16 v[192:195], a[136:139], a[176:179], v[192:195]\n"
"s_add_u32 s75, s88, 0x2000\n"
"v_mfma_f32_16x16x32_bf16 v[192:195], a[144:147], v[128:131], v[192:195]\n"
"buffer_load_dwordx4 v[80:83], v21, s[56:59], s75 offen offset:0 sc1\n"
"v_mfma_f32_16x16x32_bf16 a[244:247], v[56:59], a[176:179], a[244:247]\n"
"v_mfma_f32_16x16x32_bf16 a[244:247], v[64:67], v[128:131], a[244:247]\n"
"v_mfma_f32_16x16x32_bf16 a[248:251], a[168:171], a[176:179], a[248:251]\n"
"s_add_u32 s76, s90, 0x2000\n"
"v_mfma_f32_16x16x32_bf16 a[248:251], v[68:71], v[128:131], a[248:251]\n"
"buffer_load_dwordx4 a[180:183], v21, s[56:59], s76 offen offset:0 sc1\n"
"v_mfma_f32_16x16x32_bf16 v[224:227], v[44:47], a[176:179], v[224:227]\n"
"s_waitcnt vmcnt(13)\n"
"ds_read_b64_tr_b16 v[74:75], v142 offset:10240\n"
"v_mfma_f32_16x16x32_bf16 v[224:227], a[152:155], v[128:131], v[224:227]\n"
"v_mfma_f32_16x16x32_bf16 v[160:163], v[32:35], v[136:139], v[160:163]\n"
"s_add_u32 s84, s19, 0x40000\n"
"s_add_u32 m0, s30, 0x1000\n"
"v_mfma_f32_16x16x32_bf16 v[160:163], v[36:39], a[132:135], v[160:163]\n"
"buffer_load_dwordx4 v23, s[52:55], s84 offen offset:0 lds\n"
"v_mfma_f32_16x16x32_bf16 a[252:255], v[100:103], v[136:139], a[252:255]\n"
"v_mfma_f32_16x16x32_bf16 a[252:255], v[52:55], a[132:135], a[252:255]\n"
"v_mfma_f32_16x16x32_bf16 a[0:3], v[48:51], v[136:139], a[0:3]\n"
"v_mfma_f32_16x16x32_bf16 a[0:3], v[60:63], a[132:135], a[0:3]\n"
"buffer_load_dwordx4 v24, s[52:55], s84 offen offset:1024 lds\n"
"v_mfma_f32_16x16x32_bf16 a[4:7], v[76:79], v[136:139], a[4:7]\n"
"v_mfma_f32_16x16x32_bf16 a[4:7], v[40:43], a[132:135], a[4:7]\n"
"v_mfma_f32_16x16x32_bf16 v[196:199], a[136:139], v[136:139], v[196:199]\n"
"s_add_u32 s77, s19, 0x47800\n"
"v_mfma_f32_16x16x32_bf16 v[196:199], a[144:147], a[132:135], v[196:199]\n"
"buffer_load_dwordx4 v23, s[52:55], s77 offen offset:2048 lds\n"
"v_mfma_f32_16x16x32_bf16 v[228:231], v[56:59], v[136:139], v[228:231]\n"
"v_mfma_f32_16x16x32_bf16 v[228:231], v[64:67], a[132:135], v[228:231]\n"
"v_mfma_f32_16x16x32_bf16 v[232:235], a[168:171], v[136:139], v[232:235]\n"
"v_mfma_f32_16x16x32_bf16 v[232:235], v[68:71], a[132:135], v[232:235]\n"
"buffer_load_dwordx4 v24, s[52:55], s77 offen offset:3072 lds\n"
"v_mfma_f32_16x16x32_bf16 v[236:239], v[44:47], v[136:139], v[236:239]\n"
"v_mfma_f32_16x16x32_bf16 v[236:239], a[152:155], a[132:135], v[236:239]\n"
"v_mfma_f32_16x16x32_bf16 v[164:167], v[32:35], a[184:187], v[164:167]\n"
"s_add_u32 m0, s30, 0\n"
"v_mfma_f32_16x16x32_bf16 v[164:167], v[36:39], a[140:143], v[164:167]\n"
"s_waitcnt lgkmcnt(0)\n"
"buffer_load_dwordx4 v140, s[52:55], s84 offen offset:0 lds\n"
"v_mfma_f32_16x16x32_bf16 a[8:11], v[100:103], a[184:187], a[8:11]\n"
"v_mfma_f32_16x16x32_bf16 a[8:11], v[52:55], a[140:143], a[8:11]\n"
"v_mfma_f32_16x16x32_bf16 a[12:15], v[48:51], a[184:187], a[12:15]\n"
"v_mfma_f32_16x16x32_bf16 a[12:15], v[60:63], a[140:143], a[12:15]\n"
"buffer_load_dwordx4 v141, s[52:55], s84 offen offset:1024 lds\n"
"v_mfma_f32_16x16x32_bf16 a[16:19], v[76:79], a[184:187], a[16:19]\n"
"v_mfma_f32_16x16x32_bf16 a[16:19], v[40:43], a[140:143], a[16:19]\n"
"v_mfma_f32_16x16x32_bf16 v[200:203], a[136:139], a[184:187], v[200:203]\n"
"v_mfma_f32_16x16x32_bf16 v[200:203], a[144:147], a[140:143], v[200:203]\n"
"buffer_load_dwordx4 v140, s[52:55], s77 offen offset:2048 lds\n"
"v_mfma_f32_16x16x32_bf16 v[240:243], v[56:59], a[184:187], v[240:243]\n"
"v_mfma_f32_16x16x32_bf16 v[240:243], v[64:67], a[140:143], v[240:243]\n"
"v_mfma_f32_16x16x32_bf16 v[132:135], a[168:171], a[184:187], v[132:135]\n"
"v_mfma_f32_16x16x32_bf16 v[132:135], v[68:71], a[140:143], v[132:135]\n"
"buffer_load_dwordx4 v141, s[52:55], s77 offen offset:3072 lds\n"
"v_mfma_f32_16x16x32_bf16 v[148:151], v[44:47], a[184:187], v[148:151]\n"
"v_mfma_f32_16x16x32_bf16 v[148:151], a[152:155], a[140:143], v[148:151]\n"
"v_mfma_f32_16x16x32_bf16 v[168:171], v[32:35], v[144:147], v[168:171]\n"
"s_add_u32 s96, s21, 0x40000\n"
"s_add_u32 m0, s30, 0x3000\n"
"v_mfma_f32_16x16x32_bf16 v[168:171], v[36:39], a[148:151], v[168:171]\n"
"buffer_load_dwordx4 v140, s[52:55], s96 offen offset:0 lds\n"
"v_mfma_f32_16x16x32_bf16 a[20:23], v[100:103], v[144:147], a[20:23]\n"
"v_mfma_f32_16x16x32_bf16 a[20:23], v[52:55], a[148:151], a[20:23]\n"
"v_mfma_f32_16x16x32_bf16 a[24:27], v[48:51], v[144:147], a[24:27]\n"
"v_mfma_f32_16x16x32_bf16 a[24:27], v[60:63], a[148:151], a[24:27]\n"
"buffer_load_dwordx4 v141, s[52:55], s96 offen offset:1024 lds\n"
"v_mfma_f32_16x16x32_bf16 a[28:31], v[76:79], v[144:147], a[28:31]\n"
"v_mfma_f32_16x16x32_bf16 a[28:31], v[40:43], a[148:151], a[28:31]\n"
"s_waitcnt vmcnt(14)\n"
"v_cmp_ne_u64 s[36:37], s[86:87], v[28:29]\n"
"v_mfma_f32_16x16x32_bf16 v[204:207], a[136:139], v[144:147], v[204:207]\n"
"s_cmp_lg_u64 s[36:37], 0\n"
"s_cbranch_scc1 .Lmha_SPB\n"
".Lmha_SPBR:\n"
"s_add_u32 s78, s21, 0x47800\n"
"v_mfma_f32_16x16x32_bf16 v[204:207], a[144:147], a[148:151], v[204:207]\n"
"buffer_load_dwordx4 v140, s[52:55], s78 offen offset:2048 lds\n"
"v_mfma_f32_16x16x32_bf16 a[32:35], v[56:59], v[144:147], a[32:35]\n"
"v_mfma_f32_16x16x32_bf16 a[32:35], v[64:67], a[148:151], a[32:35]\n"
"v_mfma_f32_16x16x32_bf16 a[36:39], a[168:171], v[144:147], a[36:39]\n"
"v_mfma_f32_16x16x32_bf16 a[36:39], v[68:71], a[148:151], a[36:39]\n"
"s_getpc_b64 s[98:99]\n"
"buffer_load_dwordx4 v141, s[52:55], s78 offen offset:3072 lds\n"
"v_mfma_f32_16x16x32_bf16 a[40:43], v[44:47], v[144:147], a[40:43]\n"
"v_mfma_f32_16x16x32_bf16 a[40:43], a[152:155], a[148:151], a[40:43]\n"
"v_mfma_f32_16x16x32_bf16 v[172:175], v[32:35], v[116:119], v[172:175]\n"
"s_add_u32 s75, s14, 0x9000\n"
"global_load_dword v18, v0, s[98:99] offset:1536\n"
"v_mfma_f32_16x16x32_bf16 v[172:175], v[36:39], a[156:159], v[172:175]\n"
"buffer_load_dwordx4 v[88:91], v21, s[56:59], s75 offen offset:0 sc1\n"
"v_mfma_f32_16x16x32_bf16 a[44:47], v[100:103], v[116:119], a[44:47]\n"
"v_mfma_f32_16x16x32_bf16 a[44:47], v[52:55], a[156:159], a[44:47]\n"
"v_mfma_f32_16x16x32_bf16 a[48:51], v[48:51], v[116:119], a[48:51]\n"
"s_add_u32 s76, s16, 0x9000\n"
"v_mfma_f32_16x16x32_bf16 a[48:51], v[60:63], a[156:159], a[48:51]\n"
"buffer_load_dwordx4 a[188:191], v21, s[56:59], s76 offen offset:0 sc1\n"
"v_mfma_f32_16x16x32_bf16 a[52:55], v[76:79], v[116:119], a[52:55]\n"
"v_mfma_f32_16x16x32_bf16 a[52:55], v[40:43], a[156:159], a[52:55]\n"
"v_mfma_f32_16x16x32_bf16 v[208:211], a[136:139], v[116:119], v[208:211]\n"
"v_mfma_f32_16x16x32_bf16 v[208:211], a[144:147], a[156:159], v[208:211]\n"
"buffer_load_dwordx4 v[96:99], v21, s[56:59], s75 offen offset:1024 sc1\n"
"v_mfma_f32_16x16x32_bf16 a[56:59], v[56:59], v[116:119], a[56:59]\n"
"v_mfma_f32_16x16x32_bf16 a[56:59], v[64:67], a[156:159], a[56:59]\n"
"v_mfma_f32_16x16x32_bf16 a[60:63], a[168:171], v[116:119], a[60:63]\n"
"v_mfma_f32_16x16x32_bf16 a[60:63], v[68:71], a[156:159], a[60:63]\n"
"buffer_load_dwordx4 a[196:199], v21, s[56:59], s76 offen offset:1024 sc1\n"
"v_mfma_f32_16x16x32_bf16 a[112:115], v[44:47], v[116:119], a[112:115]\n"
"v_mfma_f32_16x16x32_bf16 a[112:115], a[152:155], a[156:159], a[112:115]\n"
"v_mfma_f32_16x16x32_bf16 v[176:179], v[32:35], a[192:195], v[176:179]\n"
"v_mfma_f32_16x16x32_bf16 v[176:179], v[36:39], a[164:167], v[176:179]\n"
"buffer_load_dwordx4 v[104:107], v21, s[56:59], s75 offen offset:2048 sc1\n"
"v_mfma_f32_16x16x32_bf16 a[116:119], v[100:103], a[192:195], a[116:119]\n"
"v_mfma_f32_16x16x32_bf16 a[116:119], v[52:55], a[164:167], a[116:119]\n"
"v_mfma_f32_16x16x32_bf16 a[120:123], v[48:51], a[192:195], a[120:123]\n"
"v_mfma_f32_16x16x32_bf16 a[120:123], v[60:63], a[164:167], a[120:123]\n"
"buffer_load_dwordx4 v[112:115], v21, s[56:59], s76 offen offset:2048 sc1\n"
"v_mfma_f32_16x16x32_bf16 a[124:127], v[76:79], a[192:195], a[124:127]\n"
"v_mfma_f32_16x16x32_bf16 a[124:127], v[40:43], a[164:167], a[124:127]\n"
"v_mfma_f32_16x16x32_bf16 v[212:215], a[136:139], a[192:195], v[212:215]\n"
"v_mfma_f32_16x16x32_bf16 v[212:215], a[144:147], a[164:167], v[212:215]\n"
"buffer_load_dwordx4 v[120:123], v21, s[56:59], s75 offen offset:3072 sc1\n"
"v_mfma_f32_16x16x32_bf16 a[200:203], v[56:59], a[192:195], a[200:203]\n"
"v_mfma_f32_16x16x32_bf16 a[200:203], v[64:67], a[164:167], a[200:203]\n"
"v_mfma_f32_16x16x32_bf16 a[204:207], a[168:171], a[192:195], a[204:207]\n"
"v_mfma_f32_16x16x32_bf16 a[204:207], v[68:71], a[164:167], a[204:207]\n"
"buffer_load_dwordx4 a[176:179], v21, s[56:59], s76 offen offset:3072 sc1\n"
"v_mfma_f32_16x16x32_bf16 a[64:67], v[44:47], a[192:195], a[64:67]\n"
"v_mfma_f32_16x16x32_bf16 a[64:67], a[152:155], a[164:167], a[64:67]\n"
"v_mfma_f32_16x16x32_bf16 v[180:183], v[32:35], v[124:127], v[180:183]\n"
"s_waitcnt vmcnt(23)\n"
"s_add_u32 s84, s14, 0xa000\n"
"v_mfma_f32_16x16x32_bf16 v[180:183], v[36:39], a[172:175], v[180:183]\n"
"buffer_load_dwordx4 v[128:131], v21, s[56:59], s84 offen offset:0 sc1\n"
"v_mfma_f32_16x16x32_bf16 a[68:71], v[100:103], v[124:127], a[68:71]\n"
"v_mfma_f32_16x16x32_bf16 a[68:71], v[52:55], a[172:175], a[68:71]\n"
"v_mfma_f32_16x16x32_bf16 a[72:75], v[48:51], v[124:127], a[72:75]\n"
"s_add_u32 s77, s16, 0xa000\n"
"v_mfma_f32_16x16x32_bf16 a[72:75], v[60:63], a[172:175], a[72:75]\n"
"buffer_load_dwordx4 v[136:139], v21, s[56:59], s77 offen offset:0 sc1\n"
"v_mfma_f32_16x16x32_bf16 a[76:79], v[76:79], v[124:127], a[76:79]\n"
"v_mfma_f32_16x16x32_bf16 a[76:79], v[40:43], a[172:175], a[76:79]\n"
"v_mfma_f32_16x16x32_bf16 v[216:219], a[136:139], v[124:127], v[216:219]\n"
"v_mfma_f32_16x16x32_bf16 v[216:219], a[144:147], a[172:175], v[216:219]\n"
"buffer_load_dwordx4 a[132:135], v21, s[56:59], s84 offen offset:1024 sc1\n"
"v_mfma_f32_16x16x32_bf16 a[80:83], v[56:59], v[124:127], a[80:83]\n"
"v_mfma_f32_16x16x32_bf16 a[80:83], v[64:67], a[172:175], a[80:83]\n"
"v_mfma_f32_16x16x32_bf16 a[84:87], a[168:171], v[124:127], a[84:87]\n"
"v_mfma_f32_16x16x32_bf16 a[84:87], v[68:71], a[172:175], a[84:87]\n"
"buffer_load_dwordx4 a[184:187], v21, s[56:59], s77 offen offset:1024 sc1\n"
"v_mfma_f32_16x16x32_bf16 a[88:91], v[44:47], v[124:127], a[88:91]\n"
"v_mfma_f32_16x16x32_bf16 a[88:91], a[152:155], a[172:175], a[88:91]\n"
"s_waitcnt vmcnt(26)\n"
"v_mfma_f32_16x16x32_bf16 v[184:187], v[32:35], v[80:83], v[184:187]\n"
"s_waitcnt vmcnt(25)\n"
"ds_read_b64_tr_b16 v[32:33], v143 offset:8192\n"
"ds_read_b64_tr_b16 v[34:35], v143 offset:10240\n"
"v_mfma_f32_16x16x32_bf16 v[184:187], v[36:39], a[180:183], v[184:187]\n"
"buffer_load_dwordx4 a[140:143], v21, s[56:59], s84 offen offset:2048 sc1\n"
"ds_read_b64_tr_b16 v[36:37], v142 offset:8704\n"
"v_mfma_f32_16x16x32_bf16 a[92:95], v[100:103], v[80:83], a[92:95]\n"
"ds_read_b64_tr_b16 v[38:39], v142 offset:10752\n"
"ds_read_b64_tr_b16 v[100:101], v143 offset:8704\n"
"v_mfma_f32_16x16x32_bf16 a[92:95], v[52:55], a[180:183], a[92:95]\n"
"ds_read_b64_tr_b16 v[52:53], v142 offset:16384\n"
"ds_read_b64_tr_b16 v[54:55], v142 offset:18432\n"
"v_mfma_f32_16x16x32_bf16 a[96:99], v[48:51], v[80:83], a[96:99]\n"
"ds_read_b64_tr_b16 v[48:49], v143 offset:16384\n"
"ds_read_b64_tr_b16 v[102:103], v143 offset:10752\n"
"v_mfma_f32_16x16x32_bf16 a[96:99], v[60:63], a[180:183], a[96:99]\n"
"buffer_load_dwordx4 v[144:147], v21, s[56:59], s77 offen offset:2048 sc1\n"
"ds_read_b64_tr_b16 v[50:51], v143 offset:18432\n"
"v_mfma_f32_16x16x32_bf16 a[100:103], v[76:79], v[80:83], a[100:103]\n"
"ds_read_b64_tr_b16 v[60:61], v142 offset:16896\n"
"ds_read_b64_tr_b16 v[62:63], v142 offset:18944\n"
"v_mfma_f32_16x16x32_bf16 a[100:103], v[40:43], a[180:183], a[100:103]\n"
"ds_read_b64_tr_b16 v[40:41], v142 offset:24576\n"
"ds_read_b64_tr_b16 v[42:43], v142 offset:26624\n"
"v_mfma_f32_16x16x32_bf16 v[220:223], a[136:139], v[80:83], v[220:223]\n"
"ds_read_b64_tr_b16 a[136:137], v143 offset:24576\n"
"ds_read_b64_tr_b16 a[138:139], v143 offset:26624\n"
"v_mfma_f32_16x16x32_bf16 v[220:223], a[144:147], a[180:183], v[220:223]\n"
"buffer_load_dwordx4 a[148:151], v21, s[56:59], s84 offen offset:3072 sc1\n"
"ds_read_b64_tr_b16 v[76:77], v143 offset:16896\n"
"v_mfma_f32_16x16x32_bf16 a[104:107], v[56:59], v[80:83], a[104:107]\n"
"ds_read_b64_tr_b16 v[78:79], v143 offset:18944\n"
"ds_read_b64_tr_b16 a[144:145], v142 offset:25088\n"
"v_mfma_f32_16x16x32_bf16 a[104:107], v[64:67], a[180:183], a[104:107]\n"
"s_waitcnt vmcnt(24)\n"
"ds_read_b64_tr_b16 v[64:65], v142 offset:4096\n"
"ds_read_b64_tr_b16 v[66:67], v142 offset:6144\n"
"v_mfma_f32_16x16x32_bf16 a[108:111], a[168:171], v[80:83], a[108:111]\n"
"ds_read_b64_tr_b16 a[146:147], v142 offset:27136\n"
"ds_read_b64_tr_b16 a[168:169], v143 offset:4096\n"
"v_mfma_f32_16x16x32_bf16 a[108:111], v[68:71], a[180:183], a[108:111]\n"
"buffer_load_dwordx4 v[116:119], v21, s[56:59], s77 offen offset:3072 sc1\n"
"ds_read_b64_tr_b16 v[56:57], v143 offset:25088\n"
"v_mfma_f32_16x16x32_bf16 a[128:131], v[44:47], v[80:83], a[128:131]\n"
"s_add_u32 s96, s14, 0xb000\n"
"ds_read_b64_tr_b16 v[58:59], v143 offset:27136\n"
"ds_read_b64_tr_b16 a[170:171], v143 offset:6144\n"
"v_mfma_f32_16x16x32_bf16 a[128:131], a[152:155], a[180:183], a[128:131]\n"
"s_waitcnt vmcnt(15)\n"
"ds_read_b64_tr_b16 a[152:153], v142 offset:0\n"
"ds_read_b64_tr_b16 a[154:155], v142 offset:2048\n"
"v_mfma_f32_16x16x32_bf16 v[152:155], v[92:95], v[88:91], v[152:155]\n"
"s_waitcnt vmcnt(14)\n"
"ds_read_b64_tr_b16 v[68:69], v142 offset:4608\n"
"ds_read_b64_tr_b16 v[70:71], v142 offset:6656\n"
"v_mfma_f32_16x16x32_bf16 v[152:155], v[72:75], a[188:191], v[152:155]\n"
"buffer_load_dwordx4 a[156:159], v21, s[56:59], s96 offen offset:0 sc1\n"
"ds_read_b64_tr_b16 v[44:45], v143 offset:4608\n"
"v_mfma_f32_16x16x32_bf16 a[208:211], v[84:87], v[88:91], a[208:211]\n"
"s_waitcnt lgkmcnt(15)\n"
"ds_read_b64_tr_b16 v[46:47], v143 offset:6656\n"
"v_mfma_f32_16x16x32_bf16 a[208:211], v[32:35], a[188:191], a[208:211]\n"
"v_mfma_f32_16x16x32_bf16 a[212:215], v[108:111], v[88:91], a[212:215]\n"
"s_add_u32 s78, s16, 0xb000\n"
"v_mfma_f32_16x16x32_bf16 a[212:215], v[36:39], a[188:191], a[212:215]\n"
"buffer_load_dwordx4 a[192:195], v21, s[56:59], s78 offen offset:0 sc1\n"
"v_mfma_f32_16x16x32_bf16 a[216:219], a[160:163], v[88:91], a[216:219]\n"
"v_mfma_f32_16x16x32_bf16 a[216:219], v[100:103], a[188:191], a[216:219]\n"
"v_mfma_f32_16x16x32_bf16 v[188:191], v[52:55], v[88:91], v[188:191]\n"
"s_add_u32 s75, s18, 0x9000\n"
"v_mfma_f32_16x16x32_bf16 v[188:191], v[40:43], a[188:191], v[188:191]\n"
"buffer_load_dwordx4 a[164:167], v21, s[56:59], s75 offen offset:0 sc1\n"
"v_mfma_f32_16x16x32_bf16 a[220:223], v[48:51], v[88:91], a[220:223]\n"
"v_mfma_f32_16x16x32_bf16 a[220:223], a[136:139], a[188:191], a[220:223]\n"
"v_mfma_f32_16x16x32_bf16 a[224:227], v[60:63], v[88:91], a[224:227]\n"
"s_waitcnt lgkmcnt(2)\n"
"v_mfma_f32_16x16x32_bf16 a[224:227], a[144:147], a[188:191], a[224:227]\n"
"buffer_load_dwordx4 v[124:127], v21, s[56:59], s75 offen offset:1024 sc1\n"
"v_mfma_f32_16x16x32_bf16 a[228:231], v[76:79], v[88:91], a[228:231]\n"
"v_mfma_f32_16x16x32_bf16 a[228:231], v[56:59], a[188:191], a[228:231]\n"
"s_waitcnt vmcnt(17)\n"
"v_mfma_f32_16x16x32_bf16 v[156:159], v[92:95], v[96:99], v[156:159]\n"
"s_waitcnt vmcnt(16)\n"
"v_mfma_f32_16x16x32_bf16 v[156:159], v[72:75], a[196:199], v[156:159]\n"
"buffer_load_dwordx4 a[172:175], v21, s[56:59], s75 offen offset:2048 sc1\n"
"v_mfma_f32_16x16x32_bf16 a[232:235], v[84:87], v[96:99], a[232:235]\n"
"v_mfma_f32_16x16x32_bf16 a[232:235], v[32:35], a[196:199], a[232:235]\n"
"v_mfma_f32_16x16x32_bf16 a[236:239], v[108:111], v[96:99], a[236:239]\n"
"v_mfma_f32_16x16x32_bf16 a[236:239], v[36:39], a[196:199], a[236:239]\n"
"buffer_load_dwordx4 v[80:83], v21, s[56:59], s75 offen offset:3072 sc1\n"
"v_mfma_f32_16x16x32_bf16 a[240:243], a[160:163], v[96:99], a[240:243]\n"
"v_mfma_f32_16x16x32_bf16 a[240:243], v[100:103], a[196:199], a[240:243]\n"
"v_mfma_f32_16x16x32_bf16 v[192:195], v[52:55], v[96:99], v[192:195]\n"
"s_add_u32 s76, s18, 0xa000\n"
"v_mfma_f32_16x16x32_bf16 v[192:195], v[40:43], a[196:199], v[192:195]\n"
"buffer_load_dwordx4 a[180:183], v21, s[56:59], s76 offen offset:0 sc1\n"
"v_mfma_f32_16x16x32_bf16 a[244:247], v[48:51], v[96:99], a[244:247]\n"
"v_mfma_f32_16x16x32_bf16 a[244:247], a[136:139], a[196:199], a[244:247]\n"
"v_mfma_f32_16x16x32_bf16 a[248:251], v[60:63], v[96:99], a[248:251]\n"
"s_add_u32 s84, s23, 0x40000\n"
"s_add_u32 m0, s30, 0x1000\n"
"v_mfma_f32_16x16x32_bf16 a[248:251], a[144:147], a[196:199], a[248:251]\n"
"s_waitcnt lgkmcnt(0)\n"
"buffer_load_dwordx4 v140, s[52:55], s84 offen offset:0 lds\n"
"v_mfma_f32_16x16x32_bf16 v[224:227], v[76:79], v[96:99], v[224:227]\n"
"v_mfma_f32_16x16x32_bf16 v[224:227], v[56:59], a[196:199], v[224:227]\n"
"s_waitcnt vmcnt(19)\n"
"v_mfma_f32_16x16x32_bf16 v[160:163], v[92:95], v[104:107], v[160:163]\n"
"s_waitcnt vmcnt(18)\n"
"v_mfma_f32_16x16x32_bf16 v[160:163], v[72:75], v[112:115], v[160:163]\n"
"buffer_load_dwordx4 v141, s[52:55], s84 offen offset:1024 lds\n"
"v_mfma_f32_16x16x32_bf16 a[252:255], v[84:87], v[104:107], a[252:255]\n"
"v_mfma_f32_16x16x32_bf16 a[252:255], v[32:35], v[112:115], a[252:255]\n"
"v_mfma_f32_16x16x32_bf16 a[0:3], v[108:111], v[104:107], a[0:3]\n"
"s_add_u32 s77, s23, 0x47800\n"
"v_mfma_f32_16x16x32_bf16 a[0:3], v[36:39], v[112:115], a[0:3]\n"
"buffer_load_dwordx4 v140, s[52:55], s77 offen offset:2048 lds\n"
"v_mfma_f32_16x16x32_bf16 a[4:7], a[160:163], v[104:107], a[4:7]\n"
"v_mfma_f32_16x16x32_bf16 a[4:7], v[100:103], v[112:115], a[4:7]\n"
"v_mfma_f32_16x16x32_bf16 v[196:199], v[52:55], v[104:107], v[196:199]\n"
"v_mfma_f32_16x16x32_bf16 v[196:199], v[40:43], v[112:115], v[196:199]\n"
"buffer_load_dwordx4 v141, s[52:55], s77 offen offset:3072 lds\n"
"v_mfma_f32_16x16x32_bf16 v[228:231], v[48:51], v[104:107], v[228:231]\n"
"v_mfma_f32_16x16x32_bf16 v[228:231], a[136:139], v[112:115], v[228:231]\n"
"v_mfma_f32_16x16x32_bf16 v[232:235], v[60:63], v[104:107], v[232:235]\n"
"s_add_u32 s96, s89, 0x40000\n"
"s_add_u32 m0, s30, 0x2000\n"
"v_mfma_f32_16x16x32_bf16 v[232:235], a[144:147], v[112:115], v[232:235]\n"
"buffer_load_dwordx4 v140, s[52:55], s96 offen offset:0 lds\n"
"v_mfma_f32_16x16x32_bf16 v[236:239], v[76:79], v[104:107], v[236:239]\n"
"v_mfma_f32_16x16x32_bf16 v[236:239], v[56:59], v[112:115], v[236:239]\n"
"s_waitcnt vmcnt(21)\n"
"v_mfma_f32_16x16x32_bf16 v[164:167], v[92:95], v[120:123], v[164:167]\n"
"s_waitcnt vmcnt(20)\n"
"v_mfma_f32_16x16x32_bf16 v[164:167], v[72:75], a[176:179], v[164:167]\n"
"buffer_load_dwordx4 v141, s[52:55], s96 offen offset:1024 lds\n"
"v_mfma_f32_16x16x32_bf16 a[8:11], v[84:87], v[120:123], a[8:11]\n"
"v_mfma_f32_16x16x32_bf16 a[8:11], v[32:35], a[176:179], a[8:11]\n"
"v_mfma_f32_16x16x32_bf16 a[12:15], v[108:111], v[120:123], a[12:15]\n"
"s_add_u32 s78, s89, 0x47800\n"
"v_mfma_f32_16x16x32_bf16 a[12:15], v[36:39], a[176:179], a[12:15]\n"
"buffer_load_dwordx4 v140, s[52:55], s78 offen offset:2048 lds\n"
"v_mfma_f32_16x16x32_bf16 a[16:19], a[160:163], v[120:123], a[16:19]\n"
"v_mfma_f32_16x16x32_bf16 a[16:19], v[100:103], a[176:179], a[16:19]\n"
"v_mfma_f32_16x16x32_bf16 v[200:203], v[52:55], v[120:123], v[200:203]\n"
"v_mfma_f32_16x16x32_bf16 v[200:203], v[40:43], a[176:179], v[200:203]\n"
"buffer_load_dwordx4 v141, s[52:55], s78 offen offset:3072 lds\n"
"v_mfma_f32_16x16x32_bf16 v[240:243], v[48:51], v[120:123], v[240:243]\n"
"v_mfma_f32_16x16x32_bf16 v[240:243], a[136:139], a[176:179], v[240:243]\n"
"v_mfma_f32_16x16x32_bf16 v[132:135], v[60:63], v[120:123], v[132:135]\n"
"s_add_u32 s75, s91, 0x40000\n"
"s_add_u32 m0, s30, 0x4000\n"
"v_mfma_f32_16x16x32_bf16 v[132:135], a[144:147], a[176:179], v[132:135]\n"
"buffer_load_dwordx4 v140, s[52:55], s75 offen offset:0 lds\n"
"v_mfma_f32_16x16x32_bf16 v[148:151], v[76:79], v[120:123], v[148:151]\n"
"v_mfma_f32_16x16x32_bf16 v[148:151], v[56:59], a[176:179], v[148:151]\n"
"s_waitcnt vmcnt(23)\n"
"v_mfma_f32_16x16x32_bf16 v[168:171], v[92:95], v[128:131], v[168:171]\n"
"s_waitcnt vmcnt(22)\n"
"v_mfma_f32_16x16x32_bf16 v[168:171], v[72:75], v[136:139], v[168:171]\n"
"buffer_load_dwordx4 v141, s[52:55], s75 offen offset:1024 lds\n"
"v_mfma_f32_16x16x32_bf16 a[20:23], v[84:87], v[128:131], a[20:23]\n"
"v_mfma_f32_16x16x32_bf16 a[20:23], v[32:35], v[136:139], a[20:23]\n"
"v_mfma_f32_16x16x32_bf16 a[24:27], v[108:111], v[128:131], a[24:27]\n"
"s_add_u32 s76, s91, 0x47800\n"
"v_mfma_f32_16x16x32_bf16 a[24:27], v[36:39], v[136:139], a[24:27]\n"
"buffer_load_dwordx4 v140, s[52:55], s76 offen offset:2048 lds\n"
"v_mfma_f32_16x16x32_bf16 a[28:31], a[160:163], v[128:131], a[28:31]\n"
"v_mfma_f32_16x16x32_bf16 a[28:31], v[100:103], v[136:139], a[28:31]\n"
"v_mfma_f32_16x16x32_bf16 v[204:207], v[52:55], v[128:131], v[204:207]\n"
"v_mfma_f32_16x16x32_bf16 v[204:207], v[40:43], v[136:139], v[204:207]\n"
"buffer_load_dwordx4 v141, s[52:55], s76 offen offset:3072 lds\n"
"v_mfma_f32_16x16x32_bf16 a[32:35], v[48:51], v[128:131], a[32:35]\n"
"v_mfma_f32_16x16x32_bf16 a[32:35], a[136:139], v[136:139], a[32:35]\n"
"v_mfma_f32_16x16x32_bf16 a[36:39], v[60:63], v[128:131], a[36:39]\n"
"s_add_u32 s84, s18, 0xa000\n"
"v_mfma_f32_16x16x32_bf16 a[36:39], a[144:147], v[136:139], a[36:39]\n"
"buffer_load_dwordx4 v[88:91], v21, s[56:59], s84 offen offset:1024 sc1\n"
"v_mfma_f32_16x16x32_bf16 a[40:43], v[76:79], v[128:131], a[40:43]\n"
"v_mfma_f32_16x16x32_bf16 a[40:43], v[56:59], v[136:139], a[40:43]\n"
"s_waitcnt vmcnt(25)\n"
"v_mfma_f32_16x16x32_bf16 v[172:175], v[92:95], a[132:135], v[172:175]\n"
"s_waitcnt vmcnt(24)\n"
"v_mfma_f32_16x16x32_bf16 v[172:175], v[72:75], a[184:187], v[172:175]\n"
"buffer_load_dwordx4 a[188:191], v21, s[56:59], s84 offen offset:2048 sc1\n"
"v_mfma_f32_16x16x32_bf16 a[44:47], v[84:87], a[132:135], a[44:47]\n"
"v_mfma_f32_16x16x32_bf16 a[44:47], v[32:35], a[184:187], a[44:47]\n"
"v_mfma_f32_16x16x32_bf16 a[48:51], v[108:111], a[132:135], a[48:51]\n"
"v_mfma_f32_16x16x32_bf16 a[48:51], v[36:39], a[184:187], a[48:51]\n"
"buffer_load_dwordx4 v[96:99], v21, s[56:59], s84 offen offset:3072 sc1\n"
"v_mfma_f32_16x16x32_bf16 a[52:55], a[160:163], a[132:135], a[52:55]\n"
"v_mfma_f32_16x16x32_bf16 a[52:55], v[100:103], a[184:187], a[52:55]\n"
"v_mfma_f32_16x16x32_bf16 v[208:211], v[52:55], a[132:135], v[208:211]\n"
"s_add_u32 s77, s18, 0xb000\n"
"v_mfma_f32_16x16x32_bf16 v[208:211], v[40:43], a[184:187], v[208:211]\n"
"buffer_load_dwordx4 a[196:199], v21, s[56:59], s77 offen offset:0 sc1\n"
"v_mfma_f32_16x16x32_bf16 a[56:59], v[48:51], a[132:135], a[56:59]\n"
"v_mfma_f32_16x16x32_bf16 a[56:59], a[136:139], a[184:187], a[56:59]\n"
"v_mfma_f32_16x16x32_bf16 a[60:63], v[60:63], a[132:135], a[60:63]\n"
"s_add_u32 s96, s20, 0x9000\n"
"v_mfma_f32_16x16x32_bf16 a[60:63], a[144:147], a[184:187], a[60:63]\n"
"buffer_load_dwordx4 v[104:107], v21, s[56:59], s96 offen offset:0 sc1\n"
"v_mfma_f32_16x16x32_bf16 a[112:115], v[76:79], a[132:135], a[112:115]\n"
"v_mfma_f32_16x16x32_bf16 a[112:115], v[56:59], a[184:187], a[112:115]\n"
"s_waitcnt vmcnt(27)\n"
"v_mfma_f32_16x16x32_bf16 v[176:179], v[92:95], a[140:143], v[176:179]\n"
"s_waitcnt vmcnt(26)\n"
"s_add_u32 s78, s22, 0x9000\n"
"v_mfma_f32_16x16x32_bf16 v[176:179], v[72:75], v[144:147], v[176:179]\n"
"buffer_load_dwordx4 v[112:115], v21, s[56:59], s78 offen offset:0 sc1\n"
"v_mfma_f32_16x16x32_bf16 a[116:119], v[84:87], a[140:143], a[116:119]\n"
"v_mfma_f32_16x16x32_bf16 a[116:119], v[32:35], v[144:147], a[116:119]\n"
"v_mfma_f32_16x16x32_bf16 a[120:123], v[108:111], a[140:143], a[120:123]\n"
"s_add_u32 s75, s88, 0x9000\n"
"v_mfma_f32_16x16x32_bf16 a[120:123], v[36:39], v[144:147], a[120:123]\n"
"buffer_load_dwordx4 v[120:123], v21, s[56:59], s75 offen offset:0 sc1\n"
"v_mfma_f32_16x16x32_bf16 a[124:127], a[160:163], a[140:143], a[124:127]\n"
"v_mfma_f32_16x16x32_bf16 a[124:127], v[100:103], v[144:147], a[124:127]\n"
"v_mfma_f32_16x16x32_bf16 v[212:215], v[52:55], a[140:143], v[212:215]\n"
"s_add_u32 s76, s90, 0x9000\n"
"v_mfma_f32_16x16x32_bf16 v[212:215], v[40:43], v[144:147], v[212:215]\n"
"buffer_load_dwordx4 a[176:179], v21, s[56:59], s76 offen offset:0 sc1\n"
"v_mfma_f32_16x16x32_bf16 a[200:203], v[48:51], a[140:143], a[200:203]\n"
"v_mfma_f32_16x16x32_bf16 a[200:203], a[136:139], v[144:147], a[200:203]\n"
"v_mfma_f32_16x16x32_bf16 a[204:207], v[60:63], a[140:143], a[204:207]\n"
"v_mfma_f32_16x16x32_bf16 a[204:207], a[144:147], v[144:147], a[204:207]\n"
"buffer_load_dwordx4 v[128:131], v21, s[56:59], s96 offen offset:1024 sc1\n"
"v_mfma_f32_16x16x32_bf16 a[64:67], v[76:79], a[140:143], a[64:67]\n"
"v_mfma_f32_16x16x32_bf16 a[64:67], v[56:59], v[144:147], a[64:67]\n"
"s_waitcnt vmcnt(29)\n"
"v_mfma_f32_16x16x32_bf16 v[180:183], v[92:95], a[148:151], v[180:183]\n"
"s_waitcnt vmcnt(28)\n"
"v_mfma_f32_16x16x32_bf16 v[180:183], v[72:75], v[116:119], v[180:183]\n"
"buffer_load_dwordx4 v[136:139], v21, s[56:59], s78 offen offset:1024 sc1\n"
"v_mfma_f32_16x16x32_bf16 a[68:71], v[84:87], a[148:151], a[68:71]\n"
"v_mfma_f32_16x16x32_bf16 a[68:71], v[32:35], v[116:119], a[68:71]\n"
"v_mfma_f32_16x16x32_bf16 a[72:75], v[108:111], a[148:151], a[72:75]\n"
"v_mfma_f32_16x16x32_bf16 a[72:75], v[36:39], v[116:119], a[72:75]\n"
"buffer_load_dwordx4 a[132:135], v21, s[56:59], s75 offen offset:1024 sc1\n"
"v_mfma_f32_16x16x32_bf16 a[76:79], a[160:163], a[148:151], a[76:79]\n"
"v_mfma_f32_16x16x32_bf16 a[76:79], v[100:103], v[116:119], a[76:79]\n"
"v_mfma_f32_16x16x32_bf16 v[216:219], v[52:55], a[148:151], v[216:219]\n"
"v_mfma_f32_16x16x32_bf16 v[216:219], v[40:43], v[116:119], v[216:219]\n"
"buffer_load_dwordx4 a[184:187], v21, s[56:59], s76 offen offset:1024 sc1\n"
"v_mfma_f32_16x16x32_bf16 a[80:83], v[48:51], a[148:151], a[80:83]\n"
"v_mfma_f32_16x16x32_bf16 a[80:83], a[136:139], v[116:119], a[80:83]\n"
"v_mfma_f32_16x16x32_bf16 a[84:87], v[60:63], a[148:151], a[84:87]\n"
"v_mfma_f32_16x16x32_bf16 a[84:87], a[144:147], v[116:119], a[84:87]\n"
"buffer_load_dwordx4 a[140:143], v21, s[56:59], s96 offen offset:2048 sc1\n"
"v_mfma_f32_16x16x32_bf16 a[88:91], v[76:79], a[148:151], a[88:91]\n"
"v_mfma_f32_16x16x32_bf16 a[88:91], v[56:59], v[116:119], a[88:91]\n"
"s_waitcnt vmcnt(31)\n"
"v_mfma_f32_16x16x32_bf16 v[184:187], v[92:95], a[156:159], v[184:187]\n"
"s_waitcnt vmcnt(30)\n"
"ds_read_b64_tr_b16 v[92:93], v143 offset:0\n"
"ds_read_b64_tr_b16 v[94:95], v143 offset:2048\n"
"v_mfma_f32_16x16x32_bf16 v[184:187], v[72:75], a[192:195], v[184:187]\n"
"buffer_load_dwordx4 v[144:147], v21, s[56:59], s78 offen offset:2048 sc1\n"
"ds_read_b64_tr_b16 v[72:73], v142 offset:512\n"
"v_mfma_f32_16x16x32_bf16 a[92:95], v[84:87], a[156:159], a[92:95]\n"
"ds_read_b64_tr_b16 v[74:75], v142 offset:2560\n"
"ds_read_b64_tr_b16 v[84:85], v143 offset:512\n"
"v_mfma_f32_16x16x32_bf16 a[92:95], v[32:35], a[192:195], a[92:95]\n"
"ds_read_b64_tr_b16 v[32:33], v143 offset:12288\n"
"ds_read_b64_tr_b16 v[34:35], v143 offset:14336\n"
"v_mfma_f32_16x16x32_bf16 a[96:99], v[108:111], a[156:159], a[96:99]\n"
"ds_read_b64_tr_b16 v[108:109], v142 offset:12800\n"
"ds_read_b64_tr_b16 v[86:87], v143 offset:2560\n"
"v_mfma_f32_16x16x32_bf16 a[96:99], v[36:39], a[192:195], a[96:99]\n"
"buffer_load_dwordx4 a[148:151], v21, s[56:59], s75 offen offset:2048 sc1\n"
"ds_read_b64_tr_b16 v[110:111], v142 offset:14848\n"
"v_mfma_f32_16x16x32_bf16 a[100:103], a[160:163], a[156:159], a[100:103]\n"
"ds_read_b64_tr_b16 v[36:37], v143 offset:12800\n"
"ds_read_b64_tr_b16 v[38:39], v143 offset:14848\n"
"v_mfma_f32_16x16x32_bf16 a[100:103], v[100:103], a[192:195], a[100:103]\n"
"s_waitcnt vmcnt(23)\n"
"ds_read_b64_tr_b16 v[100:101], v143 offset:4096\n"
"ds_read_b64_tr_b16 v[102:103], v143 offset:6144\n"
"v_mfma_f32_16x16x32_bf16 v[220:223], v[52:55], a[156:159], v[220:223]\n"
"ds_read_b64_tr_b16 v[52:53], v142 offset:4608\n"
"ds_read_b64_tr_b16 v[54:55], v142 offset:6656\n"
"v_mfma_f32_16x16x32_bf16 v[220:223], v[40:43], a[192:195], v[220:223]\n"
"buffer_load_dwordx4 v[116:119], v21, s[56:59], s76 offen offset:2048 sc1\n"
"ds_read_b64_tr_b16 a[160:161], v142 offset:12288\n"
"v_mfma_f32_16x16x32_bf16 a[104:107], v[48:51], a[156:159], a[104:107]\n"
"s_add_u32 m0, s30, 0x5000\n"
"ds_read_b64_tr_b16 a[162:163], v142 offset:14336\n"
"ds_read_b64_tr_b16 v[40:41], v143 offset:4608\n"
"v_mfma_f32_16x16x32_bf16 a[104:107], a[136:139], a[192:195], a[104:107]\n"
"s_waitcnt vmcnt(20)\n"
"ds_read_b64_tr_b16 a[136:137], v143 offset:8192\n"
"ds_read_b64_tr_b16 a[138:139], v143 offset:10240\n"
"v_mfma_f32_16x16x32_bf16 a[108:111], v[60:63], a[156:159], a[108:111]\n"
"s_add_u32 s84, s21, 0x40000\n"
"ds_read_b64_tr_b16 v[42:43], v143 offset:6656\n"
"ds_read_b64_tr_b16 v[60:61], v142 offset:8704\n"
"v_mfma_f32_16x16x32_bf16 a[108:111], a[144:147], a[192:195], a[108:111]\n"
"buffer_load_dwordx4 v23, s[52:55], s84 offen offset:0 lds\n"
"ds_read_b64_tr_b16 v[62:63], v142 offset:10752\n"
"v_mfma_f32_16x16x32_bf16 a[128:131], v[76:79], a[156:159], a[128:131]\n"
"ds_read_b64_tr_b16 v[48:49], v142 offset:4096\n"
"ds_read_b64_tr_b16 v[50:51], v142 offset:6144\n"
"v_mfma_f32_16x16x32_bf16 a[128:131], v[56:59], a[192:195], a[128:131]\n"
"s_waitcnt vmcnt(17)\n"
"ds_read_b64_tr_b16 v[56:57], v143 offset:16384\n"
"ds_read_b64_tr_b16 v[58:59], v143 offset:18432\n"
"v_mfma_f32_16x16x32_bf16 v[152:155], v[64:67], a[164:167], v[152:155]\n"
"ds_read_b64_tr_b16 a[144:145], v143 offset:8704\n"
"ds_read_b64_tr_b16 a[146:147], v143 offset:10752\n"
"v_mfma_f32_16x16x32_bf16 a[208:211], a[168:171], a[164:167], a[208:211]\n"
"buffer_load_dwordx4 v24, s[52:55], s84 offen offset:1024 lds\n"
"ds_read_b64_tr_b16 v[76:77], v142 offset:8192\n"
"v_mfma_f32_16x16x32_bf16 a[212:215], v[68:71], a[164:167], a[212:215]\n"
"ds_read_b64_tr_b16 v[78:79], v142 offset:10240\n"
"v_mfma_f32_16x16x32_bf16 a[216:219], v[44:47], a[164:167], a[216:219]\n"
"v_mfma_f32_16x16x32_bf16 v[188:191], a[152:155], a[164:167], v[188:191]\n"
"s_waitcnt lgkmcnt(15)\n"
"s_add_u32 s77, s21, 0x47800\n"
"v_mfma_f32_16x16x32_bf16 a[220:223], v[92:95], a[164:167], a[220:223]\n"
"buffer_load_dwordx4 v23, s[52:55], s77 offen offset:2048 lds\n"
"v_mfma_f32_16x16x32_bf16 a[224:227], v[72:75], a[164:167], a[224:227]\n"
"v_mfma_f32_16x16x32_bf16 a[228:231], v[84:87], a[164:167], a[228:231]\n"
"v_mfma_f32_16x16x32_bf16 v[156:159], v[64:67], v[124:127], v[156:159]\n"
"v_mfma_f32_16x16x32_bf16 a[232:235], a[168:171], v[124:127], a[232:235]\n"
"buffer_load_dwordx4 v24, s[52:55], s77 offen offset:3072 lds\n"
"v_mfma_f32_16x16x32_bf16 a[236:239], v[68:71], v[124:127], a[236:239]\n"
"v_mfma_f32_16x16x32_bf16 a[240:243], v[44:47], v[124:127], a[240:243]\n"
"v_mfma_f32_16x16x32_bf16 v[192:195], a[152:155], v[124:127], v[192:195]\n"
"v_mfma_f32_16x16x32_bf16 a[244:247], v[92:95], v[124:127], a[244:247]\n"
"buffer_load_dwordx4 a[156:159], v21, s[56:59], s96 offen offset:3072 sc1\n"
"v_mfma_f32_16x16x32_bf16 a[248:251], v[72:75], v[124:127], a[248:251]\n"
"v_mfma_f32_16x16x32_bf16 v[224:227], v[84:87], v[124:127], v[224:227]\n"
"v_mfma_f32_16x16x32_bf16 v[160:163], v[64:67], a[172:175], v[160:163]\n"
"v_mfma_f32_16x16x32_bf16 a[252:255], a[168:171], a[172:175], a[252:255]\n"
"buffer_load_dwordx4 a[192:195], v21, s[56:59], s78 offen offset:3072 sc1\n"
"v_mfma_f32_16x16x32_bf16 a[0:3], v[68:71], a[172:175], a[0:3]\n"
"v_mfma_f32_16x16x32_bf16 a[4:7], v[44:47], a[172:175], a[4:7]\n"
"v_mfma_f32_16x16x32_bf16 v[196:199], a[152:155], a[172:175], v[196:199]\n"
"v_mfma_f32_16x16x32_bf16 v[228:231], v[92:95], a[172:175], v[228:231]\n"
"buffer_load_dwordx4 a[164:167], v21, s[56:59], s75 offen offset:3072 sc1\n"
"v_mfma_f32_16x16x32_bf16 v[232:235], v[72:75], a[172:175], v[232:235]\n"
"v_mfma_f32_16x16x32_bf16 v[236:239], v[84:87], a[172:175], v[236:239]\n"
"v_mfma_f32_16x16x32_bf16 v[164:167], v[64:67], v[80:83], v[164:167]\n"
"v_mfma_f32_16x16x32_bf16 a[8:11], a[168:171], v[80:83], a[8:11]\n"
"buffer_load_dwordx4 v[124:127], v21, s[56:59], s76 offen offset:3072 sc1\n"
"v_mfma_f32_16x16x32_bf16 a[12:15], v[68:71], v[80:83], a[12:15]\n"
"v_mfma_f32_16x16x32_bf16 a[16:19], v[44:47], v[80:83], a[16:19]\n"
"v_mfma_f32_16x16x32_bf16 v[200:203], a[152:155], v[80:83], v[200:203]\n"
"s_add_u32 s84, s20, 0xa000\n"
"v_mfma_f32_16x16x32_bf16 v[240:243], v[92:95], v[80:83], v[240:243]\n"
"buffer_load_dwordx4 a[172:175], v21, s[56:59], s84 offen offset:0 sc1\n"
"v_mfma_f32_16x16x32_bf16 v[132:135], v[72:75], v[80:83], v[132:135]\n"
"v_mfma_f32_16x16x32_bf16 v[148:151], v[84:87], v[80:83], v[148:151]\n"
"v_mfma_f32_16x16x32_bf16 v[168:171], v[64:67], a[180:183], v[168:171]\n"
"s_add_u32 s77, s22, 0xa000\n"
"v_mfma_f32_16x16x32_bf16 a[20:23], a[168:171], a[180:183], a[20:23]\n"
"buffer_load_dwordx4 v[80:83], v21, s[56:59], s77 offen offset:0 sc1\n"
"v_mfma_f32_16x16x32_bf16 a[24:27], v[68:71], a[180:183], a[24:27]\n"
"v_mfma_f32_16x16x32_bf16 a[28:31], v[44:47], a[180:183], a[28:31]\n"
"v_mfma_f32_16x16x32_bf16 v[204:207], a[152:155], a[180:183], v[204:207]\n"
"s_add_u32 s96, s23, 0x40000\n"
"s_add_u32 m0, s30, 0x7000\n"
"v_mfma_f32_16x16x32_bf16 a[32:35], v[92:95], a[180:183], a[32:35]\n"
"buffer_load_dwordx4 v23, s[52:55], s96 offen offset:0 lds\n"
"v_mfma_f32_16x16x32_bf16 a[36:39], v[72:75], a[180:183], a[36:39]\n"
"v_mfma_f32_16x16x32_bf16 a[40:43], v[84:87], a[180:183], a[40:43]\n"
"s_waitcnt vmcnt(26)\n"
"v_mfma_f32_16x16x32_bf16 v[172:175], v[64:67], v[88:91], v[172:175]\n"
"v_mfma_f32_16x16x32_bf16 a[44:47], a[168:171], v[88:91], a[44:47]\n"
"buffer_load_dwordx4 v24, s[52:55], s96 offen offset:1024 lds\n"
"v_mfma_f32_16x16x32_bf16 a[48:51], v[68:71], v[88:91], a[48:51]\n"
"v_mfma_f32_16x16x32_bf16 a[52:55], v[44:47], v[88:91], a[52:55]\n"
"v_mfma_f32_16x16x32_bf16 v[208:211], a[152:155], v[88:91], v[208:211]\n"
"s_add_u32 s78, s23, 0x47800\n"
"v_mfma_f32_16x16x32_bf16 a[56:59], v[92:95], v[88:91], a[56:59]\n"
"buffer_load_dwordx4 v23, s[52:55], s78 offen offset:2048 lds\n"
"v_mfma_f32_16x16x32_bf16 a[60:63], v[72:75], v[88:91], a[60:63]\n"
"v_mfma_f32_16x16x32_bf16 a[112:115], v[84:87], v[88:91], a[112:115]\n"
"s_waitcnt vmcnt(27)\n"
"v_mfma_f32_16x16x32_bf16 v[176:179], v[64:67], a[188:191], v[176:179]\n"
"v_mfma_f32_16x16x32_bf16 a[116:119], a[168:171], a[188:191], a[116:119]\n"
"buffer_load_dwordx4 v24, s[52:55], s78 offen offset:3072 lds\n"
"v_mfma_f32_16x16x32_bf16 a[120:123], v[68:71], a[188:191], a[120:123]\n"
"v_mfma_f32_16x16x32_bf16 a[124:127], v[44:47], a[188:191], a[124:127]\n"
"v_mfma_f32_16x16x32_bf16 v[212:215], a[152:155], a[188:191], v[212:215]\n"
"s_add_u32 s75, s88, 0xa000\n"
"v_mfma_f32_16x16x32_bf16 a[200:203], v[92:95], a[188:191], a[200:203]\n"
"buffer_load_dwordx4 a[180:183], v21, s[56:59], s75 offen offset:0 sc1\n"
"v_mfma_f32_16x16x32_bf16 a[204:207], v[72:75], a[188:191], a[204:207]\n"
"v_mfma_f32_16x16x32_bf16 a[64:67], v[84:87], a[188:191], a[64:67]\n"
"s_waitcnt vmcnt(28)\n"
"v_mfma_f32_16x16x32_bf16 v[180:183], v[64:67], v[96:99], v[180:183]\n"
"s_add_u32 s76, s90, 0xa000\n"
"v_mfma_f32_16x16x32_bf16 a[68:71], a[168:171], v[96:99], a[68:71]\n"
"buffer_load_dwordx4 v[88:91], v21, s[56:59], s76 offen offset:0 sc1\n"
"v_mfma_f32_16x16x32_bf16 a[72:75], v[68:71], v[96:99], a[72:75]\n"
"v_mfma_f32_16x16x32_bf16 a[76:79], v[44:47], v[96:99], a[76:79]\n"
"v_mfma_f32_16x16x32_bf16 v[216:219], a[152:155], v[96:99], v[216:219]\n"
"v_mfma_f32_16x16x32_bf16 a[80:83], v[92:95], v[96:99], a[80:83]\n"
"buffer_load_dwordx4 a[188:191], v21, s[56:59], s84 offen offset:1024 sc1\n"
"v_mfma_f32_16x16x32_bf16 a[84:87], v[72:75], v[96:99], a[84:87]\n"
"v_mfma_f32_16x16x32_bf16 a[88:91], v[84:87], v[96:99], a[88:91]\n"
"s_waitcnt vmcnt(29)\n"
"v_mfma_f32_16x16x32_bf16 v[184:187], v[64:67], a[196:199], v[184:187]\n"
"ds_read_b64_tr_b16 v[64:65], v142 offset:16896\n"
"ds_read_b64_tr_b16 v[66:67], v142 offset:18944\n"
"v_mfma_f32_16x16x32_bf16 a[92:95], a[168:171], a[196:199], a[92:95]\n"
"buffer_load_dwordx4 v[96:99], v21, s[56:59], s77 offen offset:1024 sc1\n"
"ds_read_b64_tr_b16 a[168:169], v143 offset:16896\n"
"v_mfma_f32_16x16x32_bf16 a[96:99], v[68:71], a[196:199], a[96:99]\n"
"s_add_u32 m0, s30, 0x8000\n"
"ds_read_b64_tr_b16 a[170:171], v143 offset:18944\n"
"ds_read_b64_tr_b16 v[68:69], v142 offset:16384\n"
"v_mfma_f32_16x16x32_bf16 a[100:103], v[44:47], a[196:199], a[100:103]\n"
"s_waitcnt vmcnt(14)\n"
"ds_read_b64_tr_b16 v[44:45], v143 offset:20480\n"
"ds_read_b64_tr_b16 v[46:47], v143 offset:22528\n"
"v_mfma_f32_16x16x32_bf16 v[220:223], a[152:155], a[196:199], v[220:223]\n"
"s_add_u32 s96, s89, 0x40000\n"
"ds_read_b64_tr_b16 a[152:153], v142 offset:20992\n"
"ds_read_b64_tr_b16 v[70:71], v142 offset:18432\n"
"v_mfma_f32_16x16x32_bf16 a[104:107], v[92:95], a[196:199], a[104:107]\n"
"buffer_load_dwordx4 v23, s[52:55], s96 offen offset:0 lds\n"
"ds_read_b64_tr_b16 a[154:155], v142 offset:23040\n"
"v_mfma_f32_16x16x32_bf16 a[108:111], v[72:75], a[196:199], a[108:111]\n"
"ds_read_b64_tr_b16 v[92:93], v143 offset:20992\n"
"ds_read_b64_tr_b16 v[94:95], v143 offset:23040\n"
"v_mfma_f32_16x16x32_bf16 a[128:131], v[84:87], a[196:199], a[128:131]\n"
"ds_read_b64_tr_b16 v[72:73], v142 offset:20480\n"
"ds_read_b64_tr_b16 v[74:75], v142 offset:22528\n"
"v_mfma_f32_16x16x32_bf16 a[220:223], v[32:35], v[104:107], a[220:223]\n"
"v_mfma_f32_16x16x32_bf16 a[220:223], v[100:103], v[112:115], a[220:223]\n"
"s_waitcnt lgkmcnt(14)\n"
"buffer_load_dwordx4 v24, s[52:55], s96 offen offset:1024 lds\n"
"v_mfma_f32_16x16x32_bf16 a[220:223], a[136:139], v[120:123], a[220:223]\n"
"v_mfma_f32_16x16x32_bf16 a[220:223], v[56:59], a[176:179], a[220:223]\n"
"v_mfma_f32_16x16x32_bf16 a[224:227], v[108:111], v[104:107], a[224:227]\n"
"s_add_u32 s78, s89, 0x47800\n"
"v_mfma_f32_16x16x32_bf16 a[224:227], v[52:55], v[112:115], a[224:227]\n"
"buffer_load_dwordx4 v23, s[52:55], s78 offen offset:2048 lds\n"
"v_mfma_f32_16x16x32_bf16 a[224:227], v[60:63], v[120:123], a[224:227]\n"
"s_waitcnt lgkmcnt(7)\n"
"v_mfma_f32_16x16x32_bf16 a[224:227], v[64:67], a[176:179], a[224:227]\n"
"v_mfma_f32_16x16x32_bf16 a[228:231], v[36:39], v[104:107], a[228:231]\n"
"v_mfma_f32_16x16x32_bf16 a[228:231], v[40:43], v[112:115], a[228:231]\n"
"buffer_load_dwordx4 v24, s[52:55], s78 offen offset:3072 lds\n"
"v_mfma_f32_16x16x32_bf16 a[228:231], a[144:147], v[120:123], a[228:231]\n"
"ds_write_b128 v13, a[220:223] offset:0\n"
"v_mfma_f32_16x16x32_bf16 a[228:231], a[168:171], a[176:179], a[228:231]\n"
"v_mfma_f32_16x16x32_bf16 v[188:191], a[160:163], v[104:107], v[188:191]\n"
"v_mfma_f32_16x16x32_bf16 v[188:191], v[48:51], v[112:115], v[188:191]\n"
"buffer_load_dwordx4 a[196:199], v21, s[56:59], s75 offen offset:1024 sc1\n"
"v_mfma_f32_16x16x32_bf16 v[188:191], v[76:79], v[120:123], v[188:191]\n"
"s_waitcnt lgkmcnt(1)\n"
"ds_write_b128 v13, a[224:227] offset:1024\n"
"v_mfma_f32_16x16x32_bf16 v[188:191], v[68:71], a[176:179], v[188:191]\n"
"v_mfma_f32_16x16x32_bf16 a[244:247], v[32:35], v[128:131], a[244:247]\n"
"v_mfma_f32_16x16x32_bf16 a[244:247], v[100:103], v[136:139], a[244:247]\n"
"s_waitcnt vmcnt(11)\n"
"buffer_load_dwordx4 a[220:223], v21, s[56:59], s76 offen offset:1024 sc1\n"
"ds_read_b64_tr_b16 v[84:85], v143 offset:28672\n"
"v_mfma_f32_16x16x32_bf16 a[244:247], a[136:139], a[132:135], a[244:247]\n"
"s_getpc_b64 s[98:99]\n"
"ds_write_b128 v13, a[228:231] offset:2048\n"
"v_mfma_f32_16x16x32_bf16 a[244:247], v[56:59], a[184:187], a[244:247]\n"
"v_mfma_f32_16x16x32_bf16 a[248:251], v[108:111], v[128:131], a[248:251]\n"
"global_load_dword v18, v0, s[98:99] offset:1536\n"
"v_mfma_f32_16x16x32_bf16 a[248:251], v[52:55], v[136:139], a[248:251]\n"
"buffer_load_dwordx4 a[224:227], v21, s[56:59], s84 offen offset:2048 sc1\n"
"v_mfma_f32_16x16x32_bf16 a[248:251], v[60:63], a[132:135], a[248:251]\n"
"v_mfma_f32_16x16x32_bf16 a[248:251], v[64:67], a[184:187], a[248:251]\n"
"v_mfma_f32_16x16x32_bf16 v[224:227], v[36:39], v[128:131], v[224:227]\n"
"s_waitcnt vmcnt(12)\n"
"ds_read_b64_tr_b16 v[86:87], v143 offset:30720\n"
"v_mfma_f32_16x16x32_bf16 v[224:227], v[40:43], v[136:139], v[224:227]\n"
"buffer_load_dwordx4 a[228:231], v21, s[56:59], s77 offen offset:2048 sc1\n"
"v_mfma_f32_16x16x32_bf16 v[224:227], a[144:147], a[132:135], v[224:227]\n"
"ds_write_b128 v13, a[244:247] offset:3072\n"
"v_mfma_f32_16x16x32_bf16 v[224:227], a[168:171], a[184:187], v[224:227]\n"
"v_mfma_f32_16x16x32_bf16 v[192:195], a[160:163], v[128:131], v[192:195]\n"
"v_mfma_f32_16x16x32_bf16 v[192:195], v[48:51], v[136:139], v[192:195]\n"
"buffer_load_dwordx4 a[244:247], v21, s[56:59], s75 offen offset:2048 sc1\n"
"v_mfma_f32_16x16x32_bf16 v[192:195], v[76:79], a[132:135], v[192:195]\n"
"ds_write_b128 v13, a[248:251] offset:4096\n"
"v_mfma_f32_16x16x32_bf16 v[192:195], v[68:71], a[184:187], v[192:195]\n"
"v_mfma_f32_16x16x32_bf16 v[228:231], v[32:35], a[140:143], v[228:231]\n"
"v_mfma_f32_16x16x32_bf16 v[228:231], v[100:103], v[144:147], v[228:231]\n"
"buffer_load_dwordx4 a[248:251], v21, s[56:59], s76 offen offset:2048 sc1\n"
"v_mfma_f32_16x16x32_bf16 v[228:231], a[136:139], a[148:151], v[228:231]\n"
"ds_write_b128 v13, v[224:227] offset:5120\n"
"v_mfma_f32_16x16x32_bf16 v[228:231], v[56:59], v[116:119], v[228:231]\n"
"v_mfma_f32_16x16x32_bf16 v[232:235], v[108:111], a[140:143], v[232:235]\n"
"s_add_u32 s96, s91, 0x40000\n"
"s_add_u32 m0, s30, 0x9000\n"
"v_mfma_f32_16x16x32_bf16 v[232:235], v[52:55], v[144:147], v[232:235]\n"
"buffer_load_dwordx4 v23, s[52:55], s96 offen offset:0 lds\n"
"v_mfma_f32_16x16x32_bf16 v[232:235], v[60:63], a[148:151], v[232:235]\n"
"v_mfma_f32_16x16x32_bf16 v[232:235], v[64:67], v[116:119], v[232:235]\n"
"v_mfma_f32_16x16x32_bf16 v[236:239], v[36:39], a[140:143], v[236:239]\n"
"s_nop 0\n"
"s_nop 0\n"
"s_nop 0\n"
"v_mfma_f32_16x16x32_bf16 v[236:239], v[40:43], v[144:147], v[236:239]\n"
"buffer_load_dwordx4 v24, s[52:55], s96 offen offset:1024 lds\n"
"v_mfma_f32_16x16x32_bf16 v[236:239], a[144:147], a[148:151], v[236:239]\n"
"ds_write_b128 v13, v[228:231] offset:6144\n"
"v_mfma_f32_16x16x32_bf16 v[236:239], a[168:171], v[116:119], v[236:239]\n"
"v_mfma_f32_16x16x32_bf16 v[196:199], a[160:163], a[140:143], v[196:199]\n"
"s_add_u32 s78, s91, 0x47800\n"
"v_mfma_f32_16x16x32_bf16 v[196:199], v[48:51], v[144:147], v[196:199]\n"
"buffer_load_dwordx4 v23, s[52:55], s78 offen offset:2048 lds\n"
"v_mfma_f32_16x16x32_bf16 v[196:199], v[76:79], a[148:151], v[196:199]\n"
"ds_write_b128 v13, v[232:235] offset:7168\n"
"v_mfma_f32_16x16x32_bf16 v[196:199], v[68:71], v[116:119], v[196:199]\n"
"v_mfma_f32_16x16x32_bf16 v[240:243], v[32:35], a[156:159], v[240:243]\n"
"v_mfma_f32_16x16x32_bf16 v[240:243], v[100:103], a[192:195], v[240:243]\n"
"buffer_load_dwordx4 v24, s[52:55], s78 offen offset:3072 lds\n"
"v_mfma_f32_16x16x32_bf16 v[240:243], a[136:139], a[164:167], v[240:243]\n"
"ds_write_b128 v13, v[236:239] offset:8192\n"
"v_mfma_f32_16x16x32_bf16 v[240:243], v[56:59], v[124:127], v[240:243]\n"
"v_mfma_f32_16x16x32_bf16 v[132:135], v[108:111], a[156:159], v[132:135]\n"
"v_mfma_f32_16x16x32_bf16 v[132:135], v[52:55], a[192:195], v[132:135]\n"
"buffer_load_dwordx4 v[224:227], v21, s[56:59], s84 offen offset:3072 sc1\n"
"v_mfma_f32_16x16x32_bf16 v[132:135], v[60:63], a[164:167], v[132:135]\n"
"v_mfma_f32_16x16x32_bf16 v[132:135], v[64:67], v[124:127], v[132:135]\n"
"v_mfma_f32_16x16x32_bf16 v[148:151], v[36:39], a[156:159], v[148:151]\n"
"s_nop 0\n"
"s_nop 0\n"
"s_nop 0\n"
"v_mfma_f32_16x16x32_bf16 v[148:151], v[40:43], a[192:195], v[148:151]\n"
"buffer_load_dwordx4 v[228:231], v21, s[56:59], s77 offen offset:3072 sc1\n"
"v_mfma_f32_16x16x32_bf16 v[148:151], a[144:147], a[164:167], v[148:151]\n"
"ds_write_b128 v13, v[240:243] offset:9216\n"
"v_mfma_f32_16x16x32_bf16 v[148:151], a[168:171], v[124:127], v[148:151]\n"
"v_mfma_f32_16x16x32_bf16 v[200:203], a[160:163], a[156:159], v[200:203]\n"
"v_mfma_f32_16x16x32_bf16 v[200:203], v[48:51], a[192:195], v[200:203]\n"
"buffer_load_dwordx4 v[232:235], v21, s[56:59], s75 offen offset:3072 sc1\n"
"v_mfma_f32_16x16x32_bf16 v[200:203], v[76:79], a[164:167], v[200:203]\n"
"ds_write_b128 v13, v[132:135] offset:10240\n"
"v_mfma_f32_16x16x32_bf16 v[200:203], v[68:71], v[124:127], v[200:203]\n"
"v_mfma_f32_16x16x32_bf16 a[32:35], v[32:35], a[172:175], a[32:35]\n"
"v_mfma_f32_16x16x32_bf16 a[32:35], v[100:103], v[80:83], a[32:35]\n"
"s_waitcnt vmcnt(21)\n"
"buffer_load_dwordx4 v[236:239], v21, s[56:59], s76 offen offset:3072 sc1\n"
"v_mfma_f32_16x16x32_bf16 a[32:35], a[136:139], a[180:183], a[32:35]\n"
"s_waitcnt vmcnt(21)\n"
"ds_write_b128 v13, v[148:151] offset:11264\n"
"v_mfma_f32_16x16x32_bf16 a[32:35], v[56:59], v[88:91], a[32:35]\n"
"v_mfma_f32_16x16x32_bf16 a[36:39], v[108:111], a[172:175], a[36:39]\n"
"v_mfma_f32_16x16x32_bf16 a[36:39], v[52:55], v[80:83], a[36:39]\n"
"v_mfma_f32_16x16x32_bf16 a[36:39], v[60:63], a[180:183], a[36:39]\n"
"v_mfma_f32_16x16x32_bf16 a[36:39], v[64:67], v[88:91], a[36:39]\n"
"v_mfma_f32_16x16x32_bf16 a[40:43], v[36:39], a[172:175], a[40:43]\n"
"s_nop 0\n"
"s_nop 0\n"
"s_nop 0\n"
"v_mfma_f32_16x16x32_bf16 a[40:43], v[40:43], v[80:83], a[40:43]\n"
"ds_write_b128 v13, a[32:35] offset:12288\n"
"v_mfma_f32_16x16x32_bf16 a[40:43], a[144:147], a[180:183], a[40:43]\n"
"s_waitcnt lgkmcnt(1)\n"
"s_barrier\n"
"s_add_u32 s96, s20, 0xb000\n"
"v_mfma_f32_16x16x32_bf16 a[40:43], a[168:171], v[88:91], a[40:43]\n"
"buffer_load_dwordx4 a[32:35], v21, s[56:59], s96 offen offset:0 sc1\n"
"v_mfma_f32_16x16x32_bf16 v[204:207], a[160:163], a[172:175], v[204:207]\n"
"ds_read_b128 v[244:247], v14 offset:0\n"
"ds_read_b128 v[248:251], v15 offset:1024\n"
"v_mfma_f32_16x16x32_bf16 v[204:207], v[48:51], v[80:83], v[204:207]\n"
"ds_write_b128 v13, a[36:39] offset:13312\n"
"v_mfma_f32_16x16x32_bf16 v[204:207], v[76:79], a[180:183], v[204:207]\n"
"s_add_u32 s78, s22, 0xb000\n"
"s_add_u32 s84, s88, 0xb000\n"
"v_mfma_f32_16x16x32_bf16 v[204:207], v[68:71], v[88:91], v[204:207]\n"
"s_waitcnt vmcnt(21)\n"
"buffer_load_dwordx4 a[36:39], v21, s[56:59], s78 offen offset:0 sc1\n"
"ds_write_b128 v13, a[40:43] offset:14336\n"
"v_mfma_f32_16x16x32_bf16 a[56:59], v[32:35], a[188:191], a[56:59]\n"
"s_waitcnt vmcnt(21)\n"
"ds_read_b128 v[240:243], v16 offset:2048\n"
"buffer_load_dwordx4 a[40:43], v21, s[56:59], s84 offen offset:0 sc1\n"
"v_mfma_f32_16x16x32_bf16 a[56:59], v[100:103], v[96:99], a[56:59]\n"
"s_waitcnt vmcnt(17)\n"
"s_add_u32 s77, s90, 0xb000\n"
"ds_read_b128 v[132:135], v14 offset:3072\n"
"v_mfma_f32_16x16x32_bf16 a[56:59], a[136:139], a[196:199], a[56:59]\n"
"s_waitcnt vmcnt(16) lgkmcnt(5)\n"
"ds_read_b128 v[148:151], v15 offset:4096\n"
"v_add_f32 v188, v188, v244\n"
"v_add_f32 v189, v189, v245\n"
"v_mfma_f32_16x16x32_bf16 a[56:59], v[56:59], a[220:223], a[56:59]\n"
"s_waitcnt lgkmcnt(5)\n"
"v_add_f32 v190, v190, v246\n"
"v_add_f32 v191, v191, v247\n"
"v_add_f32 v188, v188, v248\n"
"v_mfma_f32_16x16x32_bf16 a[60:63], v[108:111], a[188:191], a[60:63]\n"
"ds_read_b128 v[244:247], v16 offset:5120\n"
"v_add_f32 v189, v189, v249\n"
"v_add_f32 v190, v190, v250\n"
"ds_write_b128 v13, a[56:59] offset:15360\n"
"buffer_load_dwordx4 a[56:59], v21, s[56:59], s77 offen offset:0 sc1\n"
"v_mfma_f32_16x16x32_bf16 a[60:63], v[52:55], v[96:99], a[60:63]\n"
"s_waitcnt lgkmcnt(4)\n"
"v_add_f32 v191, v191, v251\n"
"v_add_f32 v188, v188, v240\n"
"v_mfma_f32_16x16x32_bf16 a[60:63], v[60:63], a[196:199], a[60:63]\n"
"ds_read_b128 v[248:251], v14 offset:6144\n"
"v_add_f32 v189, v189, v241\n"
"v_mfma_f32_16x16x32_bf16 a[60:63], v[64:67], a[220:223], a[60:63]\n"
"v_add_f32 v190, v190, v242\n"
"v_add_f32 v191, v191, v243\n"
"v_mfma_f32_16x16x32_bf16 a[112:115], v[36:39], a[188:191], a[112:115]\n"
"s_min_u32 s70, s27, 1\n"
"ds_read_b128 v[240:243], v15 offset:7168\n"
"v_cvt_pk_bf16_f32 v188, v188, v189\n"
"v_mfma_f32_16x16x32_bf16 a[112:115], v[40:43], v[96:99], a[112:115]\n"
"s_lshl_b32 s70, s70, 31\n"
"v_cvt_pk_bf16_f32 v190, v190, v191\n"
"v_mov_b32 v189, v26\n"
"v_mfma_f32_16x16x32_bf16 a[112:115], a[144:147], a[196:199], a[112:115]\n"
"s_waitcnt lgkmcnt(5)\n"
"v_mov_b32 v191, v26\n"
"v_add_f32 v192, v192, v132\n"
"v_mfma_f32_16x16x32_bf16 a[112:115], a[168:171], a[220:223], a[112:115]\n"
"buffer_store_dwordx4 v[188:191], v21, s[64:67], s40 offen offset:0 sc1\n"
"v_mfma_f32_16x16x32_bf16 v[208:211], a[160:163], a[188:191], v[208:211]\n"
"v_add_f32 v193, v193, v133\n"
"v_add_f32 v194, v194, v134\n"
"v_mfma_f32_16x16x32_bf16 v[208:211], v[48:51], v[96:99], v[208:211]\n"
"ds_write_b128 v13, a[60:63] offset:16384\n"
"v_mfma_f32_16x16x32_bf16 v[208:211], v[76:79], a[196:199], v[208:211]\n"
"s_waitcnt lgkmcnt(3)\n"
"v_add_f32 v195, v195, v135\n"
"v_add_f32 v192, v192, v148\n"
"v_mfma_f32_16x16x32_bf16 v[208:211], v[68:71], a[220:223], v[208:211]\n"
"s_waitcnt vmcnt(16)\n"
"v_add_f32 v193, v193, v149\n"
"v_add_f32 v194, v194, v150\n"
"v_mfma_f32_16x16x32_bf16 a[200:203], v[32:35], a[224:227], a[200:203]\n"
"s_waitcnt vmcnt(15)\n"
"ds_read_b128 v[132:135], v16 offset:8192\n"
"v_add_f32 v195, v195, v151\n"
"v_mfma_f32_16x16x32_bf16 a[200:203], v[100:103], a[228:231], a[200:203]\n"
"s_waitcnt vmcnt(14)\n"
"ds_read_b128 v[148:151], v14 offset:9216\n"
"v_add_f32 v192, v192, v244\n"
"v_mfma_f32_16x16x32_bf16 a[200:203], a[136:139], a[244:247], a[200:203]\n"
"s_waitcnt vmcnt(13)\n"
"ds_write_b128 v13, a[112:115] offset:17408\n"
"v_mfma_f32_16x16x32_bf16 a[200:203], v[56:59], a[248:251], a[200:203]\n"
"v_add_f32 v193, v193, v245\n"
"v_add_f32 v194, v194, v246\n"
"v_mfma_f32_16x16x32_bf16 a[204:207], v[108:111], a[224:227], a[204:207]\n"
"v_add_f32 v195, v195, v247\n"
"v_cvt_pk_bf16_f32 v192, v192, v193\n"
"v_mfma_f32_16x16x32_bf16 a[204:207], v[52:55], a[228:231], a[204:207]\n"
"ds_read_b128 v[244:247], v15 offset:10240\n"
"v_cvt_pk_bf16_f32 v194, v194, v195\n"
"v_mfma_f32_16x16x32_bf16 a[204:207], v[60:63], a[244:247], a[204:207]\n"
"v_mov_b32 v193, v26\n"
"v_mov_b32 v195, v26\n"
"v_mfma_f32_16x16x32_bf16 a[204:207], v[64:67], a[248:251], a[204:207]\n"
"buffer_store_dwordx4 v[192:195], v21, s[64:67], s40 offen offset:1024 sc1\n"
"v_mfma_f32_16x16x32_bf16 a[64:67], v[36:39], a[224:227], a[64:67]\n"
"ds_write_b128 v13, a[200:203] offset:18432\n"
"v_mfma_f32_16x16x32_bf16 a[64:67], v[40:43], a[228:231], a[64:67]\n"
"s_waitcnt lgkmcnt(4)\n"
"v_add_f32 v196, v196, v248\n"
"v_add_f32 v197, v197, v249\n"
"v_mfma_f32_16x16x32_bf16 a[64:67], a[144:147], a[244:247], a[64:67]\n"
"v_add_f32 v198, v198, v250\n"
"v_add_f32 v199, v199, v251\n"
"v_mfma_f32_16x16x32_bf16 a[64:67], a[168:171], a[248:251], a[64:67]\n"
"ds_read_b128 v[248:251], v16 offset:11264\n"
"v_add_f32 v196, v196, v240\n"
"v_mfma_f32_16x16x32_bf16 v[212:215], a[160:163], a[224:227], v[212:215]\n"
"v_add_f32 v197, v197, v241\n"
"v_add_f32 v198, v198, v242\n"
"v_mfma_f32_16x16x32_bf16 v[212:215], v[48:51], a[228:231], v[212:215]\n"
"ds_write_b128 v13, a[204:207] offset:19456\n"
"v_mfma_f32_16x16x32_bf16 v[212:215], v[76:79], a[244:247], v[212:215]\n"
"v_add_f32 v199, v199, v243\n"
"v_add_f32 v196, v196, v132\n"
"v_mfma_f32_16x16x32_bf16 v[212:215], v[68:71], a[248:251], v[212:215]\n"
"s_waitcnt vmcnt(9)\n"
"v_add_f32 v197, v197, v133\n"
"v_add_f32 v198, v198, v134\n"
"v_mfma_f32_16x16x32_bf16 a[80:83], v[32:35], v[224:227], a[80:83]\n"
"s_waitcnt vmcnt(8)\n"
"ds_read_b64_tr_b16 a[204:205], v142 offset:29184\n"
"v_add_f32 v199, v199, v135\n"
"v_mfma_f32_16x16x32_bf16 a[80:83], v[100:103], v[228:231], a[80:83]\n"
"s_waitcnt vmcnt(7)\n"
"ds_read_b64_tr_b16 a[206:207], v142 offset:31232\n"
"v_cvt_pk_bf16_f32 v196, v196, v197\n"
"v_mfma_f32_16x16x32_bf16 a[80:83], a[136:139], v[232:235], a[80:83]\n"
"s_waitcnt vmcnt(6)\n"
"ds_write_b128 v13, a[64:67] offset:20480\n"
"v_mfma_f32_16x16x32_bf16 a[80:83], v[56:59], v[236:239], a[80:83]\n"
"v_cvt_pk_bf16_f32 v198, v198, v199\n"
"v_mov_b32 v197, v26\n"
"v_mfma_f32_16x16x32_bf16 a[84:87], v[108:111], v[224:227], a[84:87]\n"
"s_waitcnt lgkmcnt(5)\n"
"v_mov_b32 v199, v26\n"
"v_add_f32 v200, v200, v148\n"
"v_mfma_f32_16x16x32_bf16 a[84:87], v[52:55], v[228:231], a[84:87]\n"
"buffer_store_dwordx4 v[196:199], v21, s[64:67], s40 offen offset:2048 sc1\n"
"v_mfma_f32_16x16x32_bf16 a[84:87], v[60:63], v[232:235], a[84:87]\n"
"ds_read_b64_tr_b16 a[64:65], v143 offset:29184\n"
"v_add_f32 v201, v201, v149\n"
"v_mfma_f32_16x16x32_bf16 a[84:87], v[64:67], v[236:239], a[84:87]\n"
"ds_read_b64_tr_b16 a[66:67], v143 offset:31232\n"
"v_add_f32 v202, v202, v150\n"
"v_mfma_f32_16x16x32_bf16 a[88:91], v[36:39], v[224:227], a[88:91]\n"
"ds_write_b128 v13, a[80:83] offset:21504\n"
"v_mfma_f32_16x16x32_bf16 a[88:91], v[40:43], v[228:231], a[88:91]\n"
"s_waitcnt lgkmcnt(3)\n"
"s_barrier\n"
"v_add_f32 v203, v203, v151\n"
"v_mfma_f32_16x16x32_bf16 a[88:91], a[144:147], v[232:235], a[88:91]\n"
"v_add_f32 v200, v200, v244\n"
"v_add_f32 v201, v201, v245\n"
"v_add_f32 v202, v202, v246\n"
"v_mfma_f32_16x16x32_bf16 a[88:91], a[168:171], v[236:239], a[88:91]\n"
"ds_read_b128 v[240:243], v14 offset:12288\n"
"v_add_f32 v203, v203, v247\n"
"v_add_f32 v200, v200, v248\n"
"v_mfma_f32_16x16x32_bf16 v[216:219], a[160:163], v[224:227], v[216:219]\n"
"ds_read_b64_tr_b16 a[80:81], v142 offset:28672\n"
"v_add_f32 v201, v201, v249\n"
"v_add_f32 v202, v202, v250\n"
"v_mfma_f32_16x16x32_bf16 v[216:219], v[48:51], v[228:231], v[216:219]\n"
"ds_write_b128 v13, a[84:87] offset:22528\n"
"v_mfma_f32_16x16x32_bf16 v[216:219], v[76:79], v[232:235], v[216:219]\n"
"v_add_f32 v203, v203, v251\n"
"v_cvt_pk_bf16_f32 v200, v200, v201\n"
"v_or_b32 v25, s70, v25\n"
"v_mfma_f32_16x16x32_bf16 v[216:219], v[68:71], v[236:239], v[216:219]\n"
"s_waitcnt vmcnt(6)\n"
"v_cvt_pk_bf16_f32 v202, v202, v203\n"
"v_mov_b32 v201, v26\n"
"v_mfma_f32_16x16x32_bf16 a[104:107], v[32:35], a[32:35], a[104:107]\n"
"s_waitcnt vmcnt(5)\n"
"ds_read_b64_tr_b16 a[84:85], v143 offset:32768\n"
"ds_read_b64_tr_b16 a[86:87], v143 offset:34816\n"
"v_mov_b32 v203, v26\n"
"v_mfma_f32_16x16x32_bf16 a[104:107], v[100:103], a[36:39], a[104:107]\n"
"s_waitcnt vmcnt(4)\n"
"buffer_store_dwordx4 v[200:203], v21, s[64:67], s40 offen offset:3072 sc1\n"
"v_mfma_f32_16x16x32_bf16 a[104:107], a[136:139], a[40:43], a[104:107]\n"
"s_waitcnt vmcnt(4)\n"
"ds_write_b128 v13, a[88:91] offset:23552\n"
"v_mfma_f32_16x16x32_bf16 a[104:107], v[56:59], a[56:59], a[104:107]\n"
"s_waitcnt lgkmcnt(5)\n"
"v_add_f32 v204, v204, v240\n"
"v_add_f32 v205, v205, v241\n"
"v_add_f32 v206, v206, v242\n"
"v_mfma_f32_16x16x32_bf16 a[108:111], v[108:111], a[32:35], a[108:111]\n"
"v_add_f32 v207, v207, v243\n"
"v_mfma_f32_16x16x32_bf16 a[108:111], v[52:55], a[36:39], a[108:111]\n"
"ds_read_b64_tr_b16 a[82:83], v142 offset:30720\n"
"ds_read_b128 v[132:135], v15 offset:13312\n"
"v_mfma_f32_16x16x32_bf16 a[108:111], v[60:63], a[40:43], a[108:111]\n"
"ds_read_b64_tr_b16 v[32:33], v142 offset:33280\n"
"ds_read_b64_tr_b16 v[34:35], v142 offset:35328\n"
"v_mfma_f32_16x16x32_bf16 a[108:111], v[64:67], a[56:59], a[108:111]\n"
"ds_read_b64_tr_b16 v[56:57], v143 offset:36864\n"
"ds_read_b64_tr_b16 v[58:59], v143 offset:38912\n"
"v_mfma_f32_16x16x32_bf16 a[128:131], v[36:39], a[32:35], a[128:131]\n"
"ds_write_b128 v13, a[104:107] offset:24576\n"
"v_mfma_f32_16x16x32_bf16 a[128:131], v[40:43], a[36:39], a[128:131]\n"
"v_mfma_f32_16x16x32_bf16 a[128:131], a[144:147], a[40:43], a[128:131]\n"
"v_bfe_u32 v20, v22, 4, 2\n"
"v_lshrrev_b32 v17, 6, v22\n"
"v_mfma_f32_16x16x32_bf16 a[128:131], a[168:171], a[56:59], a[128:131]\n"
"ds_read_b128 v[148:151], v16 offset:14336\n"
"ds_read_b64_tr_b16 v[100:101], v143 offset:33280\n"
"v_mfma_f32_16x16x32_bf16 v[220:223], a[160:163], a[32:35], v[220:223]\n"
"s_waitcnt lgkmcnt(7)\n"
"ds_read_b64_tr_b16 v[102:103], v143 offset:35328\n"
"ds_read_b64_tr_b16 v[108:109], v142 offset:37376\n"
"v_add_f32 v204, v204, v132\n"
"v_add_f32 v205, v205, v133\n"
"v_add_f32 v206, v206, v134\n"
"v_mfma_f32_16x16x32_bf16 v[220:223], v[48:51], a[36:39], v[220:223]\n"
"ds_read_b64_tr_b16 v[110:111], v142 offset:39424\n"
"ds_read_b64_tr_b16 a[136:137], v142 offset:32768\n"
"v_add_f32 v207, v207, v135\n"
"v_mfma_f32_16x16x32_bf16 v[220:223], v[76:79], a[40:43], v[220:223]\n"
"ds_write_b128 v13, a[108:111] offset:25600\n"
"v_mfma_f32_16x16x32_bf16 v[220:223], v[68:71], a[56:59], v[220:223]\n"
"ds_read_b64_tr_b16 a[88:89], v143 offset:37376\n"
"ds_read_b128 v[244:247], v14 offset:15360\n"
"v_mfma_f32_16x16x32_bf16 a[208:211], v[44:47], v[104:107], a[208:211]\n"
"ds_read_b64_tr_b16 a[138:139], v142 offset:34816\n"
"ds_read_b64_tr_b16 a[90:91], v143 offset:39424\n"
"v_mfma_f32_16x16x32_bf16 a[208:211], v[84:87], v[112:115], a[208:211]\n"
"s_waitcnt lgkmcnt(10)\n"
"ds_read_b64_tr_b16 v[52:53], v142 offset:36864\n"
"v_add_f32 v204, v204, v148\n"
"v_add_f32 v205, v205, v149\n"
"v_add_f32 v206, v206, v150\n"
"v_add_f32 v207, v207, v151\n"
"v_mfma_f32_16x16x32_bf16 a[208:211], a[84:87], v[120:123], a[208:211]\n"
"ds_read_b128 v[248:251], v15 offset:16384\n"
"ds_read_b64_tr_b16 v[54:55], v142 offset:38912\n"
"v_cvt_pk_bf16_f32 v204, v204, v205\n"
"v_cvt_pk_bf16_f32 v206, v206, v207\n"
"v_mfma_f32_16x16x32_bf16 a[208:211], v[56:59], a[176:179], a[208:211]\n"
"ds_write_b128 v13, a[128:131] offset:26624\n"
"v_mfma_f32_16x16x32_bf16 a[212:215], a[152:155], v[104:107], a[212:215]\n"
"s_add_u32 s75, s40, 0x1000\n"
"v_mov_b32 v205, v26\n"
"v_mov_b32 v207, v26\n"
"v_mfma_f32_16x16x32_bf16 a[212:215], a[204:207], v[112:115], a[212:215]\n"
"s_waitcnt lgkmcnt(6)\n"
"ds_read_b128 v[240:243], v16 offset:17408\n"
"v_add_f32 v208, v208, v244\n"
"v_add_f32 v209, v209, v245\n"
"v_add_f32 v210, v210, v246\n"
"v_mfma_f32_16x16x32_bf16 a[212:215], v[32:35], v[120:123], a[212:215]\n"
"buffer_store_dwordx4 v[204:207], v21, s[64:67], s75 offen offset:0 sc1\n"
"v_mfma_f32_16x16x32_bf16 a[212:215], v[108:111], a[176:179], a[212:215]\n"
"s_waitcnt lgkmcnt(3)\n"
"ds_read_b128 v[132:135], v14 offset:18432\n"
"v_add_f32 v211, v211, v247\n"
"v_add_f32 v208, v208, v248\n"
"v_mfma_f32_16x16x32_bf16 a[216:219], v[92:95], v[104:107], a[216:219]\n"
"ds_write_b128 v13, a[208:211] offset:27648\n"
"v_mfma_f32_16x16x32_bf16 a[216:219], a[64:67], v[112:115], a[216:219]\n"
"s_waitcnt lgkmcnt(3)\n"
"s_barrier\n"
"v_add_f32 v209, v209, v249\n"
"v_mfma_f32_16x16x32_bf16 a[216:219], v[100:103], v[120:123], a[216:219]\n"
"v_add_f32 v210, v210, v250\n"
"v_add_f32 v211, v211, v251\n"
"v_mfma_f32_16x16x32_bf16 a[216:219], a[88:91], a[176:179], a[216:219]\n"
"s_waitcnt lgkmcnt(2)\n"
"ds_read_b128 v[148:151], v15 offset:19456\n"
"v_add_f32 v208, v208, v240\n"
"v_add_f32 v209, v209, v241\n"
"v_mfma_f32_16x16x32_bf16 v[152:155], v[72:75], v[104:107], v[152:155]\n"
"ds_read_b128 v[244:247], v16 offset:20480\n"
"v_add_f32 v210, v210, v242\n"
"v_add_f32 v211, v211, v243\n"
"v_mfma_f32_16x16x32_bf16 v[152:155], a[80:83], v[112:115], v[152:155]\n"
"ds_read_b128 v[248:251], v14 offset:21504\n"
"v_cvt_pk_bf16_f32 v208, v208, v209\n"
"v_cvt_pk_bf16_f32 v210, v210, v211\n"
"v_mfma_f32_16x16x32_bf16 v[152:155], a[136:139], v[120:123], v[152:155]\n"
"ds_read_b128 v[240:243], v15 offset:22528\n"
"v_mov_b32 v209, v26\n"
"v_mov_b32 v211, v26\n"
"v_mfma_f32_16x16x32_bf16 v[152:155], v[52:55], a[176:179], v[152:155]\n"
"ds_write_b128 v13, a[212:215] offset:28672\n"
"v_mfma_f32_16x16x32_bf16 a[232:235], v[44:47], v[128:131], a[232:235]\n"
"buffer_store_dwordx4 v[208:211], v21, s[64:67], s75 offen offset:1024 sc1\n"
"v_mfma_f32_16x16x32_bf16 a[232:235], v[84:87], v[136:139], a[232:235]\n"
"s_waitcnt lgkmcnt(5)\n"
"v_add_f32 v212, v212, v132\n"
"v_add_f32 v213, v213, v133\n"
"v_add_f32 v214, v214, v134\n"
"v_mfma_f32_16x16x32_bf16 a[232:235], a[84:87], a[132:135], a[232:235]\n"
"s_waitcnt lgkmcnt(4)\n"
"v_add_f32 v215, v215, v135\n"
"v_add_f32 v212, v212, v148\n"
"v_add_f32 v213, v213, v149\n"
"v_mfma_f32_16x16x32_bf16 a[232:235], v[56:59], a[184:187], a[232:235]\n"
"ds_write_b128 v13, a[216:219] offset:29696\n"
"v_mfma_f32_16x16x32_bf16 a[236:239], a[152:155], v[128:131], a[236:239]\n"
"s_waitcnt lgkmcnt(4)\n"
"v_add_f32 v214, v214, v150\n"
"v_add_f32 v215, v215, v151\n"
"v_add_f32 v212, v212, v244\n"
"v_mfma_f32_16x16x32_bf16 a[236:239], a[204:207], v[136:139], a[236:239]\n"
"v_add_f32 v213, v213, v245\n"
"v_add_f32 v214, v214, v246\n"
"v_add_f32 v215, v215, v247\n"
"v_mfma_f32_16x16x32_bf16 a[236:239], v[32:35], a[132:135], a[236:239]\n"
"ds_read_b128 v[132:135], v16 offset:23552\n"
"v_cvt_pk_bf16_f32 v212, v212, v213\n"
"v_cvt_pk_bf16_f32 v214, v214, v215\n"
"v_mfma_f32_16x16x32_bf16 a[236:239], v[108:111], a[184:187], a[236:239]\n"
"ds_read_b128 v[148:151], v14 offset:24576\n"
"v_mov_b32 v213, v26\n"
"v_mov_b32 v215, v26\n"
"v_mfma_f32_16x16x32_bf16 a[240:243], v[92:95], v[128:131], a[240:243]\n"
"buffer_store_dwordx4 v[212:215], v21, s[64:67], s75 offen offset:2048 sc1\n"
"v_mfma_f32_16x16x32_bf16 a[240:243], a[64:67], v[136:139], a[240:243]\n"
"ds_write_b128 v13, a[232:235] offset:30720\n"
"v_mfma_f32_16x16x32_bf16 a[240:243], v[100:103], a[132:135], a[240:243]\n"
"s_waitcnt lgkmcnt(4)\n"
"v_add_f32 v216, v216, v248\n"
"v_add_f32 v217, v217, v249\n"
"v_add_f32 v218, v218, v250\n"
"v_mfma_f32_16x16x32_bf16 a[240:243], a[88:91], a[184:187], a[240:243]\n"
"v_add_f32 v219, v219, v251\n"
"v_add_f32 v216, v216, v240\n"
"v_add_f32 v217, v217, v241\n"
"v_mfma_f32_16x16x32_bf16 v[156:159], v[72:75], v[128:131], v[156:159]\n"
"ds_read_b128 v[244:247], v15 offset:25600\n"
"v_add_f32 v218, v218, v242\n"
"v_add_f32 v219, v219, v243\n"
"v_mfma_f32_16x16x32_bf16 v[156:159], a[80:83], v[136:139], v[156:159]\n"
"ds_write_b128 v13, a[236:239] offset:31744\n"
"v_mfma_f32_16x16x32_bf16 v[156:159], a[136:139], a[132:135], v[156:159]\n"
"s_waitcnt lgkmcnt(4)\n"
"v_add_f32 v216, v216, v132\n"
"v_add_f32 v217, v217, v133\n"
"v_mfma_f32_16x16x32_bf16 v[156:159], v[52:55], a[184:187], v[156:159]\n"
"v_add_f32 v218, v218, v134\n"
"v_add_f32 v219, v219, v135\n"
"v_mfma_f32_16x16x32_bf16 a[252:255], v[44:47], a[140:143], a[252:255]\n"
"ds_read_b128 v[248:251], v16 offset:26624\n"
"v_cvt_pk_bf16_f32 v216, v216, v217\n"
"v_mfma_f32_16x16x32_bf16 a[252:255], v[84:87], v[144:147], a[252:255]\n"
"v_cvt_pk_bf16_f32 v218, v218, v219\n"
"v_mov_b32 v217, v26\n"
"v_mfma_f32_16x16x32_bf16 a[252:255], a[84:87], a[148:151], a[252:255]\n"
"ds_write_b128 v13, a[240:243] offset:32768\n"
"v_mfma_f32_16x16x32_bf16 a[252:255], v[56:59], v[116:119], a[252:255]\n"
"s_waitcnt lgkmcnt(4)\n"
"v_mov_b32 v219, v26\n"
"v_add_f32 v220, v220, v148\n"
"v_mfma_f32_16x16x32_bf16 a[0:3], a[152:155], a[140:143], a[0:3]\n"
"buffer_store_dwordx4 v[216:219], v21, s[64:67], s75 offen offset:3072 sc1\n"
"v_mfma_f32_16x16x32_bf16 a[0:3], a[204:207], v[144:147], a[0:3]\n"
"v_add_f32 v221, v221, v149\n"
"v_add_f32 v222, v222, v150\n"
"v_mfma_f32_16x16x32_bf16 a[0:3], v[32:35], a[148:151], a[0:3]\n"
"s_waitcnt lgkmcnt(2)\n"
"v_add_f32 v223, v223, v151\n"
"v_add_f32 v220, v220, v244\n"
"v_mfma_f32_16x16x32_bf16 a[0:3], v[108:111], v[116:119], a[0:3]\n"
"v_add_f32 v221, v221, v245\n"
"v_add_f32 v222, v222, v246\n"
"v_mfma_f32_16x16x32_bf16 a[4:7], v[92:95], a[140:143], a[4:7]\n"
"ds_write_b128 v13, a[252:255] offset:33792\n"
"v_mfma_f32_16x16x32_bf16 a[4:7], a[64:67], v[144:147], a[4:7]\n"
"v_add_f32 v223, v223, v247\n"
"v_lshlrev_b32 v19, 3, v20\n"
"v_mfma_f32_16x16x32_bf16 a[4:7], v[100:103], a[148:151], a[4:7]\n"
"v_lshl_add_u32 v20, v20, 4, v17\n"
"v_lshlrev_b32 v20, 2, v20\n"
"v_mfma_f32_16x16x32_bf16 a[4:7], a[88:91], v[116:119], a[4:7]\n"
"s_waitcnt lgkmcnt(1)\n"
"v_add_f32 v220, v220, v248\n"
"v_add_f32 v221, v221, v249\n"
"v_mfma_f32_16x16x32_bf16 v[160:163], v[72:75], a[140:143], v[160:163]\n"
"v_add_f32 v222, v222, v250\n"
"v_add_f32 v223, v223, v251\n"
"v_mfma_f32_16x16x32_bf16 v[160:163], a[80:83], v[144:147], v[160:163]\n"
"v_cvt_pk_bf16_f32 v220, v220, v221\n"
"v_cvt_pk_bf16_f32 v222, v222, v223\n"
"v_mfma_f32_16x16x32_bf16 v[160:163], a[136:139], a[148:151], v[160:163]\n"
"v_mov_b32 v221, v26\n"
"v_mov_b32 v223, v26\n"
"v_mfma_f32_16x16x32_bf16 v[160:163], v[52:55], v[116:119], v[160:163]\n"
"s_add_u32 s76, s40, 0x2000\n"
"ds_write_b128 v13, a[0:3] offset:34816\n"
"v_mfma_f32_16x16x32_bf16 a[8:11], v[44:47], a[156:159], a[8:11]\n"
"buffer_store_dwordx4 v[220:223], v21, s[64:67], s76 offen offset:0 sc1\n"
"v_mfma_f32_16x16x32_bf16 a[8:11], v[84:87], a[192:195], a[8:11]\n"
"v_lshl_add_u32 v19, v17, 11, v19\n"
"v_mfma_f32_16x16x32_bf16 a[8:11], a[84:87], a[164:167], a[8:11]\n"
"ds_write_b128 v13, a[4:7] offset:35840\n"
"v_mfma_f32_16x16x32_bf16 a[8:11], v[56:59], v[124:127], a[8:11]\n"
"v_mfma_f32_16x16x32_bf16 a[12:15], a[152:155], a[156:159], a[12:15]\n"
"v_mfma_f32_16x16x32_bf16 a[12:15], a[204:207], a[192:195], a[12:15]\n"
"v_mfma_f32_16x16x32_bf16 a[12:15], v[32:35], a[164:167], a[12:15]\n"
"v_mfma_f32_16x16x32_bf16 a[12:15], v[108:111], v[124:127], a[12:15]\n"
"v_mfma_f32_16x16x32_bf16 a[16:19], v[92:95], a[156:159], a[16:19]\n"
"s_nop 0\n"
"s_nop 0\n"
"s_nop 0\n"
"v_mfma_f32_16x16x32_bf16 a[16:19], a[64:67], a[192:195], a[16:19]\n"
"ds_write_b128 v13, a[8:11] offset:36864\n"
"v_mfma_f32_16x16x32_bf16 a[16:19], v[100:103], a[164:167], a[16:19]\n"
"v_mfma_f32_16x16x32_bf16 a[16:19], a[88:91], v[124:127], a[16:19]\n"
"v_mfma_f32_16x16x32_bf16 v[164:167], v[72:75], a[156:159], v[164:167]\n"
"v_mfma_f32_16x16x32_bf16 v[164:167], a[80:83], a[192:195], v[164:167]\n"
"ds_write_b128 v13, a[12:15] offset:37888\n"
"v_mfma_f32_16x16x32_bf16 v[164:167], a[136:139], a[164:167], v[164:167]\n"
"v_mfma_f32_16x16x32_bf16 v[164:167], v[52:55], v[124:127], v[164:167]\n"
"v_mfma_f32_16x16x32_bf16 a[20:23], v[44:47], a[172:175], a[20:23]\n"
"v_mfma_f32_16x16x32_bf16 a[20:23], v[84:87], v[80:83], a[20:23]\n"
"v_mfma_f32_16x16x32_bf16 a[20:23], a[84:87], a[180:183], a[20:23]\n"
"ds_write_b128 v13, a[16:19] offset:38912\n"
"v_mfma_f32_16x16x32_bf16 a[20:23], v[56:59], v[88:91], a[20:23]\n"
"v_mfma_f32_16x16x32_bf16 a[24:27], a[152:155], a[172:175], a[24:27]\n"
"v_mfma_f32_16x16x32_bf16 a[24:27], a[204:207], v[80:83], a[24:27]\n"
"v_mfma_f32_16x16x32_bf16 a[24:27], v[32:35], a[180:183], a[24:27]\n"
"v_mfma_f32_16x16x32_bf16 a[24:27], v[108:111], v[88:91], a[24:27]\n"
"v_mfma_f32_16x16x32_bf16 a[28:31], v[92:95], a[172:175], a[28:31]\n"
"s_nop 0\n"
"s_nop 0\n"
"s_nop 0\n"
"v_mfma_f32_16x16x32_bf16 a[28:31], a[64:67], v[80:83], a[28:31]\n"
"ds_write_b128 v13, a[20:23] offset:39936\n"
"v_mfma_f32_16x16x32_bf16 a[28:31], v[100:103], a[180:183], a[28:31]\n"
"s_waitcnt lgkmcnt(1)\n"
"s_barrier\n"
"v_mfma_f32_16x16x32_bf16 a[28:31], a[88:91], v[88:91], a[28:31]\n"
"v_mfma_f32_16x16x32_bf16 v[168:171], v[72:75], a[172:175], v[168:171]\n"
"ds_read_b128 v[240:243], v14 offset:27648\n"
"ds_read_b128 v[132:135], v15 offset:28672\n"
"v_mfma_f32_16x16x32_bf16 v[168:171], a[80:83], v[80:83], v[168:171]\n"
"ds_write_b128 v13, a[24:27] offset:0\n"
"v_mfma_f32_16x16x32_bf16 v[168:171], a[136:139], a[180:183], v[168:171]\n"
"v_mfma_f32_16x16x32_bf16 v[168:171], v[52:55], v[88:91], v[168:171]\n"
"v_mfma_f32_16x16x32_bf16 a[44:47], v[44:47], a[188:191], a[44:47]\n"
"ds_read_b128 v[148:151], v16 offset:29696\n"
"v_mfma_f32_16x16x32_bf16 a[44:47], v[84:87], v[96:99], a[44:47]\n"
"ds_read_b128 v[244:247], v14 offset:30720\n"
"v_mfma_f32_16x16x32_bf16 a[44:47], a[84:87], a[196:199], a[44:47]\n"
"ds_write_b128 v13, a[28:31] offset:1024\n"
"v_mfma_f32_16x16x32_bf16 a[44:47], v[56:59], a[220:223], a[44:47]\n"
"s_waitcnt lgkmcnt(5)\n"
"buffer_load_dwordx4 v[188:191], v21, s[64:67], s41 offen offset:0 sc1\n"
"v_mfma_f32_16x16x32_bf16 a[48:51], a[152:155], a[188:191], a[48:51]\n"
"v_add_f32 v152, v152, v240\n"
"v_add_f32 v153, v153, v241\n"
"v_mfma_f32_16x16x32_bf16 a[48:51], a[204:207], v[96:99], a[48:51]\n"
"buffer_load_dwordx4 v[192:195], v21, s[64:67], s41 offen offset:1024 sc1\n"
"v_add_f32 v154, v154, v242\n"
"v_mfma_f32_16x16x32_bf16 a[48:51], v[32:35], a[196:199], a[48:51]\n"
"ds_read_b128 v[248:251], v15 offset:31744\n"
"v_add_f32 v155, v155, v243\n"
"v_mfma_f32_16x16x32_bf16 a[48:51], v[108:111], a[220:223], a[48:51]\n"
"s_waitcnt lgkmcnt(4)\n"
"buffer_load_dwordx4 v[196:199], v21, s[64:67], s41 offen offset:2048 sc1\n"
"v_mfma_f32_16x16x32_bf16 a[52:55], v[92:95], a[188:191], a[52:55]\n"
"ds_write_b128 v13, a[44:47] offset:2048\n"
"v_mfma_f32_16x16x32_bf16 a[52:55], a[64:67], v[96:99], a[52:55]\n"
"buffer_load_dwordx4 v[200:203], v21, s[64:67], s41 offen offset:3072 sc1\n"
"v_add_f32 v152, v152, v132\n"
"v_mfma_f32_16x16x32_bf16 a[52:55], v[100:103], a[196:199], a[52:55]\n"
"v_add_f32 v153, v153, v133\n"
"v_add_f32 v154, v154, v134\n"
"v_mfma_f32_16x16x32_bf16 a[52:55], a[88:91], a[220:223], a[52:55]\n"
"ds_read_b128 v[240:243], v16 offset:32768\n"
"v_add_f32 v155, v155, v135\n"
"v_mfma_f32_16x16x32_bf16 v[172:175], v[72:75], a[188:191], v[172:175]\n"
"s_waitcnt lgkmcnt(3)\n"
"ds_read_b128 v[132:135], v14 offset:33792\n"
"v_add_f32 v152, v152, v148\n"
"v_mfma_f32_16x16x32_bf16 v[172:175], a[80:83], v[96:99], v[172:175]\n"
"ds_write_b128 v13, a[48:51] offset:3072\n"
"v_mfma_f32_16x16x32_bf16 v[172:175], a[136:139], a[196:199], v[172:175]\n"
"v_add_f32 v153, v153, v149\n"
"v_add_f32 v154, v154, v150\n"
"v_mfma_f32_16x16x32_bf16 v[172:175], v[52:55], a[220:223], v[172:175]\n"
"v_add_f32 v155, v155, v151\n"
"v_add_f32 v156, v156, v244\n"
"v_mfma_f32_16x16x32_bf16 a[116:119], v[44:47], a[224:227], a[116:119]\n"
"ds_read_b128 v[148:151], v15 offset:34816\n"
"v_add_f32 v157, v157, v245\n"
"v_mfma_f32_16x16x32_bf16 a[116:119], v[84:87], a[228:231], a[116:119]\n"
"v_add_f32 v158, v158, v246\n"
"v_add_f32 v159, v159, v247\n"
"v_mfma_f32_16x16x32_bf16 a[116:119], a[84:87], a[244:247], a[116:119]\n"
"ds_write_b128 v13, a[52:55] offset:4096\n"
"v_mfma_f32_16x16x32_bf16 a[116:119], v[56:59], a[248:251], a[116:119]\n"
"s_waitcnt lgkmcnt(5)\n"
"v_add_f32 v156, v156, v248\n"
"v_add_f32 v157, v157, v249\n"
"v_mfma_f32_16x16x32_bf16 a[120:123], a[152:155], a[224:227], a[120:123]\n"
"v_add_f32 v158, v158, v250\n"
"v_add_f32 v159, v159, v251\n"
"v_mfma_f32_16x16x32_bf16 a[120:123], a[204:207], a[228:231], a[120:123]\n"
"s_waitcnt lgkmcnt(4)\n"
"ds_read_b128 v[244:247], v16 offset:35840\n"
"v_add_f32 v156, v156, v240\n"
"v_mfma_f32_16x16x32_bf16 a[120:123], v[32:35], a[244:247], a[120:123]\n"
"ds_read_b128 v[248:251], v14 offset:36864\n"
"v_add_f32 v157, v157, v241\n"
"v_mfma_f32_16x16x32_bf16 a[120:123], v[108:111], a[248:251], a[120:123]\n"
"v_add_f32 v158, v158, v242\n"
"v_add_f32 v159, v159, v243\n"
"v_mfma_f32_16x16x32_bf16 a[124:127], v[92:95], a[224:227], a[124:127]\n"
"ds_write_b128 v13, a[116:119] offset:5120\n"
"v_mfma_f32_16x16x32_bf16 a[124:127], a[64:67], a[228:231], a[124:127]\n"
"s_waitcnt lgkmcnt(5)\n"
"v_add_f32 v160, v160, v132\n"
"v_add_f32 v161, v161, v133\n"
"v_mfma_f32_16x16x32_bf16 a[124:127], v[100:103], a[244:247], a[124:127]\n"
"v_add_f32 v162, v162, v134\n"
"v_add_f32 v163, v163, v135\n"
"v_mfma_f32_16x16x32_bf16 a[124:127], a[88:91], a[248:251], a[124:127]\n"
"s_waitcnt lgkmcnt(4)\n"
"ds_read_b128 v[240:243], v15 offset:37888\n"
"v_add_f32 v160, v160, v148\n"
"v_mfma_f32_16x16x32_bf16 v[176:179], v[72:75], a[224:227], v[176:179]\n"
"ds_read_b128 v[132:135], v16 offset:38912\n"
"v_add_f32 v161, v161, v149\n"
"v_mfma_f32_16x16x32_bf16 v[176:179], a[80:83], a[228:231], v[176:179]\n"
"ds_write_b128 v13, a[120:123] offset:6144\n"
"v_mfma_f32_16x16x32_bf16 v[176:179], a[136:139], a[244:247], v[176:179]\n"
"v_add_f32 v162, v162, v150\n"
"v_add_f32 v163, v163, v151\n"
"v_mfma_f32_16x16x32_bf16 v[176:179], v[52:55], a[248:251], v[176:179]\n"
"s_waitcnt lgkmcnt(5)\n"
"v_add_f32 v160, v160, v244\n"
"v_add_f32 v161, v161, v245\n"
"v_mfma_f32_16x16x32_bf16 a[68:71], v[44:47], v[224:227], a[68:71]\n"
"v_add_f32 v162, v162, v246\n"
"v_add_f32 v163, v163, v247\n"
"v_mfma_f32_16x16x32_bf16 a[68:71], v[84:87], v[228:231], a[68:71]\n"
"s_waitcnt lgkmcnt(4)\n"
"v_add_f32 v164, v164, v248\n"
"v_add_f32 v165, v165, v249\n"
"v_mfma_f32_16x16x32_bf16 a[68:71], a[84:87], v[232:235], a[68:71]\n"
"ds_write_b128 v13, a[124:127] offset:7168\n"
"v_mfma_f32_16x16x32_bf16 a[68:71], v[56:59], v[236:239], a[68:71]\n"
"v_add_f32 v166, v166, v250\n"
"v_add_f32 v167, v167, v251\n"
"v_mfma_f32_16x16x32_bf16 a[72:75], a[152:155], v[224:227], a[72:75]\n"
"s_waitcnt lgkmcnt(3)\n"
"v_add_f32 v164, v164, v240\n"
"v_add_f32 v165, v165, v241\n"
"v_mfma_f32_16x16x32_bf16 a[72:75], a[204:207], v[228:231], a[72:75]\n"
"v_add_f32 v166, v166, v242\n"
"v_add_f32 v167, v167, v243\n"
"v_mfma_f32_16x16x32_bf16 a[72:75], v[32:35], v[232:235], a[72:75]\n"
"s_waitcnt lgkmcnt(2)\n"
"v_add_f32 v164, v164, v132\n"
"v_add_f32 v165, v165, v133\n"
"v_mfma_f32_16x16x32_bf16 a[72:75], v[108:111], v[236:239], a[72:75]\n"
"v_add_f32 v166, v166, v134\n"
"v_add_f32 v167, v167, v135\n"
"v_mfma_f32_16x16x32_bf16 a[76:79], v[92:95], v[224:227], a[76:79]\n"
"ds_write_b128 v13, a[68:71] offset:8192\n"
"v_mfma_f32_16x16x32_bf16 a[76:79], a[64:67], v[228:231], a[76:79]\n"
"s_waitcnt lgkmcnt(1)\n"
"s_barrier\n"
"v_mfma_f32_16x16x32_bf16 a[76:79], v[100:103], v[232:235], a[76:79]\n"
"v_mfma_f32_16x16x32_bf16 a[76:79], a[88:91], v[236:239], a[76:79]\n"
"ds_read_b128 v[148:151], v14 offset:39936\n"
"ds_read_b128 v[244:247], v15 offset:0\n"
"v_mfma_f32_16x16x32_bf16 v[180:183], v[72:75], v[224:227], v[180:183]\n"
"ds_read_b128 v[248:251], v16 offset:1024\n"
"v_mfma_f32_16x16x32_bf16 v[180:183], a[80:83], v[228:231], v[180:183]\n"
"ds_write_b128 v13, a[72:75] offset:9216\n"
"v_mfma_f32_16x16x32_bf16 v[180:183], a[136:139], v[232:235], v[180:183]\n"
"v_mfma_f32_16x16x32_bf16 v[180:183], v[52:55], v[236:239], v[180:183]\n"
"s_waitcnt vmcnt(3)\n"
"v_min3_u32 v28, v26, v189, v191\n"
"v_max3_u32 v29, v26, v189, v191\n"
"v_mfma_f32_16x16x32_bf16 a[92:95], v[44:47], a[32:35], a[92:95]\n"
"ds_read_b128 v[240:243], v14 offset:2048\n"
"v_mfma_f32_16x16x32_bf16 a[92:95], v[84:87], a[36:39], a[92:95]\n"
"s_waitcnt vmcnt(2)\n"
"ds_read_b128 v[132:135], v15 offset:3072\n"
"v_min3_u32 v28, v28, v193, v195\n"
"v_mfma_f32_16x16x32_bf16 a[92:95], a[84:87], a[40:43], a[92:95]\n"
"ds_write_b128 v13, a[76:79] offset:10240\n"
"v_mfma_f32_16x16x32_bf16 a[92:95], v[56:59], a[56:59], a[92:95]\n"
"s_waitcnt lgkmcnt(6)\n"
"v_add_f32 v168, v168, v148\n"
"v_add_f32 v169, v169, v149\n"
"v_mfma_f32_16x16x32_bf16 a[96:99], a[152:155], a[32:35], a[96:99]\n"
"v_add_f32 v170, v170, v150\n"
"v_add_f32 v171, v171, v151\n"
"v_mfma_f32_16x16x32_bf16 a[96:99], a[204:207], a[36:39], a[96:99]\n"
"s_waitcnt lgkmcnt(5)\n"
"ds_read_b128 v[148:151], v16 offset:4096\n"
"v_add_f32 v168, v168, v244\n"
"v_mfma_f32_16x16x32_bf16 a[96:99], v[32:35], a[40:43], a[96:99]\n"
"v_add_f32 v169, v169, v245\n"
"v_add_f32 v170, v170, v246\n"
"v_mfma_f32_16x16x32_bf16 a[96:99], v[108:111], a[56:59], a[96:99]\n"
"s_waitcnt lgkmcnt(4)\n"
"v_add_f32 v171, v171, v247\n"
"v_add_f32 v168, v168, v248\n"
"v_mfma_f32_16x16x32_bf16 a[100:103], v[92:95], a[32:35], a[100:103]\n"
"s_add_u32 s75, s42, 0x18000\n"
"ds_write_b128 v13, a[92:95] offset:11264\n"
"v_mfma_f32_16x16x32_bf16 a[100:103], a[64:67], a[36:39], a[100:103]\n"
"s_add_u32 s77, s42, 0x10000\n"
"v_add_f32 v169, v169, v249\n"
"v_add_f32 v170, v170, v250\n"
"v_mfma_f32_16x16x32_bf16 a[100:103], v[100:103], a[40:43], a[100:103]\n"
"s_add_u32 s84, s42, 0x8000\n"
"v_add_f32 v171, v171, v251\n"
"v_max3_u32 v29, v29, v193, v195\n"
"v_mfma_f32_16x16x32_bf16 a[100:103], a[88:91], a[56:59], a[100:103]\n"
"s_waitcnt vmcnt(1)\n"
"ds_read_b128 v[244:247], v14 offset:5120\n"
"v_min3_u32 v28, v28, v197, v199\n"
"v_mfma_f32_16x16x32_bf16 v[184:187], v[72:75], a[32:35], v[184:187]\n"
"s_add_u32 s78, s41, 0x2000\n"
"ds_read_b128 v[248:251], v15 offset:6144\n"
"v_max3_u32 v29, v29, v197, v199\n"
"v_mfma_f32_16x16x32_bf16 v[184:187], a[80:83], a[36:39], v[184:187]\n"
"s_add_u32 s96, s41, 0x1000\n"
"ds_write_b128 v13, a[96:99] offset:12288\n"
"v_mfma_f32_16x16x32_bf16 v[184:187], a[136:139], a[40:43], v[184:187]\n"
"s_waitcnt lgkmcnt(5)\n"
"v_add_f32 v172, v172, v240\n"
"v_add_f32 v173, v173, v241\n"
"v_mfma_f32_16x16x32_bf16 v[184:187], v[52:55], a[56:59], v[184:187]\n"
"s_add_u32 s76, s42, 0x20000\n"
"buffer_load_dwordx4 v[204:207], v21, s[64:67], s96 offen offset:0 sc1\n"
"v_add_f32 v174, v174, v242\n"
"buffer_load_dwordx4 v[208:211], v21, s[64:67], s96 offen offset:1024 sc1\n"
"buffer_load_dwordx4 v[212:215], v21, s[64:67], s96 offen offset:2048 sc1\n"
"ds_write_b128 v13, a[100:103] offset:13312\n"
"buffer_load_dwordx4 v[216:219], v21, s[64:67], s96 offen offset:3072 sc1\n"
"v_add_f32 v175, v175, v243\n"
"s_waitcnt vmcnt(4)\n"
"v_min3_u32 v28, v28, v201, v203\n"
"v_max3_u32 v29, v29, v201, v203\n"
"v_cmp_ne_u32 s[36:37], v28, v29\n"
"buffer_load_dwordx4 v[220:223], v21, s[64:67], s78 offen offset:0 sc1\n"
"s_cmp_lg_u64 s[36:37], 0\n"
"s_cbranch_scc1 .Lmha_SPP0\n"
".Lmha_SPP0R:\n"
"v_pk_add_f32 v[172:173], v[172:173], v[132:133]\n"
"v_pk_add_f32 v[174:175], v[174:175], v[134:135]\n"
"s_waitcnt lgkmcnt(3)\n"
"v_pk_add_f32 v[172:173], v[172:173], v[148:149]\n"
"v_pk_add_f32 v[174:175], v[174:175], v[150:151]\n"
"ds_read_b128 v[240:243], v16 offset:7168\n"
"v_pk_add_f32 v[176:177], v[176:177], v[244:245]\n"
"v_add_f32 v178, v178, v246\n"
"s_waitcnt lgkmcnt(1)\n"
"s_barrier\n"
"v_add_f32 v179, v179, v247\n"
"v_pk_add_f32 v[176:177], v[176:177], v[248:249]\n"
"v_pk_add_f32 v[178:179], v[178:179], v[250:251]\n"
"ds_read_b128 v[132:135], v14 offset:8192\n"
"ds_read_b128 v[148:151], v15 offset:9216\n"
"ds_read_b128 v[244:247], v16 offset:10240\n"
"ds_read_b128 v[248:251], v14 offset:11264\n"
"v_and_b32 v189, 0xffff0000, v188\n"
"v_and_b32 v193, 0xffff0000, v192\n"
"v_and_b32 v197, 0xffff0000, v196\n"
"v_and_b32 v201, 0xffff0000, v200\n"
"v_lshlrev_b32 v188, 16, v188\n"
"s_waitcnt lgkmcnt(4)\n"
"v_pk_add_f32 v[176:177], v[176:177], v[240:241]\n"
"v_pk_add_f32 v[178:179], v[178:179], v[242:243]\n"
"ds_read_b128 v[240:243], v15 offset:12288\n"
"v_and_b32 v191, 0xffff0000, v190\n"
"v_lshlrev_b32 v192, 16, v192\n"
"v_and_b32 v195, 0xffff0000, v194\n"
"v_lshlrev_b32 v196, 16, v196\n"
"v_and_b32 v199, 0xffff0000, v198\n"
"v_lshlrev_b32 v200, 16, v200\n"
"s_waitcnt lgkmcnt(4)\n"
"v_pk_add_f32 v[180:181], v[180:181], v[132:133]\n"
"v_pk_add_f32 v[182:183], v[182:183], v[134:135]\n"
"s_waitcnt lgkmcnt(3)\n"
"v_pk_add_f32 v[180:181], v[180:181], v[148:149]\n"
"v_pk_add_f32 v[182:183], v[182:183], v[150:151]\n"
"s_waitcnt lgkmcnt(2)\n"
"v_pk_add_f32 v[180:181], v[180:181], v[244:245]\n"
"v_pk_add_f32 v[182:183], v[182:183], v[246:247]\n"
"ds_read_b128 v[132:135], v16 offset:13312\n"
"s_waitcnt lgkmcnt(2)\n"
"v_pk_add_f32 v[184:185], v[184:185], v[248:249]\n"
"v_pk_add_f32 v[186:187], v[186:187], v[250:251]\n"
"s_waitcnt lgkmcnt(1)\n"
"v_pk_add_f32 v[184:185], v[184:185], v[240:241]\n"
"v_pk_add_f32 v[186:187], v[186:187], v[242:243]\n"
"v_and_b32 v203, 0xffff0000, v202\n"
"v_lshlrev_b32 v190, 16, v190\n"
"v_pk_add_f32 v[152:153], v[152:153], v[188:189]\n"
"v_lshlrev_b32 v194, 16, v194\n"
"v_pk_add_f32 v[156:157], v[156:157], v[192:193]\n"
"v_lshlrev_b32 v198, 16, v198\n"
"v_pk_add_f32 v[160:161], v[160:161], v[196:197]\n"
"v_lshlrev_b32 v202, 16, v202\n"
"v_pk_add_f32 v[164:165], v[164:165], v[200:201]\n"
"v_pk_add_f32 v[154:155], v[154:155], v[190:191]\n"
"v_cvt_pk_bf16_f32 v152, v152, v153\n"
"v_pk_add_f32 v[158:159], v[158:159], v[194:195]\n"
"s_waitcnt lgkmcnt(0)\n"
"v_pk_add_f32 v[184:185], v[184:185], v[132:133]\n"
"v_pk_add_f32 v[186:187], v[186:187], v[134:135]\n"
"v_cvt_pk_bf16_f32 v156, v156, v157\n"
"v_pk_add_f32 v[162:163], v[162:163], v[198:199]\n"
"v_cvt_pk_bf16_f32 v160, v160, v161\n"
"v_pk_add_f32 v[166:167], v[166:167], v[202:203]\n"
"v_cvt_pk_bf16_f32 v164, v164, v165\n"
"v_cvt_pk_bf16_f32 v153, v154, v155\n"
"v_cvt_pk_bf16_f32 v157, v158, v159\n"
"v_cvt_pk_bf16_f32 v161, v162, v163\n"
"v_cvt_pk_bf16_f32 v165, v166, v167\n"
"ds_bpermute_b32 v154, v20, v152\n"
"ds_bpermute_b32 v155, v20, v153\n"
"ds_bpermute_b32 v158, v20, v156\n"
"ds_bpermute_b32 v159, v20, v157\n"
"ds_bpermute_b32 v162, v20, v160\n"
"ds_bpermute_b32 v163, v20, v161\n"
"ds_bpermute_b32 v166, v20, v164\n"
"ds_bpermute_b32 v167, v20, v165\n"
"buffer_store_dwordx2 v[26:27], v25, s[64:67], s94 offen offset:0 sc1\n"
"s_waitcnt vmcnt(5)\n"
"v_min3_u32 v28, v26, v205, v207\n"
"v_max3_u32 v29, v26, v205, v207\n"
"s_waitcnt vmcnt(4)\n"
"v_min3_u32 v28, v28, v209, v211\n"
"v_max3_u32 v29, v29, v209, v211\n"
"s_waitcnt vmcnt(3)\n"
"v_min3_u32 v28, v28, v213, v215\n"
"v_max3_u32 v29, v29, v213, v215\n"
"s_waitcnt lgkmcnt(0)\n"
"buffer_store_dwordx2 v[154:155], v19, s[60:63], s42 offen offset:0\n"
"buffer_store_dwordx2 v[158:159], v19, s[60:63], s84 offen offset:0\n"
"buffer_store_dwordx2 v[162:163], v19, s[60:63], s77 offen offset:0\n"
"s_waitcnt vmcnt(5)\n"
"v_min3_u32 v28, v28, v217, v219\n"
"v_max3_u32 v29, v29, v217, v219\n"
"buffer_store_dwordx2 v[166:167], v19, s[60:63], s75 offen offset:0\n"
"s_waitcnt vmcnt(5)\n"
"v_min3_u32 v28, v28, v221, v223\n"
"v_max3_u32 v29, v29, v221, v223\n"
"v_cmp_ne_u32 s[36:37], v28, v29\n"
"s_nop 0\n"
"s_cmp_lg_u64 s[36:37], 0\n"
"s_cbranch_scc1 .Lmha_SPP1\n"
".Lmha_SPP1R:\n"
"v_and_b32 v205, 0xffff0000, v204\n"
"v_and_b32 v209, 0xffff0000, v208\n"
"v_and_b32 v213, 0xffff0000, v212\n"
"v_and_b32 v217, 0xffff0000, v216\n"
"v_and_b32 v221, 0xffff0000, v220\n"
"v_lshlrev_b32 v204, 16, v204\n"
"v_and_b32 v207, 0xffff0000, v206\n"
"v_lshlrev_b32 v208, 16, v208\n"
"v_and_b32 v211, 0xffff0000, v210\n"
"v_lshlrev_b32 v212, 16, v212\n"
"v_and_b32 v215, 0xffff0000, v214\n"
"v_lshlrev_b32 v216, 16, v216\n"
"v_and_b32 v219, 0xffff0000, v218\n"
"v_lshlrev_b32 v220, 16, v220\n"
"v_and_b32 v223, 0xffff0000, v222\n"
"v_lshlrev_b32 v206, 16, v206\n"
"v_pk_add_f32 v[168:169], v[168:169], v[204:205]\n"
"v_lshlrev_b32 v210, 16, v210\n"
"v_pk_add_f32 v[172:173], v[172:173], v[208:209]\n"
"v_lshlrev_b32 v214, 16, v214\n"
"v_pk_add_f32 v[176:177], v[176:177], v[212:213]\n"
"v_lshlrev_b32 v218, 16, v218\n"
"v_pk_add_f32 v[180:181], v[180:181], v[216:217]\n"
"v_lshlrev_b32 v222, 16, v222\n"
"v_pk_add_f32 v[184:185], v[184:185], v[220:221]\n"
"v_pk_add_f32 v[170:171], v[170:171], v[206:207]\n"
"v_cvt_pk_bf16_f32 v168, v168, v169\n"
"v_pk_add_f32 v[174:175], v[174:175], v[210:211]\n"
"v_cvt_pk_bf16_f32 v172, v172, v173\n"
"v_pk_add_f32 v[178:179], v[178:179], v[214:215]\n"
"v_cvt_pk_bf16_f32 v176, v176, v177\n"
"v_pk_add_f32 v[182:183], v[182:183], v[218:219]\n"
"v_cvt_pk_bf16_f32 v180, v180, v181\n"
"v_pk_add_f32 v[186:187], v[186:187], v[222:223]\n"
"v_cvt_pk_bf16_f32 v184, v184, v185\n"
"v_cvt_pk_bf16_f32 v169, v170, v171\n"
"v_cvt_pk_bf16_f32 v173, v174, v175\n"
"v_cvt_pk_bf16_f32 v177, v178, v179\n"
"v_cvt_pk_bf16_f32 v181, v182, v183\n"
"v_cvt_pk_bf16_f32 v185, v186, v187\n"
"ds_bpermute_b32 v170, v20, v168\n"
"ds_bpermute_b32 v171, v20, v169\n"
"ds_bpermute_b32 v174, v20, v172\n"
"ds_bpermute_b32 v175, v20, v173\n"
"ds_bpermute_b32 v178, v20, v176\n"
"ds_bpermute_b32 v179, v20, v177\n"
"ds_bpermute_b32 v182, v20, v180\n"
"ds_bpermute_b32 v183, v20, v181\n"
"ds_bpermute_b32 v186, v20, v184\n"
"ds_bpermute_b32 v187, v20, v185\n"
"s_waitcnt lgkmcnt(8)\n"
"buffer_store_dwordx2 v[170:171], v19, s[60:63], s76 offen offset:0\n"
"s_waitcnt lgkmcnt(6)\n"
"s_add_u32 s96, s42, 0x28000\n"
"buffer_store_dwordx2 v[174:175], v19, s[60:63], s96 offen offset:0\n"
"s_waitcnt lgkmcnt(4)\n"
"s_add_u32 s78, s42, 0x30000\n"
"buffer_store_dwordx2 v[178:179], v19, s[60:63], s78 offen offset:0\n"
"s_waitcnt lgkmcnt(2)\n"
"s_add_u32 s84, s42, 0x38000\n"
"buffer_store_dwordx2 v[182:183], v19, s[60:63], s84 offen offset:0\n"
"s_waitcnt lgkmcnt(0)\n"
"s_add_u32 s77, s42, 0x40000\n"
"buffer_store_dwordx2 v[186:187], v19, s[60:63], s77 offen offset:0\n"
"s_branch .Lmha_END\n"
".Lmha_VAR1:\n"
"ds_read_b128 v[68:71], v41 offset:33280\n"
"v_mfma_f32_16x16x32_bf16 v[240:243], a[32:35], v[72:75], v[240:243]\n"
"s_waitcnt vmcnt(13)\n"
"ds_read_b64_tr_b16 a[32:33], v45 offset:24576\n"
"v_mfma_f32_16x16x32_bf16 v[240:243], a[48:51], v[76:79], v[240:243]\n"
"s_add_u32 s73, s72, 6\n"
"buffer_load_dwordx4 v38, s[40:43], s75 offen offset:1024 lds\n"
"ds_read_b64_tr_b16 a[34:35], v45 offset:24832\n"
"v_mfma_f32_16x16x32_bf16 v[244:247], a[36:39], v[72:75], v[244:247]\n"
"s_and_b32 s73, s73, 15\n"
"ds_read_b64_tr_b16 a[36:37], v46 offset:24576\n"
"v_mfma_f32_16x16x32_bf16 v[244:247], a[52:55], v[76:79], v[244:247]\n"
"s_lshl_b32 s73, s73, 7\n"
"ds_read_b64_tr_b16 a[38:39], v46 offset:24832\n"
"v_mfma_f32_16x16x32_bf16 v[248:251], a[40:43], v[72:75], v[248:251]\n"
"ds_read_b64_tr_b16 a[40:41], v45 offset:25088\n"
"v_mfma_f32_16x16x32_bf16 v[248:251], a[56:59], v[76:79], v[248:251]\n"
"buffer_load_dwordx4 v39, s[40:43], s75 offen offset:2048 lds\n"
"ds_read_b64_tr_b16 a[42:43], v45 offset:25344\n"
"v_mfma_f32_16x16x32_bf16 v[252:255], a[44:47], v[72:75], v[252:255]\n"
"ds_read_b128 v[72:75], v41 offset:34816\n"
"v_mfma_f32_16x16x32_bf16 v[252:255], a[60:63], v[76:79], v[252:255]\n"
"s_waitcnt lgkmcnt(9)\n"
"ds_read_b128 v[76:79], v41 offset:35328\n"
"v_mfma_f32_16x16x32_bf16 v[112:115], a[0:3], v[48:51], v[112:115]\n"
"ds_read_b64_tr_b16 a[44:45], v46 offset:25088\n"
"v_mfma_f32_16x16x32_bf16 v[112:115], a[16:19], v[52:55], v[112:115]\n"
"buffer_load_dwordx4 v40, s[40:43], s75 offen offset:3072 lds\n"
"ds_read_b64_tr_b16 a[46:47], v46 offset:25344\n"
"v_mfma_f32_16x16x32_bf16 v[116:119], a[4:7], v[48:51], v[116:119]\n"
"v_mfma_f32_16x16x32_bf16 v[116:119], a[20:23], v[52:55], v[116:119]\n"
"v_mfma_f32_16x16x32_bf16 v[120:123], a[8:11], v[48:51], v[120:123]\n"
"s_waitcnt vmcnt(12)\n"
"s_add_u32 m0, s30, 0x2000\n"
"ds_read_b64_tr_b16 a[48:49], v45 offset:28672\n"
"v_mfma_f32_16x16x32_bf16 v[120:123], a[24:27], v[52:55], v[120:123]\n"
"buffer_load_dwordx4 v32, s[36:39], s73 offen offset:0 lds\n"
"ds_read_b64_tr_b16 a[50:51], v45 offset:28928\n"
"v_mfma_f32_16x16x32_bf16 v[124:127], a[12:15], v[48:51], v[124:127]\n"
"ds_read_b128 v[48:51], v42 offset:32768\n"
"v_mfma_f32_16x16x32_bf16 v[124:127], a[28:31], v[52:55], v[124:127]\n"
"ds_read_b128 v[52:55], v42 offset:33280\n"
"v_mfma_f32_16x16x32_bf16 v[128:131], a[0:3], v[56:59], v[128:131]\n"
"ds_read_b64_tr_b16 a[52:53], v46 offset:28672\n"
"v_mfma_f32_16x16x32_bf16 v[128:131], a[16:19], v[60:63], v[128:131]\n"
"buffer_load_dwordx4 v33, s[36:39], s73 offen offset:1024 lds\n"
"ds_read_b64_tr_b16 a[54:55], v46 offset:28928\n"
"v_mfma_f32_16x16x32_bf16 v[132:135], a[4:7], v[56:59], v[132:135]\n"
"ds_read_b64_tr_b16 a[56:57], v45 offset:29184\n"
"v_mfma_f32_16x16x32_bf16 v[132:135], a[20:23], v[60:63], v[132:135]\n"
"ds_read_b64_tr_b16 a[58:59], v45 offset:29440\n"
"v_mfma_f32_16x16x32_bf16 v[136:139], a[8:11], v[56:59], v[136:139]\n"
"ds_read_b64_tr_b16 a[60:61], v46 offset:29184\n"
"v_mfma_f32_16x16x32_bf16 v[136:139], a[24:27], v[60:63], v[136:139]\n"
"s_add_u32 s90, s72, 7\n"
"buffer_load_dwordx4 v34, s[36:39], s73 offen offset:2048 lds\n"
"ds_read_b64_tr_b16 a[62:63], v46 offset:29440\n"
"v_mfma_f32_16x16x32_bf16 v[140:143], a[12:15], v[56:59], v[140:143]\n"
"s_and_b32 s90, s90, 15\n"
"ds_read_b128 v[56:59], v42 offset:34816\n"
"v_mfma_f32_16x16x32_bf16 v[140:143], a[28:31], v[60:63], v[140:143]\n"
"s_waitcnt lgkmcnt(10)\n"
"s_lshl_b32 s90, s90, 19\n"
"ds_read_b128 v[60:63], v42 offset:35328\n"
"v_mfma_f32_16x16x32_bf16 v[144:147], a[0:3], v[64:67], v[144:147]\n"
"v_mfma_f32_16x16x32_bf16 v[144:147], a[16:19], v[68:71], v[144:147]\n"
"buffer_load_dwordx4 v35, s[36:39], s73 offen offset:3072 lds\n"
"v_mfma_f32_16x16x32_bf16 v[148:151], a[4:7], v[64:67], v[148:151]\n"
"v_mfma_f32_16x16x32_bf16 v[148:151], a[20:23], v[68:71], v[148:151]\n"
"v_mfma_f32_16x16x32_bf16 v[152:155], a[8:11], v[64:67], v[152:155]\n"
"s_add_u32 m0, s30, 0x9400\n"
"v_mfma_f32_16x16x32_bf16 v[152:155], a[24:27], v[68:71], v[152:155]\n"
"buffer_load_dwordx4 v36, s[36:39], s73 offen offset:0 lds\n"
"v_mfma_f32_16x16x32_bf16 v[156:159], a[12:15], v[64:67], v[156:159]\n"
"ds_read_b128 v[64:67], v43 offset:32768\n"
"v_mfma_f32_16x16x32_bf16 v[156:159], a[28:31], v[68:71], v[156:159]\n"
"ds_read_b128 v[68:71], v43 offset:33280\n"
"v_mfma_f32_16x16x32_bf16 v[160:163], a[0:3], v[72:75], v[160:163]\n"
"s_add_u32 m0, s30, 0x6000\n"
"v_mfma_f32_16x16x32_bf16 v[160:163], a[16:19], v[76:79], v[160:163]\n"
"buffer_load_dwordx4 v37, s[40:43], s90 offen offset:0 lds\n"
"v_mfma_f32_16x16x32_bf16 v[164:167], a[4:7], v[72:75], v[164:167]\n"
"v_mfma_f32_16x16x32_bf16 v[164:167], a[20:23], v[76:79], v[164:167]\n"
"v_mfma_f32_16x16x32_bf16 v[168:171], a[8:11], v[72:75], v[168:171]\n"
"s_add_u32 m0, s30, 0\n"
"v_mfma_f32_16x16x32_bf16 v[168:171], a[24:27], v[76:79], v[168:171]\n"
"buffer_load_dwordx4 v17, s[44:47], 0 offen offset:0 nt lds\n"
"v_mfma_f32_16x16x32_bf16 v[172:175], a[12:15], v[72:75], v[172:175]\n"
"ds_read_b128 v[72:75], v43 offset:34816\n"
"v_mfma_f32_16x16x32_bf16 v[172:175], a[28:31], v[76:79], v[172:175]\n"
"s_waitcnt lgkmcnt(3)\n"
"ds_read_b128 v[76:79], v43 offset:35328\n"
"v_mfma_f32_16x16x32_bf16 v[176:179], a[0:3], v[48:51], v[176:179]\n"
"v_mfma_f32_16x16x32_bf16 v[176:179], a[16:19], v[52:55], v[176:179]\n"
"buffer_load_dwordx4 v17, s[44:47], 0 offen offset:1024 nt lds\n"
"v_mfma_f32_16x16x32_bf16 v[180:183], a[4:7], v[48:51], v[180:183]\n"
"v_mfma_f32_16x16x32_bf16 v[180:183], a[20:23], v[52:55], v[180:183]\n"
"v_mfma_f32_16x16x32_bf16 v[184:187], a[8:11], v[48:51], v[184:187]\n"
"v_mfma_f32_16x16x32_bf16 v[184:187], a[24:27], v[52:55], v[184:187]\n"
"buffer_load_dwordx4 v18, s[44:47], 0 offen offset:2048 nt lds\n"
"v_mfma_f32_16x16x32_bf16 v[188:191], a[12:15], v[48:51], v[188:191]\n"
"ds_read_b128 v[48:51], v44 offset:0\n"
"v_mfma_f32_16x16x32_bf16 v[188:191], a[28:31], v[52:55], v[188:191]\n"
"ds_read_b128 v[52:55], v44 offset:512\n"
"v_mfma_f32_16x16x32_bf16 v[192:195], a[0:3], v[56:59], v[192:195]\n"
"v_mfma_f32_16x16x32_bf16 v[192:195], a[16:19], v[60:63], v[192:195]\n"
"buffer_load_dwordx4 v18, s[44:47], 0 offen offset:3072 nt lds\n"
"v_mfma_f32_16x16x32_bf16 v[196:199], a[4:7], v[56:59], v[196:199]\n"
"v_mfma_f32_16x16x32_bf16 v[196:199], a[20:23], v[60:63], v[196:199]\n"
"v_mfma_f32_16x16x32_bf16 v[200:203], a[8:11], v[56:59], v[200:203]\n"
"s_mov_b32 s76, 0x4000\n"
"s_add_u32 m0, s30, 0x1000\n"
"v_mfma_f32_16x16x32_bf16 v[200:203], a[24:27], v[60:63], v[200:203]\n"
"buffer_load_dwordx4 v17, s[44:47], s76 offen offset:0 nt lds\n"
"v_mfma_f32_16x16x32_bf16 v[204:207], a[12:15], v[56:59], v[204:207]\n"
"v_mfma_f32_16x16x32_bf16 v[204:207], a[28:31], v[60:63], v[204:207]\n"
"s_waitcnt lgkmcnt(2)\n"
"v_mfma_f32_16x16x32_bf16 v[208:211], a[0:3], v[64:67], v[208:211]\n"
"v_mfma_f32_16x16x32_bf16 v[208:211], a[16:19], v[68:71], v[208:211]\n"
"buffer_load_dwordx4 v17, s[44:47], s76 offen offset:1024 nt lds\n"
"v_mfma_f32_16x16x32_bf16 v[212:215], a[4:7], v[64:67], v[212:215]\n"
"v_mfma_f32_16x16x32_bf16 v[212:215], a[20:23], v[68:71], v[212:215]\n"
"v_mfma_f32_16x16x32_bf16 v[216:219], a[8:11], v[64:67], v[216:219]\n"
"v_mfma_f32_16x16x32_bf16 v[216:219], a[24:27], v[68:71], v[216:219]\n"
"buffer_load_dwordx4 v18, s[44:47], s76 offen offset:2048 nt lds\n"
"v_mfma_f32_16x16x32_bf16 v[220:223], a[12:15], v[64:67], v[220:223]\n"
"v_mfma_f32_16x16x32_bf16 v[220:223], a[28:31], v[68:71], v[220:223]\n"
"v_mfma_f32_16x16x32_bf16 v[224:227], a[0:3], v[72:75], v[224:227]\n"
"v_mfma_f32_16x16x32_bf16 v[224:227], a[16:19], v[76:79], v[224:227]\n"
"s_waitcnt vmcnt(21) lgkmcnt(0)\n"
"s_barrier\n"
"buffer_load_dwordx4 v18, s[44:47], s76 offen offset:3072 nt lds\n"
"v_mfma_f32_16x16x32_bf16 v[228:231], a[4:7], v[72:75], v[228:231]\n"
"ds_read_b128 v[56:59], v27 offset:12288\n"
"ds_read_b128 v[60:63], v27 offset:12800\n"
"v_mfma_f32_16x16x32_bf16 v[228:231], a[20:23], v[76:79], v[228:231]\n"
"ds_read_b128 v[64:67], v27 offset:14336\n"
"ds_read_b128 v[68:71], v27 offset:14848\n"
"v_mfma_f32_16x16x32_bf16 v[232:235], a[8:11], v[72:75], v[232:235]\n"
"v_mfma_f32_16x16x32_bf16 v[232:235], a[24:27], v[76:79], v[232:235]\n"
"v_mfma_f32_16x16x32_bf16 v[236:239], a[12:15], v[72:75], v[236:239]\n"
"ds_read_b128 v[72:75], v41 offset:12288\n"
"v_mfma_f32_16x16x32_bf16 v[236:239], a[28:31], v[76:79], v[236:239]\n"
"ds_read_b128 v[76:79], v41 offset:12800\n"
"v_mfma_f32_16x16x32_bf16 v[240:243], a[0:3], v[48:51], v[240:243]\n"
"s_waitcnt vmcnt(18)\n"
"ds_read_b64_tr_b16 a[0:1], v45 offset:16384\n"
"v_mfma_f32_16x16x32_bf16 v[240:243], a[16:19], v[52:55], v[240:243]\n"
"ds_read_b64_tr_b16 a[2:3], v45 offset:16640\n"
"v_mfma_f32_16x16x32_bf16 v[244:247], a[4:7], v[48:51], v[244:247]\n"
"s_waitcnt vmcnt(14)\n"
"ds_read_b64_tr_b16 a[16:17], v45 offset:20480\n"
"v_mfma_f32_16x16x32_bf16 v[244:247], a[20:23], v[52:55], v[244:247]\n"
"ds_read_b64_tr_b16 a[18:19], v45 offset:20736\n"
"v_mfma_f32_16x16x32_bf16 v[248:251], a[8:11], v[48:51], v[248:251]\n"
"ds_read_b64_tr_b16 a[4:5], v46 offset:16384\n"
"v_mfma_f32_16x16x32_bf16 v[248:251], a[24:27], v[52:55], v[248:251]\n"
"ds_read_b64_tr_b16 a[6:7], v46 offset:16640\n"
"v_mfma_f32_16x16x32_bf16 v[252:255], a[12:15], v[48:51], v[252:255]\n"
"ds_read_b128 v[48:51], v41 offset:14336\n"
"v_mfma_f32_16x16x32_bf16 v[252:255], a[28:31], v[52:55], v[252:255]\n"
"s_waitcnt lgkmcnt(9)\n"
"ds_read_b128 v[52:55], v41 offset:14848\n"
"v_mfma_f32_16x16x32_bf16 v[112:115], a[32:35], v[56:59], v[112:115]\n"
"ds_read_b64_tr_b16 a[20:21], v46 offset:20480\n"
"v_mfma_f32_16x16x32_bf16 v[112:115], a[48:51], v[60:63], v[112:115]\n"
"ds_read_b64_tr_b16 a[22:23], v46 offset:20736\n"
"v_mfma_f32_16x16x32_bf16 v[116:119], a[36:39], v[56:59], v[116:119]\n"
"ds_read_b64_tr_b16 a[8:9], v45 offset:16896\n"
"v_mfma_f32_16x16x32_bf16 v[116:119], a[52:55], v[60:63], v[116:119]\n"
"ds_read_b64_tr_b16 a[10:11], v45 offset:17152\n"
"v_mfma_f32_16x16x32_bf16 v[120:123], a[40:43], v[56:59], v[120:123]\n"
"ds_read_b64_tr_b16 a[24:25], v45 offset:20992\n"
"v_mfma_f32_16x16x32_bf16 v[120:123], a[56:59], v[60:63], v[120:123]\n"
"ds_read_b64_tr_b16 a[26:27], v45 offset:21248\n"
"v_mfma_f32_16x16x32_bf16 v[124:127], a[44:47], v[56:59], v[124:127]\n"
"ds_read_b128 v[56:59], v42 offset:12288\n"
"v_mfma_f32_16x16x32_bf16 v[124:127], a[60:63], v[60:63], v[124:127]\n"
"ds_read_b128 v[60:63], v42 offset:12800\n"
"v_mfma_f32_16x16x32_bf16 v[128:131], a[32:35], v[64:67], v[128:131]\n"
"s_add_u32 m0, s30, 0x6000\n"
"ds_read_b64_tr_b16 a[12:13], v46 offset:16896\n"
"v_mfma_f32_16x16x32_bf16 v[128:131], a[48:51], v[68:71], v[128:131]\n"
"buffer_load_dwordx4 v38, s[40:43], s90 offen offset:1024 lds\n"
"ds_read_b64_tr_b16 a[14:15], v46 offset:17152\n"
"v_mfma_f32_16x16x32_bf16 v[132:135], a[36:39], v[64:67], v[132:135]\n"
"ds_read_b64_tr_b16 a[28:29], v46 offset:20992\n"
"v_mfma_f32_16x16x32_bf16 v[132:135], a[52:55], v[68:71], v[132:135]\n"
"ds_read_b64_tr_b16 a[30:31], v46 offset:21248\n"
"v_mfma_f32_16x16x32_bf16 v[136:139], a[40:43], v[64:67], v[136:139]\n"
"v_mfma_f32_16x16x32_bf16 v[136:139], a[56:59], v[68:71], v[136:139]\n"
"buffer_load_dwordx4 v39, s[40:43], s90 offen offset:2048 lds\n"
"v_mfma_f32_16x16x32_bf16 v[140:143], a[44:47], v[64:67], v[140:143]\n"
"ds_read_b128 v[64:67], v42 offset:14336\n"
"v_mfma_f32_16x16x32_bf16 v[140:143], a[60:63], v[68:71], v[140:143]\n"
"s_waitcnt lgkmcnt(8)\n"
"ds_read_b128 v[68:71], v42 offset:14848\n"
"v_mfma_f32_16x16x32_bf16 v[144:147], a[32:35], v[72:75], v[144:147]\n"
"v_mfma_f32_16x16x32_bf16 v[144:147], a[48:51], v[76:79], v[144:147]\n"
"buffer_load_dwordx4 v40, s[40:43], s90 offen offset:3072 lds\n"
"v_mfma_f32_16x16x32_bf16 v[148:151], a[36:39], v[72:75], v[148:151]\n"
"v_mfma_f32_16x16x32_bf16 v[148:151], a[52:55], v[76:79], v[148:151]\n"
"v_mfma_f32_16x16x32_bf16 v[152:155], a[40:43], v[72:75], v[152:155]\n"
"s_add_u32 s77, s90, 0x40000\n"
"s_add_u32 m0, s30, 0x7000\n"
"v_mfma_f32_16x16x32_bf16 v[152:155], a[56:59], v[76:79], v[152:155]\n"
"buffer_load_dwordx4 v37, s[40:43], s77 offen offset:0 lds\n"
"v_mfma_f32_16x16x32_bf16 v[156:159], a[44:47], v[72:75], v[156:159]\n"
"ds_read_b128 v[72:75], v43 offset:12288\n"
"v_mfma_f32_16x16x32_bf16 v[156:159], a[60:63], v[76:79], v[156:159]\n"
"ds_read_b128 v[76:79], v43 offset:12800\n"
"v_mfma_f32_16x16x32_bf16 v[160:163], a[32:35], v[48:51], v[160:163]\n"
"s_add_u32 s88, s72, 7\n"
"v_mfma_f32_16x16x32_bf16 v[160:163], a[48:51], v[52:55], v[160:163]\n"
"s_and_b32 s88, s88, 15\n"
"buffer_load_dwordx4 v38, s[40:43], s77 offen offset:1024 lds\n"
"v_mfma_f32_16x16x32_bf16 v[164:167], a[36:39], v[48:51], v[164:167]\n"
"s_lshl_b32 s88, s88, 7\n"
"v_mfma_f32_16x16x32_bf16 v[164:167], a[52:55], v[52:55], v[164:167]\n"
"v_mfma_f32_16x16x32_bf16 v[168:171], a[40:43], v[48:51], v[168:171]\n"
"buffer_load_dwordx4 v39, s[40:43], s77 offen offset:2048 lds\n"
"v_mfma_f32_16x16x32_bf16 v[168:171], a[56:59], v[52:55], v[168:171]\n"
"v_mfma_f32_16x16x32_bf16 v[172:175], a[44:47], v[48:51], v[172:175]\n"
"ds_read_b128 v[48:51], v43 offset:14336\n"
"v_mfma_f32_16x16x32_bf16 v[172:175], a[60:63], v[52:55], v[172:175]\n"
"s_waitcnt lgkmcnt(3)\n"
"ds_read_b128 v[52:55], v43 offset:14848\n"
"v_mfma_f32_16x16x32_bf16 v[176:179], a[32:35], v[56:59], v[176:179]\n"
"buffer_load_dwordx4 v40, s[40:43], s77 offen offset:3072 lds\n"
"v_mfma_f32_16x16x32_bf16 v[176:179], a[48:51], v[60:63], v[176:179]\n"
"v_mfma_f32_16x16x32_bf16 v[180:183], a[36:39], v[56:59], v[180:183]\n"
"v_mfma_f32_16x16x32_bf16 v[180:183], a[52:55], v[60:63], v[180:183]\n"
"s_add_u32 m0, s30, 0x8000\n"
"v_mfma_f32_16x16x32_bf16 v[184:187], a[40:43], v[56:59], v[184:187]\n"
"buffer_load_dwordx4 v32, s[36:39], s88 offen offset:0 lds\n"
"v_mfma_f32_16x16x32_bf16 v[184:187], a[56:59], v[60:63], v[184:187]\n"
"v_mfma_f32_16x16x32_bf16 v[188:191], a[44:47], v[56:59], v[188:191]\n"
"ds_read_b128 v[56:59], v30 offset:0\n"
"v_mfma_f32_16x16x32_bf16 v[188:191], a[60:63], v[60:63], v[188:191]\n"
"ds_read_b128 v[60:63], v30 offset:512\n"
"v_mfma_f32_16x16x32_bf16 v[192:195], a[32:35], v[64:67], v[192:195]\n"
"s_add_u32 s91, s72, 8\n"
"buffer_load_dwordx4 v33, s[36:39], s88 offen offset:1024 lds\n"
"v_mfma_f32_16x16x32_bf16 v[192:195], a[48:51], v[68:71], v[192:195]\n"
"s_and_b32 s91, s91, 15\n"
"v_mfma_f32_16x16x32_bf16 v[196:199], a[36:39], v[64:67], v[196:199]\n"
"s_lshl_b32 s91, s91, 19\n"
"v_mfma_f32_16x16x32_bf16 v[196:199], a[52:55], v[68:71], v[196:199]\n"
"v_mfma_f32_16x16x32_bf16 v[200:203], a[40:43], v[64:67], v[200:203]\n"
"buffer_load_dwordx4 v34, s[36:39], s88 offen offset:2048 lds\n"
"v_mfma_f32_16x16x32_bf16 v[200:203], a[56:59], v[68:71], v[200:203]\n"
"v_mfma_f32_16x16x32_bf16 v[204:207], a[44:47], v[64:67], v[204:207]\n"
"v_mfma_f32_16x16x32_bf16 v[204:207], a[60:63], v[68:71], v[204:207]\n"
"s_waitcnt lgkmcnt(2)\n"
"v_mfma_f32_16x16x32_bf16 v[208:211], a[32:35], v[72:75], v[208:211]\n"
"buffer_load_dwordx4 v35, s[36:39], s88 offen offset:3072 lds\n"
"v_mfma_f32_16x16x32_bf16 v[208:211], a[48:51], v[76:79], v[208:211]\n"
"v_mfma_f32_16x16x32_bf16 v[212:215], a[36:39], v[72:75], v[212:215]\n"
"v_mfma_f32_16x16x32_bf16 v[212:215], a[52:55], v[76:79], v[212:215]\n"
"s_add_u32 m0, s30, 0x4000\n"
"v_mfma_f32_16x16x32_bf16 v[216:219], a[40:43], v[72:75], v[216:219]\n"
"buffer_load_dwordx4 v37, s[40:43], s91 offen offset:0 lds\n"
"v_mfma_f32_16x16x32_bf16 v[216:219], a[56:59], v[76:79], v[216:219]\n"
"v_mfma_f32_16x16x32_bf16 v[220:223], a[44:47], v[72:75], v[220:223]\n"
"v_mfma_f32_16x16x32_bf16 v[220:223], a[60:63], v[76:79], v[220:223]\n"
"v_mfma_f32_16x16x32_bf16 v[224:227], a[32:35], v[48:51], v[224:227]\n"
"buffer_load_dwordx4 v38, s[40:43], s91 offen offset:1024 lds\n"
"v_mfma_f32_16x16x32_bf16 v[224:227], a[48:51], v[52:55], v[224:227]\n"
"s_waitcnt vmcnt(22) lgkmcnt(0)\n"
"s_barrier\n"
"v_mfma_f32_16x16x32_bf16 v[228:231], a[36:39], v[48:51], v[228:231]\n"
"ds_read_b128 v[64:67], v27 offset:8192\n"
"ds_read_b128 v[68:71], v27 offset:8704\n"
"v_mfma_f32_16x16x32_bf16 v[228:231], a[52:55], v[52:55], v[228:231]\n"
"ds_read_b128 v[72:75], v27 offset:10240\n"
"ds_read_b128 v[76:79], v27 offset:10752\n"
"v_mfma_f32_16x16x32_bf16 v[232:235], a[40:43], v[48:51], v[232:235]\n"
"buffer_load_dwordx4 v39, s[40:43], s91 offen offset:2048 lds\n"
"v_mfma_f32_16x16x32_bf16 v[232:235], a[56:59], v[52:55], v[232:235]\n"
"v_mfma_f32_16x16x32_bf16 v[236:239], a[44:47], v[48:51], v[236:239]\n"
"ds_read_b128 v[48:51], v41 offset:8192\n"
"v_mfma_f32_16x16x32_bf16 v[236:239], a[60:63], v[52:55], v[236:239]\n"
"ds_read_b128 v[52:55], v41 offset:8704\n"
"v_mfma_f32_16x16x32_bf16 v[240:243], a[32:35], v[56:59], v[240:243]\n"
"buffer_load_dwordx4 v40, s[40:43], s91 offen offset:3072 lds\n"
"v_mfma_f32_16x16x32_bf16 v[240:243], a[48:51], v[60:63], v[240:243]\n"
"v_mfma_f32_16x16x32_bf16 v[244:247], a[36:39], v[56:59], v[244:247]\n"
"s_add_u32 m0, s30, 0x5000\n"
"v_mfma_f32_16x16x32_bf16 v[244:247], a[52:55], v[60:63], v[244:247]\n"
"s_waitcnt vmcnt(12)\n"
"s_add_u32 s78, s91, 0x40000\n"
"ds_read_b64_tr_b16 a[32:33], v45 offset:24576\n"
"v_mfma_f32_16x16x32_bf16 v[248:251], a[40:43], v[56:59], v[248:251]\n"
"buffer_load_dwordx4 v37, s[40:43], s78 offen offset:0 lds\n"
"ds_read_b64_tr_b16 a[34:35], v45 offset:24832\n"
"v_mfma_f32_16x16x32_bf16 v[248:251], a[56:59], v[60:63], v[248:251]\n"
"ds_read_b64_tr_b16 a[36:37], v46 offset:24576\n"
"v_mfma_f32_16x16x32_bf16 v[252:255], a[44:47], v[56:59], v[252:255]\n"
"ds_read_b128 v[56:59], v41 offset:10240\n"
"v_mfma_f32_16x16x32_bf16 v[252:255], a[60:63], v[60:63], v[252:255]\n"
"s_waitcnt lgkmcnt(6)\n"
"ds_read_b128 v[60:63], v41 offset:10752\n"
"v_mfma_f32_16x16x32_bf16 v[112:115], a[0:3], v[64:67], v[112:115]\n"
"s_add_u32 s89, s72, 8\n"
"buffer_load_dwordx4 v38, s[40:43], s78 offen offset:1024 lds\n"
"ds_read_b64_tr_b16 a[38:39], v46 offset:24832\n"
"v_mfma_f32_16x16x32_bf16 v[112:115], a[16:19], v[68:71], v[112:115]\n"
"s_and_b32 s89, s89, 15\n"
"ds_read_b64_tr_b16 a[40:41], v45 offset:25088\n"
"v_mfma_f32_16x16x32_bf16 v[116:119], a[4:7], v[64:67], v[116:119]\n"
"s_lshl_b32 s89, s89, 7\n"
"ds_read_b64_tr_b16 a[42:43], v45 offset:25344\n"
"v_mfma_f32_16x16x32_bf16 v[116:119], a[20:23], v[68:71], v[116:119]\n"
"ds_read_b64_tr_b16 a[44:45], v46 offset:25088\n"
"v_mfma_f32_16x16x32_bf16 v[120:123], a[8:11], v[64:67], v[120:123]\n"
"buffer_load_dwordx4 v39, s[40:43], s78 offen offset:2048 lds\n"
"ds_read_b64_tr_b16 a[46:47], v46 offset:25344\n"
"v_mfma_f32_16x16x32_bf16 v[120:123], a[24:27], v[68:71], v[120:123]\n"
"v_mfma_f32_16x16x32_bf16 v[124:127], a[12:15], v[64:67], v[124:127]\n"
"ds_read_b128 v[64:67], v42 offset:8192\n"
"v_mfma_f32_16x16x32_bf16 v[124:127], a[28:31], v[68:71], v[124:127]\n"
"ds_read_b128 v[68:71], v42 offset:8704\n"
"v_mfma_f32_16x16x32_bf16 v[128:131], a[0:3], v[72:75], v[128:131]\n"
"buffer_load_dwordx4 v40, s[40:43], s78 offen offset:3072 lds\n"
"v_mfma_f32_16x16x32_bf16 v[128:131], a[16:19], v[76:79], v[128:131]\n"
"v_mfma_f32_16x16x32_bf16 v[132:135], a[4:7], v[72:75], v[132:135]\n"
"s_waitcnt vmcnt(12)\n"
"ds_read_b64_tr_b16 a[48:49], v45 offset:28672\n"
"v_mfma_f32_16x16x32_bf16 v[132:135], a[20:23], v[76:79], v[132:135]\n"
"s_add_u32 m0, s30, 0x3000\n"
"ds_read_b64_tr_b16 a[50:51], v45 offset:28928\n"
"v_mfma_f32_16x16x32_bf16 v[136:139], a[8:11], v[72:75], v[136:139]\n"
"buffer_load_dwordx4 v32, s[36:39], s89 offen offset:0 lds\n"
"ds_read_b64_tr_b16 a[52:53], v46 offset:28672\n"
"v_mfma_f32_16x16x32_bf16 v[136:139], a[24:27], v[76:79], v[136:139]\n"
"ds_read_b64_tr_b16 a[54:55], v46 offset:28928\n"
"v_mfma_f32_16x16x32_bf16 v[140:143], a[12:15], v[72:75], v[140:143]\n"
"ds_read_b128 v[72:75], v42 offset:10240\n"
"v_mfma_f32_16x16x32_bf16 v[140:143], a[28:31], v[76:79], v[140:143]\n"
"s_waitcnt lgkmcnt(8)\n"
"ds_read_b128 v[76:79], v42 offset:10752\n"
"v_mfma_f32_16x16x32_bf16 v[144:147], a[0:3], v[48:51], v[144:147]\n"
"buffer_load_dwordx4 v33, s[36:39], s89 offen offset:1024 lds\n"
"ds_read_b64_tr_b16 a[56:57], v45 offset:29184\n"
"v_mfma_f32_16x16x32_bf16 v[144:147], a[16:19], v[52:55], v[144:147]\n"
"ds_read_b64_tr_b16 a[58:59], v45 offset:29440\n"
"v_mfma_f32_16x16x32_bf16 v[148:151], a[4:7], v[48:51], v[148:151]\n"
"ds_read_b64_tr_b16 a[60:61], v46 offset:29184\n"
"v_mfma_f32_16x16x32_bf16 v[148:151], a[20:23], v[52:55], v[148:151]\n"
"ds_read_b64_tr_b16 a[62:63], v46 offset:29440\n"
"v_mfma_f32_16x16x32_bf16 v[152:155], a[8:11], v[48:51], v[152:155]\n"
"buffer_load_dwordx4 v34, s[36:39], s89 offen offset:2048 lds\n"
"v_mfma_f32_16x16x32_bf16 v[152:155], a[24:27], v[52:55], v[152:155]\n"
"v_mfma_f32_16x16x32_bf16 v[156:159], a[12:15], v[48:51], v[156:159]\n"
"ds_read_b128 v[48:51], v43 offset:8192\n"
"v_mfma_f32_16x16x32_bf16 v[156:159], a[28:31], v[52:55], v[156:159]\n"
"ds_read_b128 v[52:55], v43 offset:8704\n"
"v_mfma_f32_16x16x32_bf16 v[160:163], a[0:3], v[56:59], v[160:163]\n"
"buffer_load_dwordx4 v35, s[36:39], s89 offen offset:3072 lds\n"
"v_mfma_f32_16x16x32_bf16 v[160:163], a[16:19], v[60:63], v[160:163]\n"
"v_mfma_f32_16x16x32_bf16 v[164:167], a[4:7], v[56:59], v[164:167]\n"
"v_mfma_f32_16x16x32_bf16 v[164:167], a[20:23], v[60:63], v[164:167]\n"
"s_add_u32 m0, s30, 0x9000\n"
"v_mfma_f32_16x16x32_bf16 v[168:171], a[8:11], v[56:59], v[168:171]\n"
"buffer_load_dwordx4 v36, s[36:39], s89 offen offset:0 lds\n"
"v_mfma_f32_16x16x32_bf16 v[168:171], a[24:27], v[60:63], v[168:171]\n"
"v_mfma_f32_16x16x32_bf16 v[172:175], a[12:15], v[56:59], v[172:175]\n"
"buffer_load_dwordx4 a[64:67], v19, s[48:51], 0 offen offset:0 nt\n"
"ds_read_b128 v[56:59], v43 offset:10240\n"
"v_mfma_f32_16x16x32_bf16 v[172:175], a[28:31], v[60:63], v[172:175]\n"
"s_waitcnt lgkmcnt(3)\n"
"ds_read_b128 v[60:63], v43 offset:10752\n"
"v_mfma_f32_16x16x32_bf16 v[176:179], a[0:3], v[64:67], v[176:179]\n"
"v_mfma_f32_16x16x32_bf16 v[176:179], a[16:19], v[68:71], v[176:179]\n"
"v_mfma_f32_16x16x32_bf16 v[180:183], a[4:7], v[64:67], v[180:183]\n"
"buffer_load_dwordx4 a[68:71], v20, s[48:51], 0 offen offset:1024 nt\n"
"v_mfma_f32_16x16x32_bf16 v[180:183], a[20:23], v[68:71], v[180:183]\n"
"v_mfma_f32_16x16x32_bf16 v[184:187], a[8:11], v[64:67], v[184:187]\n"
"v_mfma_f32_16x16x32_bf16 v[184:187], a[24:27], v[68:71], v[184:187]\n"
"v_mfma_f32_16x16x32_bf16 v[188:191], a[12:15], v[64:67], v[188:191]\n"
"buffer_load_dwordx4 a[72:75], v19, s[48:51], 0 offen offset:2048 nt\n"
"ds_read_b128 v[64:67], v44 offset:1024\n"
"v_mfma_f32_16x16x32_bf16 v[188:191], a[28:31], v[68:71], v[188:191]\n"
"ds_read_b128 v[68:71], v44 offset:1536\n"
"v_mfma_f32_16x16x32_bf16 v[192:195], a[0:3], v[72:75], v[192:195]\n"
"v_mfma_f32_16x16x32_bf16 v[192:195], a[16:19], v[76:79], v[192:195]\n"
"v_mfma_f32_16x16x32_bf16 v[196:199], a[4:7], v[72:75], v[196:199]\n"
"buffer_load_dwordx4 a[76:79], v20, s[48:51], 0 offen offset:3072 nt\n"
"v_mfma_f32_16x16x32_bf16 v[196:199], a[20:23], v[76:79], v[196:199]\n"
"v_mfma_f32_16x16x32_bf16 v[200:203], a[8:11], v[72:75], v[200:203]\n"
"v_mfma_f32_16x16x32_bf16 v[200:203], a[24:27], v[76:79], v[200:203]\n"
"s_mov_b32 s84, 0x8000\n"
"v_mfma_f32_16x16x32_bf16 v[204:207], a[12:15], v[72:75], v[204:207]\n"
"buffer_load_dwordx4 a[80:83], v17, s[44:47], s84 offen offset:0 nt\n"
"v_mfma_f32_16x16x32_bf16 v[204:207], a[28:31], v[76:79], v[204:207]\n"
"s_waitcnt lgkmcnt(2)\n"
"v_mfma_f32_16x16x32_bf16 v[208:211], a[0:3], v[48:51], v[208:211]\n"
"v_mfma_f32_16x16x32_bf16 v[208:211], a[16:19], v[52:55], v[208:211]\n"
"v_mfma_f32_16x16x32_bf16 v[212:215], a[4:7], v[48:51], v[212:215]\n"
"buffer_load_dwordx4 a[84:87], v17, s[44:47], s84 offen offset:1024 nt\n"
"v_mfma_f32_16x16x32_bf16 v[212:215], a[20:23], v[52:55], v[212:215]\n"
"v_mfma_f32_16x16x32_bf16 v[216:219], a[8:11], v[48:51], v[216:219]\n"
"v_mfma_f32_16x16x32_bf16 v[216:219], a[24:27], v[52:55], v[216:219]\n"
"v_mfma_f32_16x16x32_bf16 v[220:223], a[12:15], v[48:51], v[220:223]\n"
"buffer_load_dwordx4 a[88:91], v18, s[44:47], s84 offen offset:2048 nt\n"
"v_mfma_f32_16x16x32_bf16 v[220:223], a[28:31], v[52:55], v[220:223]\n"
"v_mfma_f32_16x16x32_bf16 v[224:227], a[0:3], v[56:59], v[224:227]\n"
"v_mfma_f32_16x16x32_bf16 v[224:227], a[16:19], v[60:63], v[224:227]\n"
"s_waitcnt vmcnt(20) lgkmcnt(0)\n"
"s_barrier\n"
"v_mfma_f32_16x16x32_bf16 v[228:231], a[4:7], v[56:59], v[228:231]\n"
"buffer_load_dwordx4 a[92:95], v18, s[44:47], s84 offen offset:3072 nt\n"
"ds_read_b128 v[72:75], v27 offset:32768\n"
"v_mfma_f32_16x16x32_bf16 v[228:231], a[20:23], v[60:63], v[228:231]\n"
"ds_read_b128 v[76:79], v27 offset:33280\n"
"ds_read_b128 v[48:51], v27 offset:34816\n"
"v_mfma_f32_16x16x32_bf16 v[232:235], a[8:11], v[56:59], v[232:235]\n"
"ds_read_b128 v[52:55], v27 offset:35328\n"
"v_mfma_f32_16x16x32_bf16 v[232:235], a[24:27], v[60:63], v[232:235]\n"
"v_mfma_f32_16x16x32_bf16 v[236:239], a[12:15], v[56:59], v[236:239]\n"
"ds_read_b128 v[56:59], v41 offset:32768\n"
"v_mfma_f32_16x16x32_bf16 v[236:239], a[28:31], v[60:63], v[236:239]\n"
"ds_read_b128 v[60:63], v41 offset:33280\n"
"v_mfma_f32_16x16x32_bf16 v[240:243], a[0:3], v[64:67], v[240:243]\n"
"s_waitcnt vmcnt(17)\n"
"ds_read_b64_tr_b16 a[0:1], v45 offset:16384\n"
"v_mfma_f32_16x16x32_bf16 v[240:243], a[16:19], v[68:71], v[240:243]\n"
"ds_read_b64_tr_b16 a[2:3], v45 offset:16640\n"
"v_mfma_f32_16x16x32_bf16 v[244:247], a[4:7], v[64:67], v[244:247]\n"
"s_waitcnt vmcnt(13)\n"
"ds_read_b64_tr_b16 a[16:17], v45 offset:20480\n"
"v_mfma_f32_16x16x32_bf16 v[244:247], a[20:23], v[68:71], v[244:247]\n"
"ds_read_b64_tr_b16 a[18:19], v45 offset:20736\n"
"v_mfma_f32_16x16x32_bf16 v[248:251], a[8:11], v[64:67], v[248:251]\n"
"ds_read_b64_tr_b16 a[4:5], v46 offset:16384\n"
"v_mfma_f32_16x16x32_bf16 v[248:251], a[24:27], v[68:71], v[248:251]\n"
"ds_read_b64_tr_b16 a[6:7], v46 offset:16640\n"
"v_mfma_f32_16x16x32_bf16 v[252:255], a[12:15], v[64:67], v[252:255]\n"
"s_add_u32 s74, s72, 9\n"
"ds_read_b128 v[64:67], v41 offset:34816\n"
"v_mfma_f32_16x16x32_bf16 v[252:255], a[28:31], v[68:71], v[252:255]\n"
"s_waitcnt lgkmcnt(9)\n"
"s_and_b32 s74, s74, 15\n"
"ds_read_b128 v[68:71], v41 offset:35328\n"
"v_mfma_f32_16x16x32_bf16 v[112:115], a[32:35], v[72:75], v[112:115]\n"
"s_lshl_b32 s74, s74, 19\n"
"ds_read_b64_tr_b16 a[20:21], v46 offset:20480\n"
"v_mfma_f32_16x16x32_bf16 v[112:115], a[48:51], v[76:79], v[112:115]\n"
"ds_read_b64_tr_b16 a[22:23], v46 offset:20736\n"
"v_mfma_f32_16x16x32_bf16 v[116:119], a[36:39], v[72:75], v[116:119]\n"
"ds_read_b64_tr_b16 a[8:9], v45 offset:16896\n"
"v_mfma_f32_16x16x32_bf16 v[116:119], a[52:55], v[76:79], v[116:119]\n"
"ds_read_b64_tr_b16 a[10:11], v45 offset:17152\n"
"v_mfma_f32_16x16x32_bf16 v[120:123], a[40:43], v[72:75], v[120:123]\n"
"ds_read_b64_tr_b16 a[24:25], v45 offset:20992\n"
"v_mfma_f32_16x16x32_bf16 v[120:123], a[56:59], v[76:79], v[120:123]\n"
"ds_read_b64_tr_b16 a[26:27], v45 offset:21248\n"
"v_mfma_f32_16x16x32_bf16 v[124:127], a[44:47], v[72:75], v[124:127]\n"
"ds_read_b128 v[72:75], v42 offset:32768\n"
"v_mfma_f32_16x16x32_bf16 v[124:127], a[60:63], v[76:79], v[124:127]\n"
"ds_read_b128 v[76:79], v42 offset:33280\n"
"v_mfma_f32_16x16x32_bf16 v[128:131], a[32:35], v[48:51], v[128:131]\n"
"ds_read_b64_tr_b16 a[12:13], v46 offset:16896\n"
"v_mfma_f32_16x16x32_bf16 v[128:131], a[48:51], v[52:55], v[128:131]\n"
"s_add_u32 m0, s30, 0x6000\n"
"ds_read_b64_tr_b16 a[14:15], v46 offset:17152\n"
"v_mfma_f32_16x16x32_bf16 v[132:135], a[36:39], v[48:51], v[132:135]\n"
"buffer_load_dwordx4 v37, s[40:43], s74 offen offset:0 lds\n"
"ds_read_b64_tr_b16 a[28:29], v46 offset:20992\n"
"v_mfma_f32_16x16x32_bf16 v[132:135], a[52:55], v[52:55], v[132:135]\n"
"ds_read_b64_tr_b16 a[30:31], v46 offset:21248\n"
"v_mfma_f32_16x16x32_bf16 v[136:139], a[40:43], v[48:51], v[136:139]\n"
"v_mfma_f32_16x16x32_bf16 v[136:139], a[56:59], v[52:55], v[136:139]\n"
"v_mfma_f32_16x16x32_bf16 v[140:143], a[44:47], v[48:51], v[140:143]\n"
"buffer_load_dwordx4 v38, s[40:43], s74 offen offset:1024 lds\n"
"ds_read_b128 v[48:51], v42 offset:34816\n"
"v_mfma_f32_16x16x32_bf16 v[140:143], a[60:63], v[52:55], v[140:143]\n"
"s_waitcnt lgkmcnt(8)\n"
"ds_read_b128 v[52:55], v42 offset:35328\n"
"v_mfma_f32_16x16x32_bf16 v[144:147], a[32:35], v[56:59], v[144:147]\n"
"v_mfma_f32_16x16x32_bf16 v[144:147], a[48:51], v[60:63], v[144:147]\n"
"v_mfma_f32_16x16x32_bf16 v[148:151], a[36:39], v[56:59], v[148:151]\n"
"buffer_load_dwordx4 v39, s[40:43], s74 offen offset:2048 lds\n"
"v_mfma_f32_16x16x32_bf16 v[148:151], a[52:55], v[60:63], v[148:151]\n"
"v_mfma_f32_16x16x32_bf16 v[152:155], a[40:43], v[56:59], v[152:155]\n"
"v_mfma_f32_16x16x32_bf16 v[152:155], a[56:59], v[60:63], v[152:155]\n"
"v_mfma_f32_16x16x32_bf16 v[156:159], a[44:47], v[56:59], v[156:159]\n"
"buffer_load_dwordx4 v40, s[40:43], s74 offen offset:3072 lds\n"
"ds_read_b128 v[56:59], v43 offset:32768\n"
"v_mfma_f32_16x16x32_bf16 v[156:159], a[60:63], v[60:63], v[156:159]\n"
"ds_read_b128 v[60:63], v43 offset:33280\n"
"v_mfma_f32_16x16x32_bf16 v[160:163], a[32:35], v[64:67], v[160:163]\n"
"v_mfma_f32_16x16x32_bf16 v[160:163], a[48:51], v[68:71], v[160:163]\n"
"s_add_u32 s96, s74, 0x40000\n"
"s_add_u32 m0, s30, 0x7000\n"
"v_mfma_f32_16x16x32_bf16 v[164:167], a[36:39], v[64:67], v[164:167]\n"
"buffer_load_dwordx4 v37, s[40:43], s96 offen offset:0 lds\n"
"v_mfma_f32_16x16x32_bf16 v[164:167], a[52:55], v[68:71], v[164:167]\n"
"v_mfma_f32_16x16x32_bf16 v[168:171], a[40:43], v[64:67], v[168:171]\n"
"v_mfma_f32_16x16x32_bf16 v[168:171], a[56:59], v[68:71], v[168:171]\n"
"s_add_u32 s73, s72, 9\n"
"v_mfma_f32_16x16x32_bf16 v[172:175], a[44:47], v[64:67], v[172:175]\n"
"s_and_b32 s73, s73, 15\n"
"buffer_load_dwordx4 v38, s[40:43], s96 offen offset:1024 lds\n"
"ds_read_b128 v[64:67], v43 offset:34816\n"
"v_mfma_f32_16x16x32_bf16 v[172:175], a[60:63], v[68:71], v[172:175]\n"
"s_waitcnt lgkmcnt(3)\n"
"s_lshl_b32 s73, s73, 7\n"
"ds_read_b128 v[68:71], v43 offset:35328\n"
"v_mfma_f32_16x16x32_bf16 v[176:179], a[32:35], v[72:75], v[176:179]\n"
"v_mfma_f32_16x16x32_bf16 v[176:179], a[48:51], v[76:79], v[176:179]\n"
"buffer_load_dwordx4 v39, s[40:43], s96 offen offset:2048 lds\n"
"v_mfma_f32_16x16x32_bf16 v[180:183], a[36:39], v[72:75], v[180:183]\n"
"v_mfma_f32_16x16x32_bf16 v[180:183], a[52:55], v[76:79], v[180:183]\n"
"v_mfma_f32_16x16x32_bf16 v[184:187], a[40:43], v[72:75], v[184:187]\n"
"v_mfma_f32_16x16x32_bf16 v[184:187], a[56:59], v[76:79], v[184:187]\n"
"buffer_load_dwordx4 v40, s[40:43], s96 offen offset:3072 lds\n"
"v_mfma_f32_16x16x32_bf16 v[188:191], a[44:47], v[72:75], v[188:191]\n"
"ds_read_b128 v[72:75], v30 offset:1024\n"
"v_mfma_f32_16x16x32_bf16 v[188:191], a[60:63], v[76:79], v[188:191]\n"
"ds_read_b128 v[76:79], v30 offset:1536\n"
"v_mfma_f32_16x16x32_bf16 v[192:195], a[32:35], v[48:51], v[192:195]\n"
"s_add_u32 m0, s30, 0x2000\n"
"v_mfma_f32_16x16x32_bf16 v[192:195], a[48:51], v[52:55], v[192:195]\n"
"buffer_load_dwordx4 v32, s[36:39], s73 offen offset:0 lds\n"
"v_mfma_f32_16x16x32_bf16 v[196:199], a[36:39], v[48:51], v[196:199]\n"
"v_mfma_f32_16x16x32_bf16 v[196:199], a[52:55], v[52:55], v[196:199]\n"
"v_mfma_f32_16x16x32_bf16 v[200:203], a[40:43], v[48:51], v[200:203]\n"
"v_mfma_f32_16x16x32_bf16 v[200:203], a[56:59], v[52:55], v[200:203]\n"
"s_add_u32 s90, s72, 10\n"
"buffer_load_dwordx4 v33, s[36:39], s73 offen offset:1024 lds\n"
"v_mfma_f32_16x16x32_bf16 v[204:207], a[44:47], v[48:51], v[204:207]\n"
"s_and_b32 s90, s90, 15\n"
"v_mfma_f32_16x16x32_bf16 v[204:207], a[60:63], v[52:55], v[204:207]\n"
"s_waitcnt lgkmcnt(2)\n"
"s_lshl_b32 s90, s90, 19\n"
"v_mfma_f32_16x16x32_bf16 v[208:211], a[32:35], v[56:59], v[208:211]\n"
"v_mfma_f32_16x16x32_bf16 v[208:211], a[48:51], v[60:63], v[208:211]\n"
"buffer_load_dwordx4 v34, s[36:39], s73 offen offset:2048 lds\n"
"v_mfma_f32_16x16x32_bf16 v[212:215], a[36:39], v[56:59], v[212:215]\n"
"v_mfma_f32_16x16x32_bf16 v[212:215], a[52:55], v[60:63], v[212:215]\n"
"v_mfma_f32_16x16x32_bf16 v[216:219], a[40:43], v[56:59], v[216:219]\n"
"v_mfma_f32_16x16x32_bf16 v[216:219], a[56:59], v[60:63], v[216:219]\n"
"s_getpc_b64 s[98:99]\n"
"buffer_load_dwordx4 v35, s[36:39], s73 offen offset:3072 lds\n"
"v_mfma_f32_16x16x32_bf16 v[220:223], a[44:47], v[56:59], v[220:223]\n"
"v_mfma_f32_16x16x32_bf16 v[220:223], a[60:63], v[60:63], v[220:223]\n"
"v_mfma_f32_16x16x32_bf16 v[224:227], a[32:35], v[64:67], v[224:227]\n"
"s_add_u32 m0, s30, 0x4000\n"
"global_load_dword v26, v0, s[98:99] offset:1536\n"
"v_mfma_f32_16x16x32_bf16 v[224:227], a[48:51], v[68:71], v[224:227]\n"
"s_waitcnt vmcnt(21) lgkmcnt(0)\n"
"s_barrier\n"
"buffer_load_dwordx4 v37, s[40:43], s90 offen offset:0 lds\n"
"v_mfma_f32_16x16x32_bf16 v[228:231], a[36:39], v[64:67], v[228:231]\n"
"ds_read_b128 v[48:51], v27 offset:12288\n"
"ds_read_b128 v[52:55], v27 offset:12800\n"
"v_mfma_f32_16x16x32_bf16 v[228:231], a[52:55], v[68:71], v[228:231]\n"
"ds_read_b128 v[56:59], v27 offset:14336\n"
"ds_read_b128 v[60:63], v27 offset:14848\n"
"v_mfma_f32_16x16x32_bf16 v[232:235], a[40:43], v[64:67], v[232:235]\n"
"v_mfma_f32_16x16x32_bf16 v[232:235], a[56:59], v[68:71], v[232:235]\n"
"buffer_load_dwordx4 v38, s[40:43], s90 offen offset:1024 lds\n"
"v_mfma_f32_16x16x32_bf16 v[236:239], a[44:47], v[64:67], v[236:239]\n"
"ds_read_b128 v[64:67], v41 offset:12288\n"
"v_mfma_f32_16x16x32_bf16 v[236:239], a[60:63], v[68:71], v[236:239]\n"
"ds_read_b128 v[68:71], v41 offset:12800\n"
"v_mfma_f32_16x16x32_bf16 v[240:243], a[32:35], v[72:75], v[240:243]\n"
"v_mfma_f32_16x16x32_bf16 v[240:243], a[48:51], v[76:79], v[240:243]\n"
"buffer_load_dwordx4 v39, s[40:43], s90 offen offset:2048 lds\n"
"v_mfma_f32_16x16x32_bf16 v[244:247], a[36:39], v[72:75], v[244:247]\n"
"v_mfma_f32_16x16x32_bf16 v[244:247], a[52:55], v[76:79], v[244:247]\n"
"v_mfma_f32_16x16x32_bf16 v[248:251], a[40:43], v[72:75], v[248:251]\n"
"v_mfma_f32_16x16x32_bf16 v[248:251], a[56:59], v[76:79], v[248:251]\n"
"buffer_load_dwordx4 v40, s[40:43], s90 offen offset:3072 lds\n"
"v_mfma_f32_16x16x32_bf16 v[252:255], a[44:47], v[72:75], v[252:255]\n"
"ds_read_b128 v[72:75], v41 offset:14336\n"
"v_mfma_f32_16x16x32_bf16 v[252:255], a[60:63], v[76:79], v[252:255]\n"
"s_waitcnt lgkmcnt(3)\n"
"s_add_u32 m0, s30, 0x5000\n"
"ds_read_b128 v[76:79], v41 offset:14848\n"
"v_mfma_f32_16x16x32_bf16 v[112:115], a[0:3], v[48:51], v[112:115]\n"
"s_waitcnt vmcnt(13)\n"
"s_add_u32 s75, s90, 0x40000\n"
"ds_read_b64_tr_b16 a[32:33], v45 offset:24576\n"
"v_mfma_f32_16x16x32_bf16 v[112:115], a[16:19], v[52:55], v[112:115]\n"
"buffer_load_dwordx4 v37, s[40:43], s75 offen offset:0 lds\n"
"ds_read_b64_tr_b16 a[34:35], v45 offset:24832\n"
"v_mfma_f32_16x16x32_bf16 v[116:119], a[4:7], v[48:51], v[116:119]\n"
"ds_read_b64_tr_b16 a[36:37], v46 offset:24576\n"
"v_mfma_f32_16x16x32_bf16 v[116:119], a[20:23], v[52:55], v[116:119]\n"
"ds_read_b64_tr_b16 a[38:39], v46 offset:24832\n"
"v_mfma_f32_16x16x32_bf16 v[120:123], a[8:11], v[48:51], v[120:123]\n"
"ds_read_b64_tr_b16 a[40:41], v45 offset:25088\n"
"v_mfma_f32_16x16x32_bf16 v[120:123], a[24:27], v[52:55], v[120:123]\n"
"s_add_u32 s88, s72, 10\n"
"buffer_load_dwordx4 v38, s[40:43], s75 offen offset:1024 lds\n"
"ds_read_b64_tr_b16 a[42:43], v45 offset:25344\n"
"v_mfma_f32_16x16x32_bf16 v[124:127], a[12:15], v[48:51], v[124:127]\n"
"s_and_b32 s88, s88, 15\n"
"ds_read_b128 v[48:51], v42 offset:12288\n"
"v_mfma_f32_16x16x32_bf16 v[124:127], a[28:31], v[52:55], v[124:127]\n"
"s_lshl_b32 s88, s88, 7\n"
"ds_read_b128 v[52:55], v42 offset:12800\n"
"v_mfma_f32_16x16x32_bf16 v[128:131], a[0:3], v[56:59], v[128:131]\n"
"ds_read_b64_tr_b16 a[44:45], v46 offset:25088\n"
"v_mfma_f32_16x16x32_bf16 v[128:131], a[16:19], v[60:63], v[128:131]\n"
"buffer_load_dwordx4 v39, s[40:43], s75 offen offset:2048 lds\n"
"ds_read_b64_tr_b16 a[46:47], v46 offset:25344\n"
"v_mfma_f32_16x16x32_bf16 v[132:135], a[4:7], v[56:59], v[132:135]\n"
"v_mfma_f32_16x16x32_bf16 v[132:135], a[20:23], v[60:63], v[132:135]\n"
"v_mfma_f32_16x16x32_bf16 v[136:139], a[8:11], v[56:59], v[136:139]\n"
"v_mfma_f32_16x16x32_bf16 v[136:139], a[24:27], v[60:63], v[136:139]\n"
"buffer_load_dwordx4 v40, s[40:43], s75 offen offset:3072 lds\n"
"v_mfma_f32_16x16x32_bf16 v[140:143], a[12:15], v[56:59], v[140:143]\n"
"ds_read_b128 v[56:59], v42 offset:14336\n"
"v_mfma_f32_16x16x32_bf16 v[140:143], a[28:31], v[60:63], v[140:143]\n"
"s_waitcnt lgkmcnt(6)\n"
"ds_read_b128 v[60:63], v42 offset:14848\n"
"v_mfma_f32_16x16x32_bf16 v[144:147], a[0:3], v[64:67], v[144:147]\n"
"s_waitcnt vmcnt(13)\n"
"s_add_u32 m0, s30, 0x8000\n"
"ds_read_b64_tr_b16 a[48:49], v45 offset:28672\n"
"v_mfma_f32_16x16x32_bf16 v[144:147], a[16:19], v[68:71], v[144:147]\n"
"buffer_load_dwordx4 v32, s[36:39], s88 offen offset:0 lds\n"
"ds_read_b64_tr_b16 a[50:51], v45 offset:28928\n"
"v_mfma_f32_16x16x32_bf16 v[148:151], a[4:7], v[64:67], v[148:151]\n"
"ds_read_b64_tr_b16 a[52:53], v46 offset:28672\n"
"v_mfma_f32_16x16x32_bf16 v[148:151], a[20:23], v[68:71], v[148:151]\n"
"ds_read_b64_tr_b16 a[54:55], v46 offset:28928\n"
"v_mfma_f32_16x16x32_bf16 v[152:155], a[8:11], v[64:67], v[152:155]\n"
"ds_read_b64_tr_b16 a[56:57], v45 offset:29184\n"
"v_mfma_f32_16x16x32_bf16 v[152:155], a[24:27], v[68:71], v[152:155]\n"
"buffer_load_dwordx4 v33, s[36:39], s88 offen offset:1024 lds\n"
"ds_read_b64_tr_b16 a[58:59], v45 offset:29440\n"
"v_mfma_f32_16x16x32_bf16 v[156:159], a[12:15], v[64:67], v[156:159]\n"
"ds_read_b128 v[64:67], v43 offset:12288\n"
"v_mfma_f32_16x16x32_bf16 v[156:159], a[28:31], v[68:71], v[156:159]\n"
"ds_read_b128 v[68:71], v43 offset:12800\n"
"v_mfma_f32_16x16x32_bf16 v[160:163], a[0:3], v[72:75], v[160:163]\n"
"ds_read_b64_tr_b16 a[60:61], v46 offset:29184\n"
"v_mfma_f32_16x16x32_bf16 v[160:163], a[16:19], v[76:79], v[160:163]\n"
"buffer_load_dwordx4 v34, s[36:39], s88 offen offset:2048 lds\n"
"ds_read_b64_tr_b16 a[62:63], v46 offset:29440\n"
"v_mfma_f32_16x16x32_bf16 v[164:167], a[4:7], v[72:75], v[164:167]\n"
"v_mfma_f32_16x16x32_bf16 v[164:167], a[20:23], v[76:79], v[164:167]\n"
"v_mfma_f32_16x16x32_bf16 v[168:171], a[8:11], v[72:75], v[168:171]\n"
"v_mfma_f32_16x16x32_bf16 v[168:171], a[24:27], v[76:79], v[168:171]\n"
"buffer_load_dwordx4 v35, s[36:39], s88 offen offset:3072 lds\n"
"v_mfma_f32_16x16x32_bf16 v[172:175], a[12:15], v[72:75], v[172:175]\n"
"ds_read_b128 v[72:75], v43 offset:14336\n"
"v_mfma_f32_16x16x32_bf16 v[172:175], a[28:31], v[76:79], v[172:175]\n"
"s_waitcnt lgkmcnt(6)\n"
"buffer_load_dwordx4 a[128:131], v19, s[48:51], s76 offen offset:0 nt\n"
"ds_read_b128 v[76:79], v43 offset:14848\n"
"v_mfma_f32_16x16x32_bf16 v[176:179], a[0:3], v[48:51], v[176:179]\n"
"v_mfma_f32_16x16x32_bf16 v[176:179], a[16:19], v[52:55], v[176:179]\n"
"v_mfma_f32_16x16x32_bf16 v[180:183], a[4:7], v[48:51], v[180:183]\n"
"v_mfma_f32_16x16x32_bf16 v[180:183], a[20:23], v[52:55], v[180:183]\n"
"buffer_load_dwordx4 a[132:135], v20, s[48:51], s76 offen offset:1024 nt\n"
"v_mfma_f32_16x16x32_bf16 v[184:187], a[8:11], v[48:51], v[184:187]\n"
"v_mfma_f32_16x16x32_bf16 v[184:187], a[24:27], v[52:55], v[184:187]\n"
"v_mfma_f32_16x16x32_bf16 v[188:191], a[12:15], v[48:51], v[188:191]\n"
"ds_read_b128 v[48:51], v44 offset:0\n"
"v_mfma_f32_16x16x32_bf16 v[188:191], a[28:31], v[52:55], v[188:191]\n"
"buffer_load_dwordx4 a[136:139], v19, s[48:51], s76 offen offset:2048 nt\n"
"ds_read_b128 v[52:55], v44 offset:512\n"
"v_mfma_f32_16x16x32_bf16 v[192:195], a[0:3], v[56:59], v[192:195]\n"
"v_mfma_f32_16x16x32_bf16 v[192:195], a[16:19], v[60:63], v[192:195]\n"
"v_mfma_f32_16x16x32_bf16 v[196:199], a[4:7], v[56:59], v[196:199]\n"
"v_mfma_f32_16x16x32_bf16 v[196:199], a[20:23], v[60:63], v[196:199]\n"
"buffer_load_dwordx4 a[140:143], v20, s[48:51], s76 offen offset:3072 nt\n"
"v_mfma_f32_16x16x32_bf16 v[200:203], a[8:11], v[56:59], v[200:203]\n"
"v_mfma_f32_16x16x32_bf16 v[200:203], a[24:27], v[60:63], v[200:203]\n"
"v_mfma_f32_16x16x32_bf16 v[204:207], a[12:15], v[56:59], v[204:207]\n"
"s_mov_b32 s77, 0xc000\n"
"v_mfma_f32_16x16x32_bf16 v[204:207], a[28:31], v[60:63], v[204:207]\n"
"s_waitcnt lgkmcnt(2)\n"
"buffer_load_dwordx4 a[144:147], v17, s[44:47], s77 offen offset:0 nt\n"
"v_mfma_f32_16x16x32_bf16 v[208:211], a[0:3], v[64:67], v[208:211]\n"
"v_mfma_f32_16x16x32_bf16 v[208:211], a[16:19], v[68:71], v[208:211]\n"
"v_mfma_f32_16x16x32_bf16 v[212:215], a[4:7], v[64:67], v[212:215]\n"
"v_mfma_f32_16x16x32_bf16 v[212:215], a[20:23], v[68:71], v[212:215]\n"
"buffer_load_dwordx4 a[148:151], v17, s[44:47], s77 offen offset:1024 nt\n"
"v_mfma_f32_16x16x32_bf16 v[216:219], a[8:11], v[64:67], v[216:219]\n"
"v_mfma_f32_16x16x32_bf16 v[216:219], a[24:27], v[68:71], v[216:219]\n"
"v_mfma_f32_16x16x32_bf16 v[220:223], a[12:15], v[64:67], v[220:223]\n"
"v_mfma_f32_16x16x32_bf16 v[220:223], a[28:31], v[68:71], v[220:223]\n"
"buffer_load_dwordx4 a[152:155], v18, s[44:47], s77 offen offset:2048 nt\n"
"v_mfma_f32_16x16x32_bf16 v[224:227], a[0:3], v[72:75], v[224:227]\n"
"v_mfma_f32_16x16x32_bf16 v[224:227], a[16:19], v[76:79], v[224:227]\n"
"s_waitcnt vmcnt(20) lgkmcnt(0)\n"
"s_barrier\n"
"v_mfma_f32_16x16x32_bf16 v[228:231], a[4:7], v[72:75], v[228:231]\n"
"ds_read_b128 v[56:59], v27 offset:8192\n"
"ds_read_b128 v[60:63], v27 offset:8704\n"
"v_mfma_f32_16x16x32_bf16 v[228:231], a[20:23], v[76:79], v[228:231]\n"
"buffer_load_dwordx4 a[156:159], v18, s[44:47], s77 offen offset:3072 nt\n"
"ds_read_b128 v[64:67], v27 offset:10240\n"
"v_mfma_f32_16x16x32_bf16 v[232:235], a[8:11], v[72:75], v[232:235]\n"
"ds_read_b128 v[68:71], v27 offset:10752\n"
"v_mfma_f32_16x16x32_bf16 v[232:235], a[24:27], v[76:79], v[232:235]\n"
"v_mfma_f32_16x16x32_bf16 v[236:239], a[12:15], v[72:75], v[236:239]\n"
"ds_read_b128 v[72:75], v41 offset:8192\n"
"v_mfma_f32_16x16x32_bf16 v[236:239], a[28:31], v[76:79], v[236:239]\n"
"ds_read_b128 v[76:79], v41 offset:8704\n"
"v_mfma_f32_16x16x32_bf16 v[240:243], a[0:3], v[48:51], v[240:243]\n"
"s_waitcnt vmcnt(16)\n"
"ds_read_b64_tr_b16 a[0:1], v45 offset:16384\n"
"v_mfma_f32_16x16x32_bf16 v[240:243], a[16:19], v[52:55], v[240:243]\n"
"ds_read_b64_tr_b16 a[2:3], v45 offset:16640\n"
"v_mfma_f32_16x16x32_bf16 v[244:247], a[4:7], v[48:51], v[244:247]\n"
"s_waitcnt vmcnt(12)\n"
"ds_read_b64_tr_b16 a[16:17], v45 offset:20480\n"
"v_mfma_f32_16x16x32_bf16 v[244:247], a[20:23], v[52:55], v[244:247]\n"
"ds_read_b64_tr_b16 a[18:19], v45 offset:20736\n"
"v_mfma_f32_16x16x32_bf16 v[248:251], a[8:11], v[48:51], v[248:251]\n"
"ds_read_b64_tr_b16 a[4:5], v46 offset:16384\n"
"v_mfma_f32_16x16x32_bf16 v[248:251], a[24:27], v[52:55], v[248:251]\n"
"ds_read_b64_tr_b16 a[6:7], v46 offset:16640\n"
"v_mfma_f32_16x16x32_bf16 v[252:255], a[12:15], v[48:51], v[252:255]\n"
"ds_read_b128 v[48:51], v41 offset:10240\n"
"v_mfma_f32_16x16x32_bf16 v[252:255], a[28:31], v[52:55], v[252:255]\n"
"s_waitcnt lgkmcnt(9)\n"
"ds_read_b128 v[52:55], v41 offset:10752\n"
"v_mfma_f32_16x16x32_bf16 v[112:115], a[32:35], v[56:59], v[112:115]\n"
"ds_read_b64_tr_b16 a[20:21], v46 offset:20480\n"
"v_mfma_f32_16x16x32_bf16 v[112:115], a[48:51], v[60:63], v[112:115]\n"
"ds_read_b64_tr_b16 a[22:23], v46 offset:20736\n"
"v_mfma_f32_16x16x32_bf16 v[116:119], a[36:39], v[56:59], v[116:119]\n"
"ds_read_b64_tr_b16 a[8:9], v45 offset:16896\n"
"v_mfma_f32_16x16x32_bf16 v[116:119], a[52:55], v[60:63], v[116:119]\n"
"s_add_u32 s91, s72, 11\n"
"ds_read_b64_tr_b16 a[10:11], v45 offset:17152\n"
"v_mfma_f32_16x16x32_bf16 v[120:123], a[40:43], v[56:59], v[120:123]\n"
"s_and_b32 s91, s91, 15\n"
"ds_read_b64_tr_b16 a[24:25], v45 offset:20992\n"
"v_mfma_f32_16x16x32_bf16 v[120:123], a[56:59], v[60:63], v[120:123]\n"
"s_lshl_b32 s91, s91, 19\n"
"ds_read_b64_tr_b16 a[26:27], v45 offset:21248\n"
"v_mfma_f32_16x16x32_bf16 v[124:127], a[44:47], v[56:59], v[124:127]\n"
"ds_read_b128 v[56:59], v42 offset:8192\n"
"v_mfma_f32_16x16x32_bf16 v[124:127], a[60:63], v[60:63], v[124:127]\n"
"ds_read_b128 v[60:63], v42 offset:8704\n"
"v_mfma_f32_16x16x32_bf16 v[128:131], a[32:35], v[64:67], v[128:131]\n"
"ds_read_b64_tr_b16 a[12:13], v46 offset:16896\n"
"v_mfma_f32_16x16x32_bf16 v[128:131], a[48:51], v[68:71], v[128:131]\n"
"ds_read_b64_tr_b16 a[14:15], v46 offset:17152\n"
"v_mfma_f32_16x16x32_bf16 v[132:135], a[36:39], v[64:67], v[132:135]\n"
"s_add_u32 m0, s30, 0x9400\n"
"ds_read_b64_tr_b16 a[28:29], v46 offset:20992\n"
"v_mfma_f32_16x16x32_bf16 v[132:135], a[52:55], v[68:71], v[132:135]\n"
"buffer_load_dwordx4 v36, s[36:39], s88 offen offset:0 lds\n"
"ds_read_b64_tr_b16 a[30:31], v46 offset:21248\n"
"v_mfma_f32_16x16x32_bf16 v[136:139], a[40:43], v[64:67], v[136:139]\n"
"v_mfma_f32_16x16x32_bf16 v[136:139], a[56:59], v[68:71], v[136:139]\n"
"v_mfma_f32_16x16x32_bf16 v[140:143], a[44:47], v[64:67], v[140:143]\n"
"s_add_u32 m0, s30, 0x6000\n"
"ds_read_b128 v[64:67], v42 offset:10240\n"
"v_mfma_f32_16x16x32_bf16 v[140:143], a[60:63], v[68:71], v[140:143]\n"
"s_waitcnt lgkmcnt(8)\n"
"buffer_load_dwordx4 v37, s[40:43], s91 offen offset:0 lds\n"
"ds_read_b128 v[68:71], v42 offset:10752\n"
"v_mfma_f32_16x16x32_bf16 v[144:147], a[32:35], v[72:75], v[144:147]\n"
"v_mfma_f32_16x16x32_bf16 v[144:147], a[48:51], v[76:79], v[144:147]\n"
"v_mfma_f32_16x16x32_bf16 v[148:151], a[36:39], v[72:75], v[148:151]\n"
"v_mfma_f32_16x16x32_bf16 v[148:151], a[52:55], v[76:79], v[148:151]\n"
"buffer_load_dwordx4 v38, s[40:43], s91 offen offset:1024 lds\n"
"v_mfma_f32_16x16x32_bf16 v[152:155], a[40:43], v[72:75], v[152:155]\n"
"v_mfma_f32_16x16x32_bf16 v[152:155], a[56:59], v[76:79], v[152:155]\n"
"v_mfma_f32_16x16x32_bf16 v[156:159], a[44:47], v[72:75], v[156:159]\n"
"ds_read_b128 v[72:75], v43 offset:8192\n"
"v_mfma_f32_16x16x32_bf16 v[156:159], a[60:63], v[76:79], v[156:159]\n"
"buffer_load_dwordx4 v39, s[40:43], s91 offen offset:2048 lds\n"
"ds_read_b128 v[76:79], v43 offset:8704\n"
"v_mfma_f32_16x16x32_bf16 v[160:163], a[32:35], v[48:51], v[160:163]\n"
"v_mfma_f32_16x16x32_bf16 v[160:163], a[48:51], v[52:55], v[160:163]\n"
"v_mfma_f32_16x16x32_bf16 v[164:167], a[36:39], v[48:51], v[164:167]\n"
"v_mfma_f32_16x16x32_bf16 v[164:167], a[52:55], v[52:55], v[164:167]\n"
"buffer_load_dwordx4 v40, s[40:43], s91 offen offset:3072 lds\n"
"v_mfma_f32_16x16x32_bf16 v[168:171], a[40:43], v[48:51], v[168:171]\n"
"v_mfma_f32_16x16x32_bf16 v[168:171], a[56:59], v[52:55], v[168:171]\n"
"v_mfma_f32_16x16x32_bf16 v[172:175], a[44:47], v[48:51], v[172:175]\n"
"s_add_u32 s78, s91, 0x40000\n"
"s_add_u32 m0, s30, 0x7000\n"
"ds_read_b128 v[48:51], v43 offset:10240\n"
"v_mfma_f32_16x16x32_bf16 v[172:175], a[60:63], v[52:55], v[172:175]\n"
"s_waitcnt lgkmcnt(3)\n"
"buffer_load_dwordx4 v37, s[40:43], s78 offen offset:0 lds\n"
"ds_read_b128 v[52:55], v43 offset:10752\n"
"v_mfma_f32_16x16x32_bf16 v[176:179], a[32:35], v[56:59], v[176:179]\n"
"v_mfma_f32_16x16x32_bf16 v[176:179], a[48:51], v[60:63], v[176:179]\n"
"v_mfma_f32_16x16x32_bf16 v[180:183], a[36:39], v[56:59], v[180:183]\n"
"s_add_u32 s89, s72, 11\n"
"buffer_load_dwordx4 v38, s[40:43], s78 offen offset:1024 lds\n"
"v_mfma_f32_16x16x32_bf16 v[180:183], a[52:55], v[60:63], v[180:183]\n"
"s_and_b32 s89, s89, 15\n"
"v_mfma_f32_16x16x32_bf16 v[184:187], a[40:43], v[56:59], v[184:187]\n"
"s_lshl_b32 s89, s89, 7\n"
"v_mfma_f32_16x16x32_bf16 v[184:187], a[56:59], v[60:63], v[184:187]\n"
"v_mfma_f32_16x16x32_bf16 v[188:191], a[44:47], v[56:59], v[188:191]\n"
"buffer_load_dwordx4 v39, s[40:43], s78 offen offset:2048 lds\n"
"ds_read_b128 v[56:59], v30 offset:0\n"
"v_mfma_f32_16x16x32_bf16 v[188:191], a[60:63], v[60:63], v[188:191]\n"
"ds_read_b128 v[60:63], v30 offset:512\n"
"v_mfma_f32_16x16x32_bf16 v[192:195], a[32:35], v[64:67], v[192:195]\n"
"v_mfma_f32_16x16x32_bf16 v[192:195], a[48:51], v[68:71], v[192:195]\n"
"v_mfma_f32_16x16x32_bf16 v[196:199], a[36:39], v[64:67], v[196:199]\n"
"buffer_load_dwordx4 v40, s[40:43], s78 offen offset:3072 lds\n"
"v_mfma_f32_16x16x32_bf16 v[196:199], a[52:55], v[68:71], v[196:199]\n"
"v_mfma_f32_16x16x32_bf16 v[200:203], a[40:43], v[64:67], v[200:203]\n"
"v_mfma_f32_16x16x32_bf16 v[200:203], a[56:59], v[68:71], v[200:203]\n"
"s_add_u32 m0, s30, 0x3000\n"
"v_mfma_f32_16x16x32_bf16 v[204:207], a[44:47], v[64:67], v[204:207]\n"
"buffer_load_dwordx4 v32, s[36:39], s89 offen offset:0 lds\n"
"v_mfma_f32_16x16x32_bf16 v[204:207], a[60:63], v[68:71], v[204:207]\n"
"s_waitcnt lgkmcnt(2)\n"
"v_mfma_f32_16x16x32_bf16 v[208:211], a[32:35], v[72:75], v[208:211]\n"
"v_mfma_f32_16x16x32_bf16 v[208:211], a[48:51], v[76:79], v[208:211]\n"
"v_mfma_f32_16x16x32_bf16 v[212:215], a[36:39], v[72:75], v[212:215]\n"
"s_add_u32 s74, s72, 12\n"
"buffer_load_dwordx4 v33, s[36:39], s89 offen offset:1024 lds\n"
"v_mfma_f32_16x16x32_bf16 v[212:215], a[52:55], v[76:79], v[212:215]\n"
"s_and_b32 s74, s74, 15\n"
"v_mfma_f32_16x16x32_bf16 v[216:219], a[40:43], v[72:75], v[216:219]\n"
"s_lshl_b32 s74, s74, 19\n"
"v_mfma_f32_16x16x32_bf16 v[216:219], a[56:59], v[76:79], v[216:219]\n"
"v_mfma_f32_16x16x32_bf16 v[220:223], a[44:47], v[72:75], v[220:223]\n"
"buffer_load_dwordx4 v34, s[36:39], s89 offen offset:2048 lds\n"
"v_mfma_f32_16x16x32_bf16 v[220:223], a[60:63], v[76:79], v[220:223]\n"
"v_mfma_f32_16x16x32_bf16 v[224:227], a[32:35], v[48:51], v[224:227]\n"
"v_mfma_f32_16x16x32_bf16 v[224:227], a[48:51], v[52:55], v[224:227]\n"
"v_mfma_f32_16x16x32_bf16 v[228:231], a[36:39], v[48:51], v[228:231]\n"
"buffer_load_dwordx4 v35, s[36:39], s89 offen offset:3072 lds\n"
"v_mfma_f32_16x16x32_bf16 v[228:231], a[52:55], v[52:55], v[228:231]\n"
"v_mfma_f32_16x16x32_bf16 v[232:235], a[40:43], v[48:51], v[232:235]\n"
"v_mfma_f32_16x16x32_bf16 v[232:235], a[56:59], v[52:55], v[232:235]\n"
"s_add_u32 m0, s30, 0x4000\n"
"v_mfma_f32_16x16x32_bf16 v[236:239], a[44:47], v[48:51], v[236:239]\n"
"s_waitcnt vmcnt(12) lgkmcnt(0)\n"
"s_barrier\n"
"buffer_load_dwordx4 v37, s[40:43], s74 offen offset:0 lds\n"
"v_mfma_f32_16x16x32_bf16 v[236:239], a[60:63], v[52:55], v[236:239]\n"
"ds_read_b128 v[64:67], v27 offset:32768\n"
"ds_read_b128 v[68:71], v27 offset:33280\n"
"v_mfma_f32_16x16x32_bf16 v[240:243], a[32:35], v[56:59], v[240:243]\n"
"ds_read_b128 v[72:75], v27 offset:34816\n"
"ds_read_b128 v[76:79], v27 offset:35328\n"
"v_mfma_f32_16x16x32_bf16 v[240:243], a[48:51], v[60:63], v[240:243]\n"
"ds_read_b128 v[48:51], v41 offset:32768\n"
"v_mfma_f32_16x16x32_bf16 v[244:247], a[36:39], v[56:59], v[244:247]\n"
"buffer_load_dwordx4 v38, s[40:43], s74 offen offset:1024 lds\n"
"ds_read_b128 v[52:55], v41 offset:33280\n"
"v_mfma_f32_16x16x32_bf16 v[244:247], a[52:55], v[60:63], v[244:247]\n"
"v_mfma_f32_16x16x32_bf16 v[248:251], a[40:43], v[56:59], v[248:251]\n"
"v_mfma_f32_16x16x32_bf16 v[248:251], a[56:59], v[60:63], v[248:251]\n"
"v_mfma_f32_16x16x32_bf16 v[252:255], a[44:47], v[56:59], v[252:255]\n"
"buffer_load_dwordx4 v39, s[40:43], s74 offen offset:2048 lds\n"
"ds_read_b128 v[56:59], v41 offset:34816\n"
"v_mfma_f32_16x16x32_bf16 v[252:255], a[60:63], v[60:63], v[252:255]\n"
"s_waitcnt lgkmcnt(6)\n"
"ds_read_b128 v[60:63], v41 offset:35328\n"
"v_mfma_f32_16x16x32_bf16 v[112:115], a[0:3], v[64:67], v[112:115]\n"
"s_waitcnt lgkmcnt(6)\n"
"v_mfma_f32_16x16x32_bf16 v[112:115], a[16:19], v[68:71], v[112:115]\n"
"v_mfma_f32_16x16x32_bf16 v[116:119], a[4:7], v[64:67], v[116:119]\n"
"buffer_load_dwordx4 v40, s[40:43], s74 offen offset:3072 lds\n"
"v_mfma_f32_16x16x32_bf16 v[116:119], a[20:23], v[68:71], v[116:119]\n"
"v_mfma_f32_16x16x32_bf16 v[120:123], a[8:11], v[64:67], v[120:123]\n"
"s_add_u32 m0, s30, 0x5000\n"
"v_mfma_f32_16x16x32_bf16 v[120:123], a[24:27], v[68:71], v[120:123]\n"
"s_waitcnt vmcnt(12)\n"
"s_add_u32 s84, s74, 0x40000\n"
"ds_read_b64_tr_b16 a[32:33], v45 offset:24576\n"
"v_mfma_f32_16x16x32_bf16 v[124:127], a[12:15], v[64:67], v[124:127]\n"
"buffer_load_dwordx4 v37, s[40:43], s84 offen offset:0 lds\n"
"ds_read_b128 v[64:67], v42 offset:32768\n"
"v_mfma_f32_16x16x32_bf16 v[124:127], a[28:31], v[68:71], v[124:127]\n"
"s_waitcnt lgkmcnt(4)\n"
"ds_read_b128 v[68:71], v42 offset:33280\n"
"v_mfma_f32_16x16x32_bf16 v[128:131], a[0:3], v[72:75], v[128:131]\n"
"ds_read_b64_tr_b16 a[34:35], v45 offset:24832\n"
"v_mfma_f32_16x16x32_bf16 v[128:131], a[16:19], v[76:79], v[128:131]\n"
"ds_read_b64_tr_b16 a[36:37], v46 offset:24576\n"
"v_mfma_f32_16x16x32_bf16 v[132:135], a[4:7], v[72:75], v[132:135]\n"
"s_add_u32 s73, s72, 12\n"
"buffer_load_dwordx4 v38, s[40:43], s84 offen offset:1024 lds\n"
"ds_read_b64_tr_b16 a[38:39], v46 offset:24832\n"
"v_mfma_f32_16x16x32_bf16 v[132:135], a[20:23], v[76:79], v[132:135]\n"
"s_and_b32 s73, s73, 15\n"
"ds_read_b64_tr_b16 a[40:41], v45 offset:25088\n"
"v_mfma_f32_16x16x32_bf16 v[136:139], a[8:11], v[72:75], v[136:139]\n"
"s_lshl_b32 s73, s73, 7\n"
"ds_read_b64_tr_b16 a[42:43], v45 offset:25344\n"
"v_mfma_f32_16x16x32_bf16 v[136:139], a[24:27], v[76:79], v[136:139]\n"
"ds_read_b64_tr_b16 a[44:45], v46 offset:25088\n"
"v_mfma_f32_16x16x32_bf16 v[140:143], a[12:15], v[72:75], v[140:143]\n"
"buffer_load_dwordx4 v39, s[40:43], s84 offen offset:2048 lds\n"
"ds_read_b128 v[72:75], v42 offset:34816\n"
"v_mfma_f32_16x16x32_bf16 v[140:143], a[28:31], v[76:79], v[140:143]\n"
"ds_read_b128 v[76:79], v42 offset:35328\n"
"v_mfma_f32_16x16x32_bf16 v[144:147], a[0:3], v[48:51], v[144:147]\n"
"ds_read_b64_tr_b16 a[46:47], v46 offset:25344\n"
"v_mfma_f32_16x16x32_bf16 v[144:147], a[16:19], v[52:55], v[144:147]\n"
"v_mfma_f32_16x16x32_bf16 v[148:151], a[4:7], v[48:51], v[148:151]\n"
"buffer_load_dwordx4 v40, s[40:43], s84 offen offset:3072 lds\n"
"v_mfma_f32_16x16x32_bf16 v[148:151], a[20:23], v[52:55], v[148:151]\n"
"v_mfma_f32_16x16x32_bf16 v[152:155], a[8:11], v[48:51], v[152:155]\n"
"s_waitcnt vmcnt(12)\n"
"ds_read_b64_tr_b16 a[48:49], v45 offset:28672\n"
"v_mfma_f32_16x16x32_bf16 v[152:155], a[24:27], v[52:55], v[152:155]\n"
"s_add_u32 m0, s30, 0x2000\n"
"ds_read_b64_tr_b16 a[50:51], v45 offset:28928\n"
"v_mfma_f32_16x16x32_bf16 v[156:159], a[12:15], v[48:51], v[156:159]\n"
"buffer_load_dwordx4 v32, s[36:39], s73 offen offset:0 lds\n"
"ds_read_b128 v[48:51], v43 offset:32768\n"
"v_mfma_f32_16x16x32_bf16 v[156:159], a[28:31], v[52:55], v[156:159]\n"
"s_waitcnt lgkmcnt(7)\n"
"ds_read_b128 v[52:55], v43 offset:33280\n"
"v_mfma_f32_16x16x32_bf16 v[160:163], a[0:3], v[56:59], v[160:163]\n"
"ds_read_b64_tr_b16 a[52:53], v46 offset:28672\n"
"v_mfma_f32_16x16x32_bf16 v[160:163], a[16:19], v[60:63], v[160:163]\n"
"ds_read_b64_tr_b16 a[54:55], v46 offset:28928\n"
"v_mfma_f32_16x16x32_bf16 v[164:167], a[4:7], v[56:59], v[164:167]\n"
"buffer_load_dwordx4 v33, s[36:39], s73 offen offset:1024 lds\n"
"ds_read_b64_tr_b16 a[56:57], v45 offset:29184\n"
"v_mfma_f32_16x16x32_bf16 v[164:167], a[20:23], v[60:63], v[164:167]\n"
"ds_read_b64_tr_b16 a[58:59], v45 offset:29440\n"
"v_mfma_f32_16x16x32_bf16 v[168:171], a[8:11], v[56:59], v[168:171]\n"
"ds_read_b64_tr_b16 a[60:61], v46 offset:29184\n"
"v_mfma_f32_16x16x32_bf16 v[168:171], a[24:27], v[60:63], v[168:171]\n"
"ds_read_b64_tr_b16 a[62:63], v46 offset:29440\n"
"v_mfma_f32_16x16x32_bf16 v[172:175], a[12:15], v[56:59], v[172:175]\n"
"buffer_load_dwordx4 v34, s[36:39], s73 offen offset:2048 lds\n"
"ds_read_b128 v[56:59], v43 offset:34816\n"
"v_mfma_f32_16x16x32_bf16 v[172:175], a[28:31], v[60:63], v[172:175]\n"
"ds_read_b128 v[60:63], v43 offset:35328\n"
"v_mfma_f32_16x16x32_bf16 v[176:179], a[0:3], v[64:67], v[176:179]\n"
"buffer_load_dword v26, v22, s[52:55], s31 offen offset:0\n"
"v_mfma_f32_16x16x32_bf16 v[176:179], a[16:19], v[68:71], v[176:179]\n"
"v_mfma_f32_16x16x32_bf16 v[180:183], a[4:7], v[64:67], v[180:183]\n"
"v_mfma_f32_16x16x32_bf16 v[180:183], a[20:23], v[68:71], v[180:183]\n"
"v_mfma_f32_16x16x32_bf16 v[184:187], a[8:11], v[64:67], v[184:187]\n"
"buffer_load_dword v26, v22, s[52:55], s31 offen offset:1024\n"
"v_mfma_f32_16x16x32_bf16 v[184:187], a[24:27], v[68:71], v[184:187]\n"
"v_mfma_f32_16x16x32_bf16 v[188:191], a[12:15], v[64:67], v[188:191]\n"
"ds_read_b128 v[64:67], v44 offset:1024\n"
"v_mfma_f32_16x16x32_bf16 v[188:191], a[28:31], v[68:71], v[188:191]\n"
"s_waitcnt lgkmcnt(4)\n"
"s_add_u32 s96, s31, 0x200000\n"
"ds_read_b128 v[68:71], v44 offset:1536\n"
"v_mfma_f32_16x16x32_bf16 v[192:195], a[0:3], v[72:75], v[192:195]\n"
"buffer_load_dword v26, v22, s[52:55], s96 offen offset:0\n"
"v_mfma_f32_16x16x32_bf16 v[192:195], a[16:19], v[76:79], v[192:195]\n"
"v_mfma_f32_16x16x32_bf16 v[196:199], a[4:7], v[72:75], v[196:199]\n"
"v_mfma_f32_16x16x32_bf16 v[196:199], a[20:23], v[76:79], v[196:199]\n"
"v_mfma_f32_16x16x32_bf16 v[200:203], a[8:11], v[72:75], v[200:203]\n"
"buffer_load_dword v26, v22, s[52:55], s96 offen offset:1024\n"
"v_mfma_f32_16x16x32_bf16 v[200:203], a[24:27], v[76:79], v[200:203]\n"
"v_mfma_f32_16x16x32_bf16 v[204:207], a[12:15], v[72:75], v[204:207]\n"
"v_mfma_f32_16x16x32_bf16 v[204:207], a[28:31], v[76:79], v[204:207]\n"
"s_add_u32 s75, s31, 0x40000\n"
"v_mfma_f32_16x16x32_bf16 v[208:211], a[0:3], v[48:51], v[208:211]\n"
"buffer_load_dword v26, v22, s[52:55], s75 offen offset:0\n"
"v_mfma_f32_16x16x32_bf16 v[208:211], a[16:19], v[52:55], v[208:211]\n"
"v_mfma_f32_16x16x32_bf16 v[212:215], a[4:7], v[48:51], v[212:215]\n"
"v_mfma_f32_16x16x32_bf16 v[212:215], a[20:23], v[52:55], v[212:215]\n"
"v_mfma_f32_16x16x32_bf16 v[216:219], a[8:11], v[48:51], v[216:219]\n"
"buffer_load_dword v26, v22, s[52:55], s75 offen offset:1024\n"
"v_mfma_f32_16x16x32_bf16 v[216:219], a[24:27], v[52:55], v[216:219]\n"
"v_mfma_f32_16x16x32_bf16 v[220:223], a[12:15], v[48:51], v[220:223]\n"
"v_mfma_f32_16x16x32_bf16 v[220:223], a[28:31], v[52:55], v[220:223]\n"
"s_waitcnt lgkmcnt(0)\n"
"s_add_u32 s76, s31, 0x240000\n"
"v_mfma_f32_16x16x32_bf16 v[224:227], a[0:3], v[56:59], v[224:227]\n"
"buffer_load_dword v26, v22, s[52:55], s76 offen offset:0\n"
"v_mfma_f32_16x16x32_bf16 v[224:227], a[16:19], v[60:63], v[224:227]\n"
"s_waitcnt vmcnt(18)\n"
"s_barrier\n"
"v_mfma_f32_16x16x32_bf16 v[228:231], a[4:7], v[56:59], v[228:231]\n"
"ds_read_b128 v[72:75], v27 offset:12288\n"
"ds_read_b128 v[76:79], v27 offset:12800\n"
"v_mfma_f32_16x16x32_bf16 v[228:231], a[20:23], v[60:63], v[228:231]\n"
"ds_read_b128 v[48:51], v27 offset:14336\n"
"ds_read_b128 v[52:55], v27 offset:14848\n"
"v_mfma_f32_16x16x32_bf16 v[232:235], a[8:11], v[56:59], v[232:235]\n"
"buffer_load_dword v26, v22, s[52:55], s76 offen offset:1024\n"
"v_mfma_f32_16x16x32_bf16 v[232:235], a[24:27], v[60:63], v[232:235]\n"
"v_mfma_f32_16x16x32_bf16 v[236:239], a[12:15], v[56:59], v[236:239]\n"
"ds_read_b128 v[56:59], v41 offset:12288\n"
"v_mfma_f32_16x16x32_bf16 v[236:239], a[28:31], v[60:63], v[236:239]\n"
"ds_read_b128 v[60:63], v41 offset:12800\n"
"v_mfma_f32_16x16x32_bf16 v[240:243], a[0:3], v[64:67], v[240:243]\n"
"s_waitcnt vmcnt(15)\n"
"ds_read_b64_tr_b16 a[0:1], v45 offset:16384\n"
"v_mfma_f32_16x16x32_bf16 v[240:243], a[16:19], v[68:71], v[240:243]\n"
"ds_read_b64_tr_b16 a[2:3], v45 offset:16640\n"
"v_mfma_f32_16x16x32_bf16 v[244:247], a[4:7], v[64:67], v[244:247]\n"
"ds_read_b64_tr_b16 a[4:5], v46 offset:16384\n"
"v_mfma_f32_16x16x32_bf16 v[244:247], a[20:23], v[68:71], v[244:247]\n"
"ds_read_b64_tr_b16 a[6:7], v46 offset:16640\n"
"v_mfma_f32_16x16x32_bf16 v[248:251], a[8:11], v[64:67], v[248:251]\n"
"s_waitcnt vmcnt(11)\n"
"ds_read_b64_tr_b16 a[16:17], v45 offset:20480\n"
"v_mfma_f32_16x16x32_bf16 v[248:251], a[24:27], v[68:71], v[248:251]\n"
"ds_read_b64_tr_b16 a[18:19], v45 offset:20736\n"
"v_mfma_f32_16x16x32_bf16 v[252:255], a[12:15], v[64:67], v[252:255]\n"
"ds_read_b128 v[64:67], v41 offset:14336\n"
"v_mfma_f32_16x16x32_bf16 v[252:255], a[28:31], v[68:71], v[252:255]\n"
"s_waitcnt lgkmcnt(9)\n"
"ds_read_b128 v[68:71], v41 offset:14848\n"
"v_mfma_f32_16x16x32_bf16 v[112:115], a[32:35], v[72:75], v[112:115]\n"
"ds_read_b64_tr_b16 a[20:21], v46 offset:20480\n"
"v_mfma_f32_16x16x32_bf16 v[112:115], a[48:51], v[76:79], v[112:115]\n"
"ds_read_b64_tr_b16 a[22:23], v46 offset:20736\n"
"v_mfma_f32_16x16x32_bf16 v[116:119], a[36:39], v[72:75], v[116:119]\n"
"ds_read_b64_tr_b16 a[8:9], v45 offset:16896\n"
"v_mfma_f32_16x16x32_bf16 v[116:119], a[52:55], v[76:79], v[116:119]\n"
"ds_read_b64_tr_b16 a[10:11], v45 offset:17152\n"
"v_mfma_f32_16x16x32_bf16 v[120:123], a[40:43], v[72:75], v[120:123]\n"
"ds_read_b64_tr_b16 a[24:25], v45 offset:20992\n"
"v_mfma_f32_16x16x32_bf16 v[120:123], a[56:59], v[76:79], v[120:123]\n"
"ds_read_b64_tr_b16 a[26:27], v45 offset:21248\n"
"v_mfma_f32_16x16x32_bf16 v[124:127], a[44:47], v[72:75], v[124:127]\n"
"ds_read_b128 v[72:75], v42 offset:12288\n"
"v_mfma_f32_16x16x32_bf16 v[124:127], a[60:63], v[76:79], v[124:127]\n"
"ds_read_b128 v[76:79], v42 offset:12800\n"
"v_mfma_f32_16x16x32_bf16 v[128:131], a[32:35], v[48:51], v[128:131]\n"
"s_add_u32 s90, s72, 13\n"
"ds_read_b64_tr_b16 a[12:13], v46 offset:16896\n"
"v_mfma_f32_16x16x32_bf16 v[128:131], a[48:51], v[52:55], v[128:131]\n"
"s_and_b32 s90, s90, 15\n"
"ds_read_b64_tr_b16 a[14:15], v46 offset:17152\n"
"v_mfma_f32_16x16x32_bf16 v[132:135], a[36:39], v[48:51], v[132:135]\n"
"s_lshl_b32 s90, s90, 19\n"
"ds_read_b64_tr_b16 a[28:29], v46 offset:20992\n"
"v_mfma_f32_16x16x32_bf16 v[132:135], a[52:55], v[52:55], v[132:135]\n"
"ds_read_b64_tr_b16 a[30:31], v46 offset:21248\n"
"v_mfma_f32_16x16x32_bf16 v[136:139], a[40:43], v[48:51], v[136:139]\n"
"buffer_load_dwordx4 v35, s[36:39], s73 offen offset:3072 lds\n"
"v_mfma_f32_16x16x32_bf16 v[136:139], a[56:59], v[52:55], v[136:139]\n"
"s_add_u32 m0, s30, 0x9000\n"
"s_nop 0\n"
"buffer_load_dwordx4 v36, s[36:39], s73 offen offset:0 lds\n"
"v_mfma_f32_16x16x32_bf16 v[140:143], a[44:47], v[48:51], v[140:143]\n"
"ds_read_b128 v[48:51], v42 offset:14336\n"
"v_mfma_f32_16x16x32_bf16 v[140:143], a[60:63], v[52:55], v[140:143]\n"
"s_waitcnt lgkmcnt(8)\n"
"ds_read_b128 v[52:55], v42 offset:14848\n"
"v_mfma_f32_16x16x32_bf16 v[144:147], a[32:35], v[56:59], v[144:147]\n"
"v_mfma_f32_16x16x32_bf16 v[144:147], a[48:51], v[60:63], v[144:147]\n"
"v_mfma_f32_16x16x32_bf16 v[148:151], a[36:39], v[56:59], v[148:151]\n"
"v_mfma_f32_16x16x32_bf16 v[148:151], a[52:55], v[60:63], v[148:151]\n"
"s_add_u32 m0, s30, 0x6000\n"
"v_mfma_f32_16x16x32_bf16 v[152:155], a[40:43], v[56:59], v[152:155]\n"
"buffer_load_dwordx4 v37, s[40:43], s90 offen offset:0 lds\n"
"v_mfma_f32_16x16x32_bf16 v[152:155], a[56:59], v[60:63], v[152:155]\n"
"v_mfma_f32_16x16x32_bf16 v[156:159], a[44:47], v[56:59], v[156:159]\n"
"ds_read_b128 v[56:59], v43 offset:12288\n"
"v_mfma_f32_16x16x32_bf16 v[156:159], a[60:63], v[60:63], v[156:159]\n"
"ds_read_b128 v[60:63], v43 offset:12800\n"
"v_mfma_f32_16x16x32_bf16 v[160:163], a[32:35], v[64:67], v[160:163]\n"
"buffer_load_dwordx4 v38, s[40:43], s90 offen offset:1024 lds\n"
"v_mfma_f32_16x16x32_bf16 v[160:163], a[48:51], v[68:71], v[160:163]\n"
"v_mfma_f32_16x16x32_bf16 v[164:167], a[36:39], v[64:67], v[164:167]\n"
"v_mfma_f32_16x16x32_bf16 v[164:167], a[52:55], v[68:71], v[164:167]\n"
"v_mfma_f32_16x16x32_bf16 v[168:171], a[40:43], v[64:67], v[168:171]\n"
"buffer_load_dwordx4 v39, s[40:43], s90 offen offset:2048 lds\n"
"v_mfma_f32_16x16x32_bf16 v[168:171], a[56:59], v[68:71], v[168:171]\n"
"v_mfma_f32_16x16x32_bf16 v[172:175], a[44:47], v[64:67], v[172:175]\n"
"ds_read_b128 v[64:67], v43 offset:14336\n"
"v_mfma_f32_16x16x32_bf16 v[172:175], a[60:63], v[68:71], v[172:175]\n"
"s_waitcnt lgkmcnt(3)\n"
"ds_read_b128 v[68:71], v43 offset:14848\n"
"v_mfma_f32_16x16x32_bf16 v[176:179], a[32:35], v[72:75], v[176:179]\n"
"buffer_load_dwordx4 v40, s[40:43], s90 offen offset:3072 lds\n"
"v_mfma_f32_16x16x32_bf16 v[176:179], a[48:51], v[76:79], v[176:179]\n"
"v_mfma_f32_16x16x32_bf16 v[180:183], a[36:39], v[72:75], v[180:183]\n"
"s_add_u32 s77, s90, 0x40000\n"
"s_add_u32 m0, s30, 0x7000\n"
"v_mfma_f32_16x16x32_bf16 v[180:183], a[52:55], v[76:79], v[180:183]\n"
"buffer_load_dwordx4 v37, s[40:43], s77 offen offset:0 lds\n"
"v_mfma_f32_16x16x32_bf16 v[184:187], a[40:43], v[72:75], v[184:187]\n"
"v_mfma_f32_16x16x32_bf16 v[184:187], a[56:59], v[76:79], v[184:187]\n"
"v_mfma_f32_16x16x32_bf16 v[188:191], a[44:47], v[72:75], v[188:191]\n"
"ds_read_b128 v[72:75], v30 offset:1024\n"
"v_mfma_f32_16x16x32_bf16 v[188:191], a[60:63], v[76:79], v[188:191]\n"
"s_add_u32 s88, s72, 13\n"
"buffer_load_dwordx4 v38, s[40:43], s77 offen offset:1024 lds\n"
"ds_read_b128 v[76:79], v30 offset:1536\n"
"v_mfma_f32_16x16x32_bf16 v[192:195], a[32:35], v[48:51], v[192:195]\n"
"s_and_b32 s88, s88, 15\n"
"v_mfma_f32_16x16x32_bf16 v[192:195], a[48:51], v[52:55], v[192:195]\n"
"s_lshl_b32 s88, s88, 7\n"
"v_mfma_f32_16x16x32_bf16 v[196:199], a[36:39], v[48:51], v[196:199]\n"
"v_mfma_f32_16x16x32_bf16 v[196:199], a[52:55], v[52:55], v[196:199]\n"
"buffer_load_dwordx4 v39, s[40:43], s77 offen offset:2048 lds\n"
"v_mfma_f32_16x16x32_bf16 v[200:203], a[40:43], v[48:51], v[200:203]\n"
"v_mfma_f32_16x16x32_bf16 v[200:203], a[56:59], v[52:55], v[200:203]\n"
"v_mfma_f32_16x16x32_bf16 v[204:207], a[44:47], v[48:51], v[204:207]\n"
"v_mfma_f32_16x16x32_bf16 v[204:207], a[60:63], v[52:55], v[204:207]\n"
"s_waitcnt lgkmcnt(2)\n"
"buffer_load_dwordx4 v40, s[40:43], s77 offen offset:3072 lds\n"
"v_mfma_f32_16x16x32_bf16 v[208:211], a[32:35], v[56:59], v[208:211]\n"
"v_mfma_f32_16x16x32_bf16 v[208:211], a[48:51], v[60:63], v[208:211]\n"
"v_mfma_f32_16x16x32_bf16 v[212:215], a[36:39], v[56:59], v[212:215]\n"
"s_add_u32 m0, s30, 0x8000\n"
"v_mfma_f32_16x16x32_bf16 v[212:215], a[52:55], v[60:63], v[212:215]\n"
"buffer_load_dwordx4 v32, s[36:39], s88 offen offset:0 lds\n"
"v_mfma_f32_16x16x32_bf16 v[216:219], a[40:43], v[56:59], v[216:219]\n"
"v_mfma_f32_16x16x32_bf16 v[216:219], a[56:59], v[60:63], v[216:219]\n"
"v_mfma_f32_16x16x32_bf16 v[220:223], a[44:47], v[56:59], v[220:223]\n"
"v_mfma_f32_16x16x32_bf16 v[220:223], a[60:63], v[60:63], v[220:223]\n"
"s_add_u32 s91, s72, 14\n"
"buffer_load_dwordx4 v33, s[36:39], s88 offen offset:1024 lds\n"
"v_mfma_f32_16x16x32_bf16 v[224:227], a[32:35], v[64:67], v[224:227]\n"
"s_and_b32 s91, s91, 15\n"
"v_mfma_f32_16x16x32_bf16 v[224:227], a[48:51], v[68:71], v[224:227]\n"
"s_lshl_b32 s91, s91, 19\n"
"v_mfma_f32_16x16x32_bf16 v[228:231], a[36:39], v[64:67], v[228:231]\n"
"v_mfma_f32_16x16x32_bf16 v[228:231], a[52:55], v[68:71], v[228:231]\n"
"buffer_load_dwordx4 v34, s[36:39], s88 offen offset:2048 lds\n"
"v_mfma_f32_16x16x32_bf16 v[232:235], a[40:43], v[64:67], v[232:235]\n"
"v_mfma_f32_16x16x32_bf16 v[232:235], a[56:59], v[68:71], v[232:235]\n"
"v_mfma_f32_16x16x32_bf16 v[236:239], a[44:47], v[64:67], v[236:239]\n"
"v_mfma_f32_16x16x32_bf16 v[236:239], a[60:63], v[68:71], v[236:239]\n"
"s_waitcnt vmcnt(11) lgkmcnt(0)\n"
"s_barrier\n"
"buffer_load_dwordx4 v35, s[36:39], s88 offen offset:3072 lds\n"
"v_mfma_f32_16x16x32_bf16 v[240:243], a[32:35], v[72:75], v[240:243]\n"
"s_mul_i32 s88, s70, 0x2400\n"
"ds_read_b128 v[48:51], v27 offset:8192\n"
"ds_read_b128 v[52:55], v27 offset:8704\n"
"v_mfma_f32_16x16x32_bf16 v[240:243], a[48:51], v[76:79], v[240:243]\n"
"s_getpc_b64 s[98:99]\n"
"7:\n"
"s_add_u32 s98, s98, .Lmha_VARJ-7b\n"
"s_addc_u32 s99, s99, (.Lmha_VARJ-7b)>>32\n"
"ds_read_b128 v[56:59], v27 offset:10240\n"
"ds_read_b128 v[60:63], v27 offset:10752\n"
"v_mfma_f32_16x16x32_bf16 v[244:247], a[36:39], v[72:75], v[244:247]\n"
"s_add_u32 m0, s30, 0x4000\n"
"ds_read_b128 v[64:67], v41 offset:8192\n"
"v_mfma_f32_16x16x32_bf16 v[244:247], a[52:55], v[76:79], v[244:247]\n"
"buffer_load_dwordx4 v37, s[40:43], s91 offen offset:0 lds\n"
"ds_read_b128 v[68:71], v41 offset:8704\n"
"v_mfma_f32_16x16x32_bf16 v[248:251], a[40:43], v[72:75], v[248:251]\n"
"global_load_dword v26, v0, s[98:99] offset:0\n"
"v_mfma_f32_16x16x32_bf16 v[248:251], a[56:59], v[76:79], v[248:251]\n"
"v_mfma_f32_16x16x32_bf16 v[252:255], a[44:47], v[72:75], v[252:255]\n"
"ds_read_b128 v[72:75], v41 offset:10240\n"
"v_mfma_f32_16x16x32_bf16 v[252:255], a[60:63], v[76:79], v[252:255]\n"
"s_waitcnt lgkmcnt(6)\n"
"buffer_load_dwordx4 v38, s[40:43], s91 offen offset:1024 lds\n"
"ds_read_b128 v[76:79], v41 offset:10752\n"
"v_mfma_f32_16x16x32_bf16 v[112:115], a[0:3], v[48:51], v[112:115]\n"
"s_waitcnt lgkmcnt(6)\n"
"v_mfma_f32_16x16x32_bf16 v[112:115], a[16:19], v[52:55], v[112:115]\n"
"v_mfma_f32_16x16x32_bf16 v[116:119], a[4:7], v[48:51], v[116:119]\n"
"v_mfma_f32_16x16x32_bf16 v[116:119], a[20:23], v[52:55], v[116:119]\n"
"buffer_load_dwordx4 v39, s[40:43], s91 offen offset:2048 lds\n"
"v_mfma_f32_16x16x32_bf16 v[120:123], a[8:11], v[48:51], v[120:123]\n"
"v_mfma_f32_16x16x32_bf16 v[120:123], a[24:27], v[52:55], v[120:123]\n"
"v_mfma_f32_16x16x32_bf16 v[124:127], a[12:15], v[48:51], v[124:127]\n"
"ds_read_b128 v[48:51], v42 offset:8192\n"
"v_mfma_f32_16x16x32_bf16 v[124:127], a[28:31], v[52:55], v[124:127]\n"
"s_waitcnt lgkmcnt(3)\n"
"buffer_load_dwordx4 v40, s[40:43], s91 offen offset:3072 lds\n"
"ds_read_b128 v[52:55], v42 offset:8704\n"
"v_mfma_f32_16x16x32_bf16 v[128:131], a[0:3], v[56:59], v[128:131]\n"
"v_mfma_f32_16x16x32_bf16 v[128:131], a[16:19], v[60:63], v[128:131]\n"
"s_waitcnt vmcnt(13)\n"
"ds_read_b64_tr_b16 a[32:33], v45 offset:24576\n"
"v_mfma_f32_16x16x32_bf16 v[132:135], a[4:7], v[56:59], v[132:135]\n"
"s_add_u32 s78, s91, 0x40000\n"
"s_add_u32 m0, s30, 0x5000\n"
"ds_read_b64_tr_b16 a[34:35], v45 offset:24832\n"
"v_mfma_f32_16x16x32_bf16 v[132:135], a[20:23], v[60:63], v[132:135]\n"
"buffer_load_dwordx4 v37, s[40:43], s78 offen offset:0 lds\n"
"ds_read_b64_tr_b16 a[36:37], v46 offset:24576\n"
"v_mfma_f32_16x16x32_bf16 v[136:139], a[8:11], v[56:59], v[136:139]\n"
"ds_read_b64_tr_b16 a[38:39], v46 offset:24832\n"
"v_mfma_f32_16x16x32_bf16 v[136:139], a[24:27], v[60:63], v[136:139]\n"
"ds_read_b64_tr_b16 a[40:41], v45 offset:25088\n"
"v_mfma_f32_16x16x32_bf16 v[140:143], a[12:15], v[56:59], v[140:143]\n"
"ds_read_b128 v[56:59], v42 offset:10240\n"
"v_mfma_f32_16x16x32_bf16 v[140:143], a[28:31], v[60:63], v[140:143]\n"
"s_add_u32 s74, s72, 15\n"
"buffer_load_dwordx4 v38, s[40:43], s78 offen offset:1024 lds\n"
"ds_read_b128 v[60:63], v42 offset:10752\n"
"v_mfma_f32_16x16x32_bf16 v[144:147], a[0:3], v[64:67], v[144:147]\n"
"s_and_b32 s74, s74, 15\n"
"ds_read_b64_tr_b16 a[42:43], v45 offset:25344\n"
"v_mfma_f32_16x16x32_bf16 v[144:147], a[16:19], v[68:71], v[144:147]\n"
"s_lshl_b32 s74, s74, 19\n"
"ds_read_b64_tr_b16 a[44:45], v46 offset:25088\n"
"v_mfma_f32_16x16x32_bf16 v[148:151], a[4:7], v[64:67], v[148:151]\n"
"ds_read_b64_tr_b16 a[46:47], v46 offset:25344\n"
"v_mfma_f32_16x16x32_bf16 v[148:151], a[20:23], v[68:71], v[148:151]\n"
"buffer_load_dwordx4 v39, s[40:43], s78 offen offset:2048 lds\n"
"v_mfma_f32_16x16x32_bf16 v[152:155], a[8:11], v[64:67], v[152:155]\n"
"v_mfma_f32_16x16x32_bf16 v[152:155], a[24:27], v[68:71], v[152:155]\n"
"v_mfma_f32_16x16x32_bf16 v[156:159], a[12:15], v[64:67], v[156:159]\n"
"ds_read_b128 v[64:67], v43 offset:8192\n"
"v_mfma_f32_16x16x32_bf16 v[156:159], a[28:31], v[68:71], v[156:159]\n"
"s_waitcnt lgkmcnt(7)\n"
"buffer_load_dwordx4 v40, s[40:43], s78 offen offset:3072 lds\n"
"ds_read_b128 v[68:71], v43 offset:8704\n"
"v_mfma_f32_16x16x32_bf16 v[160:163], a[0:3], v[72:75], v[160:163]\n"
"s_waitcnt vmcnt(13)\n"
"ds_read_b64_tr_b16 a[48:49], v45 offset:28672\n"
"v_mfma_f32_16x16x32_bf16 v[160:163], a[16:19], v[76:79], v[160:163]\n"
"ds_read_b64_tr_b16 a[50:51], v45 offset:28928\n"
"v_mfma_f32_16x16x32_bf16 v[164:167], a[4:7], v[72:75], v[164:167]\n"
"s_add_u32 m0, s30, 0x6000\n"
"ds_read_b64_tr_b16 a[52:53], v46 offset:28672\n"
"v_mfma_f32_16x16x32_bf16 v[164:167], a[20:23], v[76:79], v[164:167]\n"
"s_waitcnt lgkmcnt(5)\n"
"buffer_load_dwordx4 v37, s[40:43], s74 offen offset:0 lds\n"
"ds_read_b64_tr_b16 a[54:55], v46 offset:28928\n"
"v_mfma_f32_16x16x32_bf16 v[168:171], a[8:11], v[72:75], v[168:171]\n"
"ds_read_b64_tr_b16 a[56:57], v45 offset:29184\n"
"v_mfma_f32_16x16x32_bf16 v[168:171], a[24:27], v[76:79], v[168:171]\n"
"ds_read_b64_tr_b16 a[58:59], v45 offset:29440\n"
"v_mfma_f32_16x16x32_bf16 v[172:175], a[12:15], v[72:75], v[172:175]\n"
"s_add_u32 s89, s72, 14\n"
"ds_read_b128 v[72:75], v43 offset:10240\n"
"v_mfma_f32_16x16x32_bf16 v[172:175], a[28:31], v[76:79], v[172:175]\n"
"s_and_b32 s89, s89, 15\n"
"buffer_load_dwordx4 v38, s[40:43], s74 offen offset:1024 lds\n"
"ds_read_b128 v[76:79], v43 offset:10752\n"
"v_mfma_f32_16x16x32_bf16 v[176:179], a[0:3], v[48:51], v[176:179]\n"
"s_lshl_b32 s89, s89, 7\n"
"ds_read_b64_tr_b16 a[60:61], v46 offset:29184\n"
"v_mfma_f32_16x16x32_bf16 v[176:179], a[16:19], v[52:55], v[176:179]\n"
"ds_read_b64_tr_b16 a[62:63], v46 offset:29440\n"
"v_mfma_f32_16x16x32_bf16 v[180:183], a[4:7], v[48:51], v[180:183]\n"
"v_mfma_f32_16x16x32_bf16 v[180:183], a[20:23], v[52:55], v[180:183]\n"
"buffer_load_dwordx4 v39, s[40:43], s74 offen offset:2048 lds\n"
"v_mfma_f32_16x16x32_bf16 v[184:187], a[8:11], v[48:51], v[184:187]\n"
"v_mfma_f32_16x16x32_bf16 v[184:187], a[24:27], v[52:55], v[184:187]\n"
"v_mfma_f32_16x16x32_bf16 v[188:191], a[12:15], v[48:51], v[188:191]\n"
"ds_read_b128 v[48:51], v44 offset:0\n"
"v_mfma_f32_16x16x32_bf16 v[188:191], a[28:31], v[52:55], v[188:191]\n"
"buffer_load_dwordx4 v40, s[40:43], s74 offen offset:3072 lds\n"
"ds_read_b128 v[52:55], v44 offset:512\n"
"v_mfma_f32_16x16x32_bf16 v[192:195], a[0:3], v[56:59], v[192:195]\n"
"v_mfma_f32_16x16x32_bf16 v[192:195], a[16:19], v[60:63], v[192:195]\n"
"s_add_u32 m0, s30, 0x3000\n"
"v_mfma_f32_16x16x32_bf16 v[196:199], a[4:7], v[56:59], v[196:199]\n"
"buffer_load_dwordx4 v32, s[36:39], s89 offen offset:0 lds\n"
"v_mfma_f32_16x16x32_bf16 v[196:199], a[20:23], v[60:63], v[196:199]\n"
"v_mfma_f32_16x16x32_bf16 v[200:203], a[8:11], v[56:59], v[200:203]\n"
"v_mfma_f32_16x16x32_bf16 v[200:203], a[24:27], v[60:63], v[200:203]\n"
"v_mfma_f32_16x16x32_bf16 v[204:207], a[12:15], v[56:59], v[204:207]\n"
"buffer_load_dwordx4 v33, s[36:39], s89 offen offset:1024 lds\n"
"v_mfma_f32_16x16x32_bf16 v[204:207], a[28:31], v[60:63], v[204:207]\n"
"s_waitcnt lgkmcnt(2)\n"
"v_mfma_f32_16x16x32_bf16 v[208:211], a[0:3], v[64:67], v[208:211]\n"
"v_mfma_f32_16x16x32_bf16 v[208:211], a[16:19], v[68:71], v[208:211]\n"
"v_mfma_f32_16x16x32_bf16 v[212:215], a[4:7], v[64:67], v[212:215]\n"
"buffer_load_dwordx4 v34, s[36:39], s89 offen offset:2048 lds\n"
"v_mfma_f32_16x16x32_bf16 v[212:215], a[20:23], v[68:71], v[212:215]\n"
"v_mfma_f32_16x16x32_bf16 v[216:219], a[8:11], v[64:67], v[216:219]\n"
"v_mfma_f32_16x16x32_bf16 v[216:219], a[24:27], v[68:71], v[216:219]\n"
"v_mfma_f32_16x16x32_bf16 v[220:223], a[12:15], v[64:67], v[220:223]\n"
"buffer_load_dwordx4 v35, s[36:39], s89 offen offset:3072 lds\n"
"v_mfma_f32_16x16x32_bf16 v[220:223], a[28:31], v[68:71], v[220:223]\n"
"v_mfma_f32_16x16x32_bf16 v[224:227], a[0:3], v[72:75], v[224:227]\n"
"v_mfma_f32_16x16x32_bf16 v[224:227], a[16:19], v[76:79], v[224:227]\n"
"s_waitcnt vmcnt(17) lgkmcnt(0)\n"
"s_barrier\n"
"s_add_u32 m0, s30, 0x9400\n"
"v_mfma_f32_16x16x32_bf16 v[228:231], a[4:7], v[72:75], v[228:231]\n"
"buffer_load_dwordx4 v36, s[36:39], s89 offen offset:0 lds\n"
"ds_read_b128 v[56:59], v27 offset:32768\n"
"v_mfma_f32_16x16x32_bf16 v[228:231], a[20:23], v[76:79], v[228:231]\n"
"s_lshl_b32 s89, s70, 16\n"
"ds_read_b128 v[60:63], v27 offset:33280\n"
"ds_read_b128 v[64:67], v27 offset:34816\n"
"v_mfma_f32_16x16x32_bf16 v[232:235], a[8:11], v[72:75], v[232:235]\n"
"s_and_b32 s70, s27, 7\n"
"ds_read_b128 v[68:71], v27 offset:35328\n"
"v_mfma_f32_16x16x32_bf16 v[232:235], a[24:27], v[76:79], v[232:235]\n"
"s_sub_u32 s70, s70, 7\n"
"s_add_u32 s84, s74, 0x40000\n"
"s_add_u32 m0, s30, 0x7000\n"
"v_mfma_f32_16x16x32_bf16 v[236:239], a[12:15], v[72:75], v[236:239]\n"
"s_lshr_b32 s70, s70, 31\n"
"buffer_load_dwordx4 v37, s[40:43], s84 offen offset:0 lds\n"
"ds_read_b128 v[72:75], v41 offset:32768\n"
"v_mfma_f32_16x16x32_bf16 v[236:239], a[28:31], v[76:79], v[236:239]\n"
"s_add_u32 s70, s70, s71\n"
"ds_read_b128 v[76:79], v41 offset:33280\n"
"v_mfma_f32_16x16x32_bf16 v[240:243], a[0:3], v[48:51], v[240:243]\n"
"s_waitcnt vmcnt(14)\n"
"s_add_u32 s70, s70, 6\n"
"ds_read_b64_tr_b16 a[0:1], v45 offset:16384\n"
"v_mfma_f32_16x16x32_bf16 v[240:243], a[16:19], v[52:55], v[240:243]\n"
"s_lshl_b32 s70, s70, 3\n"
"ds_read_b64_tr_b16 a[2:3], v45 offset:16640\n"
"v_mfma_f32_16x16x32_bf16 v[244:247], a[4:7], v[48:51], v[244:247]\n"
"s_add_u32 s73, s72, 15\n"
"buffer_load_dwordx4 v38, s[40:43], s84 offen offset:1024 lds\n"
"ds_read_b64_tr_b16 a[4:5], v46 offset:16384\n"
"v_mfma_f32_16x16x32_bf16 v[244:247], a[20:23], v[52:55], v[244:247]\n"
"s_and_b32 s73, s73, 15\n"
"s_add_u32 s70, s70, s11\n"
"ds_read_b64_tr_b16 a[6:7], v46 offset:16640\n"
"v_mfma_f32_16x16x32_bf16 v[248:251], a[8:11], v[48:51], v[248:251]\n"
"s_lshl_b32 s73, s73, 7\n"
"s_mul_i32 s90, s70, 0x2400\n"
"ds_read_b64_tr_b16 a[8:9], v45 offset:16896\n"
"v_mfma_f32_16x16x32_bf16 v[248:251], a[24:27], v[52:55], v[248:251]\n"
"s_lshl_b32 s91, s70, 16\n"
"ds_read_b64_tr_b16 a[10:11], v45 offset:17152\n"
"v_mfma_f32_16x16x32_bf16 v[252:255], a[12:15], v[48:51], v[252:255]\n"
"buffer_load_dwordx4 v39, s[40:43], s84 offen offset:2048 lds\n"
"ds_read_b128 v[48:51], v41 offset:34816\n"
"v_mfma_f32_16x16x32_bf16 v[252:255], a[28:31], v[52:55], v[252:255]\n"
"s_waitcnt lgkmcnt(9)\n"
"ds_read_b128 v[52:55], v41 offset:35328\n"
"v_mfma_f32_16x16x32_bf16 v[112:115], a[32:35], v[56:59], v[112:115]\n"
"ds_read_b64_tr_b16 a[12:13], v46 offset:16896\n"
"v_mfma_f32_16x16x32_bf16 v[112:115], a[48:51], v[60:63], v[112:115]\n"
"s_waitcnt vmcnt(12)\n"
"ds_read_b64_tr_b16 a[16:17], v45 offset:20480\n"
"v_mfma_f32_16x16x32_bf16 v[116:119], a[36:39], v[56:59], v[116:119]\n"
"buffer_load_dwordx4 v40, s[40:43], s84 offen offset:3072 lds\n"
"ds_read_b64_tr_b16 a[18:19], v45 offset:20736\n"
"v_mfma_f32_16x16x32_bf16 v[116:119], a[52:55], v[60:63], v[116:119]\n"
"s_mul_i32 s41, s68, 0x2400\n"
"ds_read_b64_tr_b16 a[20:21], v46 offset:20480\n"
"v_mfma_f32_16x16x32_bf16 v[120:123], a[40:43], v[56:59], v[120:123]\n"
"s_xor_b32 s68, s68, 32\n"
"ds_read_b64_tr_b16 a[22:23], v46 offset:20736\n"
"v_mfma_f32_16x16x32_bf16 v[120:123], a[56:59], v[60:63], v[120:123]\n"
"s_mul_i32 s40, s68, 0x2400\n"
"s_add_u32 m0, s30, 0x2000\n"
"ds_read_b64_tr_b16 a[24:25], v45 offset:20992\n"
"v_mfma_f32_16x16x32_bf16 v[124:127], a[44:47], v[56:59], v[124:127]\n"
"s_lshl_b32 s68, s29, 1\n"
"buffer_load_dwordx4 v32, s[36:39], s73 offen offset:0 lds\n"
"ds_read_b128 v[56:59], v42 offset:32768\n"
"v_mfma_f32_16x16x32_bf16 v[124:127], a[60:63], v[60:63], v[124:127]\n"
"s_add_u32 s68, s68, s28\n"
"ds_read_b128 v[60:63], v42 offset:33280\n"
"v_mfma_f32_16x16x32_bf16 v[128:131], a[32:35], v[64:67], v[128:131]\n"
"s_lshl_b32 s68, s68, 2\n"
"ds_read_b64_tr_b16 a[26:27], v45 offset:21248\n"
"v_mfma_f32_16x16x32_bf16 v[128:131], a[48:51], v[68:71], v[128:131]\n"
"s_add_u32 s68, s68, s11\n"
"ds_read_b64_tr_b16 a[14:15], v46 offset:17152\n"
"v_mfma_f32_16x16x32_bf16 v[132:135], a[36:39], v[64:67], v[132:135]\n"
"s_lshl_b32 s42, s68, 5\n"
"buffer_load_dwordx4 v33, s[36:39], s73 offen offset:1024 lds\n"
"ds_read_b64_tr_b16 a[28:29], v46 offset:20992\n"
"v_mfma_f32_16x16x32_bf16 v[132:135], a[52:55], v[68:71], v[132:135]\n"
"ds_read_b64_tr_b16 a[30:31], v46 offset:21248\n"
"v_mfma_f32_16x16x32_bf16 v[136:139], a[40:43], v[64:67], v[136:139]\n"
"v_mfma_f32_16x16x32_bf16 v[136:139], a[56:59], v[68:71], v[136:139]\n"
"buffer_load_dwordx4 v34, s[36:39], s73 offen offset:2048 lds\n"
"buffer_load_dwordx4 v35, s[36:39], s73 offen offset:3072 lds\n"
"v_mfma_f32_16x16x32_bf16 v[140:143], a[44:47], v[64:67], v[140:143]\n"
"s_lshl_b32 s39, s27, 7\n"
"ds_read_b128 v[64:67], v42 offset:34816\n"
"v_mfma_f32_16x16x32_bf16 v[140:143], a[60:63], v[68:71], v[140:143]\n"
"s_waitcnt lgkmcnt(8)\n"
"s_add_u32 s39, s39, s94\n"
"ds_read_b128 v[68:71], v42 offset:35328\n"
"v_mfma_f32_16x16x32_bf16 v[144:147], a[32:35], v[72:75], v[144:147]\n"
"v_mfma_f32_16x16x32_bf16 v[144:147], a[48:51], v[76:79], v[144:147]\n"
"s_waitcnt vmcnt(13)\n"
"ds_read_b64_tr_b16 v[80:81], v45 offset:24576\n"
"v_mfma_f32_16x16x32_bf16 v[148:151], a[36:39], v[72:75], v[148:151]\n"
"ds_read_b64_tr_b16 v[82:83], v45 offset:24832\n"
"v_mfma_f32_16x16x32_bf16 v[148:151], a[52:55], v[76:79], v[148:151]\n"
"ds_read_b64_tr_b16 v[84:85], v46 offset:24576\n"
"v_mfma_f32_16x16x32_bf16 v[152:155], a[40:43], v[72:75], v[152:155]\n"
"ds_read_b64_tr_b16 v[86:87], v46 offset:24832\n"
"v_mfma_f32_16x16x32_bf16 v[152:155], a[56:59], v[76:79], v[152:155]\n"
"ds_read_b64_tr_b16 v[88:89], v45 offset:25088\n"
"v_mfma_f32_16x16x32_bf16 v[156:159], a[44:47], v[72:75], v[156:159]\n"
"ds_read_b128 v[72:75], v43 offset:32768\n"
"v_mfma_f32_16x16x32_bf16 v[156:159], a[60:63], v[76:79], v[156:159]\n"
"ds_read_b128 v[76:79], v43 offset:33280\n"
"v_mfma_f32_16x16x32_bf16 v[160:163], a[32:35], v[48:51], v[160:163]\n"
"ds_read_b64_tr_b16 v[90:91], v45 offset:25344\n"
"v_mfma_f32_16x16x32_bf16 v[160:163], a[48:51], v[52:55], v[160:163]\n"
"ds_read_b64_tr_b16 v[92:93], v46 offset:25088\n"
"v_mfma_f32_16x16x32_bf16 v[164:167], a[36:39], v[48:51], v[164:167]\n"
"ds_read_b64_tr_b16 v[94:95], v46 offset:25344\n"
"v_mfma_f32_16x16x32_bf16 v[164:167], a[52:55], v[52:55], v[164:167]\n"
"v_mfma_f32_16x16x32_bf16 v[168:171], a[40:43], v[48:51], v[168:171]\n"
"v_mfma_f32_16x16x32_bf16 v[168:171], a[56:59], v[52:55], v[168:171]\n"
"v_mfma_f32_16x16x32_bf16 v[172:175], a[44:47], v[48:51], v[172:175]\n"
"ds_read_b128 v[48:51], v43 offset:34816\n"
"v_mfma_f32_16x16x32_bf16 v[172:175], a[60:63], v[52:55], v[172:175]\n"
"s_waitcnt lgkmcnt(7)\n"
"ds_read_b128 v[52:55], v43 offset:35328\n"
"v_mfma_f32_16x16x32_bf16 v[176:179], a[32:35], v[56:59], v[176:179]\n"
"v_mfma_f32_16x16x32_bf16 v[176:179], a[48:51], v[60:63], v[176:179]\n"
"s_mov_b32 s96, 0x8000\n"
"v_mfma_f32_16x16x32_bf16 v[180:183], a[36:39], v[56:59], v[180:183]\n"
"buffer_load_dwordx4 a[160:163], v19, s[48:51], s96 offen offset:0\n"
"v_mfma_f32_16x16x32_bf16 v[180:183], a[52:55], v[60:63], v[180:183]\n"
"v_mfma_f32_16x16x32_bf16 v[184:187], a[40:43], v[56:59], v[184:187]\n"
"v_mfma_f32_16x16x32_bf16 v[184:187], a[56:59], v[60:63], v[184:187]\n"
"v_mfma_f32_16x16x32_bf16 v[188:191], a[44:47], v[56:59], v[188:191]\n"
"ds_read_b128 v[56:59], v30 offset:0\n"
"v_mfma_f32_16x16x32_bf16 v[188:191], a[60:63], v[60:63], v[188:191]\n"
"ds_read_b128 v[60:63], v30 offset:512\n"
"v_mfma_f32_16x16x32_bf16 v[192:195], a[32:35], v[64:67], v[192:195]\n"
"v_mfma_f32_16x16x32_bf16 v[192:195], a[48:51], v[68:71], v[192:195]\n"
"v_mfma_f32_16x16x32_bf16 v[196:199], a[36:39], v[64:67], v[196:199]\n"
"v_mfma_f32_16x16x32_bf16 v[196:199], a[52:55], v[68:71], v[196:199]\n"
"v_mfma_f32_16x16x32_bf16 v[200:203], a[40:43], v[64:67], v[200:203]\n"
"v_mfma_f32_16x16x32_bf16 v[200:203], a[56:59], v[68:71], v[200:203]\n"
"v_mfma_f32_16x16x32_bf16 v[204:207], a[44:47], v[64:67], v[204:207]\n"
"v_mfma_f32_16x16x32_bf16 v[204:207], a[60:63], v[68:71], v[204:207]\n"
"s_waitcnt lgkmcnt(2)\n"
"buffer_load_dwordx4 a[164:167], v20, s[48:51], s96 offen offset:1024\n"
"v_mfma_f32_16x16x32_bf16 v[208:211], a[32:35], v[72:75], v[208:211]\n"
"v_mfma_f32_16x16x32_bf16 v[208:211], a[48:51], v[76:79], v[208:211]\n"
"v_mfma_f32_16x16x32_bf16 v[212:215], a[36:39], v[72:75], v[212:215]\n"
"v_mfma_f32_16x16x32_bf16 v[212:215], a[52:55], v[76:79], v[212:215]\n"
"v_mfma_f32_16x16x32_bf16 v[216:219], a[40:43], v[72:75], v[216:219]\n"
"s_waitcnt vmcnt(6)\n"
"ds_read_b64_tr_b16 v[96:97], v45 offset:28672\n"
"v_mfma_f32_16x16x32_bf16 v[216:219], a[56:59], v[76:79], v[216:219]\n"
"ds_read_b64_tr_b16 v[98:99], v45 offset:28928\n"
"v_mfma_f32_16x16x32_bf16 v[220:223], a[44:47], v[72:75], v[220:223]\n"
"ds_read_b64_tr_b16 v[100:101], v46 offset:28672\n"
"v_mfma_f32_16x16x32_bf16 v[220:223], a[60:63], v[76:79], v[220:223]\n"
"ds_read_b64_tr_b16 v[102:103], v46 offset:28928\n"
"v_mfma_f32_16x16x32_bf16 v[224:227], a[32:35], v[48:51], v[224:227]\n"
"ds_read_b64_tr_b16 v[104:105], v45 offset:29184\n"
"v_mfma_f32_16x16x32_bf16 v[224:227], a[48:51], v[52:55], v[224:227]\n"
"ds_read_b64_tr_b16 v[106:107], v45 offset:29440\n"
"v_mfma_f32_16x16x32_bf16 v[228:231], a[36:39], v[48:51], v[228:231]\n"
"ds_read_b64_tr_b16 v[108:109], v46 offset:29184\n"
"v_mfma_f32_16x16x32_bf16 v[228:231], a[52:55], v[52:55], v[228:231]\n"
"buffer_load_dwordx4 a[168:171], v19, s[48:51], s96 offen offset:2048\n"
"ds_read_b64_tr_b16 v[110:111], v46 offset:29440\n"
"v_mfma_f32_16x16x32_bf16 v[232:235], a[40:43], v[48:51], v[232:235]\n"
"v_mfma_f32_16x16x32_bf16 v[232:235], a[56:59], v[52:55], v[232:235]\n"
"v_mfma_f32_16x16x32_bf16 v[236:239], a[44:47], v[48:51], v[236:239]\n"
"v_mfma_f32_16x16x32_bf16 v[236:239], a[60:63], v[52:55], v[236:239]\n"
"s_waitcnt vmcnt(3) lgkmcnt(7)\n"
"s_barrier\n"
"v_mfma_f32_16x16x32_bf16 v[240:243], a[32:35], v[56:59], v[240:243]\n"
"ds_read_b128 v[64:67], v44 offset:1024\n"
"ds_read_b128 v[68:71], v44 offset:1536\n"
"v_mfma_f32_16x16x32_bf16 v[240:243], a[48:51], v[60:63], v[240:243]\n"
"ds_read_b128 v[72:75], v30 offset:1024\n"
"ds_read_b128 v[76:79], v30 offset:1536\n"
"v_mfma_f32_16x16x32_bf16 v[244:247], a[36:39], v[56:59], v[244:247]\n"
"ds_read_b128 v[48:51], v27 offset:12288\n"
"v_add_u32 v30, 0x8000, v22\n"
"v_mfma_f32_16x16x32_bf16 v[244:247], a[52:55], v[60:63], v[244:247]\n"
"ds_read_b128 v[52:55], v27 offset:12800\n"
"v_mfma_f32_16x16x32_bf16 v[248:251], a[40:43], v[56:59], v[248:251]\n"
"ds_read_b128 a[32:35], v21 offset:0\n"
"v_mfma_f32_16x16x32_bf16 v[248:251], a[56:59], v[60:63], v[248:251]\n"
"ds_read_b128 a[36:39], v21 offset:256\n"
"v_mfma_f32_16x16x32_bf16 v[252:255], a[44:47], v[56:59], v[252:255]\n"
"ds_read_b128 v[56:59], v27 offset:8192\n"
"v_mfma_f32_16x16x32_bf16 v[252:255], a[60:63], v[60:63], v[252:255]\n"
"s_mov_b32 s75, 0x8000\n"
"ds_read_b128 v[60:63], v27 offset:8704\n"
"s_waitcnt lgkmcnt(9)\n"
"v_mfma_f32_16x16x32_bf16 v[240:243], a[0:3], v[64:67], v[240:243]\n"
"s_waitcnt lgkmcnt(8)\n"
"buffer_load_dwordx4 a[172:175], v20, s[48:51], s75 offen offset:3072\n"
"ds_read_b128 a[40:43], v21 offset:512\n"
"v_mfma_f32_16x16x32_bf16 v[240:243], a[16:19], v[68:71], v[240:243]\n"
"s_waitcnt lgkmcnt(8)\n"
"ds_read_b128 a[44:47], v21 offset:768\n"
"v_mfma_f32_16x16x32_bf16 v[240:243], v[80:83], v[72:75], v[240:243]\n"
"s_waitcnt lgkmcnt(8)\n"
"ds_read_b128 a[48:51], v1 offset:0\n"
"v_mfma_f32_16x16x32_bf16 v[240:243], v[96:99], v[76:79], v[240:243]\n"
"ds_read_b128 a[52:55], v1 offset:256\n"
"v_mfma_f32_16x16x32_bf16 v[244:247], a[4:7], v[64:67], v[244:247]\n"
"ds_read_b128 a[56:59], v1 offset:512\n"
"v_mfma_f32_16x16x32_bf16 v[244:247], a[20:23], v[68:71], v[244:247]\n"
"ds_read_b128 a[60:63], v1 offset:768\n"
"v_mfma_f32_16x16x32_bf16 v[244:247], v[84:87], v[72:75], v[244:247]\n"
"s_nop 0\n"
"s_nop 0\n"
"s_nop 0\n"
"v_mfma_f32_16x16x32_bf16 v[244:247], v[100:103], v[76:79], v[244:247]\n"
"v_mul_f32 v240, s85, v240\n"
"v_mul_f32 v241, s85, v241\n"
"v_mfma_f32_16x16x32_bf16 v[248:251], a[8:11], v[64:67], v[248:251]\n"
"v_mul_f32 v242, s85, v242\n"
"v_mul_f32 v243, s85, v243\n"
"v_mfma_f32_16x16x32_bf16 v[248:251], a[24:27], v[68:71], v[248:251]\n"
"v_cvt_pk_bf16_f32 v240, v240, v241\n"
"v_cvt_pk_bf16_f32 v241, v242, v243\n"
"v_mfma_f32_16x16x32_bf16 v[248:251], v[88:91], v[72:75], v[248:251]\n"
"ds_write_b64 v47, v[240:241] offset:32768\n"
"v_mfma_f32_16x16x32_bf16 v[248:251], v[104:107], v[76:79], v[248:251]\n"
"s_mov_b32 s76, 0x10000\n"
"v_mul_f32 v244, s85, v244\n"
"v_mul_f32 v245, s85, v245\n"
"v_mfma_f32_16x16x32_bf16 v[252:255], a[12:15], v[64:67], v[252:255]\n"
"buffer_load_dwordx4 a[176:179], v17, s[44:47], s76 offen offset:0\n"
"v_mul_f32 v246, s85, v246\n"
"v_mfma_f32_16x16x32_bf16 v[252:255], a[28:31], v[68:71], v[252:255]\n"
"ds_read_b128 v[64:67], v27 offset:14336\n"
"v_mul_f32 v247, s85, v247\n"
"v_mfma_f32_16x16x32_bf16 v[252:255], v[92:95], v[72:75], v[252:255]\n"
"ds_read_b128 v[68:71], v27 offset:14848\n"
"v_cvt_pk_bf16_f32 v244, v244, v245\n"
"v_mfma_f32_16x16x32_bf16 v[252:255], v[108:111], v[76:79], v[252:255]\n"
"s_waitcnt lgkmcnt(4)\n"
"ds_read_b128 v[72:75], v27 offset:10240\n"
"v_cvt_pk_bf16_f32 v245, v246, v247\n"
"v_mfma_f32_16x16x32_bf16 v[112:115], a[0:3], v[48:51], v[112:115]\n"
"ds_write_b64 v47, v[244:245] offset:33280\n"
"v_mfma_f32_16x16x32_bf16 v[112:115], a[16:19], v[52:55], v[112:115]\n"
"v_mul_f32 v248, s85, v248\n"
"v_mul_f32 v249, s85, v249\n"
"v_mul_f32 v250, s85, v250\n"
"v_mfma_f32_16x16x32_bf16 v[112:115], v[80:83], v[56:59], v[112:115]\n"
"ds_read_b128 v[76:79], v27 offset:10752\n"
"v_mul_f32 v251, s85, v251\n"
"v_cvt_pk_bf16_f32 v248, v248, v249\n"
"s_branch .Lmha_VARJ\n"
".Lmha_VAR2:\n"
"ds_read_b128 v[68:71], v41 offset:33280\n"
"v_mfma_f32_16x16x32_bf16 v[240:243], a[32:35], v[72:75], v[240:243]\n"
"s_waitcnt vmcnt(13)\n"
"ds_read_b64_tr_b16 a[32:33], v45 offset:24576\n"
"v_mfma_f32_16x16x32_bf16 v[240:243], a[48:51], v[76:79], v[240:243]\n"
"s_add_u32 s73, s72, 6\n"
"buffer_load_dwordx4 v38, s[40:43], s75 offen offset:1024 lds\n"
"ds_read_b64_tr_b16 a[34:35], v45 offset:24832\n"
"v_mfma_f32_16x16x32_bf16 v[244:247], a[36:39], v[72:75], v[244:247]\n"
"s_and_b32 s73, s73, 15\n"
"ds_read_b64_tr_b16 a[36:37], v46 offset:24576\n"
"v_mfma_f32_16x16x32_bf16 v[244:247], a[52:55], v[76:79], v[244:247]\n"
"s_lshl_b32 s73, s73, 7\n"
"ds_read_b64_tr_b16 a[38:39], v46 offset:24832\n"
"v_mfma_f32_16x16x32_bf16 v[248:251], a[40:43], v[72:75], v[248:251]\n"
"ds_read_b64_tr_b16 a[40:41], v45 offset:25088\n"
"v_mfma_f32_16x16x32_bf16 v[248:251], a[56:59], v[76:79], v[248:251]\n"
"buffer_load_dwordx4 v39, s[40:43], s75 offen offset:2048 lds\n"
"ds_read_b64_tr_b16 a[42:43], v45 offset:25344\n"
"v_mfma_f32_16x16x32_bf16 v[252:255], a[44:47], v[72:75], v[252:255]\n"
"ds_read_b128 v[72:75], v41 offset:34816\n"
"v_mfma_f32_16x16x32_bf16 v[252:255], a[60:63], v[76:79], v[252:255]\n"
"s_waitcnt lgkmcnt(9)\n"
"ds_read_b128 v[76:79], v41 offset:35328\n"
"v_mfma_f32_16x16x32_bf16 v[112:115], a[0:3], v[48:51], v[112:115]\n"
"ds_read_b64_tr_b16 a[44:45], v46 offset:25088\n"
"v_mfma_f32_16x16x32_bf16 v[112:115], a[16:19], v[52:55], v[112:115]\n"
"buffer_load_dwordx4 v40, s[40:43], s75 offen offset:3072 lds\n"
"ds_read_b64_tr_b16 a[46:47], v46 offset:25344\n"
"v_mfma_f32_16x16x32_bf16 v[116:119], a[4:7], v[48:51], v[116:119]\n"
"v_mfma_f32_16x16x32_bf16 v[116:119], a[20:23], v[52:55], v[116:119]\n"
"v_mfma_f32_16x16x32_bf16 v[120:123], a[8:11], v[48:51], v[120:123]\n"
"s_waitcnt vmcnt(12)\n"
"s_add_u32 m0, s30, 0x2000\n"
"ds_read_b64_tr_b16 a[48:49], v45 offset:28672\n"
"v_mfma_f32_16x16x32_bf16 v[120:123], a[24:27], v[52:55], v[120:123]\n"
"buffer_load_dwordx4 v32, s[36:39], s73 offen offset:0 lds\n"
"ds_read_b64_tr_b16 a[50:51], v45 offset:28928\n"
"v_mfma_f32_16x16x32_bf16 v[124:127], a[12:15], v[48:51], v[124:127]\n"
"ds_read_b128 v[48:51], v42 offset:32768\n"
"v_mfma_f32_16x16x32_bf16 v[124:127], a[28:31], v[52:55], v[124:127]\n"
"ds_read_b128 v[52:55], v42 offset:33280\n"
"v_mfma_f32_16x16x32_bf16 v[128:131], a[0:3], v[56:59], v[128:131]\n"
"ds_read_b64_tr_b16 a[52:53], v46 offset:28672\n"
"v_mfma_f32_16x16x32_bf16 v[128:131], a[16:19], v[60:63], v[128:131]\n"
"buffer_load_dwordx4 v33, s[36:39], s73 offen offset:1024 lds\n"
"ds_read_b64_tr_b16 a[54:55], v46 offset:28928\n"
"v_mfma_f32_16x16x32_bf16 v[132:135], a[4:7], v[56:59], v[132:135]\n"
"ds_read_b64_tr_b16 a[56:57], v45 offset:29184\n"
"v_mfma_f32_16x16x32_bf16 v[132:135], a[20:23], v[60:63], v[132:135]\n"
"ds_read_b64_tr_b16 a[58:59], v45 offset:29440\n"
"v_mfma_f32_16x16x32_bf16 v[136:139], a[8:11], v[56:59], v[136:139]\n"
"ds_read_b64_tr_b16 a[60:61], v46 offset:29184\n"
"v_mfma_f32_16x16x32_bf16 v[136:139], a[24:27], v[60:63], v[136:139]\n"
"s_add_u32 s90, s72, 7\n"
"buffer_load_dwordx4 v34, s[36:39], s73 offen offset:2048 lds\n"
"ds_read_b64_tr_b16 a[62:63], v46 offset:29440\n"
"v_mfma_f32_16x16x32_bf16 v[140:143], a[12:15], v[56:59], v[140:143]\n"
"s_and_b32 s90, s90, 15\n"
"ds_read_b128 v[56:59], v42 offset:34816\n"
"v_mfma_f32_16x16x32_bf16 v[140:143], a[28:31], v[60:63], v[140:143]\n"
"s_waitcnt lgkmcnt(10)\n"
"s_lshl_b32 s90, s90, 19\n"
"ds_read_b128 v[60:63], v42 offset:35328\n"
"v_mfma_f32_16x16x32_bf16 v[144:147], a[0:3], v[64:67], v[144:147]\n"
"v_mfma_f32_16x16x32_bf16 v[144:147], a[16:19], v[68:71], v[144:147]\n"
"buffer_load_dwordx4 v35, s[36:39], s73 offen offset:3072 lds\n"
"v_mfma_f32_16x16x32_bf16 v[148:151], a[4:7], v[64:67], v[148:151]\n"
"v_mfma_f32_16x16x32_bf16 v[148:151], a[20:23], v[68:71], v[148:151]\n"
"v_mfma_f32_16x16x32_bf16 v[152:155], a[8:11], v[64:67], v[152:155]\n"
"s_add_u32 m0, s30, 0x9400\n"
"v_mfma_f32_16x16x32_bf16 v[152:155], a[24:27], v[68:71], v[152:155]\n"
"buffer_load_dwordx4 v36, s[36:39], s73 offen offset:0 lds\n"
"v_mfma_f32_16x16x32_bf16 v[156:159], a[12:15], v[64:67], v[156:159]\n"
"ds_read_b128 v[64:67], v43 offset:32768\n"
"v_mfma_f32_16x16x32_bf16 v[156:159], a[28:31], v[68:71], v[156:159]\n"
"ds_read_b128 v[68:71], v43 offset:33280\n"
"v_mfma_f32_16x16x32_bf16 v[160:163], a[0:3], v[72:75], v[160:163]\n"
"s_add_u32 m0, s30, 0x6000\n"
"v_mfma_f32_16x16x32_bf16 v[160:163], a[16:19], v[76:79], v[160:163]\n"
"buffer_load_dwordx4 v37, s[40:43], s90 offen offset:0 lds\n"
"v_mfma_f32_16x16x32_bf16 v[164:167], a[4:7], v[72:75], v[164:167]\n"
"v_mfma_f32_16x16x32_bf16 v[164:167], a[20:23], v[76:79], v[164:167]\n"
"v_mfma_f32_16x16x32_bf16 v[168:171], a[8:11], v[72:75], v[168:171]\n"
"v_mfma_f32_16x16x32_bf16 v[168:171], a[24:27], v[76:79], v[168:171]\n"
"buffer_load_dwordx4 v38, s[40:43], s90 offen offset:1024 lds\n"
"v_mfma_f32_16x16x32_bf16 v[172:175], a[12:15], v[72:75], v[172:175]\n"
"ds_read_b128 v[72:75], v43 offset:34816\n"
"v_mfma_f32_16x16x32_bf16 v[172:175], a[28:31], v[76:79], v[172:175]\n"
"s_waitcnt lgkmcnt(3)\n"
"ds_read_b128 v[76:79], v43 offset:35328\n"
"v_mfma_f32_16x16x32_bf16 v[176:179], a[0:3], v[48:51], v[176:179]\n"
"v_mfma_f32_16x16x32_bf16 v[176:179], a[16:19], v[52:55], v[176:179]\n"
"buffer_load_dwordx4 v39, s[40:43], s90 offen offset:2048 lds\n"
"v_mfma_f32_16x16x32_bf16 v[180:183], a[4:7], v[48:51], v[180:183]\n"
"v_mfma_f32_16x16x32_bf16 v[180:183], a[20:23], v[52:55], v[180:183]\n"
"v_mfma_f32_16x16x32_bf16 v[184:187], a[8:11], v[48:51], v[184:187]\n"
"v_mfma_f32_16x16x32_bf16 v[184:187], a[24:27], v[52:55], v[184:187]\n"
"buffer_load_dwordx4 v40, s[40:43], s90 offen offset:3072 lds\n"
"v_mfma_f32_16x16x32_bf16 v[188:191], a[12:15], v[48:51], v[188:191]\n"
"ds_read_b128 v[48:51], v44 offset:0\n"
"v_mfma_f32_16x16x32_bf16 v[188:191], a[28:31], v[52:55], v[188:191]\n"
"ds_read_b128 v[52:55], v44 offset:512\n"
"v_mfma_f32_16x16x32_bf16 v[192:195], a[0:3], v[56:59], v[192:195]\n"
"s_add_u32 s76, s90, 0x40000\n"
"s_add_u32 m0, s30, 0x7000\n"
"v_mfma_f32_16x16x32_bf16 v[192:195], a[16:19], v[60:63], v[192:195]\n"
"buffer_load_dwordx4 v37, s[40:43], s76 offen offset:0 lds\n"
"v_mfma_f32_16x16x32_bf16 v[196:199], a[4:7], v[56:59], v[196:199]\n"
"v_mfma_f32_16x16x32_bf16 v[196:199], a[20:23], v[60:63], v[196:199]\n"
"v_mfma_f32_16x16x32_bf16 v[200:203], a[8:11], v[56:59], v[200:203]\n"
"v_mfma_f32_16x16x32_bf16 v[200:203], a[24:27], v[60:63], v[200:203]\n"
"buffer_load_dwordx4 v38, s[40:43], s76 offen offset:1024 lds\n"
"v_mfma_f32_16x16x32_bf16 v[204:207], a[12:15], v[56:59], v[204:207]\n"
"v_mfma_f32_16x16x32_bf16 v[204:207], a[28:31], v[60:63], v[204:207]\n"
"s_waitcnt lgkmcnt(2)\n"
"v_mfma_f32_16x16x32_bf16 v[208:211], a[0:3], v[64:67], v[208:211]\n"
"v_mfma_f32_16x16x32_bf16 v[208:211], a[16:19], v[68:71], v[208:211]\n"
"buffer_load_dwordx4 v39, s[40:43], s76 offen offset:2048 lds\n"
"v_mfma_f32_16x16x32_bf16 v[212:215], a[4:7], v[64:67], v[212:215]\n"
"v_mfma_f32_16x16x32_bf16 v[212:215], a[20:23], v[68:71], v[212:215]\n"
"v_mfma_f32_16x16x32_bf16 v[216:219], a[8:11], v[64:67], v[216:219]\n"
"v_mfma_f32_16x16x32_bf16 v[216:219], a[24:27], v[68:71], v[216:219]\n"
"buffer_load_dwordx4 v40, s[40:43], s76 offen offset:3072 lds\n"
"v_mfma_f32_16x16x32_bf16 v[220:223], a[12:15], v[64:67], v[220:223]\n"
"v_mfma_f32_16x16x32_bf16 v[220:223], a[28:31], v[68:71], v[220:223]\n"
"v_mfma_f32_16x16x32_bf16 v[224:227], a[0:3], v[72:75], v[224:227]\n"
"v_mfma_f32_16x16x32_bf16 v[224:227], a[16:19], v[76:79], v[224:227]\n"
"s_waitcnt vmcnt(21) lgkmcnt(0)\n"
"s_barrier\n"
"v_mfma_f32_16x16x32_bf16 v[228:231], a[4:7], v[72:75], v[228:231]\n"
"ds_read_b128 v[56:59], v27 offset:12288\n"
"ds_read_b128 v[60:63], v27 offset:12800\n"
"v_mfma_f32_16x16x32_bf16 v[228:231], a[20:23], v[76:79], v[228:231]\n"
"ds_read_b128 v[64:67], v27 offset:14336\n"
"ds_read_b128 v[68:71], v27 offset:14848\n"
"v_mfma_f32_16x16x32_bf16 v[232:235], a[8:11], v[72:75], v[232:235]\n"
"v_mfma_f32_16x16x32_bf16 v[232:235], a[24:27], v[76:79], v[232:235]\n"
"v_mfma_f32_16x16x32_bf16 v[236:239], a[12:15], v[72:75], v[236:239]\n"
"ds_read_b128 v[72:75], v41 offset:12288\n"
"v_mfma_f32_16x16x32_bf16 v[236:239], a[28:31], v[76:79], v[236:239]\n"
"ds_read_b128 v[76:79], v41 offset:12800\n"
"v_mfma_f32_16x16x32_bf16 v[240:243], a[0:3], v[48:51], v[240:243]\n"
"s_waitcnt vmcnt(17)\n"
"s_add_u32 m0, s30, 0\n"
"ds_read_b64_tr_b16 a[0:1], v45 offset:16384\n"
"v_mfma_f32_16x16x32_bf16 v[240:243], a[16:19], v[52:55], v[240:243]\n"
"buffer_load_dwordx4 v17, s[44:47], 0 offen offset:0 nt lds\n"
"ds_read_b64_tr_b16 a[2:3], v45 offset:16640\n"
"v_mfma_f32_16x16x32_bf16 v[244:247], a[4:7], v[48:51], v[244:247]\n"
"s_waitcnt vmcnt(14)\n"
"ds_read_b64_tr_b16 a[16:17], v45 offset:20480\n"
"v_mfma_f32_16x16x32_bf16 v[244:247], a[20:23], v[52:55], v[244:247]\n"
"ds_read_b64_tr_b16 a[18:19], v45 offset:20736\n"
"v_mfma_f32_16x16x32_bf16 v[248:251], a[8:11], v[48:51], v[248:251]\n"
"ds_read_b64_tr_b16 a[4:5], v46 offset:16384\n"
"v_mfma_f32_16x16x32_bf16 v[248:251], a[24:27], v[52:55], v[248:251]\n"
"buffer_load_dwordx4 v17, s[44:47], 0 offen offset:1024 nt lds\n"
"ds_read_b64_tr_b16 a[6:7], v46 offset:16640\n"
"v_mfma_f32_16x16x32_bf16 v[252:255], a[12:15], v[48:51], v[252:255]\n"
"ds_read_b128 v[48:51], v41 offset:14336\n"
"v_mfma_f32_16x16x32_bf16 v[252:255], a[28:31], v[52:55], v[252:255]\n"
"s_waitcnt lgkmcnt(9)\n"
"ds_read_b128 v[52:55], v41 offset:14848\n"
"v_mfma_f32_16x16x32_bf16 v[112:115], a[32:35], v[56:59], v[112:115]\n"
"ds_read_b64_tr_b16 a[20:21], v46 offset:20480\n"
"v_mfma_f32_16x16x32_bf16 v[112:115], a[48:51], v[60:63], v[112:115]\n"
"buffer_load_dwordx4 v18, s[44:47], 0 offen offset:2048 nt lds\n"
"ds_read_b64_tr_b16 a[22:23], v46 offset:20736\n"
"v_mfma_f32_16x16x32_bf16 v[116:119], a[36:39], v[56:59], v[116:119]\n"
"ds_read_b64_tr_b16 a[8:9], v45 offset:16896\n"
"v_mfma_f32_16x16x32_bf16 v[116:119], a[52:55], v[60:63], v[116:119]\n"
"ds_read_b64_tr_b16 a[10:11], v45 offset:17152\n"
"v_mfma_f32_16x16x32_bf16 v[120:123], a[40:43], v[56:59], v[120:123]\n"
"ds_read_b64_tr_b16 a[24:25], v45 offset:20992\n"
"v_mfma_f32_16x16x32_bf16 v[120:123], a[56:59], v[60:63], v[120:123]\n"
"buffer_load_dwordx4 v18, s[44:47], 0 offen offset:3072 nt lds\n"
"ds_read_b64_tr_b16 a[26:27], v45 offset:21248\n"
"v_mfma_f32_16x16x32_bf16 v[124:127], a[44:47], v[56:59], v[124:127]\n"
"ds_read_b128 v[56:59], v42 offset:12288\n"
"v_mfma_f32_16x16x32_bf16 v[124:127], a[60:63], v[60:63], v[124:127]\n"
"ds_read_b128 v[60:63], v42 offset:12800\n"
"v_mfma_f32_16x16x32_bf16 v[128:131], a[32:35], v[64:67], v[128:131]\n"
"s_mov_b32 s77, 0x4000\n"
"s_add_u32 m0, s30, 0x1000\n"
"ds_read_b64_tr_b16 a[12:13], v46 offset:16896\n"
"v_mfma_f32_16x16x32_bf16 v[128:131], a[48:51], v[68:71], v[128:131]\n"
"buffer_load_dwordx4 v17, s[44:47], s77 offen offset:0 nt lds\n"
"ds_read_b64_tr_b16 a[14:15], v46 offset:17152\n"
"v_mfma_f32_16x16x32_bf16 v[132:135], a[36:39], v[64:67], v[132:135]\n"
"ds_read_b64_tr_b16 a[28:29], v46 offset:20992\n"
"v_mfma_f32_16x16x32_bf16 v[132:135], a[52:55], v[68:71], v[132:135]\n"
"ds_read_b64_tr_b16 a[30:31], v46 offset:21248\n"
"v_mfma_f32_16x16x32_bf16 v[136:139], a[40:43], v[64:67], v[136:139]\n"
"v_mfma_f32_16x16x32_bf16 v[136:139], a[56:59], v[68:71], v[136:139]\n"
"buffer_load_dwordx4 v17, s[44:47], s77 offen offset:1024 nt lds\n"
"v_mfma_f32_16x16x32_bf16 v[140:143], a[44:47], v[64:67], v[140:143]\n"
"ds_read_b128 v[64:67], v42 offset:14336\n"
"v_mfma_f32_16x16x32_bf16 v[140:143], a[60:63], v[68:71], v[140:143]\n"
"s_waitcnt lgkmcnt(8)\n"
"ds_read_b128 v[68:71], v42 offset:14848\n"
"v_mfma_f32_16x16x32_bf16 v[144:147], a[32:35], v[72:75], v[144:147]\n"
"v_mfma_f32_16x16x32_bf16 v[144:147], a[48:51], v[76:79], v[144:147]\n"
"buffer_load_dwordx4 v18, s[44:47], s77 offen offset:2048 nt lds\n"
"v_mfma_f32_16x16x32_bf16 v[148:151], a[36:39], v[72:75], v[148:151]\n"
"v_mfma_f32_16x16x32_bf16 v[148:151], a[52:55], v[76:79], v[148:151]\n"
"v_mfma_f32_16x16x32_bf16 v[152:155], a[40:43], v[72:75], v[152:155]\n"
"v_mfma_f32_16x16x32_bf16 v[152:155], a[56:59], v[76:79], v[152:155]\n"
"buffer_load_dwordx4 v18, s[44:47], s77 offen offset:3072 nt lds\n"
"v_mfma_f32_16x16x32_bf16 v[156:159], a[44:47], v[72:75], v[156:159]\n"
"ds_read_b128 v[72:75], v43 offset:12288\n"
"v_mfma_f32_16x16x32_bf16 v[156:159], a[60:63], v[76:79], v[156:159]\n"
"ds_read_b128 v[76:79], v43 offset:12800\n"
"v_mfma_f32_16x16x32_bf16 v[160:163], a[32:35], v[48:51], v[160:163]\n"
"v_mfma_f32_16x16x32_bf16 v[160:163], a[48:51], v[52:55], v[160:163]\n"
"v_mfma_f32_16x16x32_bf16 v[164:167], a[36:39], v[48:51], v[164:167]\n"
"v_mfma_f32_16x16x32_bf16 v[164:167], a[52:55], v[52:55], v[164:167]\n"
"v_mfma_f32_16x16x32_bf16 v[168:171], a[40:43], v[48:51], v[168:171]\n"
"v_mfma_f32_16x16x32_bf16 v[168:171], a[56:59], v[52:55], v[168:171]\n"
"v_mfma_f32_16x16x32_bf16 v[172:175], a[44:47], v[48:51], v[172:175]\n"
"ds_read_b128 v[48:51], v43 offset:14336\n"
"v_mfma_f32_16x16x32_bf16 v[172:175], a[60:63], v[52:55], v[172:175]\n"
"s_waitcnt lgkmcnt(3)\n"
"ds_read_b128 v[52:55], v43 offset:14848\n"
"v_mfma_f32_16x16x32_bf16 v[176:179], a[32:35], v[56:59], v[176:179]\n"
"v_mfma_f32_16x16x32_bf16 v[176:179], a[48:51], v[60:63], v[176:179]\n"
"s_add_u32 s88, s72, 7\n"
"v_mfma_f32_16x16x32_bf16 v[180:183], a[36:39], v[56:59], v[180:183]\n"
"s_and_b32 s88, s88, 15\n"
"v_mfma_f32_16x16x32_bf16 v[180:183], a[52:55], v[60:63], v[180:183]\n"
"s_lshl_b32 s88, s88, 7\n"
"v_mfma_f32_16x16x32_bf16 v[184:187], a[40:43], v[56:59], v[184:187]\n"
"v_mfma_f32_16x16x32_bf16 v[184:187], a[56:59], v[60:63], v[184:187]\n"
"v_mfma_f32_16x16x32_bf16 v[188:191], a[44:47], v[56:59], v[188:191]\n"
"ds_read_b128 v[56:59], v30 offset:0\n"
"v_mfma_f32_16x16x32_bf16 v[188:191], a[60:63], v[60:63], v[188:191]\n"
"ds_read_b128 v[60:63], v30 offset:512\n"
"v_mfma_f32_16x16x32_bf16 v[192:195], a[32:35], v[64:67], v[192:195]\n"
"v_mfma_f32_16x16x32_bf16 v[192:195], a[48:51], v[68:71], v[192:195]\n"
"v_mfma_f32_16x16x32_bf16 v[196:199], a[36:39], v[64:67], v[196:199]\n"
"v_mfma_f32_16x16x32_bf16 v[196:199], a[52:55], v[68:71], v[196:199]\n"
"v_mfma_f32_16x16x32_bf16 v[200:203], a[40:43], v[64:67], v[200:203]\n"
"s_add_u32 m0, s30, 0x8000\n"
"v_mfma_f32_16x16x32_bf16 v[200:203], a[56:59], v[68:71], v[200:203]\n"
"buffer_load_dwordx4 v32, s[36:39], s88 offen offset:0 lds\n"
"v_mfma_f32_16x16x32_bf16 v[204:207], a[44:47], v[64:67], v[204:207]\n"
"v_mfma_f32_16x16x32_bf16 v[204:207], a[60:63], v[68:71], v[204:207]\n"
"s_waitcnt lgkmcnt(2)\n"
"v_mfma_f32_16x16x32_bf16 v[208:211], a[32:35], v[72:75], v[208:211]\n"
"v_mfma_f32_16x16x32_bf16 v[208:211], a[48:51], v[76:79], v[208:211]\n"
"s_add_u32 s91, s72, 8\n"
"buffer_load_dwordx4 v33, s[36:39], s88 offen offset:1024 lds\n"
"v_mfma_f32_16x16x32_bf16 v[212:215], a[36:39], v[72:75], v[212:215]\n"
"s_and_b32 s91, s91, 15\n"
"v_mfma_f32_16x16x32_bf16 v[212:215], a[52:55], v[76:79], v[212:215]\n"
"s_lshl_b32 s91, s91, 19\n"
"v_mfma_f32_16x16x32_bf16 v[216:219], a[40:43], v[72:75], v[216:219]\n"
"v_mfma_f32_16x16x32_bf16 v[216:219], a[56:59], v[76:79], v[216:219]\n"
"buffer_load_dwordx4 v34, s[36:39], s88 offen offset:2048 lds\n"
"v_mfma_f32_16x16x32_bf16 v[220:223], a[44:47], v[72:75], v[220:223]\n"
"v_mfma_f32_16x16x32_bf16 v[220:223], a[60:63], v[76:79], v[220:223]\n"
"v_mfma_f32_16x16x32_bf16 v[224:227], a[32:35], v[48:51], v[224:227]\n"
"v_mfma_f32_16x16x32_bf16 v[224:227], a[48:51], v[52:55], v[224:227]\n"
"s_waitcnt vmcnt(19) lgkmcnt(0)\n"
"s_barrier\n"
"buffer_load_dwordx4 v35, s[36:39], s88 offen offset:3072 lds\n"
"v_mfma_f32_16x16x32_bf16 v[228:231], a[36:39], v[48:51], v[228:231]\n"
"ds_read_b128 v[64:67], v27 offset:8192\n"
"ds_read_b128 v[68:71], v27 offset:8704\n"
"v_mfma_f32_16x16x32_bf16 v[228:231], a[52:55], v[52:55], v[228:231]\n"
"ds_read_b128 v[72:75], v27 offset:10240\n"
"ds_read_b128 v[76:79], v27 offset:10752\n"
"v_mfma_f32_16x16x32_bf16 v[232:235], a[40:43], v[48:51], v[232:235]\n"
"s_add_u32 m0, s30, 0x4000\n"
"v_mfma_f32_16x16x32_bf16 v[232:235], a[56:59], v[52:55], v[232:235]\n"
"buffer_load_dwordx4 v37, s[40:43], s91 offen offset:0 lds\n"
"v_mfma_f32_16x16x32_bf16 v[236:239], a[44:47], v[48:51], v[236:239]\n"
"ds_read_b128 v[48:51], v41 offset:8192\n"
"v_mfma_f32_16x16x32_bf16 v[236:239], a[60:63], v[52:55], v[236:239]\n"
"ds_read_b128 v[52:55], v41 offset:8704\n"
"v_mfma_f32_16x16x32_bf16 v[240:243], a[32:35], v[56:59], v[240:243]\n"
"s_waitcnt vmcnt(17)\n"
"ds_read_b64_tr_b16 a[32:33], v45 offset:24576\n"
"v_mfma_f32_16x16x32_bf16 v[240:243], a[48:51], v[60:63], v[240:243]\n"
"buffer_load_dwordx4 v38, s[40:43], s91 offen offset:1024 lds\n"
"ds_read_b64_tr_b16 a[34:35], v45 offset:24832\n"
"v_mfma_f32_16x16x32_bf16 v[244:247], a[36:39], v[56:59], v[244:247]\n"
"s_waitcnt vmcnt(14)\n"
"ds_read_b64_tr_b16 a[48:49], v45 offset:28672\n"
"v_mfma_f32_16x16x32_bf16 v[244:247], a[52:55], v[60:63], v[244:247]\n"
"ds_read_b64_tr_b16 a[50:51], v45 offset:28928\n"
"v_mfma_f32_16x16x32_bf16 v[248:251], a[40:43], v[56:59], v[248:251]\n"
"ds_read_b64_tr_b16 a[36:37], v46 offset:24576\n"
"v_mfma_f32_16x16x32_bf16 v[248:251], a[56:59], v[60:63], v[248:251]\n"
"buffer_load_dwordx4 v39, s[40:43], s91 offen offset:2048 lds\n"
"ds_read_b64_tr_b16 a[38:39], v46 offset:24832\n"
"v_mfma_f32_16x16x32_bf16 v[252:255], a[44:47], v[56:59], v[252:255]\n"
"ds_read_b128 v[56:59], v41 offset:10240\n"
"v_mfma_f32_16x16x32_bf16 v[252:255], a[60:63], v[60:63], v[252:255]\n"
"s_waitcnt lgkmcnt(9)\n"
"ds_read_b128 v[60:63], v41 offset:10752\n"
"v_mfma_f32_16x16x32_bf16 v[112:115], a[0:3], v[64:67], v[112:115]\n"
"ds_read_b64_tr_b16 a[52:53], v46 offset:28672\n"
"v_mfma_f32_16x16x32_bf16 v[112:115], a[16:19], v[68:71], v[112:115]\n"
"buffer_load_dwordx4 v40, s[40:43], s91 offen offset:3072 lds\n"
"ds_read_b64_tr_b16 a[54:55], v46 offset:28928\n"
"v_mfma_f32_16x16x32_bf16 v[116:119], a[4:7], v[64:67], v[116:119]\n"
"ds_read_b64_tr_b16 a[40:41], v45 offset:25088\n"
"v_mfma_f32_16x16x32_bf16 v[116:119], a[20:23], v[68:71], v[116:119]\n"
"ds_read_b64_tr_b16 a[42:43], v45 offset:25344\n"
"v_mfma_f32_16x16x32_bf16 v[120:123], a[8:11], v[64:67], v[120:123]\n"
"s_add_u32 s78, s91, 0x40000\n"
"s_add_u32 m0, s30, 0x5000\n"
"ds_read_b64_tr_b16 a[56:57], v45 offset:29184\n"
"v_mfma_f32_16x16x32_bf16 v[120:123], a[24:27], v[68:71], v[120:123]\n"
"buffer_load_dwordx4 v37, s[40:43], s78 offen offset:0 lds\n"
"ds_read_b64_tr_b16 a[58:59], v45 offset:29440\n"
"v_mfma_f32_16x16x32_bf16 v[124:127], a[12:15], v[64:67], v[124:127]\n"
"ds_read_b128 v[64:67], v42 offset:8192\n"
"v_mfma_f32_16x16x32_bf16 v[124:127], a[28:31], v[68:71], v[124:127]\n"
"ds_read_b128 v[68:71], v42 offset:8704\n"
"v_mfma_f32_16x16x32_bf16 v[128:131], a[0:3], v[72:75], v[128:131]\n"
"s_add_u32 s89, s72, 8\n"
"ds_read_b64_tr_b16 a[44:45], v46 offset:25088\n"
"v_mfma_f32_16x16x32_bf16 v[128:131], a[16:19], v[76:79], v[128:131]\n"
"s_and_b32 s89, s89, 15\n"
"buffer_load_dwordx4 v38, s[40:43], s78 offen offset:1024 lds\n"
"ds_read_b64_tr_b16 a[46:47], v46 offset:25344\n"
"v_mfma_f32_16x16x32_bf16 v[132:135], a[4:7], v[72:75], v[132:135]\n"
"s_lshl_b32 s89, s89, 7\n"
"ds_read_b64_tr_b16 a[60:61], v46 offset:29184\n"
"v_mfma_f32_16x16x32_bf16 v[132:135], a[20:23], v[76:79], v[132:135]\n"
"ds_read_b64_tr_b16 a[62:63], v46 offset:29440\n"
"v_mfma_f32_16x16x32_bf16 v[136:139], a[8:11], v[72:75], v[136:139]\n"
"buffer_load_dwordx4 v39, s[40:43], s78 offen offset:2048 lds\n"
"v_mfma_f32_16x16x32_bf16 v[136:139], a[24:27], v[76:79], v[136:139]\n"
"v_mfma_f32_16x16x32_bf16 v[140:143], a[12:15], v[72:75], v[140:143]\n"
"ds_read_b128 v[72:75], v42 offset:10240\n"
"v_mfma_f32_16x16x32_bf16 v[140:143], a[28:31], v[76:79], v[140:143]\n"
"s_waitcnt lgkmcnt(8)\n"
"ds_read_b128 v[76:79], v42 offset:10752\n"
"v_mfma_f32_16x16x32_bf16 v[144:147], a[0:3], v[48:51], v[144:147]\n"
"buffer_load_dwordx4 v40, s[40:43], s78 offen offset:3072 lds\n"
"v_mfma_f32_16x16x32_bf16 v[144:147], a[16:19], v[52:55], v[144:147]\n"
"v_mfma_f32_16x16x32_bf16 v[148:151], a[4:7], v[48:51], v[148:151]\n"
"v_mfma_f32_16x16x32_bf16 v[148:151], a[20:23], v[52:55], v[148:151]\n"
"s_add_u32 m0, s30, 0x3000\n"
"v_mfma_f32_16x16x32_bf16 v[152:155], a[8:11], v[48:51], v[152:155]\n"
"buffer_load_dwordx4 v32, s[36:39], s89 offen offset:0 lds\n"
"v_mfma_f32_16x16x32_bf16 v[152:155], a[24:27], v[52:55], v[152:155]\n"
"v_mfma_f32_16x16x32_bf16 v[156:159], a[12:15], v[48:51], v[156:159]\n"
"ds_read_b128 v[48:51], v43 offset:8192\n"
"v_mfma_f32_16x16x32_bf16 v[156:159], a[28:31], v[52:55], v[156:159]\n"
"ds_read_b128 v[52:55], v43 offset:8704\n"
"v_mfma_f32_16x16x32_bf16 v[160:163], a[0:3], v[56:59], v[160:163]\n"
"buffer_load_dwordx4 v33, s[36:39], s89 offen offset:1024 lds\n"
"v_mfma_f32_16x16x32_bf16 v[160:163], a[16:19], v[60:63], v[160:163]\n"
"v_mfma_f32_16x16x32_bf16 v[164:167], a[4:7], v[56:59], v[164:167]\n"
"v_mfma_f32_16x16x32_bf16 v[164:167], a[20:23], v[60:63], v[164:167]\n"
"v_mfma_f32_16x16x32_bf16 v[168:171], a[8:11], v[56:59], v[168:171]\n"
"s_add_u32 s74, s72, 9\n"
"buffer_load_dwordx4 v34, s[36:39], s89 offen offset:2048 lds\n"
"v_mfma_f32_16x16x32_bf16 v[168:171], a[24:27], v[60:63], v[168:171]\n"
"s_and_b32 s74, s74, 15\n"
"v_mfma_f32_16x16x32_bf16 v[172:175], a[12:15], v[56:59], v[172:175]\n"
"s_lshl_b32 s74, s74, 19\n"
"ds_read_b128 v[56:59], v43 offset:10240\n"
"v_mfma_f32_16x16x32_bf16 v[172:175], a[28:31], v[60:63], v[172:175]\n"
"s_waitcnt lgkmcnt(3)\n"
"ds_read_b128 v[60:63], v43 offset:10752\n"
"v_mfma_f32_16x16x32_bf16 v[176:179], a[0:3], v[64:67], v[176:179]\n"
"buffer_load_dwordx4 v35, s[36:39], s89 offen offset:3072 lds\n"
"v_mfma_f32_16x16x32_bf16 v[176:179], a[16:19], v[68:71], v[176:179]\n"
"v_mfma_f32_16x16x32_bf16 v[180:183], a[4:7], v[64:67], v[180:183]\n"
"v_mfma_f32_16x16x32_bf16 v[180:183], a[20:23], v[68:71], v[180:183]\n"
"s_add_u32 m0, s30, 0x9000\n"
"v_mfma_f32_16x16x32_bf16 v[184:187], a[8:11], v[64:67], v[184:187]\n"
"buffer_load_dwordx4 v36, s[36:39], s89 offen offset:0 lds\n"
"v_mfma_f32_16x16x32_bf16 v[184:187], a[24:27], v[68:71], v[184:187]\n"
"v_mfma_f32_16x16x32_bf16 v[188:191], a[12:15], v[64:67], v[188:191]\n"
"ds_read_b128 v[64:67], v44 offset:1024\n"
"v_mfma_f32_16x16x32_bf16 v[188:191], a[28:31], v[68:71], v[188:191]\n"
"s_add_u32 m0, s30, 0x6000\n"
"ds_read_b128 v[68:71], v44 offset:1536\n"
"v_mfma_f32_16x16x32_bf16 v[192:195], a[0:3], v[72:75], v[192:195]\n"
"buffer_load_dwordx4 v37, s[40:43], s74 offen offset:0 lds\n"
"v_mfma_f32_16x16x32_bf16 v[192:195], a[16:19], v[76:79], v[192:195]\n"
"v_mfma_f32_16x16x32_bf16 v[196:199], a[4:7], v[72:75], v[196:199]\n"
"v_mfma_f32_16x16x32_bf16 v[196:199], a[20:23], v[76:79], v[196:199]\n"
"v_mfma_f32_16x16x32_bf16 v[200:203], a[8:11], v[72:75], v[200:203]\n"
"buffer_load_dwordx4 v38, s[40:43], s74 offen offset:1024 lds\n"
"v_mfma_f32_16x16x32_bf16 v[200:203], a[24:27], v[76:79], v[200:203]\n"
"v_mfma_f32_16x16x32_bf16 v[204:207], a[12:15], v[72:75], v[204:207]\n"
"v_mfma_f32_16x16x32_bf16 v[204:207], a[28:31], v[76:79], v[204:207]\n"
"s_waitcnt lgkmcnt(2)\n"
"v_mfma_f32_16x16x32_bf16 v[208:211], a[0:3], v[48:51], v[208:211]\n"
"buffer_load_dwordx4 v39, s[40:43], s74 offen offset:2048 lds\n"
"v_mfma_f32_16x16x32_bf16 v[208:211], a[16:19], v[52:55], v[208:211]\n"
"v_mfma_f32_16x16x32_bf16 v[212:215], a[4:7], v[48:51], v[212:215]\n"
"v_mfma_f32_16x16x32_bf16 v[212:215], a[20:23], v[52:55], v[212:215]\n"
"v_mfma_f32_16x16x32_bf16 v[216:219], a[8:11], v[48:51], v[216:219]\n"
"buffer_load_dwordx4 v40, s[40:43], s74 offen offset:3072 lds\n"
"v_mfma_f32_16x16x32_bf16 v[216:219], a[24:27], v[52:55], v[216:219]\n"
"v_mfma_f32_16x16x32_bf16 v[220:223], a[12:15], v[48:51], v[220:223]\n"
"v_mfma_f32_16x16x32_bf16 v[220:223], a[28:31], v[52:55], v[220:223]\n"
"s_add_u32 s84, s74, 0x40000\n"
"s_add_u32 m0, s30, 0x7000\n"
"v_mfma_f32_16x16x32_bf16 v[224:227], a[0:3], v[56:59], v[224:227]\n"
"buffer_load_dwordx4 v37, s[40:43], s84 offen offset:0 lds\n"
"v_mfma_f32_16x16x32_bf16 v[224:227], a[16:19], v[60:63], v[224:227]\n"
"s_waitcnt vmcnt(18) lgkmcnt(0)\n"
"s_barrier\n"
"v_mfma_f32_16x16x32_bf16 v[228:231], a[4:7], v[56:59], v[228:231]\n"
"ds_read_b128 v[72:75], v27 offset:32768\n"
"ds_read_b128 v[76:79], v27 offset:33280\n"
"v_mfma_f32_16x16x32_bf16 v[228:231], a[20:23], v[60:63], v[228:231]\n"
"ds_read_b128 v[48:51], v27 offset:34816\n"
"ds_read_b128 v[52:55], v27 offset:35328\n"
"v_mfma_f32_16x16x32_bf16 v[232:235], a[8:11], v[56:59], v[232:235]\n"
"buffer_load_dwordx4 v38, s[40:43], s84 offen offset:1024 lds\n"
"v_mfma_f32_16x16x32_bf16 v[232:235], a[24:27], v[60:63], v[232:235]\n"
"v_mfma_f32_16x16x32_bf16 v[236:239], a[12:15], v[56:59], v[236:239]\n"
"ds_read_b128 v[56:59], v41 offset:32768\n"
"v_mfma_f32_16x16x32_bf16 v[236:239], a[28:31], v[60:63], v[236:239]\n"
"ds_read_b128 v[60:63], v41 offset:33280\n"
"v_mfma_f32_16x16x32_bf16 v[240:243], a[0:3], v[64:67], v[240:243]\n"
"s_waitcnt vmcnt(15)\n"
"buffer_load_dwordx4 v39, s[40:43], s84 offen offset:2048 lds\n"
"ds_read_b64_tr_b16 a[0:1], v45 offset:16384\n"
"v_mfma_f32_16x16x32_bf16 v[240:243], a[16:19], v[68:71], v[240:243]\n"
"ds_read_b64_tr_b16 a[2:3], v45 offset:16640\n"
"v_mfma_f32_16x16x32_bf16 v[244:247], a[4:7], v[64:67], v[244:247]\n"
"s_waitcnt vmcnt(12)\n"
"buffer_load_dwordx4 a[64:67], v19, s[48:51], 0 offen offset:0 nt\n"
"ds_read_b64_tr_b16 a[16:17], v45 offset:20480\n"
"v_mfma_f32_16x16x32_bf16 v[244:247], a[20:23], v[68:71], v[244:247]\n"
"ds_read_b64_tr_b16 a[18:19], v45 offset:20736\n"
"v_mfma_f32_16x16x32_bf16 v[248:251], a[8:11], v[64:67], v[248:251]\n"
"ds_read_b64_tr_b16 a[4:5], v46 offset:16384\n"
"v_mfma_f32_16x16x32_bf16 v[248:251], a[24:27], v[68:71], v[248:251]\n"
"ds_read_b64_tr_b16 a[6:7], v46 offset:16640\n"
"v_mfma_f32_16x16x32_bf16 v[252:255], a[12:15], v[64:67], v[252:255]\n"
"buffer_load_dwordx4 a[68:71], v20, s[48:51], 0 offen offset:1024 nt\n"
"ds_read_b128 v[64:67], v41 offset:34816\n"
"v_mfma_f32_16x16x32_bf16 v[252:255], a[28:31], v[68:71], v[252:255]\n"
"s_waitcnt lgkmcnt(9)\n"
"ds_read_b128 v[68:71], v41 offset:35328\n"
"v_mfma_f32_16x16x32_bf16 v[112:115], a[32:35], v[72:75], v[112:115]\n"
"ds_read_b64_tr_b16 a[20:21], v46 offset:20480\n"
"v_mfma_f32_16x16x32_bf16 v[112:115], a[48:51], v[76:79], v[112:115]\n"
"ds_read_b64_tr_b16 a[22:23], v46 offset:20736\n"
"v_mfma_f32_16x16x32_bf16 v[116:119], a[36:39], v[72:75], v[116:119]\n"
"buffer_load_dwordx4 a[72:75], v19, s[48:51], 0 offen offset:2048 nt\n"
"ds_read_b64_tr_b16 a[8:9], v45 offset:16896\n"
"v_mfma_f32_16x16x32_bf16 v[116:119], a[52:55], v[76:79], v[116:119]\n"
"ds_read_b64_tr_b16 a[10:11], v45 offset:17152\n"
"v_mfma_f32_16x16x32_bf16 v[120:123], a[40:43], v[72:75], v[120:123]\n"
"ds_read_b64_tr_b16 a[24:25], v45 offset:20992\n"
"v_mfma_f32_16x16x32_bf16 v[120:123], a[56:59], v[76:79], v[120:123]\n"
"ds_read_b64_tr_b16 a[26:27], v45 offset:21248\n"
"v_mfma_f32_16x16x32_bf16 v[124:127], a[44:47], v[72:75], v[124:127]\n"
"buffer_load_dwordx4 a[76:79], v20, s[48:51], 0 offen offset:3072 nt\n"
"ds_read_b128 v[72:75], v42 offset:32768\n"
"v_mfma_f32_16x16x32_bf16 v[124:127], a[60:63], v[76:79], v[124:127]\n"
"ds_read_b128 v[76:79], v42 offset:33280\n"
"v_mfma_f32_16x16x32_bf16 v[128:131], a[32:35], v[48:51], v[128:131]\n"
"ds_read_b64_tr_b16 a[12:13], v46 offset:16896\n"
"v_mfma_f32_16x16x32_bf16 v[128:131], a[48:51], v[52:55], v[128:131]\n"
"s_mov_b32 s96, 0x8000\n"
"ds_read_b64_tr_b16 a[14:15], v46 offset:17152\n"
"v_mfma_f32_16x16x32_bf16 v[132:135], a[36:39], v[48:51], v[132:135]\n"
"buffer_load_dwordx4 a[80:83], v17, s[44:47], s96 offen offset:0 nt\n"
"ds_read_b64_tr_b16 a[28:29], v46 offset:20992\n"
"v_mfma_f32_16x16x32_bf16 v[132:135], a[52:55], v[52:55], v[132:135]\n"
"ds_read_b64_tr_b16 a[30:31], v46 offset:21248\n"
"v_mfma_f32_16x16x32_bf16 v[136:139], a[40:43], v[48:51], v[136:139]\n"
"v_mfma_f32_16x16x32_bf16 v[136:139], a[56:59], v[52:55], v[136:139]\n"
"v_mfma_f32_16x16x32_bf16 v[140:143], a[44:47], v[48:51], v[140:143]\n"
"buffer_load_dwordx4 a[84:87], v17, s[44:47], s96 offen offset:1024 nt\n"
"ds_read_b128 v[48:51], v42 offset:34816\n"
"v_mfma_f32_16x16x32_bf16 v[140:143], a[60:63], v[52:55], v[140:143]\n"
"s_waitcnt lgkmcnt(8)\n"
"ds_read_b128 v[52:55], v42 offset:35328\n"
"v_mfma_f32_16x16x32_bf16 v[144:147], a[32:35], v[56:59], v[144:147]\n"
"v_mfma_f32_16x16x32_bf16 v[144:147], a[48:51], v[60:63], v[144:147]\n"
"v_mfma_f32_16x16x32_bf16 v[148:151], a[36:39], v[56:59], v[148:151]\n"
"buffer_load_dwordx4 a[88:91], v18, s[44:47], s96 offen offset:2048 nt\n"
"v_mfma_f32_16x16x32_bf16 v[148:151], a[52:55], v[60:63], v[148:151]\n"
"v_mfma_f32_16x16x32_bf16 v[152:155], a[40:43], v[56:59], v[152:155]\n"
"v_mfma_f32_16x16x32_bf16 v[152:155], a[56:59], v[60:63], v[152:155]\n"
"v_mfma_f32_16x16x32_bf16 v[156:159], a[44:47], v[56:59], v[156:159]\n"
"buffer_load_dwordx4 a[92:95], v18, s[44:47], s96 offen offset:3072 nt\n"
"ds_read_b128 v[56:59], v43 offset:32768\n"
"v_mfma_f32_16x16x32_bf16 v[156:159], a[60:63], v[60:63], v[156:159]\n"
"ds_read_b128 v[60:63], v43 offset:33280\n"
"v_mfma_f32_16x16x32_bf16 v[160:163], a[32:35], v[64:67], v[160:163]\n"
"v_mfma_f32_16x16x32_bf16 v[160:163], a[48:51], v[68:71], v[160:163]\n"
"v_mfma_f32_16x16x32_bf16 v[164:167], a[36:39], v[64:67], v[164:167]\n"
"v_mfma_f32_16x16x32_bf16 v[164:167], a[52:55], v[68:71], v[164:167]\n"
"v_mfma_f32_16x16x32_bf16 v[168:171], a[40:43], v[64:67], v[168:171]\n"
"v_mfma_f32_16x16x32_bf16 v[168:171], a[56:59], v[68:71], v[168:171]\n"
"v_mfma_f32_16x16x32_bf16 v[172:175], a[44:47], v[64:67], v[172:175]\n"
"ds_read_b128 v[64:67], v43 offset:34816\n"
"v_mfma_f32_16x16x32_bf16 v[172:175], a[60:63], v[68:71], v[172:175]\n"
"s_waitcnt lgkmcnt(3)\n"
"ds_read_b128 v[68:71], v43 offset:35328\n"
"v_mfma_f32_16x16x32_bf16 v[176:179], a[32:35], v[72:75], v[176:179]\n"
"v_mfma_f32_16x16x32_bf16 v[176:179], a[48:51], v[76:79], v[176:179]\n"
"v_mfma_f32_16x16x32_bf16 v[180:183], a[36:39], v[72:75], v[180:183]\n"
"v_mfma_f32_16x16x32_bf16 v[180:183], a[52:55], v[76:79], v[180:183]\n"
"v_mfma_f32_16x16x32_bf16 v[184:187], a[40:43], v[72:75], v[184:187]\n"
"v_mfma_f32_16x16x32_bf16 v[184:187], a[56:59], v[76:79], v[184:187]\n"
"v_mfma_f32_16x16x32_bf16 v[188:191], a[44:47], v[72:75], v[188:191]\n"
"s_add_u32 s73, s72, 9\n"
"ds_read_b128 v[72:75], v30 offset:1024\n"
"v_mfma_f32_16x16x32_bf16 v[188:191], a[60:63], v[76:79], v[188:191]\n"
"s_and_b32 s73, s73, 15\n"
"ds_read_b128 v[76:79], v30 offset:1536\n"
"v_mfma_f32_16x16x32_bf16 v[192:195], a[32:35], v[48:51], v[192:195]\n"
"s_lshl_b32 s73, s73, 7\n"
"v_mfma_f32_16x16x32_bf16 v[192:195], a[48:51], v[52:55], v[192:195]\n"
"v_mfma_f32_16x16x32_bf16 v[196:199], a[36:39], v[48:51], v[196:199]\n"
"v_mfma_f32_16x16x32_bf16 v[196:199], a[52:55], v[52:55], v[196:199]\n"
"v_mfma_f32_16x16x32_bf16 v[200:203], a[40:43], v[48:51], v[200:203]\n"
"v_mfma_f32_16x16x32_bf16 v[200:203], a[56:59], v[52:55], v[200:203]\n"
"v_mfma_f32_16x16x32_bf16 v[204:207], a[44:47], v[48:51], v[204:207]\n"
"buffer_load_dwordx4 v40, s[40:43], s84 offen offset:3072 lds\n"
"v_mfma_f32_16x16x32_bf16 v[204:207], a[60:63], v[52:55], v[204:207]\n"
"s_waitcnt lgkmcnt(2)\n"
"v_mfma_f32_16x16x32_bf16 v[208:211], a[32:35], v[56:59], v[208:211]\n"
"v_mfma_f32_16x16x32_bf16 v[208:211], a[48:51], v[60:63], v[208:211]\n"
"s_add_u32 m0, s30, 0x2000\n"
"v_mfma_f32_16x16x32_bf16 v[212:215], a[36:39], v[56:59], v[212:215]\n"
"buffer_load_dwordx4 v32, s[36:39], s73 offen offset:0 lds\n"
"v_mfma_f32_16x16x32_bf16 v[212:215], a[52:55], v[60:63], v[212:215]\n"
"v_mfma_f32_16x16x32_bf16 v[216:219], a[40:43], v[56:59], v[216:219]\n"
"v_mfma_f32_16x16x32_bf16 v[216:219], a[56:59], v[60:63], v[216:219]\n"
"s_getpc_b64 s[98:99]\n"
"v_mfma_f32_16x16x32_bf16 v[220:223], a[44:47], v[56:59], v[220:223]\n"
"s_add_u32 s90, s72, 10\n"
"buffer_load_dwordx4 v33, s[36:39], s73 offen offset:1024 lds\n"
"v_mfma_f32_16x16x32_bf16 v[220:223], a[60:63], v[60:63], v[220:223]\n"
"s_and_b32 s90, s90, 15\n"
"v_mfma_f32_16x16x32_bf16 v[224:227], a[32:35], v[64:67], v[224:227]\n"
"s_lshl_b32 s90, s90, 19\n"
"global_load_dword v26, v0, s[98:99] offset:1536\n"
"v_mfma_f32_16x16x32_bf16 v[224:227], a[48:51], v[68:71], v[224:227]\n"
"s_waitcnt vmcnt(19) lgkmcnt(0)\n"
"s_barrier\n"
"v_mfma_f32_16x16x32_bf16 v[228:231], a[36:39], v[64:67], v[228:231]\n"
"buffer_load_dwordx4 v34, s[36:39], s73 offen offset:2048 lds\n"
"ds_read_b128 v[48:51], v27 offset:12288\n"
"v_mfma_f32_16x16x32_bf16 v[228:231], a[52:55], v[68:71], v[228:231]\n"
"ds_read_b128 v[52:55], v27 offset:12800\n"
"ds_read_b128 v[56:59], v27 offset:14336\n"
"v_mfma_f32_16x16x32_bf16 v[232:235], a[40:43], v[64:67], v[232:235]\n"
"ds_read_b128 v[60:63], v27 offset:14848\n"
"v_mfma_f32_16x16x32_bf16 v[232:235], a[56:59], v[68:71], v[232:235]\n"
"v_mfma_f32_16x16x32_bf16 v[236:239], a[44:47], v[64:67], v[236:239]\n"
"buffer_load_dwordx4 v35, s[36:39], s73 offen offset:3072 lds\n"
"ds_read_b128 v[64:67], v41 offset:12288\n"
"v_mfma_f32_16x16x32_bf16 v[236:239], a[60:63], v[68:71], v[236:239]\n"
"ds_read_b128 v[68:71], v41 offset:12800\n"
"v_mfma_f32_16x16x32_bf16 v[240:243], a[32:35], v[72:75], v[240:243]\n"
"s_waitcnt vmcnt(17)\n"
"ds_read_b64_tr_b16 a[32:33], v45 offset:24576\n"
"v_mfma_f32_16x16x32_bf16 v[240:243], a[48:51], v[76:79], v[240:243]\n"
"s_add_u32 m0, s30, 0x4000\n"
"ds_read_b64_tr_b16 a[34:35], v45 offset:24832\n"
"v_mfma_f32_16x16x32_bf16 v[244:247], a[36:39], v[72:75], v[244:247]\n"
"buffer_load_dwordx4 v37, s[40:43], s90 offen offset:0 lds\n"
"ds_read_b64_tr_b16 a[36:37], v46 offset:24576\n"
"v_mfma_f32_16x16x32_bf16 v[244:247], a[52:55], v[76:79], v[244:247]\n"
"ds_read_b64_tr_b16 a[38:39], v46 offset:24832\n"
"v_mfma_f32_16x16x32_bf16 v[248:251], a[40:43], v[72:75], v[248:251]\n"
"ds_read_b64_tr_b16 a[40:41], v45 offset:25088\n"
"v_mfma_f32_16x16x32_bf16 v[248:251], a[56:59], v[76:79], v[248:251]\n"
"ds_read_b64_tr_b16 a[42:43], v45 offset:25344\n"
"v_mfma_f32_16x16x32_bf16 v[252:255], a[44:47], v[72:75], v[252:255]\n"
"buffer_load_dwordx4 v38, s[40:43], s90 offen offset:1024 lds\n"
"ds_read_b128 v[72:75], v41 offset:14336\n"
"v_mfma_f32_16x16x32_bf16 v[252:255], a[60:63], v[76:79], v[252:255]\n"
"s_waitcnt lgkmcnt(9)\n"
"ds_read_b128 v[76:79], v41 offset:14848\n"
"v_mfma_f32_16x16x32_bf16 v[112:115], a[0:3], v[48:51], v[112:115]\n"
"ds_read_b64_tr_b16 a[44:45], v46 offset:25088\n"
"v_mfma_f32_16x16x32_bf16 v[112:115], a[16:19], v[52:55], v[112:115]\n"
"ds_read_b64_tr_b16 a[46:47], v46 offset:25344\n"
"v_mfma_f32_16x16x32_bf16 v[116:119], a[4:7], v[48:51], v[116:119]\n"
"buffer_load_dwordx4 v39, s[40:43], s90 offen offset:2048 lds\n"
"v_mfma_f32_16x16x32_bf16 v[116:119], a[20:23], v[52:55], v[116:119]\n"
"v_mfma_f32_16x16x32_bf16 v[120:123], a[8:11], v[48:51], v[120:123]\n"
"v_mfma_f32_16x16x32_bf16 v[120:123], a[24:27], v[52:55], v[120:123]\n"
"v_mfma_f32_16x16x32_bf16 v[124:127], a[12:15], v[48:51], v[124:127]\n"
"buffer_load_dwordx4 v40, s[40:43], s90 offen offset:3072 lds\n"
"ds_read_b128 v[48:51], v42 offset:12288\n"
"v_mfma_f32_16x16x32_bf16 v[124:127], a[28:31], v[52:55], v[124:127]\n"
"ds_read_b128 v[52:55], v42 offset:12800\n"
"v_mfma_f32_16x16x32_bf16 v[128:131], a[0:3], v[56:59], v[128:131]\n"
"v_mfma_f32_16x16x32_bf16 v[128:131], a[16:19], v[60:63], v[128:131]\n"
"s_add_u32 s75, s90, 0x40000\n"
"s_add_u32 m0, s30, 0x5000\n"
"v_mfma_f32_16x16x32_bf16 v[132:135], a[4:7], v[56:59], v[132:135]\n"
"buffer_load_dwordx4 v37, s[40:43], s75 offen offset:0 lds\n"
"v_mfma_f32_16x16x32_bf16 v[132:135], a[20:23], v[60:63], v[132:135]\n"
"v_mfma_f32_16x16x32_bf16 v[136:139], a[8:11], v[56:59], v[136:139]\n"
"v_mfma_f32_16x16x32_bf16 v[136:139], a[24:27], v[60:63], v[136:139]\n"
"s_add_u32 s88, s72, 10\n"
"v_mfma_f32_16x16x32_bf16 v[140:143], a[12:15], v[56:59], v[140:143]\n"
"s_and_b32 s88, s88, 15\n"
"buffer_load_dwordx4 v38, s[40:43], s75 offen offset:1024 lds\n"
"ds_read_b128 v[56:59], v42 offset:14336\n"
"v_mfma_f32_16x16x32_bf16 v[140:143], a[28:31], v[60:63], v[140:143]\n"
"s_waitcnt lgkmcnt(3)\n"
"s_lshl_b32 s88, s88, 7\n"
"ds_read_b128 v[60:63], v42 offset:14848\n"
"v_mfma_f32_16x16x32_bf16 v[144:147], a[0:3], v[64:67], v[144:147]\n"
"v_mfma_f32_16x16x32_bf16 v[144:147], a[16:19], v[68:71], v[144:147]\n"
"buffer_load_dwordx4 v39, s[40:43], s75 offen offset:2048 lds\n"
"v_mfma_f32_16x16x32_bf16 v[148:151], a[4:7], v[64:67], v[148:151]\n"
"v_mfma_f32_16x16x32_bf16 v[148:151], a[20:23], v[68:71], v[148:151]\n"
"v_mfma_f32_16x16x32_bf16 v[152:155], a[8:11], v[64:67], v[152:155]\n"
"v_mfma_f32_16x16x32_bf16 v[152:155], a[24:27], v[68:71], v[152:155]\n"
"buffer_load_dwordx4 v40, s[40:43], s75 offen offset:3072 lds\n"
"v_mfma_f32_16x16x32_bf16 v[156:159], a[12:15], v[64:67], v[156:159]\n"
"ds_read_b128 v[64:67], v43 offset:12288\n"
"v_mfma_f32_16x16x32_bf16 v[156:159], a[28:31], v[68:71], v[156:159]\n"
"ds_read_b128 v[68:71], v43 offset:12800\n"
"v_mfma_f32_16x16x32_bf16 v[160:163], a[0:3], v[72:75], v[160:163]\n"
"s_waitcnt vmcnt(13)\n"
"s_add_u32 m0, s30, 0x8000\n"
"ds_read_b64_tr_b16 a[48:49], v45 offset:28672\n"
"v_mfma_f32_16x16x32_bf16 v[160:163], a[16:19], v[76:79], v[160:163]\n"
"buffer_load_dwordx4 v32, s[36:39], s88 offen offset:0 lds\n"
"ds_read_b64_tr_b16 a[50:51], v45 offset:28928\n"
"v_mfma_f32_16x16x32_bf16 v[164:167], a[4:7], v[72:75], v[164:167]\n"
"ds_read_b64_tr_b16 a[52:53], v46 offset:28672\n"
"v_mfma_f32_16x16x32_bf16 v[164:167], a[20:23], v[76:79], v[164:167]\n"
"ds_read_b64_tr_b16 a[54:55], v46 offset:28928\n"
"v_mfma_f32_16x16x32_bf16 v[168:171], a[8:11], v[72:75], v[168:171]\n"
"ds_read_b64_tr_b16 a[56:57], v45 offset:29184\n"
"v_mfma_f32_16x16x32_bf16 v[168:171], a[24:27], v[76:79], v[168:171]\n"
"buffer_load_dwordx4 v33, s[36:39], s88 offen offset:1024 lds\n"
"ds_read_b64_tr_b16 a[58:59], v45 offset:29440\n"
"v_mfma_f32_16x16x32_bf16 v[172:175], a[12:15], v[72:75], v[172:175]\n"
"ds_read_b128 v[72:75], v43 offset:14336\n"
"v_mfma_f32_16x16x32_bf16 v[172:175], a[28:31], v[76:79], v[172:175]\n"
"s_waitcnt lgkmcnt(9)\n"
"ds_read_b128 v[76:79], v43 offset:14848\n"
"v_mfma_f32_16x16x32_bf16 v[176:179], a[0:3], v[48:51], v[176:179]\n"
"ds_read_b64_tr_b16 a[60:61], v46 offset:29184\n"
"v_mfma_f32_16x16x32_bf16 v[176:179], a[16:19], v[52:55], v[176:179]\n"
"s_add_u32 s91, s72, 11\n"
"buffer_load_dwordx4 v34, s[36:39], s88 offen offset:2048 lds\n"
"ds_read_b64_tr_b16 a[62:63], v46 offset:29440\n"
"v_mfma_f32_16x16x32_bf16 v[180:183], a[4:7], v[48:51], v[180:183]\n"
"s_and_b32 s91, s91, 15\n"
"v_mfma_f32_16x16x32_bf16 v[180:183], a[20:23], v[52:55], v[180:183]\n"
"s_lshl_b32 s91, s91, 19\n"
"v_mfma_f32_16x16x32_bf16 v[184:187], a[8:11], v[48:51], v[184:187]\n"
"v_mfma_f32_16x16x32_bf16 v[184:187], a[24:27], v[52:55], v[184:187]\n"
"buffer_load_dwordx4 v35, s[36:39], s88 offen offset:3072 lds\n"
"v_mfma_f32_16x16x32_bf16 v[188:191], a[12:15], v[48:51], v[188:191]\n"
"ds_read_b128 v[48:51], v44 offset:0\n"
"v_mfma_f32_16x16x32_bf16 v[188:191], a[28:31], v[52:55], v[188:191]\n"
"ds_read_b128 v[52:55], v44 offset:512\n"
"v_mfma_f32_16x16x32_bf16 v[192:195], a[0:3], v[56:59], v[192:195]\n"
"s_add_u32 m0, s30, 0x9400\n"
"v_mfma_f32_16x16x32_bf16 v[192:195], a[16:19], v[60:63], v[192:195]\n"
"buffer_load_dwordx4 v36, s[36:39], s88 offen offset:0 lds\n"
"v_mfma_f32_16x16x32_bf16 v[196:199], a[4:7], v[56:59], v[196:199]\n"
"v_mfma_f32_16x16x32_bf16 v[196:199], a[20:23], v[60:63], v[196:199]\n"
"v_mfma_f32_16x16x32_bf16 v[200:203], a[8:11], v[56:59], v[200:203]\n"
"s_add_u32 m0, s30, 0x6000\n"
"v_mfma_f32_16x16x32_bf16 v[200:203], a[24:27], v[60:63], v[200:203]\n"
"buffer_load_dwordx4 v37, s[40:43], s91 offen offset:0 lds\n"
"v_mfma_f32_16x16x32_bf16 v[204:207], a[12:15], v[56:59], v[204:207]\n"
"v_mfma_f32_16x16x32_bf16 v[204:207], a[28:31], v[60:63], v[204:207]\n"
"s_waitcnt lgkmcnt(2)\n"
"v_mfma_f32_16x16x32_bf16 v[208:211], a[0:3], v[64:67], v[208:211]\n"
"v_mfma_f32_16x16x32_bf16 v[208:211], a[16:19], v[68:71], v[208:211]\n"
"buffer_load_dwordx4 v38, s[40:43], s91 offen offset:1024 lds\n"
"v_mfma_f32_16x16x32_bf16 v[212:215], a[4:7], v[64:67], v[212:215]\n"
"v_mfma_f32_16x16x32_bf16 v[212:215], a[20:23], v[68:71], v[212:215]\n"
"v_mfma_f32_16x16x32_bf16 v[216:219], a[8:11], v[64:67], v[216:219]\n"
"v_mfma_f32_16x16x32_bf16 v[216:219], a[24:27], v[68:71], v[216:219]\n"
"buffer_load_dwordx4 v39, s[40:43], s91 offen offset:2048 lds\n"
"v_mfma_f32_16x16x32_bf16 v[220:223], a[12:15], v[64:67], v[220:223]\n"
"v_mfma_f32_16x16x32_bf16 v[220:223], a[28:31], v[68:71], v[220:223]\n"
"v_mfma_f32_16x16x32_bf16 v[224:227], a[0:3], v[72:75], v[224:227]\n"
"v_mfma_f32_16x16x32_bf16 v[224:227], a[16:19], v[76:79], v[224:227]\n"
"s_waitcnt vmcnt(16) lgkmcnt(0)\n"
"s_barrier\n"
"buffer_load_dwordx4 v40, s[40:43], s91 offen offset:3072 lds\n"
"v_mfma_f32_16x16x32_bf16 v[228:231], a[4:7], v[72:75], v[228:231]\n"
"ds_read_b128 v[56:59], v27 offset:8192\n"
"ds_read_b128 v[60:63], v27 offset:8704\n"
"v_mfma_f32_16x16x32_bf16 v[228:231], a[20:23], v[76:79], v[228:231]\n"
"ds_read_b128 v[64:67], v27 offset:10240\n"
"ds_read_b128 v[68:71], v27 offset:10752\n"
"v_mfma_f32_16x16x32_bf16 v[232:235], a[8:11], v[72:75], v[232:235]\n"
"s_add_u32 s76, s91, 0x40000\n"
"s_add_u32 m0, s30, 0x7000\n"
"v_mfma_f32_16x16x32_bf16 v[232:235], a[24:27], v[76:79], v[232:235]\n"
"buffer_load_dwordx4 v37, s[40:43], s76 offen offset:0 lds\n"
"v_mfma_f32_16x16x32_bf16 v[236:239], a[12:15], v[72:75], v[236:239]\n"
"ds_read_b128 v[72:75], v41 offset:8192\n"
"v_mfma_f32_16x16x32_bf16 v[236:239], a[28:31], v[76:79], v[236:239]\n"
"ds_read_b128 v[76:79], v41 offset:8704\n"
"v_mfma_f32_16x16x32_bf16 v[240:243], a[0:3], v[48:51], v[240:243]\n"
"s_waitcnt vmcnt(14)\n"
"ds_read_b64_tr_b16 a[0:1], v45 offset:16384\n"
"v_mfma_f32_16x16x32_bf16 v[240:243], a[16:19], v[52:55], v[240:243]\n"
"buffer_load_dwordx4 v38, s[40:43], s76 offen offset:1024 lds\n"
"ds_read_b64_tr_b16 a[2:3], v45 offset:16640\n"
"v_mfma_f32_16x16x32_bf16 v[244:247], a[4:7], v[48:51], v[244:247]\n"
"ds_read_b64_tr_b16 a[4:5], v46 offset:16384\n"
"v_mfma_f32_16x16x32_bf16 v[244:247], a[20:23], v[52:55], v[244:247]\n"
"buffer_load_dwordx4 a[128:131], v19, s[48:51], s77 offen offset:0 nt\n"
"ds_read_b64_tr_b16 a[6:7], v46 offset:16640\n"
"v_mfma_f32_16x16x32_bf16 v[248:251], a[8:11], v[48:51], v[248:251]\n"
"ds_read_b64_tr_b16 a[8:9], v45 offset:16896\n"
"v_mfma_f32_16x16x32_bf16 v[248:251], a[24:27], v[52:55], v[248:251]\n"
"ds_read_b64_tr_b16 a[10:11], v45 offset:17152\n"
"v_mfma_f32_16x16x32_bf16 v[252:255], a[12:15], v[48:51], v[252:255]\n"
"ds_read_b128 v[48:51], v41 offset:10240\n"
"v_mfma_f32_16x16x32_bf16 v[252:255], a[28:31], v[52:55], v[252:255]\n"
"s_waitcnt lgkmcnt(9)\n"
"buffer_load_dwordx4 a[132:135], v20, s[48:51], s77 offen offset:1024 nt\n"
"ds_read_b128 v[52:55], v41 offset:10752\n"
"v_mfma_f32_16x16x32_bf16 v[112:115], a[32:35], v[56:59], v[112:115]\n"
"s_waitcnt vmcnt(13)\n"
"ds_read_b64_tr_b16 a[16:17], v45 offset:20480\n"
"v_mfma_f32_16x16x32_bf16 v[112:115], a[48:51], v[60:63], v[112:115]\n"
"ds_read_b64_tr_b16 a[18:19], v45 offset:20736\n"
"v_mfma_f32_16x16x32_bf16 v[116:119], a[36:39], v[56:59], v[116:119]\n"
"ds_read_b64_tr_b16 a[20:21], v46 offset:20480\n"
"v_mfma_f32_16x16x32_bf16 v[116:119], a[52:55], v[60:63], v[116:119]\n"
"buffer_load_dwordx4 a[136:139], v19, s[48:51], s77 offen offset:2048 nt\n"
"ds_read_b64_tr_b16 a[22:23], v46 offset:20736\n"
"v_mfma_f32_16x16x32_bf16 v[120:123], a[40:43], v[56:59], v[120:123]\n"
"ds_read_b64_tr_b16 a[24:25], v45 offset:20992\n"
"v_mfma_f32_16x16x32_bf16 v[120:123], a[56:59], v[60:63], v[120:123]\n"
"ds_read_b64_tr_b16 a[26:27], v45 offset:21248\n"
"v_mfma_f32_16x16x32_bf16 v[124:127], a[44:47], v[56:59], v[124:127]\n"
"ds_read_b128 v[56:59], v42 offset:8192\n"
"v_mfma_f32_16x16x32_bf16 v[124:127], a[60:63], v[60:63], v[124:127]\n"
"buffer_load_dwordx4 a[140:143], v20, s[48:51], s77 offen offset:3072 nt\n"
"ds_read_b128 v[60:63], v42 offset:8704\n"
"v_mfma_f32_16x16x32_bf16 v[128:131], a[32:35], v[64:67], v[128:131]\n"
"ds_read_b64_tr_b16 a[12:13], v46 offset:16896\n"
"v_mfma_f32_16x16x32_bf16 v[128:131], a[48:51], v[68:71], v[128:131]\n"
"ds_read_b64_tr_b16 a[14:15], v46 offset:17152\n"
"v_mfma_f32_16x16x32_bf16 v[132:135], a[36:39], v[64:67], v[132:135]\n"
"s_mov_b32 s78, 0xc000\n"
"ds_read_b64_tr_b16 a[28:29], v46 offset:20992\n"
"v_mfma_f32_16x16x32_bf16 v[132:135], a[52:55], v[68:71], v[132:135]\n"
"buffer_load_dwordx4 a[144:147], v17, s[44:47], s78 offen offset:0 nt\n"
"ds_read_b64_tr_b16 a[30:31], v46 offset:21248\n"
"v_mfma_f32_16x16x32_bf16 v[136:139], a[40:43], v[64:67], v[136:139]\n"
"v_mfma_f32_16x16x32_bf16 v[136:139], a[56:59], v[68:71], v[136:139]\n"
"v_mfma_f32_16x16x32_bf16 v[140:143], a[44:47], v[64:67], v[140:143]\n"
"ds_read_b128 v[64:67], v42 offset:10240\n"
"v_mfma_f32_16x16x32_bf16 v[140:143], a[60:63], v[68:71], v[140:143]\n"
"s_waitcnt lgkmcnt(8)\n"
"buffer_load_dwordx4 a[148:151], v17, s[44:47], s78 offen offset:1024 nt\n"
"ds_read_b128 v[68:71], v42 offset:10752\n"
"v_mfma_f32_16x16x32_bf16 v[144:147], a[32:35], v[72:75], v[144:147]\n"
"v_mfma_f32_16x16x32_bf16 v[144:147], a[48:51], v[76:79], v[144:147]\n"
"v_mfma_f32_16x16x32_bf16 v[148:151], a[36:39], v[72:75], v[148:151]\n"
"v_mfma_f32_16x16x32_bf16 v[148:151], a[52:55], v[76:79], v[148:151]\n"
"buffer_load_dwordx4 a[152:155], v18, s[44:47], s78 offen offset:2048 nt\n"
"v_mfma_f32_16x16x32_bf16 v[152:155], a[40:43], v[72:75], v[152:155]\n"
"v_mfma_f32_16x16x32_bf16 v[152:155], a[56:59], v[76:79], v[152:155]\n"
"v_mfma_f32_16x16x32_bf16 v[156:159], a[44:47], v[72:75], v[156:159]\n"
"ds_read_b128 v[72:75], v43 offset:8192\n"
"v_mfma_f32_16x16x32_bf16 v[156:159], a[60:63], v[76:79], v[156:159]\n"
"buffer_load_dwordx4 a[156:159], v18, s[44:47], s78 offen offset:3072 nt\n"
"ds_read_b128 v[76:79], v43 offset:8704\n"
"v_mfma_f32_16x16x32_bf16 v[160:163], a[32:35], v[48:51], v[160:163]\n"
"v_mfma_f32_16x16x32_bf16 v[160:163], a[48:51], v[52:55], v[160:163]\n"
"v_mfma_f32_16x16x32_bf16 v[164:167], a[36:39], v[48:51], v[164:167]\n"
"v_mfma_f32_16x16x32_bf16 v[164:167], a[52:55], v[52:55], v[164:167]\n"
"v_mfma_f32_16x16x32_bf16 v[168:171], a[40:43], v[48:51], v[168:171]\n"
"v_mfma_f32_16x16x32_bf16 v[168:171], a[56:59], v[52:55], v[168:171]\n"
"v_mfma_f32_16x16x32_bf16 v[172:175], a[44:47], v[48:51], v[172:175]\n"
"ds_read_b128 v[48:51], v43 offset:10240\n"
"v_mfma_f32_16x16x32_bf16 v[172:175], a[60:63], v[52:55], v[172:175]\n"
"s_waitcnt lgkmcnt(3)\n"
"ds_read_b128 v[52:55], v43 offset:10752\n"
"v_mfma_f32_16x16x32_bf16 v[176:179], a[32:35], v[56:59], v[176:179]\n"
"v_mfma_f32_16x16x32_bf16 v[176:179], a[48:51], v[60:63], v[176:179]\n"
"v_mfma_f32_16x16x32_bf16 v[180:183], a[36:39], v[56:59], v[180:183]\n"
"v_mfma_f32_16x16x32_bf16 v[180:183], a[52:55], v[60:63], v[180:183]\n"
"v_mfma_f32_16x16x32_bf16 v[184:187], a[40:43], v[56:59], v[184:187]\n"
"v_mfma_f32_16x16x32_bf16 v[184:187], a[56:59], v[60:63], v[184:187]\n"
"v_mfma_f32_16x16x32_bf16 v[188:191], a[44:47], v[56:59], v[188:191]\n"
"ds_read_b128 v[56:59], v30 offset:0\n"
"v_mfma_f32_16x16x32_bf16 v[188:191], a[60:63], v[60:63], v[188:191]\n"
"ds_read_b128 v[60:63], v30 offset:512\n"
"v_mfma_f32_16x16x32_bf16 v[192:195], a[32:35], v[64:67], v[192:195]\n"
"v_mfma_f32_16x16x32_bf16 v[192:195], a[48:51], v[68:71], v[192:195]\n"
"v_mfma_f32_16x16x32_bf16 v[196:199], a[36:39], v[64:67], v[196:199]\n"
"v_mfma_f32_16x16x32_bf16 v[196:199], a[52:55], v[68:71], v[196:199]\n"
"s_add_u32 s89, s72, 11\n"
"v_mfma_f32_16x16x32_bf16 v[200:203], a[40:43], v[64:67], v[200:203]\n"
"s_and_b32 s89, s89, 15\n"
"v_mfma_f32_16x16x32_bf16 v[200:203], a[56:59], v[68:71], v[200:203]\n"
"s_lshl_b32 s89, s89, 7\n"
"v_mfma_f32_16x16x32_bf16 v[204:207], a[44:47], v[64:67], v[204:207]\n"
"v_mfma_f32_16x16x32_bf16 v[204:207], a[60:63], v[68:71], v[204:207]\n"
"s_waitcnt lgkmcnt(2)\n"
"buffer_load_dwordx4 v39, s[40:43], s76 offen offset:2048 lds\n"
"v_mfma_f32_16x16x32_bf16 v[208:211], a[32:35], v[72:75], v[208:211]\n"
"v_mfma_f32_16x16x32_bf16 v[208:211], a[48:51], v[76:79], v[208:211]\n"
"v_mfma_f32_16x16x32_bf16 v[212:215], a[36:39], v[72:75], v[212:215]\n"
"v_mfma_f32_16x16x32_bf16 v[212:215], a[52:55], v[76:79], v[212:215]\n"
"buffer_load_dwordx4 v40, s[40:43], s76 offen offset:3072 lds\n"
"v_mfma_f32_16x16x32_bf16 v[216:219], a[40:43], v[72:75], v[216:219]\n"
"v_mfma_f32_16x16x32_bf16 v[216:219], a[56:59], v[76:79], v[216:219]\n"
"v_mfma_f32_16x16x32_bf16 v[220:223], a[44:47], v[72:75], v[220:223]\n"
"s_add_u32 m0, s30, 0x3000\n"
"v_mfma_f32_16x16x32_bf16 v[220:223], a[60:63], v[76:79], v[220:223]\n"
"buffer_load_dwordx4 v32, s[36:39], s89 offen offset:0 lds\n"
"v_mfma_f32_16x16x32_bf16 v[224:227], a[32:35], v[48:51], v[224:227]\n"
"v_mfma_f32_16x16x32_bf16 v[224:227], a[48:51], v[52:55], v[224:227]\n"
"s_waitcnt vmcnt(17) lgkmcnt(0)\n"
"s_barrier\n"
"v_mfma_f32_16x16x32_bf16 v[228:231], a[36:39], v[48:51], v[228:231]\n"
"ds_read_b128 v[64:67], v27 offset:32768\n"
"ds_read_b128 v[68:71], v27 offset:33280\n"
"v_mfma_f32_16x16x32_bf16 v[228:231], a[52:55], v[52:55], v[228:231]\n"
"s_add_u32 s74, s72, 12\n"
"buffer_load_dwordx4 v33, s[36:39], s89 offen offset:1024 lds\n"
"ds_read_b128 v[72:75], v27 offset:34816\n"
"v_mfma_f32_16x16x32_bf16 v[232:235], a[40:43], v[48:51], v[232:235]\n"
"s_and_b32 s74, s74, 15\n"
"ds_read_b128 v[76:79], v27 offset:35328\n"
"v_mfma_f32_16x16x32_bf16 v[232:235], a[56:59], v[52:55], v[232:235]\n"
"s_lshl_b32 s74, s74, 19\n"
"v_mfma_f32_16x16x32_bf16 v[236:239], a[44:47], v[48:51], v[236:239]\n"
"ds_read_b128 v[48:51], v41 offset:32768\n"
"v_mfma_f32_16x16x32_bf16 v[236:239], a[60:63], v[52:55], v[236:239]\n"
"buffer_load_dwordx4 v34, s[36:39], s89 offen offset:2048 lds\n"
"ds_read_b128 v[52:55], v41 offset:33280\n"
"v_mfma_f32_16x16x32_bf16 v[240:243], a[32:35], v[56:59], v[240:243]\n"
"s_waitcnt vmcnt(15)\n"
"ds_read_b64_tr_b16 a[32:33], v45 offset:24576\n"
"v_mfma_f32_16x16x32_bf16 v[240:243], a[48:51], v[60:63], v[240:243]\n"
"ds_read_b64_tr_b16 a[34:35], v45 offset:24832\n"
"v_mfma_f32_16x16x32_bf16 v[244:247], a[36:39], v[56:59], v[244:247]\n"
"ds_read_b64_tr_b16 a[36:37], v46 offset:24576\n"
"v_mfma_f32_16x16x32_bf16 v[244:247], a[52:55], v[60:63], v[244:247]\n"
"buffer_load_dwordx4 v35, s[36:39], s89 offen offset:3072 lds\n"
"ds_read_b64_tr_b16 a[38:39], v46 offset:24832\n"
"v_mfma_f32_16x16x32_bf16 v[248:251], a[40:43], v[56:59], v[248:251]\n"
"ds_read_b64_tr_b16 a[40:41], v45 offset:25088\n"
"v_mfma_f32_16x16x32_bf16 v[248:251], a[56:59], v[60:63], v[248:251]\n"
"ds_read_b64_tr_b16 a[42:43], v45 offset:25344\n"
"v_mfma_f32_16x16x32_bf16 v[252:255], a[44:47], v[56:59], v[252:255]\n"
"s_add_u32 m0, s30, 0x4000\n"
"ds_read_b128 v[56:59], v41 offset:34816\n"
"v_mfma_f32_16x16x32_bf16 v[252:255], a[60:63], v[60:63], v[252:255]\n"
"s_waitcnt lgkmcnt(9)\n"
"buffer_load_dwordx4 v37, s[40:43], s74 offen offset:0 lds\n"
"ds_read_b128 v[60:63], v41 offset:35328\n"
"v_mfma_f32_16x16x32_bf16 v[112:115], a[0:3], v[64:67], v[112:115]\n"
"ds_read_b64_tr_b16 a[44:45], v46 offset:25088\n"
"v_mfma_f32_16x16x32_bf16 v[112:115], a[16:19], v[68:71], v[112:115]\n"
"ds_read_b64_tr_b16 a[46:47], v46 offset:25344\n"
"v_mfma_f32_16x16x32_bf16 v[116:119], a[4:7], v[64:67], v[116:119]\n"
"v_mfma_f32_16x16x32_bf16 v[116:119], a[20:23], v[68:71], v[116:119]\n"
"buffer_load_dwordx4 v38, s[40:43], s74 offen offset:1024 lds\n"
"v_mfma_f32_16x16x32_bf16 v[120:123], a[8:11], v[64:67], v[120:123]\n"
"v_mfma_f32_16x16x32_bf16 v[120:123], a[24:27], v[68:71], v[120:123]\n"
"v_mfma_f32_16x16x32_bf16 v[124:127], a[12:15], v[64:67], v[124:127]\n"
"ds_read_b128 v[64:67], v42 offset:32768\n"
"v_mfma_f32_16x16x32_bf16 v[124:127], a[28:31], v[68:71], v[124:127]\n"
"buffer_load_dwordx4 v39, s[40:43], s74 offen offset:2048 lds\n"
"ds_read_b128 v[68:71], v42 offset:33280\n"
"v_mfma_f32_16x16x32_bf16 v[128:131], a[0:3], v[72:75], v[128:131]\n"
"v_mfma_f32_16x16x32_bf16 v[128:131], a[16:19], v[76:79], v[128:131]\n"
"v_mfma_f32_16x16x32_bf16 v[132:135], a[4:7], v[72:75], v[132:135]\n"
"v_mfma_f32_16x16x32_bf16 v[132:135], a[20:23], v[76:79], v[132:135]\n"
"buffer_load_dwordx4 v40, s[40:43], s74 offen offset:3072 lds\n"
"v_mfma_f32_16x16x32_bf16 v[136:139], a[8:11], v[72:75], v[136:139]\n"
"v_mfma_f32_16x16x32_bf16 v[136:139], a[24:27], v[76:79], v[136:139]\n"
"v_mfma_f32_16x16x32_bf16 v[140:143], a[12:15], v[72:75], v[140:143]\n"
"s_add_u32 s96, s74, 0x40000\n"
"s_add_u32 m0, s30, 0x5000\n"
"ds_read_b128 v[72:75], v42 offset:34816\n"
"v_mfma_f32_16x16x32_bf16 v[140:143], a[28:31], v[76:79], v[140:143]\n"
"s_waitcnt lgkmcnt(3)\n"
"buffer_load_dwordx4 v37, s[40:43], s96 offen offset:0 lds\n"
"ds_read_b128 v[76:79], v42 offset:35328\n"
"v_mfma_f32_16x16x32_bf16 v[144:147], a[0:3], v[48:51], v[144:147]\n"
"v_mfma_f32_16x16x32_bf16 v[144:147], a[16:19], v[52:55], v[144:147]\n"
"v_mfma_f32_16x16x32_bf16 v[148:151], a[4:7], v[48:51], v[148:151]\n"
"s_add_u32 s73, s72, 12\n"
"buffer_load_dwordx4 v38, s[40:43], s96 offen offset:1024 lds\n"
"v_mfma_f32_16x16x32_bf16 v[148:151], a[20:23], v[52:55], v[148:151]\n"
"s_and_b32 s73, s73, 15\n"
"v_mfma_f32_16x16x32_bf16 v[152:155], a[8:11], v[48:51], v[152:155]\n"
"s_lshl_b32 s73, s73, 7\n"
"v_mfma_f32_16x16x32_bf16 v[152:155], a[24:27], v[52:55], v[152:155]\n"
"v_mfma_f32_16x16x32_bf16 v[156:159], a[12:15], v[48:51], v[156:159]\n"
"buffer_load_dwordx4 v39, s[40:43], s96 offen offset:2048 lds\n"
"ds_read_b128 v[48:51], v43 offset:32768\n"
"v_mfma_f32_16x16x32_bf16 v[156:159], a[28:31], v[52:55], v[156:159]\n"
"ds_read_b128 v[52:55], v43 offset:33280\n"
"v_mfma_f32_16x16x32_bf16 v[160:163], a[0:3], v[56:59], v[160:163]\n"
"v_mfma_f32_16x16x32_bf16 v[160:163], a[16:19], v[60:63], v[160:163]\n"
"v_mfma_f32_16x16x32_bf16 v[164:167], a[4:7], v[56:59], v[164:167]\n"
"buffer_load_dwordx4 v40, s[40:43], s96 offen offset:3072 lds\n"
"v_mfma_f32_16x16x32_bf16 v[164:167], a[20:23], v[60:63], v[164:167]\n"
"v_mfma_f32_16x16x32_bf16 v[168:171], a[8:11], v[56:59], v[168:171]\n"
"v_mfma_f32_16x16x32_bf16 v[168:171], a[24:27], v[60:63], v[168:171]\n"
"s_waitcnt vmcnt(12)\n"
"s_add_u32 m0, s30, 0x2000\n"
"ds_read_b64_tr_b16 a[48:49], v45 offset:28672\n"
"v_mfma_f32_16x16x32_bf16 v[172:175], a[12:15], v[56:59], v[172:175]\n"
"buffer_load_dwordx4 v32, s[36:39], s73 offen offset:0 lds\n"
"ds_read_b128 v[56:59], v43 offset:34816\n"
"v_mfma_f32_16x16x32_bf16 v[172:175], a[28:31], v[60:63], v[172:175]\n"
"s_waitcnt lgkmcnt(4)\n"
"ds_read_b128 v[60:63], v43 offset:35328\n"
"v_mfma_f32_16x16x32_bf16 v[176:179], a[0:3], v[64:67], v[176:179]\n"
"ds_read_b64_tr_b16 a[50:51], v45 offset:28928\n"
"v_mfma_f32_16x16x32_bf16 v[176:179], a[16:19], v[68:71], v[176:179]\n"
"ds_read_b64_tr_b16 a[52:53], v46 offset:28672\n"
"v_mfma_f32_16x16x32_bf16 v[180:183], a[4:7], v[64:67], v[180:183]\n"
"buffer_load_dwordx4 v33, s[36:39], s73 offen offset:1024 lds\n"
"ds_read_b64_tr_b16 a[54:55], v46 offset:28928\n"
"v_mfma_f32_16x16x32_bf16 v[180:183], a[20:23], v[68:71], v[180:183]\n"
"ds_read_b64_tr_b16 a[56:57], v45 offset:29184\n"
"v_mfma_f32_16x16x32_bf16 v[184:187], a[8:11], v[64:67], v[184:187]\n"
"ds_read_b64_tr_b16 a[58:59], v45 offset:29440\n"
"v_mfma_f32_16x16x32_bf16 v[184:187], a[24:27], v[68:71], v[184:187]\n"
"ds_read_b64_tr_b16 a[60:61], v46 offset:29184\n"
"v_mfma_f32_16x16x32_bf16 v[188:191], a[12:15], v[64:67], v[188:191]\n"
"s_add_u32 s90, s72, 13\n"
"buffer_load_dwordx4 v34, s[36:39], s73 offen offset:2048 lds\n"
"ds_read_b128 v[64:67], v44 offset:1024\n"
"v_mfma_f32_16x16x32_bf16 v[188:191], a[28:31], v[68:71], v[188:191]\n"
"s_and_b32 s90, s90, 15\n"
"ds_read_b128 v[68:71], v44 offset:1536\n"
"v_mfma_f32_16x16x32_bf16 v[192:195], a[0:3], v[72:75], v[192:195]\n"
"s_lshl_b32 s90, s90, 19\n"
"ds_read_b64_tr_b16 a[62:63], v46 offset:29440\n"
"v_mfma_f32_16x16x32_bf16 v[192:195], a[16:19], v[76:79], v[192:195]\n"
"v_mfma_f32_16x16x32_bf16 v[196:199], a[4:7], v[72:75], v[196:199]\n"
"buffer_load_dwordx4 v35, s[36:39], s73 offen offset:3072 lds\n"
"v_mfma_f32_16x16x32_bf16 v[196:199], a[20:23], v[76:79], v[196:199]\n"
"v_mfma_f32_16x16x32_bf16 v[200:203], a[8:11], v[72:75], v[200:203]\n"
"v_mfma_f32_16x16x32_bf16 v[200:203], a[24:27], v[76:79], v[200:203]\n"
"s_add_u32 m0, s30, 0x9000\n"
"v_mfma_f32_16x16x32_bf16 v[204:207], a[12:15], v[72:75], v[204:207]\n"
"buffer_load_dwordx4 v36, s[36:39], s73 offen offset:0 lds\n"
"v_mfma_f32_16x16x32_bf16 v[204:207], a[28:31], v[76:79], v[204:207]\n"
"s_waitcnt lgkmcnt(4)\n"
"v_mfma_f32_16x16x32_bf16 v[208:211], a[0:3], v[48:51], v[208:211]\n"
"v_mfma_f32_16x16x32_bf16 v[208:211], a[16:19], v[52:55], v[208:211]\n"
"s_add_u32 m0, s30, 0x6000\n"
"v_mfma_f32_16x16x32_bf16 v[212:215], a[4:7], v[48:51], v[212:215]\n"
"buffer_load_dwordx4 v37, s[40:43], s90 offen offset:0 lds\n"
"v_mfma_f32_16x16x32_bf16 v[212:215], a[20:23], v[52:55], v[212:215]\n"
"v_mfma_f32_16x16x32_bf16 v[216:219], a[8:11], v[48:51], v[216:219]\n"
"v_mfma_f32_16x16x32_bf16 v[216:219], a[24:27], v[52:55], v[216:219]\n"
"v_mfma_f32_16x16x32_bf16 v[220:223], a[12:15], v[48:51], v[220:223]\n"
"buffer_load_dwordx4 v38, s[40:43], s90 offen offset:1024 lds\n"
"v_mfma_f32_16x16x32_bf16 v[220:223], a[28:31], v[52:55], v[220:223]\n"
"v_mfma_f32_16x16x32_bf16 v[224:227], a[0:3], v[56:59], v[224:227]\n"
"v_mfma_f32_16x16x32_bf16 v[224:227], a[16:19], v[60:63], v[224:227]\n"
"s_waitcnt vmcnt(15) lgkmcnt(0)\n"
"s_barrier\n"
"v_mfma_f32_16x16x32_bf16 v[228:231], a[4:7], v[56:59], v[228:231]\n"
"buffer_load_dwordx4 v39, s[40:43], s90 offen offset:2048 lds\n"
"ds_read_b128 v[72:75], v27 offset:12288\n"
"v_mfma_f32_16x16x32_bf16 v[228:231], a[20:23], v[60:63], v[228:231]\n"
"ds_read_b128 v[76:79], v27 offset:12800\n"
"ds_read_b128 v[48:51], v27 offset:14336\n"
"v_mfma_f32_16x16x32_bf16 v[232:235], a[8:11], v[56:59], v[232:235]\n"
"ds_read_b128 v[52:55], v27 offset:14848\n"
"v_mfma_f32_16x16x32_bf16 v[232:235], a[24:27], v[60:63], v[232:235]\n"
"v_mfma_f32_16x16x32_bf16 v[236:239], a[12:15], v[56:59], v[236:239]\n"
"buffer_load_dwordx4 v40, s[40:43], s90 offen offset:3072 lds\n"
"ds_read_b128 v[56:59], v41 offset:12288\n"
"v_mfma_f32_16x16x32_bf16 v[236:239], a[28:31], v[60:63], v[236:239]\n"
"ds_read_b128 v[60:63], v41 offset:12800\n"
"v_mfma_f32_16x16x32_bf16 v[240:243], a[0:3], v[64:67], v[240:243]\n"
"s_waitcnt vmcnt(13)\n"
"ds_read_b64_tr_b16 a[0:1], v45 offset:16384\n"
"v_mfma_f32_16x16x32_bf16 v[240:243], a[16:19], v[68:71], v[240:243]\n"
"s_add_u32 s84, s90, 0x40000\n"
"s_add_u32 m0, s30, 0x7000\n"
"ds_read_b64_tr_b16 a[2:3], v45 offset:16640\n"
"v_mfma_f32_16x16x32_bf16 v[244:247], a[4:7], v[64:67], v[244:247]\n"
"buffer_load_dwordx4 v37, s[40:43], s84 offen offset:0 lds\n"
"ds_read_b64_tr_b16 a[4:5], v46 offset:16384\n"
"v_mfma_f32_16x16x32_bf16 v[244:247], a[20:23], v[68:71], v[244:247]\n"
"ds_read_b64_tr_b16 a[6:7], v46 offset:16640\n"
"v_mfma_f32_16x16x32_bf16 v[248:251], a[8:11], v[64:67], v[248:251]\n"
"buffer_load_dword v26, v22, s[52:55], s31 offen offset:0\n"
"ds_read_b64_tr_b16 a[8:9], v45 offset:16896\n"
"v_mfma_f32_16x16x32_bf16 v[248:251], a[24:27], v[68:71], v[248:251]\n"
"ds_read_b64_tr_b16 a[10:11], v45 offset:17152\n"
"v_mfma_f32_16x16x32_bf16 v[252:255], a[12:15], v[64:67], v[252:255]\n"
"ds_read_b128 v[64:67], v41 offset:14336\n"
"v_mfma_f32_16x16x32_bf16 v[252:255], a[28:31], v[68:71], v[252:255]\n"
"s_waitcnt lgkmcnt(9)\n"
"ds_read_b128 v[68:71], v41 offset:14848\n"
"v_mfma_f32_16x16x32_bf16 v[112:115], a[32:35], v[72:75], v[112:115]\n"
"buffer_load_dword v26, v22, s[52:55], s31 offen offset:1024\n"
"ds_read_b64_tr_b16 a[12:13], v46 offset:16896\n"
"v_mfma_f32_16x16x32_bf16 v[112:115], a[48:51], v[76:79], v[112:115]\n"
"ds_read_b64_tr_b16 a[14:15], v46 offset:17152\n"
"v_mfma_f32_16x16x32_bf16 v[116:119], a[36:39], v[72:75], v[116:119]\n"
"v_mfma_f32_16x16x32_bf16 v[116:119], a[52:55], v[76:79], v[116:119]\n"
"s_add_u32 s75, s31, 0x200000\n"
"v_mfma_f32_16x16x32_bf16 v[120:123], a[40:43], v[72:75], v[120:123]\n"
"s_waitcnt vmcnt(12)\n"
"buffer_load_dword v26, v22, s[52:55], s75 offen offset:0\n"
"ds_read_b64_tr_b16 a[16:17], v45 offset:20480\n"
"v_mfma_f32_16x16x32_bf16 v[120:123], a[56:59], v[76:79], v[120:123]\n"
"ds_read_b64_tr_b16 a[18:19], v45 offset:20736\n"
"v_mfma_f32_16x16x32_bf16 v[124:127], a[44:47], v[72:75], v[124:127]\n"
"ds_read_b128 v[72:75], v42 offset:12288\n"
"v_mfma_f32_16x16x32_bf16 v[124:127], a[60:63], v[76:79], v[124:127]\n"
"ds_read_b128 v[76:79], v42 offset:12800\n"
"v_mfma_f32_16x16x32_bf16 v[128:131], a[32:35], v[48:51], v[128:131]\n"
"buffer_load_dword v26, v22, s[52:55], s75 offen offset:1024\n"
"ds_read_b64_tr_b16 a[20:21], v46 offset:20480\n"
"v_mfma_f32_16x16x32_bf16 v[128:131], a[48:51], v[52:55], v[128:131]\n"
"ds_read_b64_tr_b16 a[22:23], v46 offset:20736\n"
"v_mfma_f32_16x16x32_bf16 v[132:135], a[36:39], v[48:51], v[132:135]\n"
"ds_read_b64_tr_b16 a[24:25], v45 offset:20992\n"
"v_mfma_f32_16x16x32_bf16 v[132:135], a[52:55], v[52:55], v[132:135]\n"
"s_add_u32 s77, s31, 0x40000\n"
"ds_read_b64_tr_b16 a[26:27], v45 offset:21248\n"
"v_mfma_f32_16x16x32_bf16 v[136:139], a[40:43], v[48:51], v[136:139]\n"
"buffer_load_dword v26, v22, s[52:55], s77 offen offset:0\n"
"ds_read_b64_tr_b16 a[28:29], v46 offset:20992\n"
"v_mfma_f32_16x16x32_bf16 v[136:139], a[56:59], v[52:55], v[136:139]\n"
"ds_read_b64_tr_b16 a[30:31], v46 offset:21248\n"
"v_mfma_f32_16x16x32_bf16 v[140:143], a[44:47], v[48:51], v[140:143]\n"
"ds_read_b128 v[48:51], v42 offset:14336\n"
"v_mfma_f32_16x16x32_bf16 v[140:143], a[60:63], v[52:55], v[140:143]\n"
"s_waitcnt lgkmcnt(11)\n"
"ds_read_b128 v[52:55], v42 offset:14848\n"
"v_mfma_f32_16x16x32_bf16 v[144:147], a[32:35], v[56:59], v[144:147]\n"
"buffer_load_dword v26, v22, s[52:55], s77 offen offset:1024\n"
"v_mfma_f32_16x16x32_bf16 v[144:147], a[48:51], v[60:63], v[144:147]\n"
"v_mfma_f32_16x16x32_bf16 v[148:151], a[36:39], v[56:59], v[148:151]\n"
"v_mfma_f32_16x16x32_bf16 v[148:151], a[52:55], v[60:63], v[148:151]\n"
"s_add_u32 s78, s31, 0x240000\n"
"v_mfma_f32_16x16x32_bf16 v[152:155], a[40:43], v[56:59], v[152:155]\n"
"buffer_load_dword v26, v22, s[52:55], s78 offen offset:0\n"
"v_mfma_f32_16x16x32_bf16 v[152:155], a[56:59], v[60:63], v[152:155]\n"
"v_mfma_f32_16x16x32_bf16 v[156:159], a[44:47], v[56:59], v[156:159]\n"
"ds_read_b128 v[56:59], v43 offset:12288\n"
"v_mfma_f32_16x16x32_bf16 v[156:159], a[60:63], v[60:63], v[156:159]\n"
"ds_read_b128 v[60:63], v43 offset:12800\n"
"v_mfma_f32_16x16x32_bf16 v[160:163], a[32:35], v[64:67], v[160:163]\n"
"buffer_load_dword v26, v22, s[52:55], s78 offen offset:1024\n"
"v_mfma_f32_16x16x32_bf16 v[160:163], a[48:51], v[68:71], v[160:163]\n"
"v_mfma_f32_16x16x32_bf16 v[164:167], a[36:39], v[64:67], v[164:167]\n"
"v_mfma_f32_16x16x32_bf16 v[164:167], a[52:55], v[68:71], v[164:167]\n"
"v_mfma_f32_16x16x32_bf16 v[168:171], a[40:43], v[64:67], v[168:171]\n"
"v_mfma_f32_16x16x32_bf16 v[168:171], a[56:59], v[68:71], v[168:171]\n"
"v_mfma_f32_16x16x32_bf16 v[172:175], a[44:47], v[64:67], v[172:175]\n"
"ds_read_b128 v[64:67], v43 offset:14336\n"
"v_mfma_f32_16x16x32_bf16 v[172:175], a[60:63], v[68:71], v[172:175]\n"
"s_waitcnt lgkmcnt(3)\n"
"ds_read_b128 v[68:71], v43 offset:14848\n"
"v_mfma_f32_16x16x32_bf16 v[176:179], a[32:35], v[72:75], v[176:179]\n"
"v_mfma_f32_16x16x32_bf16 v[176:179], a[48:51], v[76:79], v[176:179]\n"
"v_mfma_f32_16x16x32_bf16 v[180:183], a[36:39], v[72:75], v[180:183]\n"
"v_mfma_f32_16x16x32_bf16 v[180:183], a[52:55], v[76:79], v[180:183]\n"
"v_mfma_f32_16x16x32_bf16 v[184:187], a[40:43], v[72:75], v[184:187]\n"
"v_mfma_f32_16x16x32_bf16 v[184:187], a[56:59], v[76:79], v[184:187]\n"
"v_mfma_f32_16x16x32_bf16 v[188:191], a[44:47], v[72:75], v[188:191]\n"
"ds_read_b128 v[72:75], v30 offset:1024\n"
"v_mfma_f32_16x16x32_bf16 v[188:191], a[60:63], v[76:79], v[188:191]\n"
"ds_read_b128 v[76:79], v30 offset:1536\n"
"v_mfma_f32_16x16x32_bf16 v[192:195], a[32:35], v[48:51], v[192:195]\n"
"v_mfma_f32_16x16x32_bf16 v[192:195], a[48:51], v[52:55], v[192:195]\n"
"v_mfma_f32_16x16x32_bf16 v[196:199], a[36:39], v[48:51], v[196:199]\n"
"v_mfma_f32_16x16x32_bf16 v[196:199], a[52:55], v[52:55], v[196:199]\n"
"v_mfma_f32_16x16x32_bf16 v[200:203], a[40:43], v[48:51], v[200:203]\n"
"v_mfma_f32_16x16x32_bf16 v[200:203], a[56:59], v[52:55], v[200:203]\n"
"v_mfma_f32_16x16x32_bf16 v[204:207], a[44:47], v[48:51], v[204:207]\n"
"v_mfma_f32_16x16x32_bf16 v[204:207], a[60:63], v[52:55], v[204:207]\n"
"s_waitcnt lgkmcnt(2)\n"
"v_mfma_f32_16x16x32_bf16 v[208:211], a[32:35], v[56:59], v[208:211]\n"
"s_add_u32 s88, s72, 13\n"
"buffer_load_dwordx4 v38, s[40:43], s84 offen offset:1024 lds\n"
"v_mfma_f32_16x16x32_bf16 v[208:211], a[48:51], v[60:63], v[208:211]\n"
"s_and_b32 s88, s88, 15\n"
"v_mfma_f32_16x16x32_bf16 v[212:215], a[36:39], v[56:59], v[212:215]\n"
"s_lshl_b32 s88, s88, 7\n"
"v_mfma_f32_16x16x32_bf16 v[212:215], a[52:55], v[60:63], v[212:215]\n"
"v_mfma_f32_16x16x32_bf16 v[216:219], a[40:43], v[56:59], v[216:219]\n"
"buffer_load_dwordx4 v39, s[40:43], s84 offen offset:2048 lds\n"
"v_mfma_f32_16x16x32_bf16 v[216:219], a[56:59], v[60:63], v[216:219]\n"
"v_mfma_f32_16x16x32_bf16 v[220:223], a[44:47], v[56:59], v[220:223]\n"
"v_mfma_f32_16x16x32_bf16 v[220:223], a[60:63], v[60:63], v[220:223]\n"
"v_mfma_f32_16x16x32_bf16 v[224:227], a[32:35], v[64:67], v[224:227]\n"
"buffer_load_dwordx4 v40, s[40:43], s84 offen offset:3072 lds\n"
"v_mfma_f32_16x16x32_bf16 v[224:227], a[48:51], v[68:71], v[224:227]\n"
"s_waitcnt vmcnt(16) lgkmcnt(0)\n"
"s_barrier\n"
"v_mfma_f32_16x16x32_bf16 v[228:231], a[36:39], v[64:67], v[228:231]\n"
"ds_read_b128 v[48:51], v27 offset:8192\n"
"ds_read_b128 v[52:55], v27 offset:8704\n"
"v_mfma_f32_16x16x32_bf16 v[228:231], a[52:55], v[68:71], v[228:231]\n"
"s_add_u32 m0, s30, 0x8000\n"
"ds_read_b128 v[56:59], v27 offset:10240\n"
"ds_read_b128 v[60:63], v27 offset:10752\n"
"v_mfma_f32_16x16x32_bf16 v[232:235], a[40:43], v[64:67], v[232:235]\n"
"buffer_load_dwordx4 v32, s[36:39], s88 offen offset:0 lds\n"
"v_mfma_f32_16x16x32_bf16 v[232:235], a[56:59], v[68:71], v[232:235]\n"
"v_mfma_f32_16x16x32_bf16 v[236:239], a[44:47], v[64:67], v[236:239]\n"
"ds_read_b128 v[64:67], v41 offset:8192\n"
"v_mfma_f32_16x16x32_bf16 v[236:239], a[60:63], v[68:71], v[236:239]\n"
"ds_read_b128 v[68:71], v41 offset:8704\n"
"v_mfma_f32_16x16x32_bf16 v[240:243], a[32:35], v[72:75], v[240:243]\n"
"s_waitcnt vmcnt(13)\n"
"buffer_load_dwordx4 v33, s[36:39], s88 offen offset:1024 lds\n"
"ds_read_b64_tr_b16 a[32:33], v45 offset:24576\n"
"v_mfma_f32_16x16x32_bf16 v[240:243], a[48:51], v[76:79], v[240:243]\n"
"s_getpc_b64 s[98:99]\n"
"7:\n"
"s_add_u32 s98, s98, .Lmha_VARJ-7b\n"
"s_addc_u32 s99, s99, (.Lmha_VARJ-7b)>>32\n"
"s_add_u32 s91, s72, 14\n"
"ds_read_b64_tr_b16 a[34:35], v45 offset:24832\n"
"v_mfma_f32_16x16x32_bf16 v[244:247], a[36:39], v[72:75], v[244:247]\n"
"s_and_b32 s91, s91, 15\n"
"ds_read_b64_tr_b16 a[36:37], v46 offset:24576\n"
"v_mfma_f32_16x16x32_bf16 v[244:247], a[52:55], v[76:79], v[244:247]\n"
"s_lshl_b32 s91, s91, 19\n"
"ds_read_b64_tr_b16 a[38:39], v46 offset:24832\n"
"v_mfma_f32_16x16x32_bf16 v[248:251], a[40:43], v[72:75], v[248:251]\n"
"buffer_load_dwordx4 v34, s[36:39], s88 offen offset:2048 lds\n"
"ds_read_b64_tr_b16 a[40:41], v45 offset:25088\n"
"v_mfma_f32_16x16x32_bf16 v[248:251], a[56:59], v[76:79], v[248:251]\n"
"global_load_dword v26, v0, s[98:99] offset:0\n"
"ds_read_b64_tr_b16 a[42:43], v45 offset:25344\n"
"v_mfma_f32_16x16x32_bf16 v[252:255], a[44:47], v[72:75], v[252:255]\n"
"ds_read_b128 v[72:75], v41 offset:10240\n"
"v_mfma_f32_16x16x32_bf16 v[252:255], a[60:63], v[76:79], v[252:255]\n"
"s_waitcnt lgkmcnt(9)\n"
"ds_read_b128 v[76:79], v41 offset:10752\n"
"v_mfma_f32_16x16x32_bf16 v[112:115], a[0:3], v[48:51], v[112:115]\n"
"buffer_load_dwordx4 v35, s[36:39], s88 offen offset:3072 lds\n"
"ds_read_b64_tr_b16 a[44:45], v46 offset:25088\n"
"v_mfma_f32_16x16x32_bf16 v[112:115], a[16:19], v[52:55], v[112:115]\n"
"s_mul_i32 s88, s70, 0x2400\n"
"ds_read_b64_tr_b16 a[46:47], v46 offset:25344\n"
"v_mfma_f32_16x16x32_bf16 v[116:119], a[4:7], v[48:51], v[116:119]\n"
"v_mfma_f32_16x16x32_bf16 v[116:119], a[20:23], v[52:55], v[116:119]\n"
"s_add_u32 m0, s30, 0x4000\n"
"v_mfma_f32_16x16x32_bf16 v[120:123], a[8:11], v[48:51], v[120:123]\n"
"buffer_load_dwordx4 v37, s[40:43], s91 offen offset:0 lds\n"
"v_mfma_f32_16x16x32_bf16 v[120:123], a[24:27], v[52:55], v[120:123]\n"
"v_mfma_f32_16x16x32_bf16 v[124:127], a[12:15], v[48:51], v[124:127]\n"
"ds_read_b128 v[48:51], v42 offset:8192\n"
"v_mfma_f32_16x16x32_bf16 v[124:127], a[28:31], v[52:55], v[124:127]\n"
"ds_read_b128 v[52:55], v42 offset:8704\n"
"v_mfma_f32_16x16x32_bf16 v[128:131], a[0:3], v[56:59], v[128:131]\n"
"buffer_load_dwordx4 v38, s[40:43], s91 offen offset:1024 lds\n"
"v_mfma_f32_16x16x32_bf16 v[128:131], a[16:19], v[60:63], v[128:131]\n"
"v_mfma_f32_16x16x32_bf16 v[132:135], a[4:7], v[56:59], v[132:135]\n"
"v_mfma_f32_16x16x32_bf16 v[132:135], a[20:23], v[60:63], v[132:135]\n"
"v_mfma_f32_16x16x32_bf16 v[136:139], a[8:11], v[56:59], v[136:139]\n"
"buffer_load_dwordx4 v39, s[40:43], s91 offen offset:2048 lds\n"
"v_mfma_f32_16x16x32_bf16 v[136:139], a[24:27], v[60:63], v[136:139]\n"
"v_mfma_f32_16x16x32_bf16 v[140:143], a[12:15], v[56:59], v[140:143]\n"
"ds_read_b128 v[56:59], v42 offset:10240\n"
"v_mfma_f32_16x16x32_bf16 v[140:143], a[28:31], v[60:63], v[140:143]\n"
"s_waitcnt lgkmcnt(3)\n"
"ds_read_b128 v[60:63], v42 offset:10752\n"
"v_mfma_f32_16x16x32_bf16 v[144:147], a[0:3], v[64:67], v[144:147]\n"
"buffer_load_dwordx4 v40, s[40:43], s91 offen offset:3072 lds\n"
"v_mfma_f32_16x16x32_bf16 v[144:147], a[16:19], v[68:71], v[144:147]\n"
"v_mfma_f32_16x16x32_bf16 v[148:151], a[4:7], v[64:67], v[148:151]\n"
"s_add_u32 s76, s91, 0x40000\n"
"s_add_u32 m0, s30, 0x5000\n"
"v_mfma_f32_16x16x32_bf16 v[148:151], a[20:23], v[68:71], v[148:151]\n"
"buffer_load_dwordx4 v37, s[40:43], s76 offen offset:0 lds\n"
"v_mfma_f32_16x16x32_bf16 v[152:155], a[8:11], v[64:67], v[152:155]\n"
"v_mfma_f32_16x16x32_bf16 v[152:155], a[24:27], v[68:71], v[152:155]\n"
"v_mfma_f32_16x16x32_bf16 v[156:159], a[12:15], v[64:67], v[156:159]\n"
"ds_read_b128 v[64:67], v43 offset:8192\n"
"v_mfma_f32_16x16x32_bf16 v[156:159], a[28:31], v[68:71], v[156:159]\n"
"s_add_u32 s74, s72, 15\n"
"buffer_load_dwordx4 v38, s[40:43], s76 offen offset:1024 lds\n"
"ds_read_b128 v[68:71], v43 offset:8704\n"
"v_mfma_f32_16x16x32_bf16 v[160:163], a[0:3], v[72:75], v[160:163]\n"
"s_and_b32 s74, s74, 15\n"
"v_mfma_f32_16x16x32_bf16 v[160:163], a[16:19], v[76:79], v[160:163]\n"
"s_lshl_b32 s74, s74, 19\n"
"v_mfma_f32_16x16x32_bf16 v[164:167], a[4:7], v[72:75], v[164:167]\n"
"v_mfma_f32_16x16x32_bf16 v[164:167], a[20:23], v[76:79], v[164:167]\n"
"buffer_load_dwordx4 v39, s[40:43], s76 offen offset:2048 lds\n"
"v_mfma_f32_16x16x32_bf16 v[168:171], a[8:11], v[72:75], v[168:171]\n"
"v_mfma_f32_16x16x32_bf16 v[168:171], a[24:27], v[76:79], v[168:171]\n"
"v_mfma_f32_16x16x32_bf16 v[172:175], a[12:15], v[72:75], v[172:175]\n"
"ds_read_b128 v[72:75], v43 offset:10240\n"
"v_mfma_f32_16x16x32_bf16 v[172:175], a[28:31], v[76:79], v[172:175]\n"
"s_waitcnt lgkmcnt(3)\n"
"buffer_load_dwordx4 v40, s[40:43], s76 offen offset:3072 lds\n"
"ds_read_b128 v[76:79], v43 offset:10752\n"
"v_mfma_f32_16x16x32_bf16 v[176:179], a[0:3], v[48:51], v[176:179]\n"
"v_mfma_f32_16x16x32_bf16 v[176:179], a[16:19], v[52:55], v[176:179]\n"
"s_waitcnt vmcnt(13)\n"
"ds_read_b64_tr_b16 a[48:49], v45 offset:28672\n"
"v_mfma_f32_16x16x32_bf16 v[180:183], a[4:7], v[48:51], v[180:183]\n"
"s_add_u32 m0, s30, 0x6000\n"
"ds_read_b64_tr_b16 a[50:51], v45 offset:28928\n"
"v_mfma_f32_16x16x32_bf16 v[180:183], a[20:23], v[52:55], v[180:183]\n"
"buffer_load_dwordx4 v37, s[40:43], s74 offen offset:0 lds\n"
"ds_read_b64_tr_b16 a[52:53], v46 offset:28672\n"
"v_mfma_f32_16x16x32_bf16 v[184:187], a[8:11], v[48:51], v[184:187]\n"
"ds_read_b64_tr_b16 a[54:55], v46 offset:28928\n"
"v_mfma_f32_16x16x32_bf16 v[184:187], a[24:27], v[52:55], v[184:187]\n"
"ds_read_b64_tr_b16 a[56:57], v45 offset:29184\n"
"v_mfma_f32_16x16x32_bf16 v[188:191], a[12:15], v[48:51], v[188:191]\n"
"ds_read_b128 v[48:51], v44 offset:0\n"
"v_mfma_f32_16x16x32_bf16 v[188:191], a[28:31], v[52:55], v[188:191]\n"
"s_add_u32 s89, s72, 14\n"
"buffer_load_dwordx4 v38, s[40:43], s74 offen offset:1024 lds\n"
"ds_read_b128 v[52:55], v44 offset:512\n"
"v_mfma_f32_16x16x32_bf16 v[192:195], a[0:3], v[56:59], v[192:195]\n"
"s_and_b32 s89, s89, 15\n"
"ds_read_b64_tr_b16 a[58:59], v45 offset:29440\n"
"v_mfma_f32_16x16x32_bf16 v[192:195], a[16:19], v[60:63], v[192:195]\n"
"s_lshl_b32 s89, s89, 7\n"
"ds_read_b64_tr_b16 a[60:61], v46 offset:29184\n"
"v_mfma_f32_16x16x32_bf16 v[196:199], a[4:7], v[56:59], v[196:199]\n"
"ds_read_b64_tr_b16 a[62:63], v46 offset:29440\n"
"v_mfma_f32_16x16x32_bf16 v[196:199], a[20:23], v[60:63], v[196:199]\n"
"buffer_load_dwordx4 v39, s[40:43], s74 offen offset:2048 lds\n"
"v_mfma_f32_16x16x32_bf16 v[200:203], a[8:11], v[56:59], v[200:203]\n"
"v_mfma_f32_16x16x32_bf16 v[200:203], a[24:27], v[60:63], v[200:203]\n"
"v_mfma_f32_16x16x32_bf16 v[204:207], a[12:15], v[56:59], v[204:207]\n"
"v_mfma_f32_16x16x32_bf16 v[204:207], a[28:31], v[60:63], v[204:207]\n"
"s_waitcnt lgkmcnt(6)\n"
"buffer_load_dwordx4 v40, s[40:43], s74 offen offset:3072 lds\n"
"v_mfma_f32_16x16x32_bf16 v[208:211], a[0:3], v[64:67], v[208:211]\n"
"v_mfma_f32_16x16x32_bf16 v[208:211], a[16:19], v[68:71], v[208:211]\n"
"v_mfma_f32_16x16x32_bf16 v[212:215], a[4:7], v[64:67], v[212:215]\n"
"s_add_u32 m0, s30, 0x3000\n"
"v_mfma_f32_16x16x32_bf16 v[212:215], a[20:23], v[68:71], v[212:215]\n"
"buffer_load_dwordx4 v32, s[36:39], s89 offen offset:0 lds\n"
"v_mfma_f32_16x16x32_bf16 v[216:219], a[8:11], v[64:67], v[216:219]\n"
"v_mfma_f32_16x16x32_bf16 v[216:219], a[24:27], v[68:71], v[216:219]\n"
"v_mfma_f32_16x16x32_bf16 v[220:223], a[12:15], v[64:67], v[220:223]\n"
"v_mfma_f32_16x16x32_bf16 v[220:223], a[28:31], v[68:71], v[220:223]\n"
"buffer_load_dwordx4 v33, s[36:39], s89 offen offset:1024 lds\n"
"v_mfma_f32_16x16x32_bf16 v[224:227], a[0:3], v[72:75], v[224:227]\n"
"v_mfma_f32_16x16x32_bf16 v[224:227], a[16:19], v[76:79], v[224:227]\n"
"s_waitcnt vmcnt(14) lgkmcnt(0)\n"
"s_barrier\n"
"v_mfma_f32_16x16x32_bf16 v[228:231], a[4:7], v[72:75], v[228:231]\n"
"ds_read_b128 v[56:59], v27 offset:32768\n"
"ds_read_b128 v[60:63], v27 offset:33280\n"
"v_mfma_f32_16x16x32_bf16 v[228:231], a[20:23], v[76:79], v[228:231]\n"
"buffer_load_dwordx4 v34, s[36:39], s89 offen offset:2048 lds\n"
"ds_read_b128 v[64:67], v27 offset:34816\n"
"v_mfma_f32_16x16x32_bf16 v[232:235], a[8:11], v[72:75], v[232:235]\n"
"ds_read_b128 v[68:71], v27 offset:35328\n"
"v_mfma_f32_16x16x32_bf16 v[232:235], a[24:27], v[76:79], v[232:235]\n"
"v_mfma_f32_16x16x32_bf16 v[236:239], a[12:15], v[72:75], v[236:239]\n"
"ds_read_b128 v[72:75], v41 offset:32768\n"
"v_mfma_f32_16x16x32_bf16 v[236:239], a[28:31], v[76:79], v[236:239]\n"
"buffer_load_dwordx4 v35, s[36:39], s89 offen offset:3072 lds\n"
"ds_read_b128 v[76:79], v41 offset:33280\n"
"v_mfma_f32_16x16x32_bf16 v[240:243], a[0:3], v[48:51], v[240:243]\n"
"v_mfma_f32_16x16x32_bf16 v[240:243], a[16:19], v[52:55], v[240:243]\n"
"v_mfma_f32_16x16x32_bf16 v[244:247], a[4:7], v[48:51], v[244:247]\n"
"s_waitcnt vmcnt(12)\n"
"s_add_u32 m0, s30, 0x9400\n"
"ds_read_b64_tr_b16 a[0:1], v45 offset:16384\n"
"v_mfma_f32_16x16x32_bf16 v[244:247], a[20:23], v[52:55], v[244:247]\n"
"buffer_load_dwordx4 v36, s[36:39], s89 offen offset:0 lds\n"
"ds_read_b64_tr_b16 a[2:3], v45 offset:16640\n"
"v_mfma_f32_16x16x32_bf16 v[248:251], a[8:11], v[48:51], v[248:251]\n"
"s_lshl_b32 s89, s70, 16\n"
"ds_read_b64_tr_b16 a[4:5], v46 offset:16384\n"
"v_mfma_f32_16x16x32_bf16 v[248:251], a[24:27], v[52:55], v[248:251]\n"
"s_and_b32 s70, s27, 7\n"
"s_add_u32 m0, s30, 0x7000\n"
"ds_read_b64_tr_b16 a[6:7], v46 offset:16640\n"
"v_mfma_f32_16x16x32_bf16 v[252:255], a[12:15], v[48:51], v[252:255]\n"
"s_sub_u32 s70, s70, 7\n"
"s_add_u32 s96, s74, 0x40000\n"
"ds_read_b128 v[48:51], v41 offset:34816\n"
"v_mfma_f32_16x16x32_bf16 v[252:255], a[28:31], v[52:55], v[252:255]\n"
"s_waitcnt lgkmcnt(7)\n"
"buffer_load_dwordx4 v37, s[40:43], s96 offen offset:0 lds\n"
"ds_read_b128 v[52:55], v41 offset:35328\n"
"v_mfma_f32_16x16x32_bf16 v[112:115], a[32:35], v[56:59], v[112:115]\n"
"s_lshr_b32 s70, s70, 31\n"
"ds_read_b64_tr_b16 a[8:9], v45 offset:16896\n"
"v_mfma_f32_16x16x32_bf16 v[112:115], a[48:51], v[60:63], v[112:115]\n"
"s_add_u32 s70, s70, s71\n"
"ds_read_b64_tr_b16 a[10:11], v45 offset:17152\n"
"v_mfma_f32_16x16x32_bf16 v[116:119], a[36:39], v[56:59], v[116:119]\n"
"s_add_u32 s70, s70, 6\n"
"ds_read_b64_tr_b16 a[12:13], v46 offset:16896\n"
"v_mfma_f32_16x16x32_bf16 v[116:119], a[52:55], v[60:63], v[116:119]\n"
"s_add_u32 s73, s72, 15\n"
"buffer_load_dwordx4 v38, s[40:43], s96 offen offset:1024 lds\n"
"ds_read_b64_tr_b16 a[14:15], v46 offset:17152\n"
"v_mfma_f32_16x16x32_bf16 v[120:123], a[40:43], v[56:59], v[120:123]\n"
"s_and_b32 s73, s73, 15\n"
"s_lshl_b32 s70, s70, 3\n"
"v_mfma_f32_16x16x32_bf16 v[120:123], a[56:59], v[60:63], v[120:123]\n"
"s_lshl_b32 s73, s73, 7\n"
"s_add_u32 s70, s70, s11\n"
"v_mfma_f32_16x16x32_bf16 v[124:127], a[44:47], v[56:59], v[124:127]\n"
"s_mul_i32 s90, s70, 0x2400\n"
"s_lshl_b32 s91, s70, 16\n"
"ds_read_b128 v[56:59], v42 offset:32768\n"
"v_mfma_f32_16x16x32_bf16 v[124:127], a[60:63], v[60:63], v[124:127]\n"
"buffer_load_dwordx4 v39, s[40:43], s96 offen offset:2048 lds\n"
"ds_read_b128 v[60:63], v42 offset:33280\n"
"v_mfma_f32_16x16x32_bf16 v[128:131], a[32:35], v[64:67], v[128:131]\n"
"v_mfma_f32_16x16x32_bf16 v[128:131], a[48:51], v[68:71], v[128:131]\n"
"s_waitcnt vmcnt(12)\n"
"ds_read_b64_tr_b16 a[16:17], v45 offset:20480\n"
"v_mfma_f32_16x16x32_bf16 v[132:135], a[36:39], v[64:67], v[132:135]\n"
"ds_read_b64_tr_b16 a[18:19], v45 offset:20736\n"
"v_mfma_f32_16x16x32_bf16 v[132:135], a[52:55], v[68:71], v[132:135]\n"
"buffer_load_dwordx4 v40, s[40:43], s96 offen offset:3072 lds\n"
"ds_read_b64_tr_b16 a[20:21], v46 offset:20480\n"
"v_mfma_f32_16x16x32_bf16 v[136:139], a[40:43], v[64:67], v[136:139]\n"
"s_mul_i32 s41, s68, 0x2400\n"
"s_add_u32 m0, s30, 0x2000\n"
"ds_read_b64_tr_b16 a[22:23], v46 offset:20736\n"
"v_mfma_f32_16x16x32_bf16 v[136:139], a[56:59], v[68:71], v[136:139]\n"
"s_xor_b32 s68, s68, 32\n"
"ds_read_b64_tr_b16 a[24:25], v45 offset:20992\n"
"buffer_load_dwordx4 v32, s[36:39], s73 offen offset:0 lds\n"
"buffer_load_dwordx4 v33, s[36:39], s73 offen offset:1024 lds\n"
"v_mfma_f32_16x16x32_bf16 v[140:143], a[44:47], v[64:67], v[140:143]\n"
"s_mul_i32 s40, s68, 0x2400\n"
"ds_read_b128 v[64:67], v42 offset:34816\n"
"buffer_load_dwordx4 v34, s[36:39], s73 offen offset:2048 lds\n"
"buffer_load_dwordx4 v35, s[36:39], s73 offen offset:3072 lds\n"
"v_mfma_f32_16x16x32_bf16 v[140:143], a[60:63], v[68:71], v[140:143]\n"
"s_waitcnt lgkmcnt(8)\n"
"s_lshl_b32 s68, s29, 1\n"
"ds_read_b128 v[68:71], v42 offset:35328\n"
"v_mfma_f32_16x16x32_bf16 v[144:147], a[32:35], v[72:75], v[144:147]\n"
"s_add_u32 s68, s68, s28\n"
"s_lshl_b32 s39, s27, 7\n"
"ds_read_b64_tr_b16 a[26:27], v45 offset:21248\n"
"v_mfma_f32_16x16x32_bf16 v[144:147], a[48:51], v[76:79], v[144:147]\n"
"s_lshl_b32 s68, s68, 2\n"
"s_add_u32 s39, s39, s94\n"
"ds_read_b64_tr_b16 a[28:29], v46 offset:20992\n"
"v_mfma_f32_16x16x32_bf16 v[148:151], a[36:39], v[72:75], v[148:151]\n"
"s_add_u32 s68, s68, s11\n"
"ds_read_b64_tr_b16 a[30:31], v46 offset:21248\n"
"v_mfma_f32_16x16x32_bf16 v[148:151], a[52:55], v[76:79], v[148:151]\n"
"s_lshl_b32 s42, s68, 5\n"
"v_mfma_f32_16x16x32_bf16 v[152:155], a[40:43], v[72:75], v[152:155]\n"
"v_mfma_f32_16x16x32_bf16 v[152:155], a[56:59], v[76:79], v[152:155]\n"
"v_mfma_f32_16x16x32_bf16 v[156:159], a[44:47], v[72:75], v[156:159]\n"
"ds_read_b128 v[72:75], v43 offset:32768\n"
"v_mfma_f32_16x16x32_bf16 v[156:159], a[60:63], v[76:79], v[156:159]\n"
"ds_read_b128 v[76:79], v43 offset:33280\n"
"v_mfma_f32_16x16x32_bf16 v[160:163], a[32:35], v[48:51], v[160:163]\n"
"v_mfma_f32_16x16x32_bf16 v[160:163], a[48:51], v[52:55], v[160:163]\n"
"s_waitcnt vmcnt(13)\n"
"ds_read_b64_tr_b16 v[80:81], v45 offset:24576\n"
"v_mfma_f32_16x16x32_bf16 v[164:167], a[36:39], v[48:51], v[164:167]\n"
"ds_read_b64_tr_b16 v[82:83], v45 offset:24832\n"
"v_mfma_f32_16x16x32_bf16 v[164:167], a[52:55], v[52:55], v[164:167]\n"
"ds_read_b64_tr_b16 v[84:85], v46 offset:24576\n"
"v_mfma_f32_16x16x32_bf16 v[168:171], a[40:43], v[48:51], v[168:171]\n"
"ds_read_b64_tr_b16 v[86:87], v46 offset:24832\n"
"v_mfma_f32_16x16x32_bf16 v[168:171], a[56:59], v[52:55], v[168:171]\n"
"ds_read_b64_tr_b16 v[88:89], v45 offset:25088\n"
"v_mfma_f32_16x16x32_bf16 v[172:175], a[44:47], v[48:51], v[172:175]\n"
"ds_read_b128 v[48:51], v43 offset:34816\n"
"v_mfma_f32_16x16x32_bf16 v[172:175], a[60:63], v[52:55], v[172:175]\n"
"s_waitcnt lgkmcnt(8)\n"
"ds_read_b128 v[52:55], v43 offset:35328\n"
"v_mfma_f32_16x16x32_bf16 v[176:179], a[32:35], v[56:59], v[176:179]\n"
"ds_read_b64_tr_b16 v[90:91], v45 offset:25344\n"
"v_mfma_f32_16x16x32_bf16 v[176:179], a[48:51], v[60:63], v[176:179]\n"
"s_mov_b32 s75, 0x8000\n"
"ds_read_b64_tr_b16 v[92:93], v46 offset:25088\n"
"v_mfma_f32_16x16x32_bf16 v[180:183], a[36:39], v[56:59], v[180:183]\n"
"buffer_load_dwordx4 a[160:163], v19, s[48:51], s75 offen offset:0\n"
"ds_read_b64_tr_b16 v[94:95], v46 offset:25344\n"
"v_mfma_f32_16x16x32_bf16 v[180:183], a[52:55], v[60:63], v[180:183]\n"
"v_mfma_f32_16x16x32_bf16 v[184:187], a[40:43], v[56:59], v[184:187]\n"
"v_mfma_f32_16x16x32_bf16 v[184:187], a[56:59], v[60:63], v[184:187]\n"
"v_mfma_f32_16x16x32_bf16 v[188:191], a[44:47], v[56:59], v[188:191]\n"
"ds_read_b128 v[56:59], v30 offset:0\n"
"v_mfma_f32_16x16x32_bf16 v[188:191], a[60:63], v[60:63], v[188:191]\n"
"ds_read_b128 v[60:63], v30 offset:512\n"
"v_mfma_f32_16x16x32_bf16 v[192:195], a[32:35], v[64:67], v[192:195]\n"
"v_mfma_f32_16x16x32_bf16 v[192:195], a[48:51], v[68:71], v[192:195]\n"
"v_mfma_f32_16x16x32_bf16 v[196:199], a[36:39], v[64:67], v[196:199]\n"
"v_mfma_f32_16x16x32_bf16 v[196:199], a[52:55], v[68:71], v[196:199]\n"
"v_mfma_f32_16x16x32_bf16 v[200:203], a[40:43], v[64:67], v[200:203]\n"
"v_mfma_f32_16x16x32_bf16 v[200:203], a[56:59], v[68:71], v[200:203]\n"
"v_mfma_f32_16x16x32_bf16 v[204:207], a[44:47], v[64:67], v[204:207]\n"
"v_mfma_f32_16x16x32_bf16 v[204:207], a[60:63], v[68:71], v[204:207]\n"
"s_waitcnt lgkmcnt(2)\n"
"buffer_load_dwordx4 a[164:167], v20, s[48:51], s75 offen offset:1024\n"
"v_mfma_f32_16x16x32_bf16 v[208:211], a[32:35], v[72:75], v[208:211]\n"
"v_mfma_f32_16x16x32_bf16 v[208:211], a[48:51], v[76:79], v[208:211]\n"
"v_mfma_f32_16x16x32_bf16 v[212:215], a[36:39], v[72:75], v[212:215]\n"
"v_mfma_f32_16x16x32_bf16 v[212:215], a[52:55], v[76:79], v[212:215]\n"
"v_mfma_f32_16x16x32_bf16 v[216:219], a[40:43], v[72:75], v[216:219]\n"
"v_mfma_f32_16x16x32_bf16 v[216:219], a[56:59], v[76:79], v[216:219]\n"
"v_mfma_f32_16x16x32_bf16 v[220:223], a[44:47], v[72:75], v[220:223]\n"
"v_mfma_f32_16x16x32_bf16 v[220:223], a[60:63], v[76:79], v[220:223]\n"
"v_mfma_f32_16x16x32_bf16 v[224:227], a[32:35], v[48:51], v[224:227]\n"
"v_mfma_f32_16x16x32_bf16 v[224:227], a[48:51], v[52:55], v[224:227]\n"
"v_mfma_f32_16x16x32_bf16 v[228:231], a[36:39], v[48:51], v[228:231]\n"
"v_mfma_f32_16x16x32_bf16 v[228:231], a[52:55], v[52:55], v[228:231]\n"
"buffer_load_dwordx4 a[168:171], v19, s[48:51], s75 offen offset:2048\n"
"v_mfma_f32_16x16x32_bf16 v[232:235], a[40:43], v[48:51], v[232:235]\n"
"v_mfma_f32_16x16x32_bf16 v[232:235], a[56:59], v[52:55], v[232:235]\n"
"s_waitcnt vmcnt(7)\n"
"ds_read_b64_tr_b16 v[96:97], v45 offset:28672\n"
"ds_read_b64_tr_b16 v[98:99], v45 offset:28928\n"
"v_mfma_f32_16x16x32_bf16 v[236:239], a[44:47], v[48:51], v[236:239]\n"
"ds_read_b64_tr_b16 v[100:101], v46 offset:28672\n"
"ds_read_b64_tr_b16 v[102:103], v46 offset:28928\n"
"v_mfma_f32_16x16x32_bf16 v[236:239], a[60:63], v[52:55], v[236:239]\n"
"s_waitcnt lgkmcnt(4)\n"
"ds_read_b64_tr_b16 v[104:105], v45 offset:29184\n"
"ds_read_b64_tr_b16 v[106:107], v45 offset:29440\n"
"v_mfma_f32_16x16x32_bf16 v[240:243], a[32:35], v[56:59], v[240:243]\n"
"ds_read_b64_tr_b16 v[108:109], v46 offset:29184\n"
"s_waitcnt vmcnt(3)\n"
"s_barrier\n"
"ds_read_b128 v[64:67], v44 offset:1024\n"
"v_mfma_f32_16x16x32_bf16 v[240:243], a[48:51], v[60:63], v[240:243]\n"
"ds_read_b128 v[68:71], v44 offset:1536\n"
"ds_read_b128 v[72:75], v30 offset:1024\n"
"v_mfma_f32_16x16x32_bf16 v[244:247], a[36:39], v[56:59], v[244:247]\n"
"ds_read_b128 v[76:79], v30 offset:1536\n"
"ds_read_b64_tr_b16 v[110:111], v46 offset:29440\n"
"v_mfma_f32_16x16x32_bf16 v[244:247], a[52:55], v[60:63], v[244:247]\n"
"ds_read_b128 v[48:51], v27 offset:12288\n"
"ds_read_b128 v[52:55], v27 offset:12800\n"
"v_mfma_f32_16x16x32_bf16 v[248:251], a[40:43], v[56:59], v[248:251]\n"
"ds_read_b128 a[32:35], v21 offset:0\n"
"v_add_u32 v30, 0x8000, v22\n"
"v_mfma_f32_16x16x32_bf16 v[248:251], a[56:59], v[60:63], v[248:251]\n"
"ds_read_b128 a[36:39], v21 offset:256\n"
"v_mfma_f32_16x16x32_bf16 v[252:255], a[44:47], v[56:59], v[252:255]\n"
"ds_read_b128 v[56:59], v27 offset:8192\n"
"v_mfma_f32_16x16x32_bf16 v[252:255], a[60:63], v[60:63], v[252:255]\n"
"s_mov_b32 s75, 0x8000\n"
"ds_read_b128 v[60:63], v27 offset:8704\n"
"s_waitcnt lgkmcnt(10)\n"
"v_mfma_f32_16x16x32_bf16 v[240:243], a[0:3], v[64:67], v[240:243]\n"
"s_waitcnt lgkmcnt(9)\n"
"buffer_load_dwordx4 a[172:175], v20, s[48:51], s75 offen offset:3072\n"
"ds_read_b128 a[40:43], v21 offset:512\n"
"v_mfma_f32_16x16x32_bf16 v[240:243], a[16:19], v[68:71], v[240:243]\n"
"s_waitcnt lgkmcnt(9)\n"
"ds_read_b128 a[44:47], v21 offset:768\n"
"v_mfma_f32_16x16x32_bf16 v[240:243], v[80:83], v[72:75], v[240:243]\n"
"s_waitcnt lgkmcnt(9)\n"
"ds_read_b128 a[48:51], v1 offset:0\n"
"v_mfma_f32_16x16x32_bf16 v[240:243], v[96:99], v[76:79], v[240:243]\n"
"ds_read_b128 a[52:55], v1 offset:256\n"
"v_mfma_f32_16x16x32_bf16 v[244:247], a[4:7], v[64:67], v[244:247]\n"
"ds_read_b128 a[56:59], v1 offset:512\n"
"v_mfma_f32_16x16x32_bf16 v[244:247], a[20:23], v[68:71], v[244:247]\n"
"ds_read_b128 a[60:63], v1 offset:768\n"
"v_mfma_f32_16x16x32_bf16 v[244:247], v[84:87], v[72:75], v[244:247]\n"
"s_nop 0\n"
"s_nop 0\n"
"s_nop 0\n"
"v_mfma_f32_16x16x32_bf16 v[244:247], v[100:103], v[76:79], v[244:247]\n"
"v_mul_f32 v240, s85, v240\n"
"v_mul_f32 v241, s85, v241\n"
"v_mfma_f32_16x16x32_bf16 v[248:251], a[8:11], v[64:67], v[248:251]\n"
"v_mul_f32 v242, s85, v242\n"
"v_mul_f32 v243, s85, v243\n"
"v_mfma_f32_16x16x32_bf16 v[248:251], a[24:27], v[68:71], v[248:251]\n"
"v_cvt_pk_bf16_f32 v240, v240, v241\n"
"v_cvt_pk_bf16_f32 v241, v242, v243\n"
"v_mfma_f32_16x16x32_bf16 v[248:251], v[88:91], v[72:75], v[248:251]\n"
"ds_write_b64 v47, v[240:241] offset:32768\n"
"v_mfma_f32_16x16x32_bf16 v[248:251], v[104:107], v[76:79], v[248:251]\n"
"s_mov_b32 s76, 0x10000\n"
"v_mul_f32 v244, s85, v244\n"
"v_mul_f32 v245, s85, v245\n"
"v_mfma_f32_16x16x32_bf16 v[252:255], a[12:15], v[64:67], v[252:255]\n"
"buffer_load_dwordx4 a[176:179], v17, s[44:47], s76 offen offset:0\n"
"v_mul_f32 v246, s85, v246\n"
"v_mfma_f32_16x16x32_bf16 v[252:255], a[28:31], v[68:71], v[252:255]\n"
"ds_read_b128 v[64:67], v27 offset:14336\n"
"v_mul_f32 v247, s85, v247\n"
"v_mfma_f32_16x16x32_bf16 v[252:255], v[92:95], v[72:75], v[252:255]\n"
"s_waitcnt lgkmcnt(4)\n"
"ds_read_b128 v[68:71], v27 offset:14848\n"
"v_cvt_pk_bf16_f32 v244, v244, v245\n"
"v_mfma_f32_16x16x32_bf16 v[252:255], v[108:111], v[76:79], v[252:255]\n"
"ds_read_b128 v[72:75], v27 offset:10240\n"
"v_cvt_pk_bf16_f32 v245, v246, v247\n"
"v_mfma_f32_16x16x32_bf16 v[112:115], a[0:3], v[48:51], v[112:115]\n"
"ds_write_b64 v47, v[244:245] offset:33280\n"
"v_mfma_f32_16x16x32_bf16 v[112:115], a[16:19], v[52:55], v[112:115]\n"
"v_mul_f32 v248, s85, v248\n"
"v_mul_f32 v249, s85, v249\n"
"v_mul_f32 v250, s85, v250\n"
"v_mfma_f32_16x16x32_bf16 v[112:115], v[80:83], v[56:59], v[112:115]\n"
"ds_read_b128 v[76:79], v27 offset:10752\n"
"v_mul_f32 v251, s85, v251\n"
"v_cvt_pk_bf16_f32 v248, v248, v249\n"
"s_branch .Lmha_VARJ\n"
".Lmha_VAR3:\n"
"ds_read_b128 v[68:71], v41 offset:33280\n"
"v_mfma_f32_16x16x32_bf16 v[240:243], a[32:35], v[72:75], v[240:243]\n"
"s_waitcnt vmcnt(13)\n"
"ds_read_b64_tr_b16 a[32:33], v45 offset:24576\n"
"v_mfma_f32_16x16x32_bf16 v[240:243], a[48:51], v[76:79], v[240:243]\n"
"s_add_u32 s73, s72, 6\n"
"buffer_load_dwordx4 v38, s[40:43], s75 offen offset:1024 lds\n"
"ds_read_b64_tr_b16 a[34:35], v45 offset:24832\n"
"v_mfma_f32_16x16x32_bf16 v[244:247], a[36:39], v[72:75], v[244:247]\n"
"s_and_b32 s73, s73, 15\n"
"ds_read_b64_tr_b16 a[36:37], v46 offset:24576\n"
"v_mfma_f32_16x16x32_bf16 v[244:247], a[52:55], v[76:79], v[244:247]\n"
"s_lshl_b32 s73, s73, 7\n"
"ds_read_b64_tr_b16 a[38:39], v46 offset:24832\n"
"v_mfma_f32_16x16x32_bf16 v[248:251], a[40:43], v[72:75], v[248:251]\n"
"ds_read_b64_tr_b16 a[40:41], v45 offset:25088\n"
"v_mfma_f32_16x16x32_bf16 v[248:251], a[56:59], v[76:79], v[248:251]\n"
"buffer_load_dwordx4 v39, s[40:43], s75 offen offset:2048 lds\n"
"ds_read_b64_tr_b16 a[42:43], v45 offset:25344\n"
"v_mfma_f32_16x16x32_bf16 v[252:255], a[44:47], v[72:75], v[252:255]\n"
"ds_read_b128 v[72:75], v41 offset:34816\n"
"v_mfma_f32_16x16x32_bf16 v[252:255], a[60:63], v[76:79], v[252:255]\n"
"s_waitcnt lgkmcnt(9)\n"
"ds_read_b128 v[76:79], v41 offset:35328\n"
"v_mfma_f32_16x16x32_bf16 v[112:115], a[0:3], v[48:51], v[112:115]\n"
"ds_read_b64_tr_b16 a[44:45], v46 offset:25088\n"
"v_mfma_f32_16x16x32_bf16 v[112:115], a[16:19], v[52:55], v[112:115]\n"
"buffer_load_dwordx4 v40, s[40:43], s75 offen offset:3072 lds\n"
"ds_read_b64_tr_b16 a[46:47], v46 offset:25344\n"
"v_mfma_f32_16x16x32_bf16 v[116:119], a[4:7], v[48:51], v[116:119]\n"
"v_mfma_f32_16x16x32_bf16 v[116:119], a[20:23], v[52:55], v[116:119]\n"
"v_mfma_f32_16x16x32_bf16 v[120:123], a[8:11], v[48:51], v[120:123]\n"
"s_waitcnt vmcnt(12)\n"
"s_add_u32 m0, s30, 0x2000\n"
"ds_read_b64_tr_b16 a[48:49], v45 offset:28672\n"
"v_mfma_f32_16x16x32_bf16 v[120:123], a[24:27], v[52:55], v[120:123]\n"
"buffer_load_dwordx4 v32, s[36:39], s73 offen offset:0 lds\n"
"ds_read_b64_tr_b16 a[50:51], v45 offset:28928\n"
"v_mfma_f32_16x16x32_bf16 v[124:127], a[12:15], v[48:51], v[124:127]\n"
"ds_read_b128 v[48:51], v42 offset:32768\n"
"v_mfma_f32_16x16x32_bf16 v[124:127], a[28:31], v[52:55], v[124:127]\n"
"ds_read_b128 v[52:55], v42 offset:33280\n"
"v_mfma_f32_16x16x32_bf16 v[128:131], a[0:3], v[56:59], v[128:131]\n"
"ds_read_b64_tr_b16 a[52:53], v46 offset:28672\n"
"v_mfma_f32_16x16x32_bf16 v[128:131], a[16:19], v[60:63], v[128:131]\n"
"buffer_load_dwordx4 v33, s[36:39], s73 offen offset:1024 lds\n"
"ds_read_b64_tr_b16 a[54:55], v46 offset:28928\n"
"v_mfma_f32_16x16x32_bf16 v[132:135], a[4:7], v[56:59], v[132:135]\n"
"ds_read_b64_tr_b16 a[56:57], v45 offset:29184\n"
"v_mfma_f32_16x16x32_bf16 v[132:135], a[20:23], v[60:63], v[132:135]\n"
"ds_read_b64_tr_b16 a[58:59], v45 offset:29440\n"
"v_mfma_f32_16x16x32_bf16 v[136:139], a[8:11], v[56:59], v[136:139]\n"
"ds_read_b64_tr_b16 a[60:61], v46 offset:29184\n"
"v_mfma_f32_16x16x32_bf16 v[136:139], a[24:27], v[60:63], v[136:139]\n"
"s_add_u32 s90, s72, 7\n"
"buffer_load_dwordx4 v34, s[36:39], s73 offen offset:2048 lds\n"
"ds_read_b64_tr_b16 a[62:63], v46 offset:29440\n"
"v_mfma_f32_16x16x32_bf16 v[140:143], a[12:15], v[56:59], v[140:143]\n"
"s_and_b32 s90, s90, 15\n"
"ds_read_b128 v[56:59], v42 offset:34816\n"
"v_mfma_f32_16x16x32_bf16 v[140:143], a[28:31], v[60:63], v[140:143]\n"
"s_waitcnt lgkmcnt(10)\n"
"s_lshl_b32 s90, s90, 19\n"
"ds_read_b128 v[60:63], v42 offset:35328\n"
"v_mfma_f32_16x16x32_bf16 v[144:147], a[0:3], v[64:67], v[144:147]\n"
"v_mfma_f32_16x16x32_bf16 v[144:147], a[16:19], v[68:71], v[144:147]\n"
"buffer_load_dwordx4 v35, s[36:39], s73 offen offset:3072 lds\n"
"v_mfma_f32_16x16x32_bf16 v[148:151], a[4:7], v[64:67], v[148:151]\n"
"v_mfma_f32_16x16x32_bf16 v[148:151], a[20:23], v[68:71], v[148:151]\n"
"v_mfma_f32_16x16x32_bf16 v[152:155], a[8:11], v[64:67], v[152:155]\n"
"s_add_u32 m0, s30, 0x9400\n"
"v_mfma_f32_16x16x32_bf16 v[152:155], a[24:27], v[68:71], v[152:155]\n"
"buffer_load_dwordx4 v36, s[36:39], s73 offen offset:0 lds\n"
"v_mfma_f32_16x16x32_bf16 v[156:159], a[12:15], v[64:67], v[156:159]\n"
"ds_read_b128 v[64:67], v43 offset:32768\n"
"v_mfma_f32_16x16x32_bf16 v[156:159], a[28:31], v[68:71], v[156:159]\n"
"ds_read_b128 v[68:71], v43 offset:33280\n"
"v_mfma_f32_16x16x32_bf16 v[160:163], a[0:3], v[72:75], v[160:163]\n"
"s_add_u32 m0, s30, 0x6000\n"
"v_mfma_f32_16x16x32_bf16 v[160:163], a[16:19], v[76:79], v[160:163]\n"
"buffer_load_dwordx4 v37, s[40:43], s90 offen offset:0 lds\n"
"v_mfma_f32_16x16x32_bf16 v[164:167], a[4:7], v[72:75], v[164:167]\n"
"v_mfma_f32_16x16x32_bf16 v[164:167], a[20:23], v[76:79], v[164:167]\n"
"v_mfma_f32_16x16x32_bf16 v[168:171], a[8:11], v[72:75], v[168:171]\n"
"v_mfma_f32_16x16x32_bf16 v[168:171], a[24:27], v[76:79], v[168:171]\n"
"buffer_load_dwordx4 v38, s[40:43], s90 offen offset:1024 lds\n"
"v_mfma_f32_16x16x32_bf16 v[172:175], a[12:15], v[72:75], v[172:175]\n"
"ds_read_b128 v[72:75], v43 offset:34816\n"
"v_mfma_f32_16x16x32_bf16 v[172:175], a[28:31], v[76:79], v[172:175]\n"
"s_waitcnt lgkmcnt(3)\n"
"ds_read_b128 v[76:79], v43 offset:35328\n"
"v_mfma_f32_16x16x32_bf16 v[176:179], a[0:3], v[48:51], v[176:179]\n"
"v_mfma_f32_16x16x32_bf16 v[176:179], a[16:19], v[52:55], v[176:179]\n"
"buffer_load_dwordx4 v39, s[40:43], s90 offen offset:2048 lds\n"
"v_mfma_f32_16x16x32_bf16 v[180:183], a[4:7], v[48:51], v[180:183]\n"
"v_mfma_f32_16x16x32_bf16 v[180:183], a[20:23], v[52:55], v[180:183]\n"
"v_mfma_f32_16x16x32_bf16 v[184:187], a[8:11], v[48:51], v[184:187]\n"
"v_mfma_f32_16x16x32_bf16 v[184:187], a[24:27], v[52:55], v[184:187]\n"
"buffer_load_dwordx4 v40, s[40:43], s90 offen offset:3072 lds\n"
"v_mfma_f32_16x16x32_bf16 v[188:191], a[12:15], v[48:51], v[188:191]\n"
"ds_read_b128 v[48:51], v44 offset:0\n"
"v_mfma_f32_16x16x32_bf16 v[188:191], a[28:31], v[52:55], v[188:191]\n"
"ds_read_b128 v[52:55], v44 offset:512\n"
"v_mfma_f32_16x16x32_bf16 v[192:195], a[0:3], v[56:59], v[192:195]\n"
"s_add_u32 s76, s90, 0x40000\n"
"s_add_u32 m0, s30, 0x7000\n"
"v_mfma_f32_16x16x32_bf16 v[192:195], a[16:19], v[60:63], v[192:195]\n"
"buffer_load_dwordx4 v37, s[40:43], s76 offen offset:0 lds\n"
"v_mfma_f32_16x16x32_bf16 v[196:199], a[4:7], v[56:59], v[196:199]\n"
"v_mfma_f32_16x16x32_bf16 v[196:199], a[20:23], v[60:63], v[196:199]\n"
"v_mfma_f32_16x16x32_bf16 v[200:203], a[8:11], v[56:59], v[200:203]\n"
"v_mfma_f32_16x16x32_bf16 v[200:203], a[24:27], v[60:63], v[200:203]\n"
"buffer_load_dwordx4 v38, s[40:43], s76 offen offset:1024 lds\n"
"v_mfma_f32_16x16x32_bf16 v[204:207], a[12:15], v[56:59], v[204:207]\n"
"v_mfma_f32_16x16x32_bf16 v[204:207], a[28:31], v[60:63], v[204:207]\n"
"s_waitcnt lgkmcnt(2)\n"
"v_mfma_f32_16x16x32_bf16 v[208:211], a[0:3], v[64:67], v[208:211]\n"
"v_mfma_f32_16x16x32_bf16 v[208:211], a[16:19], v[68:71], v[208:211]\n"
"buffer_load_dwordx4 v39, s[40:43], s76 offen offset:2048 lds\n"
"v_mfma_f32_16x16x32_bf16 v[212:215], a[4:7], v[64:67], v[212:215]\n"
"v_mfma_f32_16x16x32_bf16 v[212:215], a[20:23], v[68:71], v[212:215]\n"
"v_mfma_f32_16x16x32_bf16 v[216:219], a[8:11], v[64:67], v[216:219]\n"
"v_mfma_f32_16x16x32_bf16 v[216:219], a[24:27], v[68:71], v[216:219]\n"
"buffer_load_dwordx4 v40, s[40:43], s76 offen offset:3072 lds\n"
"v_mfma_f32_16x16x32_bf16 v[220:223], a[12:15], v[64:67], v[220:223]\n"
"v_mfma_f32_16x16x32_bf16 v[220:223], a[28:31], v[68:71], v[220:223]\n"
"s_add_u32 s88, s72, 7\n"
"v_mfma_f32_16x16x32_bf16 v[224:227], a[0:3], v[72:75], v[224:227]\n"
"s_and_b32 s88, s88, 15\n"
"v_mfma_f32_16x16x32_bf16 v[224:227], a[16:19], v[76:79], v[224:227]\n"
"s_waitcnt vmcnt(21) lgkmcnt(0)\n"
"s_barrier\n"
"s_lshl_b32 s88, s88, 7\n"
"v_mfma_f32_16x16x32_bf16 v[228:231], a[4:7], v[72:75], v[228:231]\n"
"ds_read_b128 v[56:59], v27 offset:12288\n"
"ds_read_b128 v[60:63], v27 offset:12800\n"
"v_mfma_f32_16x16x32_bf16 v[228:231], a[20:23], v[76:79], v[228:231]\n"
"ds_read_b128 v[64:67], v27 offset:14336\n"
"ds_read_b128 v[68:71], v27 offset:14848\n"
"v_mfma_f32_16x16x32_bf16 v[232:235], a[8:11], v[72:75], v[232:235]\n"
"v_mfma_f32_16x16x32_bf16 v[232:235], a[24:27], v[76:79], v[232:235]\n"
"v_mfma_f32_16x16x32_bf16 v[236:239], a[12:15], v[72:75], v[236:239]\n"
"ds_read_b128 v[72:75], v41 offset:12288\n"
"v_mfma_f32_16x16x32_bf16 v[236:239], a[28:31], v[76:79], v[236:239]\n"
"ds_read_b128 v[76:79], v41 offset:12800\n"
"v_mfma_f32_16x16x32_bf16 v[240:243], a[0:3], v[48:51], v[240:243]\n"
"s_waitcnt vmcnt(17)\n"
"ds_read_b64_tr_b16 a[0:1], v45 offset:16384\n"
"v_mfma_f32_16x16x32_bf16 v[240:243], a[16:19], v[52:55], v[240:243]\n"
"ds_read_b64_tr_b16 a[2:3], v45 offset:16640\n"
"v_mfma_f32_16x16x32_bf16 v[244:247], a[4:7], v[48:51], v[244:247]\n"
"s_waitcnt vmcnt(13)\n"
"s_add_u32 m0, s30, 0x8000\n"
"ds_read_b64_tr_b16 a[16:17], v45 offset:20480\n"
"v_mfma_f32_16x16x32_bf16 v[244:247], a[20:23], v[52:55], v[244:247]\n"
"buffer_load_dwordx4 v32, s[36:39], s88 offen offset:0 lds\n"
"ds_read_b64_tr_b16 a[18:19], v45 offset:20736\n"
"v_mfma_f32_16x16x32_bf16 v[248:251], a[8:11], v[48:51], v[248:251]\n"
"ds_read_b64_tr_b16 a[4:5], v46 offset:16384\n"
"v_mfma_f32_16x16x32_bf16 v[248:251], a[24:27], v[52:55], v[248:251]\n"
"ds_read_b64_tr_b16 a[6:7], v46 offset:16640\n"
"v_mfma_f32_16x16x32_bf16 v[252:255], a[12:15], v[48:51], v[252:255]\n"
"ds_read_b128 v[48:51], v41 offset:14336\n"
"v_mfma_f32_16x16x32_bf16 v[252:255], a[28:31], v[52:55], v[252:255]\n"
"s_waitcnt lgkmcnt(9)\n"
"buffer_load_dwordx4 v33, s[36:39], s88 offen offset:1024 lds\n"
"ds_read_b128 v[52:55], v41 offset:14848\n"
"v_mfma_f32_16x16x32_bf16 v[112:115], a[32:35], v[56:59], v[112:115]\n"
"ds_read_b64_tr_b16 a[20:21], v46 offset:20480\n"
"v_mfma_f32_16x16x32_bf16 v[112:115], a[48:51], v[60:63], v[112:115]\n"
"ds_read_b64_tr_b16 a[22:23], v46 offset:20736\n"
"v_mfma_f32_16x16x32_bf16 v[116:119], a[36:39], v[56:59], v[116:119]\n"
"ds_read_b64_tr_b16 a[8:9], v45 offset:16896\n"
"v_mfma_f32_16x16x32_bf16 v[116:119], a[52:55], v[60:63], v[116:119]\n"
"buffer_load_dwordx4 v34, s[36:39], s88 offen offset:2048 lds\n"
"ds_read_b64_tr_b16 a[10:11], v45 offset:17152\n"
"v_mfma_f32_16x16x32_bf16 v[120:123], a[40:43], v[56:59], v[120:123]\n"
"ds_read_b64_tr_b16 a[24:25], v45 offset:20992\n"
"v_mfma_f32_16x16x32_bf16 v[120:123], a[56:59], v[60:63], v[120:123]\n"
"s_add_u32 s91, s72, 8\n"
"ds_read_b64_tr_b16 a[26:27], v45 offset:21248\n"
"v_mfma_f32_16x16x32_bf16 v[124:127], a[44:47], v[56:59], v[124:127]\n"
"s_and_b32 s91, s91, 15\n"
"ds_read_b128 v[56:59], v42 offset:12288\n"
"v_mfma_f32_16x16x32_bf16 v[124:127], a[60:63], v[60:63], v[124:127]\n"
"s_lshl_b32 s91, s91, 19\n"
"buffer_load_dwordx4 v35, s[36:39], s88 offen offset:3072 lds\n"
"ds_read_b128 v[60:63], v42 offset:12800\n"
"v_mfma_f32_16x16x32_bf16 v[128:131], a[32:35], v[64:67], v[128:131]\n"
"ds_read_b64_tr_b16 a[12:13], v46 offset:16896\n"
"v_mfma_f32_16x16x32_bf16 v[128:131], a[48:51], v[68:71], v[128:131]\n"
"ds_read_b64_tr_b16 a[14:15], v46 offset:17152\n"
"v_mfma_f32_16x16x32_bf16 v[132:135], a[36:39], v[64:67], v[132:135]\n"
"ds_read_b64_tr_b16 a[28:29], v46 offset:20992\n"
"v_mfma_f32_16x16x32_bf16 v[132:135], a[52:55], v[68:71], v[132:135]\n"
"ds_read_b64_tr_b16 a[30:31], v46 offset:21248\n"
"v_mfma_f32_16x16x32_bf16 v[136:139], a[40:43], v[64:67], v[136:139]\n"
"v_mfma_f32_16x16x32_bf16 v[136:139], a[56:59], v[68:71], v[136:139]\n"
"v_mfma_f32_16x16x32_bf16 v[140:143], a[44:47], v[64:67], v[140:143]\n"
"ds_read_b128 v[64:67], v42 offset:14336\n"
"v_mfma_f32_16x16x32_bf16 v[140:143], a[60:63], v[68:71], v[140:143]\n"
"s_waitcnt lgkmcnt(8)\n"
"ds_read_b128 v[68:71], v42 offset:14848\n"
"v_mfma_f32_16x16x32_bf16 v[144:147], a[32:35], v[72:75], v[144:147]\n"
"s_add_u32 m0, s30, 0x4000\n"
"v_mfma_f32_16x16x32_bf16 v[144:147], a[48:51], v[76:79], v[144:147]\n"
"s_waitcnt lgkmcnt(4)\n"
"buffer_load_dwordx4 v37, s[40:43], s91 offen offset:0 lds\n"
"v_mfma_f32_16x16x32_bf16 v[148:151], a[36:39], v[72:75], v[148:151]\n"
"v_mfma_f32_16x16x32_bf16 v[148:151], a[52:55], v[76:79], v[148:151]\n"
"v_mfma_f32_16x16x32_bf16 v[152:155], a[40:43], v[72:75], v[152:155]\n"
"v_mfma_f32_16x16x32_bf16 v[152:155], a[56:59], v[76:79], v[152:155]\n"
"buffer_load_dwordx4 v38, s[40:43], s91 offen offset:1024 lds\n"
"v_mfma_f32_16x16x32_bf16 v[156:159], a[44:47], v[72:75], v[156:159]\n"
"ds_read_b128 v[72:75], v43 offset:12288\n"
"v_mfma_f32_16x16x32_bf16 v[156:159], a[60:63], v[76:79], v[156:159]\n"
"ds_read_b128 v[76:79], v43 offset:12800\n"
"v_mfma_f32_16x16x32_bf16 v[160:163], a[32:35], v[48:51], v[160:163]\n"
"v_mfma_f32_16x16x32_bf16 v[160:163], a[48:51], v[52:55], v[160:163]\n"
"buffer_load_dwordx4 v39, s[40:43], s91 offen offset:2048 lds\n"
"v_mfma_f32_16x16x32_bf16 v[164:167], a[36:39], v[48:51], v[164:167]\n"
"v_mfma_f32_16x16x32_bf16 v[164:167], a[52:55], v[52:55], v[164:167]\n"
"v_mfma_f32_16x16x32_bf16 v[168:171], a[40:43], v[48:51], v[168:171]\n"
"s_add_u32 m0, s30, 0\n"
"v_mfma_f32_16x16x32_bf16 v[168:171], a[56:59], v[52:55], v[168:171]\n"
"buffer_load_dwordx4 v17, s[44:47], 0 offen offset:0 nt lds\n"
"v_mfma_f32_16x16x32_bf16 v[172:175], a[44:47], v[48:51], v[172:175]\n"
"ds_read_b128 v[48:51], v43 offset:14336\n"
"v_mfma_f32_16x16x32_bf16 v[172:175], a[60:63], v[52:55], v[172:175]\n"
"ds_read_b128 v[52:55], v43 offset:14848\n"
"v_mfma_f32_16x16x32_bf16 v[176:179], a[32:35], v[56:59], v[176:179]\n"
"v_mfma_f32_16x16x32_bf16 v[176:179], a[48:51], v[60:63], v[176:179]\n"
"buffer_load_dwordx4 v17, s[44:47], 0 offen offset:1024 nt lds\n"
"v_mfma_f32_16x16x32_bf16 v[180:183], a[36:39], v[56:59], v[180:183]\n"
"v_mfma_f32_16x16x32_bf16 v[180:183], a[52:55], v[60:63], v[180:183]\n"
"v_mfma_f32_16x16x32_bf16 v[184:187], a[40:43], v[56:59], v[184:187]\n"
"v_mfma_f32_16x16x32_bf16 v[184:187], a[56:59], v[60:63], v[184:187]\n"
"buffer_load_dwordx4 v18, s[44:47], 0 offen offset:2048 nt lds\n"
"v_mfma_f32_16x16x32_bf16 v[188:191], a[44:47], v[56:59], v[188:191]\n"
"ds_read_b128 v[56:59], v30 offset:0\n"
"v_mfma_f32_16x16x32_bf16 v[188:191], a[60:63], v[60:63], v[188:191]\n"
"s_waitcnt lgkmcnt(3)\n"
"ds_read_b128 v[60:63], v30 offset:512\n"
"v_mfma_f32_16x16x32_bf16 v[192:195], a[32:35], v[64:67], v[192:195]\n"
"v_mfma_f32_16x16x32_bf16 v[192:195], a[48:51], v[68:71], v[192:195]\n"
"buffer_load_dwordx4 v18, s[44:47], 0 offen offset:3072 nt lds\n"
"v_mfma_f32_16x16x32_bf16 v[196:199], a[36:39], v[64:67], v[196:199]\n"
"v_mfma_f32_16x16x32_bf16 v[196:199], a[52:55], v[68:71], v[196:199]\n"
"v_mfma_f32_16x16x32_bf16 v[200:203], a[40:43], v[64:67], v[200:203]\n"
"s_mov_b32 s77, 0x4000\n"
"s_add_u32 m0, s30, 0x1000\n"
"v_mfma_f32_16x16x32_bf16 v[200:203], a[56:59], v[68:71], v[200:203]\n"
"buffer_load_dwordx4 v17, s[44:47], s77 offen offset:0 nt lds\n"
"v_mfma_f32_16x16x32_bf16 v[204:207], a[44:47], v[64:67], v[204:207]\n"
"v_mfma_f32_16x16x32_bf16 v[204:207], a[60:63], v[68:71], v[204:207]\n"
"v_mfma_f32_16x16x32_bf16 v[208:211], a[32:35], v[72:75], v[208:211]\n"
"v_mfma_f32_16x16x32_bf16 v[208:211], a[48:51], v[76:79], v[208:211]\n"
"buffer_load_dwordx4 v17, s[44:47], s77 offen offset:1024 nt lds\n"
"v_mfma_f32_16x16x32_bf16 v[212:215], a[36:39], v[72:75], v[212:215]\n"
"v_mfma_f32_16x16x32_bf16 v[212:215], a[52:55], v[76:79], v[212:215]\n"
"v_mfma_f32_16x16x32_bf16 v[216:219], a[40:43], v[72:75], v[216:219]\n"
"v_mfma_f32_16x16x32_bf16 v[216:219], a[56:59], v[76:79], v[216:219]\n"
"buffer_load_dwordx4 v18, s[44:47], s77 offen offset:2048 nt lds\n"
"v_mfma_f32_16x16x32_bf16 v[220:223], a[44:47], v[72:75], v[220:223]\n"
"v_mfma_f32_16x16x32_bf16 v[220:223], a[60:63], v[76:79], v[220:223]\n"
"s_waitcnt lgkmcnt(0)\n"
"v_mfma_f32_16x16x32_bf16 v[224:227], a[32:35], v[48:51], v[224:227]\n"
"v_mfma_f32_16x16x32_bf16 v[224:227], a[48:51], v[52:55], v[224:227]\n"
"s_waitcnt vmcnt(22)\n"
"s_barrier\n"
"buffer_load_dwordx4 v18, s[44:47], s77 offen offset:3072 nt lds\n"
"v_mfma_f32_16x16x32_bf16 v[228:231], a[36:39], v[48:51], v[228:231]\n"
"ds_read_b128 v[64:67], v27 offset:8192\n"
"ds_read_b128 v[68:71], v27 offset:8704\n"
"v_mfma_f32_16x16x32_bf16 v[228:231], a[52:55], v[52:55], v[228:231]\n"
"ds_read_b128 v[72:75], v27 offset:10240\n"
"ds_read_b128 v[76:79], v27 offset:10752\n"
"v_mfma_f32_16x16x32_bf16 v[232:235], a[40:43], v[48:51], v[232:235]\n"
"v_mfma_f32_16x16x32_bf16 v[232:235], a[56:59], v[52:55], v[232:235]\n"
"v_mfma_f32_16x16x32_bf16 v[236:239], a[44:47], v[48:51], v[236:239]\n"
"ds_read_b128 v[48:51], v41 offset:8192\n"
"v_mfma_f32_16x16x32_bf16 v[236:239], a[60:63], v[52:55], v[236:239]\n"
"ds_read_b128 v[52:55], v41 offset:8704\n"
"v_mfma_f32_16x16x32_bf16 v[240:243], a[32:35], v[56:59], v[240:243]\n"
"s_waitcnt vmcnt(19)\n"
"ds_read_b64_tr_b16 a[32:33], v45 offset:24576\n"
"v_mfma_f32_16x16x32_bf16 v[240:243], a[48:51], v[60:63], v[240:243]\n"
"ds_read_b64_tr_b16 a[34:35], v45 offset:24832\n"
"v_mfma_f32_16x16x32_bf16 v[244:247], a[36:39], v[56:59], v[244:247]\n"
"s_waitcnt vmcnt(15)\n"
"ds_read_b64_tr_b16 a[48:49], v45 offset:28672\n"
"v_mfma_f32_16x16x32_bf16 v[244:247], a[52:55], v[60:63], v[244:247]\n"
"ds_read_b64_tr_b16 a[50:51], v45 offset:28928\n"
"v_mfma_f32_16x16x32_bf16 v[248:251], a[40:43], v[56:59], v[248:251]\n"
"ds_read_b64_tr_b16 a[36:37], v46 offset:24576\n"
"v_mfma_f32_16x16x32_bf16 v[248:251], a[56:59], v[60:63], v[248:251]\n"
"ds_read_b64_tr_b16 a[38:39], v46 offset:24832\n"
"v_mfma_f32_16x16x32_bf16 v[252:255], a[44:47], v[56:59], v[252:255]\n"
"ds_read_b128 v[56:59], v41 offset:10240\n"
"v_mfma_f32_16x16x32_bf16 v[252:255], a[60:63], v[60:63], v[252:255]\n"
"s_waitcnt lgkmcnt(9)\n"
"ds_read_b128 v[60:63], v41 offset:10752\n"
"v_mfma_f32_16x16x32_bf16 v[112:115], a[0:3], v[64:67], v[112:115]\n"
"ds_read_b64_tr_b16 a[52:53], v46 offset:28672\n"
"v_mfma_f32_16x16x32_bf16 v[112:115], a[16:19], v[68:71], v[112:115]\n"
"ds_read_b64_tr_b16 a[54:55], v46 offset:28928\n"
"v_mfma_f32_16x16x32_bf16 v[116:119], a[4:7], v[64:67], v[116:119]\n"
"ds_read_b64_tr_b16 a[40:41], v45 offset:25088\n"
"v_mfma_f32_16x16x32_bf16 v[116:119], a[20:23], v[68:71], v[116:119]\n"
"ds_read_b64_tr_b16 a[42:43], v45 offset:25344\n"
"v_mfma_f32_16x16x32_bf16 v[120:123], a[8:11], v[64:67], v[120:123]\n"
"ds_read_b64_tr_b16 a[56:57], v45 offset:29184\n"
"v_mfma_f32_16x16x32_bf16 v[120:123], a[24:27], v[68:71], v[120:123]\n"
"ds_read_b64_tr_b16 a[58:59], v45 offset:29440\n"
"v_mfma_f32_16x16x32_bf16 v[124:127], a[12:15], v[64:67], v[124:127]\n"
"ds_read_b128 v[64:67], v42 offset:8192\n"
"v_mfma_f32_16x16x32_bf16 v[124:127], a[28:31], v[68:71], v[124:127]\n"
"ds_read_b128 v[68:71], v42 offset:8704\n"
"v_mfma_f32_16x16x32_bf16 v[128:131], a[0:3], v[72:75], v[128:131]\n"
"s_add_u32 m0, s30, 0x4000\n"
"ds_read_b64_tr_b16 a[44:45], v46 offset:25088\n"
"v_mfma_f32_16x16x32_bf16 v[128:131], a[16:19], v[76:79], v[128:131]\n"
"buffer_load_dwordx4 v40, s[40:43], s91 offen offset:3072 lds\n"
"ds_read_b64_tr_b16 a[46:47], v46 offset:25344\n"
"v_mfma_f32_16x16x32_bf16 v[132:135], a[4:7], v[72:75], v[132:135]\n"
"ds_read_b64_tr_b16 a[60:61], v46 offset:29184\n"
"v_mfma_f32_16x16x32_bf16 v[132:135], a[20:23], v[76:79], v[132:135]\n"
"ds_read_b64_tr_b16 a[62:63], v46 offset:29440\n"
"v_mfma_f32_16x16x32_bf16 v[136:139], a[8:11], v[72:75], v[136:139]\n"
"s_add_u32 s78, s91, 0x40000\n"
"s_add_u32 m0, s30, 0x5000\n"
"v_mfma_f32_16x16x32_bf16 v[136:139], a[24:27], v[76:79], v[136:139]\n"
"buffer_load_dwordx4 v37, s[40:43], s78 offen offset:0 lds\n"
"v_mfma_f32_16x16x32_bf16 v[140:143], a[12:15], v[72:75], v[140:143]\n"
"ds_read_b128 v[72:75], v42 offset:10240\n"
"v_mfma_f32_16x16x32_bf16 v[140:143], a[28:31], v[76:79], v[140:143]\n"
"s_waitcnt lgkmcnt(8)\n"
"ds_read_b128 v[76:79], v42 offset:10752\n"
"v_mfma_f32_16x16x32_bf16 v[144:147], a[0:3], v[48:51], v[144:147]\n"
"v_mfma_f32_16x16x32_bf16 v[144:147], a[16:19], v[52:55], v[144:147]\n"
"s_add_u32 s89, s72, 8\n"
"buffer_load_dwordx4 v38, s[40:43], s78 offen offset:1024 lds\n"
"v_mfma_f32_16x16x32_bf16 v[148:151], a[4:7], v[48:51], v[148:151]\n"
"s_and_b32 s89, s89, 15\n"
"v_mfma_f32_16x16x32_bf16 v[148:151], a[20:23], v[52:55], v[148:151]\n"
"s_lshl_b32 s89, s89, 7\n"
"v_mfma_f32_16x16x32_bf16 v[152:155], a[8:11], v[48:51], v[152:155]\n"
"v_mfma_f32_16x16x32_bf16 v[152:155], a[24:27], v[52:55], v[152:155]\n"
"buffer_load_dwordx4 v39, s[40:43], s78 offen offset:2048 lds\n"
"v_mfma_f32_16x16x32_bf16 v[156:159], a[12:15], v[48:51], v[156:159]\n"
"ds_read_b128 v[48:51], v43 offset:8192\n"
"v_mfma_f32_16x16x32_bf16 v[156:159], a[28:31], v[52:55], v[156:159]\n"
"ds_read_b128 v[52:55], v43 offset:8704\n"
"v_mfma_f32_16x16x32_bf16 v[160:163], a[0:3], v[56:59], v[160:163]\n"
"v_mfma_f32_16x16x32_bf16 v[160:163], a[16:19], v[60:63], v[160:163]\n"
"buffer_load_dwordx4 v40, s[40:43], s78 offen offset:3072 lds\n"
"v_mfma_f32_16x16x32_bf16 v[164:167], a[4:7], v[56:59], v[164:167]\n"
"v_mfma_f32_16x16x32_bf16 v[164:167], a[20:23], v[60:63], v[164:167]\n"
"v_mfma_f32_16x16x32_bf16 v[168:171], a[8:11], v[56:59], v[168:171]\n"
"s_add_u32 m0, s30, 0x3000\n"
"v_mfma_f32_16x16x32_bf16 v[168:171], a[24:27], v[60:63], v[168:171]\n"
"buffer_load_dwordx4 v32, s[36:39], s89 offen offset:0 lds\n"
"v_mfma_f32_16x16x32_bf16 v[172:175], a[12:15], v[56:59], v[172:175]\n"
"ds_read_b128 v[56:59], v43 offset:10240\n"
"v_mfma_f32_16x16x32_bf16 v[172:175], a[28:31], v[60:63], v[172:175]\n"
"s_waitcnt lgkmcnt(3)\n"
"ds_read_b128 v[60:63], v43 offset:10752\n"
"v_mfma_f32_16x16x32_bf16 v[176:179], a[0:3], v[64:67], v[176:179]\n"
"v_mfma_f32_16x16x32_bf16 v[176:179], a[16:19], v[68:71], v[176:179]\n"
"buffer_load_dwordx4 v33, s[36:39], s89 offen offset:1024 lds\n"
"v_mfma_f32_16x16x32_bf16 v[180:183], a[4:7], v[64:67], v[180:183]\n"
"v_mfma_f32_16x16x32_bf16 v[180:183], a[20:23], v[68:71], v[180:183]\n"
"v_mfma_f32_16x16x32_bf16 v[184:187], a[8:11], v[64:67], v[184:187]\n"
"v_mfma_f32_16x16x32_bf16 v[184:187], a[24:27], v[68:71], v[184:187]\n"
"s_add_u32 s74, s72, 9\n"
"buffer_load_dwordx4 v34, s[36:39], s89 offen offset:2048 lds\n"
"v_mfma_f32_16x16x32_bf16 v[188:191], a[12:15], v[64:67], v[188:191]\n"
"s_and_b32 s74, s74, 15\n"
"ds_read_b128 v[64:67], v44 offset:1024\n"
"v_mfma_f32_16x16x32_bf16 v[188:191], a[28:31], v[68:71], v[188:191]\n"
"s_lshl_b32 s74, s74, 19\n"
"ds_read_b128 v[68:71], v44 offset:1536\n"
"v_mfma_f32_16x16x32_bf16 v[192:195], a[0:3], v[72:75], v[192:195]\n"
"v_mfma_f32_16x16x32_bf16 v[192:195], a[16:19], v[76:79], v[192:195]\n"
"buffer_load_dwordx4 v35, s[36:39], s89 offen offset:3072 lds\n"
"v_mfma_f32_16x16x32_bf16 v[196:199], a[4:7], v[72:75], v[196:199]\n"
"v_mfma_f32_16x16x32_bf16 v[196:199], a[20:23], v[76:79], v[196:199]\n"
"v_mfma_f32_16x16x32_bf16 v[200:203], a[8:11], v[72:75], v[200:203]\n"
"s_add_u32 m0, s30, 0x9000\n"
"v_mfma_f32_16x16x32_bf16 v[200:203], a[24:27], v[76:79], v[200:203]\n"
"buffer_load_dwordx4 v36, s[36:39], s89 offen offset:0 lds\n"
"v_mfma_f32_16x16x32_bf16 v[204:207], a[12:15], v[72:75], v[204:207]\n"
"v_mfma_f32_16x16x32_bf16 v[204:207], a[28:31], v[76:79], v[204:207]\n"
"s_waitcnt lgkmcnt(2)\n"
"v_mfma_f32_16x16x32_bf16 v[208:211], a[0:3], v[48:51], v[208:211]\n"
"s_add_u32 m0, s30, 0x6000\n"
"v_mfma_f32_16x16x32_bf16 v[208:211], a[16:19], v[52:55], v[208:211]\n"
"buffer_load_dwordx4 v37, s[40:43], s74 offen offset:0 lds\n"
"v_mfma_f32_16x16x32_bf16 v[212:215], a[4:7], v[48:51], v[212:215]\n"
"v_mfma_f32_16x16x32_bf16 v[212:215], a[20:23], v[52:55], v[212:215]\n"
"v_mfma_f32_16x16x32_bf16 v[216:219], a[8:11], v[48:51], v[216:219]\n"
"v_mfma_f32_16x16x32_bf16 v[216:219], a[24:27], v[52:55], v[216:219]\n"
"buffer_load_dwordx4 v38, s[40:43], s74 offen offset:1024 lds\n"
"v_mfma_f32_16x16x32_bf16 v[220:223], a[12:15], v[48:51], v[220:223]\n"
"v_mfma_f32_16x16x32_bf16 v[220:223], a[28:31], v[52:55], v[220:223]\n"
"v_mfma_f32_16x16x32_bf16 v[224:227], a[0:3], v[56:59], v[224:227]\n"
"v_mfma_f32_16x16x32_bf16 v[224:227], a[16:19], v[60:63], v[224:227]\n"
"s_waitcnt vmcnt(23) lgkmcnt(0)\n"
"s_barrier\n"
"buffer_load_dwordx4 v39, s[40:43], s74 offen offset:2048 lds\n"
"v_mfma_f32_16x16x32_bf16 v[228:231], a[4:7], v[56:59], v[228:231]\n"
"ds_read_b128 v[72:75], v27 offset:32768\n"
"ds_read_b128 v[76:79], v27 offset:33280\n"
"v_mfma_f32_16x16x32_bf16 v[228:231], a[20:23], v[60:63], v[228:231]\n"
"ds_read_b128 v[48:51], v27 offset:34816\n"
"ds_read_b128 v[52:55], v27 offset:35328\n"
"v_mfma_f32_16x16x32_bf16 v[232:235], a[8:11], v[56:59], v[232:235]\n"
"v_mfma_f32_16x16x32_bf16 v[232:235], a[24:27], v[60:63], v[232:235]\n"
"buffer_load_dwordx4 v40, s[40:43], s74 offen offset:3072 lds\n"
"v_mfma_f32_16x16x32_bf16 v[236:239], a[12:15], v[56:59], v[236:239]\n"
"ds_read_b128 v[56:59], v41 offset:32768\n"
"v_mfma_f32_16x16x32_bf16 v[236:239], a[28:31], v[60:63], v[236:239]\n"
"s_add_u32 m0, s30, 0x7000\n"
"ds_read_b128 v[60:63], v41 offset:33280\n"
"v_mfma_f32_16x16x32_bf16 v[240:243], a[0:3], v[64:67], v[240:243]\n"
"s_waitcnt vmcnt(13)\n"
"s_add_u32 s84, s74, 0x40000\n"
"ds_read_b64_tr_b16 a[0:1], v45 offset:16384\n"
"v_mfma_f32_16x16x32_bf16 v[240:243], a[16:19], v[68:71], v[240:243]\n"
"buffer_load_dwordx4 v37, s[40:43], s84 offen offset:0 lds\n"
"ds_read_b64_tr_b16 a[2:3], v45 offset:16640\n"
"v_mfma_f32_16x16x32_bf16 v[244:247], a[4:7], v[64:67], v[244:247]\n"
"ds_read_b64_tr_b16 a[4:5], v46 offset:16384\n"
"v_mfma_f32_16x16x32_bf16 v[244:247], a[20:23], v[68:71], v[244:247]\n"
"ds_read_b64_tr_b16 a[6:7], v46 offset:16640\n"
"v_mfma_f32_16x16x32_bf16 v[248:251], a[8:11], v[64:67], v[248:251]\n"
"s_add_u32 s73, s72, 9\n"
"buffer_load_dwordx4 v38, s[40:43], s84 offen offset:1024 lds\n"
"ds_read_b64_tr_b16 a[8:9], v45 offset:16896\n"
"v_mfma_f32_16x16x32_bf16 v[248:251], a[24:27], v[68:71], v[248:251]\n"
"s_and_b32 s73, s73, 15\n"
"ds_read_b64_tr_b16 a[10:11], v45 offset:17152\n"
"v_mfma_f32_16x16x32_bf16 v[252:255], a[12:15], v[64:67], v[252:255]\n"
"s_lshl_b32 s73, s73, 7\n"
"ds_read_b128 v[64:67], v41 offset:34816\n"
"v_mfma_f32_16x16x32_bf16 v[252:255], a[28:31], v[68:71], v[252:255]\n"
"s_waitcnt lgkmcnt(9)\n"
"ds_read_b128 v[68:71], v41 offset:35328\n"
"v_mfma_f32_16x16x32_bf16 v[112:115], a[32:35], v[72:75], v[112:115]\n"
"buffer_load_dwordx4 v39, s[40:43], s84 offen offset:2048 lds\n"
"ds_read_b64_tr_b16 a[12:13], v46 offset:16896\n"
"v_mfma_f32_16x16x32_bf16 v[112:115], a[48:51], v[76:79], v[112:115]\n"
"ds_read_b64_tr_b16 a[14:15], v46 offset:17152\n"
"v_mfma_f32_16x16x32_bf16 v[116:119], a[36:39], v[72:75], v[116:119]\n"
"v_mfma_f32_16x16x32_bf16 v[116:119], a[52:55], v[76:79], v[116:119]\n"
"s_waitcnt vmcnt(12)\n"
"ds_read_b64_tr_b16 a[16:17], v45 offset:20480\n"
"v_mfma_f32_16x16x32_bf16 v[120:123], a[40:43], v[72:75], v[120:123]\n"
"buffer_load_dwordx4 v40, s[40:43], s84 offen offset:3072 lds\n"
"ds_read_b64_tr_b16 a[18:19], v45 offset:20736\n"
"v_mfma_f32_16x16x32_bf16 v[120:123], a[56:59], v[76:79], v[120:123]\n"
"ds_read_b64_tr_b16 a[20:21], v46 offset:20480\n"
"v_mfma_f32_16x16x32_bf16 v[124:127], a[44:47], v[72:75], v[124:127]\n"
"ds_read_b128 v[72:75], v42 offset:32768\n"
"v_mfma_f32_16x16x32_bf16 v[124:127], a[60:63], v[76:79], v[124:127]\n"
"s_add_u32 m0, s30, 0x2000\n"
"ds_read_b128 v[76:79], v42 offset:33280\n"
"v_mfma_f32_16x16x32_bf16 v[128:131], a[32:35], v[48:51], v[128:131]\n"
"buffer_load_dwordx4 v32, s[36:39], s73 offen offset:0 lds\n"
"ds_read_b64_tr_b16 a[22:23], v46 offset:20736\n"
"v_mfma_f32_16x16x32_bf16 v[128:131], a[48:51], v[52:55], v[128:131]\n"
"ds_read_b64_tr_b16 a[24:25], v45 offset:20992\n"
"v_mfma_f32_16x16x32_bf16 v[132:135], a[36:39], v[48:51], v[132:135]\n"
"ds_read_b64_tr_b16 a[26:27], v45 offset:21248\n"
"v_mfma_f32_16x16x32_bf16 v[132:135], a[52:55], v[52:55], v[132:135]\n"
"ds_read_b64_tr_b16 a[28:29], v46 offset:20992\n"
"v_mfma_f32_16x16x32_bf16 v[136:139], a[40:43], v[48:51], v[136:139]\n"
"s_add_u32 s90, s72, 10\n"
"buffer_load_dwordx4 v33, s[36:39], s73 offen offset:1024 lds\n"
"ds_read_b64_tr_b16 a[30:31], v46 offset:21248\n"
"v_mfma_f32_16x16x32_bf16 v[136:139], a[56:59], v[52:55], v[136:139]\n"
"s_and_b32 s90, s90, 15\n"
"v_mfma_f32_16x16x32_bf16 v[140:143], a[44:47], v[48:51], v[140:143]\n"
"s_lshl_b32 s90, s90, 19\n"
"ds_read_b128 v[48:51], v42 offset:34816\n"
"v_mfma_f32_16x16x32_bf16 v[140:143], a[60:63], v[52:55], v[140:143]\n"
"s_waitcnt lgkmcnt(10)\n"
"ds_read_b128 v[52:55], v42 offset:35328\n"
"v_mfma_f32_16x16x32_bf16 v[144:147], a[32:35], v[56:59], v[144:147]\n"
"buffer_load_dwordx4 v34, s[36:39], s73 offen offset:2048 lds\n"
"v_mfma_f32_16x16x32_bf16 v[144:147], a[48:51], v[60:63], v[144:147]\n"
"v_mfma_f32_16x16x32_bf16 v[148:151], a[36:39], v[56:59], v[148:151]\n"
"v_mfma_f32_16x16x32_bf16 v[148:151], a[52:55], v[60:63], v[148:151]\n"
"v_mfma_f32_16x16x32_bf16 v[152:155], a[40:43], v[56:59], v[152:155]\n"
"buffer_load_dwordx4 v35, s[36:39], s73 offen offset:3072 lds\n"
"v_mfma_f32_16x16x32_bf16 v[152:155], a[56:59], v[60:63], v[152:155]\n"
"v_mfma_f32_16x16x32_bf16 v[156:159], a[44:47], v[56:59], v[156:159]\n"
"ds_read_b128 v[56:59], v43 offset:32768\n"
"v_mfma_f32_16x16x32_bf16 v[156:159], a[60:63], v[60:63], v[156:159]\n"
"s_add_u32 m0, s30, 0x4000\n"
"ds_read_b128 v[60:63], v43 offset:33280\n"
"v_mfma_f32_16x16x32_bf16 v[160:163], a[32:35], v[64:67], v[160:163]\n"
"buffer_load_dwordx4 v37, s[40:43], s90 offen offset:0 lds\n"
"v_mfma_f32_16x16x32_bf16 v[160:163], a[48:51], v[68:71], v[160:163]\n"
"v_mfma_f32_16x16x32_bf16 v[164:167], a[36:39], v[64:67], v[164:167]\n"
"v_mfma_f32_16x16x32_bf16 v[164:167], a[52:55], v[68:71], v[164:167]\n"
"v_mfma_f32_16x16x32_bf16 v[168:171], a[40:43], v[64:67], v[168:171]\n"
"buffer_load_dwordx4 v38, s[40:43], s90 offen offset:1024 lds\n"
"v_mfma_f32_16x16x32_bf16 v[168:171], a[56:59], v[68:71], v[168:171]\n"
"v_mfma_f32_16x16x32_bf16 v[172:175], a[44:47], v[64:67], v[172:175]\n"
"buffer_load_dwordx4 a[64:67], v19, s[48:51], 0 offen offset:0 nt\n"
"ds_read_b128 v[64:67], v43 offset:34816\n"
"v_mfma_f32_16x16x32_bf16 v[172:175], a[60:63], v[68:71], v[172:175]\n"
"s_waitcnt lgkmcnt(3)\n"
"ds_read_b128 v[68:71], v43 offset:35328\n"
"v_mfma_f32_16x16x32_bf16 v[176:179], a[32:35], v[72:75], v[176:179]\n"
"v_mfma_f32_16x16x32_bf16 v[176:179], a[48:51], v[76:79], v[176:179]\n"
"v_mfma_f32_16x16x32_bf16 v[180:183], a[36:39], v[72:75], v[180:183]\n"
"buffer_load_dwordx4 a[68:71], v20, s[48:51], 0 offen offset:1024 nt\n"
"v_mfma_f32_16x16x32_bf16 v[180:183], a[52:55], v[76:79], v[180:183]\n"
"v_mfma_f32_16x16x32_bf16 v[184:187], a[40:43], v[72:75], v[184:187]\n"
"v_mfma_f32_16x16x32_bf16 v[184:187], a[56:59], v[76:79], v[184:187]\n"
"v_mfma_f32_16x16x32_bf16 v[188:191], a[44:47], v[72:75], v[188:191]\n"
"buffer_load_dwordx4 a[72:75], v19, s[48:51], 0 offen offset:2048 nt\n"
"ds_read_b128 v[72:75], v30 offset:1024\n"
"v_mfma_f32_16x16x32_bf16 v[188:191], a[60:63], v[76:79], v[188:191]\n"
"ds_read_b128 v[76:79], v30 offset:1536\n"
"v_mfma_f32_16x16x32_bf16 v[192:195], a[32:35], v[48:51], v[192:195]\n"
"v_mfma_f32_16x16x32_bf16 v[192:195], a[48:51], v[52:55], v[192:195]\n"
"v_mfma_f32_16x16x32_bf16 v[196:199], a[36:39], v[48:51], v[196:199]\n"
"buffer_load_dwordx4 a[76:79], v20, s[48:51], 0 offen offset:3072 nt\n"
"v_mfma_f32_16x16x32_bf16 v[196:199], a[52:55], v[52:55], v[196:199]\n"
"v_mfma_f32_16x16x32_bf16 v[200:203], a[40:43], v[48:51], v[200:203]\n"
"v_mfma_f32_16x16x32_bf16 v[200:203], a[56:59], v[52:55], v[200:203]\n"
"s_mov_b32 s96, 0x8000\n"
"v_mfma_f32_16x16x32_bf16 v[204:207], a[44:47], v[48:51], v[204:207]\n"
"buffer_load_dwordx4 a[80:83], v17, s[44:47], s96 offen offset:0 nt\n"
"v_mfma_f32_16x16x32_bf16 v[204:207], a[60:63], v[52:55], v[204:207]\n"
"s_waitcnt lgkmcnt(2)\n"
"v_mfma_f32_16x16x32_bf16 v[208:211], a[32:35], v[56:59], v[208:211]\n"
"v_mfma_f32_16x16x32_bf16 v[208:211], a[48:51], v[60:63], v[208:211]\n"
"v_mfma_f32_16x16x32_bf16 v[212:215], a[36:39], v[56:59], v[212:215]\n"
"buffer_load_dwordx4 a[84:87], v17, s[44:47], s96 offen offset:1024 nt\n"
"v_mfma_f32_16x16x32_bf16 v[212:215], a[52:55], v[60:63], v[212:215]\n"
"v_mfma_f32_16x16x32_bf16 v[216:219], a[40:43], v[56:59], v[216:219]\n"
"v_mfma_f32_16x16x32_bf16 v[216:219], a[56:59], v[60:63], v[216:219]\n"
"s_getpc_b64 s[98:99]\n"
"v_mfma_f32_16x16x32_bf16 v[220:223], a[44:47], v[56:59], v[220:223]\n"
"buffer_load_dwordx4 a[88:91], v18, s[44:47], s96 offen offset:2048 nt\n"
"v_mfma_f32_16x16x32_bf16 v[220:223], a[60:63], v[60:63], v[220:223]\n"
"v_mfma_f32_16x16x32_bf16 v[224:227], a[32:35], v[64:67], v[224:227]\n"
"global_load_dword v26, v0, s[98:99] offset:1536\n"
"v_mfma_f32_16x16x32_bf16 v[224:227], a[48:51], v[68:71], v[224:227]\n"
"s_waitcnt vmcnt(22) lgkmcnt(0)\n"
"s_barrier\n"
"v_mfma_f32_16x16x32_bf16 v[228:231], a[36:39], v[64:67], v[228:231]\n"
"buffer_load_dwordx4 a[92:95], v18, s[44:47], s96 offen offset:3072 nt\n"
"ds_read_b128 v[48:51], v27 offset:12288\n"
"v_mfma_f32_16x16x32_bf16 v[228:231], a[52:55], v[68:71], v[228:231]\n"
"ds_read_b128 v[52:55], v27 offset:12800\n"
"ds_read_b128 v[56:59], v27 offset:14336\n"
"v_mfma_f32_16x16x32_bf16 v[232:235], a[40:43], v[64:67], v[232:235]\n"
"ds_read_b128 v[60:63], v27 offset:14848\n"
"v_mfma_f32_16x16x32_bf16 v[232:235], a[56:59], v[68:71], v[232:235]\n"
"v_mfma_f32_16x16x32_bf16 v[236:239], a[44:47], v[64:67], v[236:239]\n"
"ds_read_b128 v[64:67], v41 offset:12288\n"
"v_mfma_f32_16x16x32_bf16 v[236:239], a[60:63], v[68:71], v[236:239]\n"
"ds_read_b128 v[68:71], v41 offset:12800\n"
"v_mfma_f32_16x16x32_bf16 v[240:243], a[32:35], v[72:75], v[240:243]\n"
"s_waitcnt vmcnt(19)\n"
"ds_read_b64_tr_b16 a[32:33], v45 offset:24576\n"
"v_mfma_f32_16x16x32_bf16 v[240:243], a[48:51], v[76:79], v[240:243]\n"
"ds_read_b64_tr_b16 a[34:35], v45 offset:24832\n"
"v_mfma_f32_16x16x32_bf16 v[244:247], a[36:39], v[72:75], v[244:247]\n"
"s_waitcnt vmcnt(15)\n"
"ds_read_b64_tr_b16 a[48:49], v45 offset:28672\n"
"v_mfma_f32_16x16x32_bf16 v[244:247], a[52:55], v[76:79], v[244:247]\n"
"ds_read_b64_tr_b16 a[50:51], v45 offset:28928\n"
"v_mfma_f32_16x16x32_bf16 v[248:251], a[40:43], v[72:75], v[248:251]\n"
"ds_read_b64_tr_b16 a[36:37], v46 offset:24576\n"
"v_mfma_f32_16x16x32_bf16 v[248:251], a[56:59], v[76:79], v[248:251]\n"
"ds_read_b64_tr_b16 a[38:39], v46 offset:24832\n"
"v_mfma_f32_16x16x32_bf16 v[252:255], a[44:47], v[72:75], v[252:255]\n"
"ds_read_b128 v[72:75], v41 offset:14336\n"
"v_mfma_f32_16x16x32_bf16 v[252:255], a[60:63], v[76:79], v[252:255]\n"
"s_waitcnt lgkmcnt(9)\n"
"ds_read_b128 v[76:79], v41 offset:14848\n"
"v_mfma_f32_16x16x32_bf16 v[112:115], a[0:3], v[48:51], v[112:115]\n"
"ds_read_b64_tr_b16 a[52:53], v46 offset:28672\n"
"v_mfma_f32_16x16x32_bf16 v[112:115], a[16:19], v[52:55], v[112:115]\n"
"ds_read_b64_tr_b16 a[54:55], v46 offset:28928\n"
"v_mfma_f32_16x16x32_bf16 v[116:119], a[4:7], v[48:51], v[116:119]\n"
"ds_read_b64_tr_b16 a[40:41], v45 offset:25088\n"
"v_mfma_f32_16x16x32_bf16 v[116:119], a[20:23], v[52:55], v[116:119]\n"
"ds_read_b64_tr_b16 a[42:43], v45 offset:25344\n"
"v_mfma_f32_16x16x32_bf16 v[120:123], a[8:11], v[48:51], v[120:123]\n"
"ds_read_b64_tr_b16 a[56:57], v45 offset:29184\n"
"v_mfma_f32_16x16x32_bf16 v[120:123], a[24:27], v[52:55], v[120:123]\n"
"ds_read_b64_tr_b16 a[58:59], v45 offset:29440\n"
"v_mfma_f32_16x16x32_bf16 v[124:127], a[12:15], v[48:51], v[124:127]\n"
"ds_read_b128 v[48:51], v42 offset:12288\n"
"v_mfma_f32_16x16x32_bf16 v[124:127], a[28:31], v[52:55], v[124:127]\n"
"ds_read_b128 v[52:55], v42 offset:12800\n"
"v_mfma_f32_16x16x32_bf16 v[128:131], a[0:3], v[56:59], v[128:131]\n"
"ds_read_b64_tr_b16 a[44:45], v46 offset:25088\n"
"v_mfma_f32_16x16x32_bf16 v[128:131], a[16:19], v[60:63], v[128:131]\n"
"ds_read_b64_tr_b16 a[46:47], v46 offset:25344\n"
"v_mfma_f32_16x16x32_bf16 v[132:135], a[4:7], v[56:59], v[132:135]\n"
"buffer_load_dwordx4 v39, s[40:43], s90 offen offset:2048 lds\n"
"ds_read_b64_tr_b16 a[60:61], v46 offset:29184\n"
"v_mfma_f32_16x16x32_bf16 v[132:135], a[20:23], v[60:63], v[132:135]\n"
"ds_read_b64_tr_b16 a[62:63], v46 offset:29440\n"
"v_mfma_f32_16x16x32_bf16 v[136:139], a[8:11], v[56:59], v[136:139]\n"
"v_mfma_f32_16x16x32_bf16 v[136:139], a[24:27], v[60:63], v[136:139]\n"
"v_mfma_f32_16x16x32_bf16 v[140:143], a[12:15], v[56:59], v[140:143]\n"
"buffer_load_dwordx4 v40, s[40:43], s90 offen offset:3072 lds\n"
"ds_read_b128 v[56:59], v42 offset:14336\n"
"v_mfma_f32_16x16x32_bf16 v[140:143], a[28:31], v[60:63], v[140:143]\n"
"s_waitcnt lgkmcnt(8)\n"
"ds_read_b128 v[60:63], v42 offset:14848\n"
"v_mfma_f32_16x16x32_bf16 v[144:147], a[0:3], v[64:67], v[144:147]\n"
"v_mfma_f32_16x16x32_bf16 v[144:147], a[16:19], v[68:71], v[144:147]\n"
"s_add_u32 s75, s90, 0x40000\n"
"s_add_u32 m0, s30, 0x5000\n"
"v_mfma_f32_16x16x32_bf16 v[148:151], a[4:7], v[64:67], v[148:151]\n"
"buffer_load_dwordx4 v37, s[40:43], s75 offen offset:0 lds\n"
"v_mfma_f32_16x16x32_bf16 v[148:151], a[20:23], v[68:71], v[148:151]\n"
"v_mfma_f32_16x16x32_bf16 v[152:155], a[8:11], v[64:67], v[152:155]\n"
"v_mfma_f32_16x16x32_bf16 v[152:155], a[24:27], v[68:71], v[152:155]\n"
"v_mfma_f32_16x16x32_bf16 v[156:159], a[12:15], v[64:67], v[156:159]\n"
"s_add_u32 s88, s72, 10\n"
"buffer_load_dwordx4 v38, s[40:43], s75 offen offset:1024 lds\n"
"ds_read_b128 v[64:67], v43 offset:12288\n"
"v_mfma_f32_16x16x32_bf16 v[156:159], a[28:31], v[68:71], v[156:159]\n"
"s_and_b32 s88, s88, 15\n"
"ds_read_b128 v[68:71], v43 offset:12800\n"
"v_mfma_f32_16x16x32_bf16 v[160:163], a[0:3], v[72:75], v[160:163]\n"
"s_lshl_b32 s88, s88, 7\n"
"v_mfma_f32_16x16x32_bf16 v[160:163], a[16:19], v[76:79], v[160:163]\n"
"v_mfma_f32_16x16x32_bf16 v[164:167], a[4:7], v[72:75], v[164:167]\n"
"buffer_load_dwordx4 v39, s[40:43], s75 offen offset:2048 lds\n"
"v_mfma_f32_16x16x32_bf16 v[164:167], a[20:23], v[76:79], v[164:167]\n"
"v_mfma_f32_16x16x32_bf16 v[168:171], a[8:11], v[72:75], v[168:171]\n"
"v_mfma_f32_16x16x32_bf16 v[168:171], a[24:27], v[76:79], v[168:171]\n"
"v_mfma_f32_16x16x32_bf16 v[172:175], a[12:15], v[72:75], v[172:175]\n"
"buffer_load_dwordx4 v40, s[40:43], s75 offen offset:3072 lds\n"
"ds_read_b128 v[72:75], v43 offset:14336\n"
"v_mfma_f32_16x16x32_bf16 v[172:175], a[28:31], v[76:79], v[172:175]\n"
"s_waitcnt lgkmcnt(3)\n"
"ds_read_b128 v[76:79], v43 offset:14848\n"
"v_mfma_f32_16x16x32_bf16 v[176:179], a[0:3], v[48:51], v[176:179]\n"
"v_mfma_f32_16x16x32_bf16 v[176:179], a[16:19], v[52:55], v[176:179]\n"
"s_add_u32 m0, s30, 0x8000\n"
"v_mfma_f32_16x16x32_bf16 v[180:183], a[4:7], v[48:51], v[180:183]\n"
"buffer_load_dwordx4 v32, s[36:39], s88 offen offset:0 lds\n"
"v_mfma_f32_16x16x32_bf16 v[180:183], a[20:23], v[52:55], v[180:183]\n"
"v_mfma_f32_16x16x32_bf16 v[184:187], a[8:11], v[48:51], v[184:187]\n"
"v_mfma_f32_16x16x32_bf16 v[184:187], a[24:27], v[52:55], v[184:187]\n"
"v_mfma_f32_16x16x32_bf16 v[188:191], a[12:15], v[48:51], v[188:191]\n"
"buffer_load_dwordx4 v33, s[36:39], s88 offen offset:1024 lds\n"
"ds_read_b128 v[48:51], v44 offset:0\n"
"v_mfma_f32_16x16x32_bf16 v[188:191], a[28:31], v[52:55], v[188:191]\n"
"ds_read_b128 v[52:55], v44 offset:512\n"
"v_mfma_f32_16x16x32_bf16 v[192:195], a[0:3], v[56:59], v[192:195]\n"
"v_mfma_f32_16x16x32_bf16 v[192:195], a[16:19], v[60:63], v[192:195]\n"
"v_mfma_f32_16x16x32_bf16 v[196:199], a[4:7], v[56:59], v[196:199]\n"
"s_add_u32 s91, s72, 11\n"
"buffer_load_dwordx4 v34, s[36:39], s88 offen offset:2048 lds\n"
"v_mfma_f32_16x16x32_bf16 v[196:199], a[20:23], v[60:63], v[196:199]\n"
"s_and_b32 s91, s91, 15\n"
"v_mfma_f32_16x16x32_bf16 v[200:203], a[8:11], v[56:59], v[200:203]\n"
"s_lshl_b32 s91, s91, 19\n"
"v_mfma_f32_16x16x32_bf16 v[200:203], a[24:27], v[60:63], v[200:203]\n"
"v_mfma_f32_16x16x32_bf16 v[204:207], a[12:15], v[56:59], v[204:207]\n"
"buffer_load_dwordx4 v35, s[36:39], s88 offen offset:3072 lds\n"
"v_mfma_f32_16x16x32_bf16 v[204:207], a[28:31], v[60:63], v[204:207]\n"
"s_waitcnt lgkmcnt(2)\n"
"v_mfma_f32_16x16x32_bf16 v[208:211], a[0:3], v[64:67], v[208:211]\n"
"v_mfma_f32_16x16x32_bf16 v[208:211], a[16:19], v[68:71], v[208:211]\n"
"s_add_u32 m0, s30, 0x9400\n"
"v_mfma_f32_16x16x32_bf16 v[212:215], a[4:7], v[64:67], v[212:215]\n"
"buffer_load_dwordx4 v36, s[36:39], s88 offen offset:0 lds\n"
"v_mfma_f32_16x16x32_bf16 v[212:215], a[20:23], v[68:71], v[212:215]\n"
"v_mfma_f32_16x16x32_bf16 v[216:219], a[8:11], v[64:67], v[216:219]\n"
"v_mfma_f32_16x16x32_bf16 v[216:219], a[24:27], v[68:71], v[216:219]\n"
"s_add_u32 m0, s30, 0x6000\n"
"v_mfma_f32_16x16x32_bf16 v[220:223], a[12:15], v[64:67], v[220:223]\n"
"buffer_load_dwordx4 v37, s[40:43], s91 offen offset:0 lds\n"
"v_mfma_f32_16x16x32_bf16 v[220:223], a[28:31], v[68:71], v[220:223]\n"
"v_mfma_f32_16x16x32_bf16 v[224:227], a[0:3], v[72:75], v[224:227]\n"
"v_mfma_f32_16x16x32_bf16 v[224:227], a[16:19], v[76:79], v[224:227]\n"
"s_waitcnt vmcnt(23) lgkmcnt(0)\n"
"s_barrier\n"
"v_mfma_f32_16x16x32_bf16 v[228:231], a[4:7], v[72:75], v[228:231]\n"
"buffer_load_dwordx4 v38, s[40:43], s91 offen offset:1024 lds\n"
"ds_read_b128 v[56:59], v27 offset:8192\n"
"v_mfma_f32_16x16x32_bf16 v[228:231], a[20:23], v[76:79], v[228:231]\n"
"ds_read_b128 v[60:63], v27 offset:8704\n"
"ds_read_b128 v[64:67], v27 offset:10240\n"
"v_mfma_f32_16x16x32_bf16 v[232:235], a[8:11], v[72:75], v[232:235]\n"
"ds_read_b128 v[68:71], v27 offset:10752\n"
"v_mfma_f32_16x16x32_bf16 v[232:235], a[24:27], v[76:79], v[232:235]\n"
"v_mfma_f32_16x16x32_bf16 v[236:239], a[12:15], v[72:75], v[236:239]\n"
"buffer_load_dwordx4 v39, s[40:43], s91 offen offset:2048 lds\n"
"ds_read_b128 v[72:75], v41 offset:8192\n"
"v_mfma_f32_16x16x32_bf16 v[236:239], a[28:31], v[76:79], v[236:239]\n"
"ds_read_b128 v[76:79], v41 offset:8704\n"
"v_mfma_f32_16x16x32_bf16 v[240:243], a[0:3], v[48:51], v[240:243]\n"
"s_waitcnt vmcnt(12)\n"
"ds_read_b64_tr_b16 a[0:1], v45 offset:16384\n"
"v_mfma_f32_16x16x32_bf16 v[240:243], a[16:19], v[52:55], v[240:243]\n"
"ds_read_b64_tr_b16 a[2:3], v45 offset:16640\n"
"v_mfma_f32_16x16x32_bf16 v[244:247], a[4:7], v[48:51], v[244:247]\n"
"buffer_load_dwordx4 v40, s[40:43], s91 offen offset:3072 lds\n"
"ds_read_b64_tr_b16 a[4:5], v46 offset:16384\n"
"v_mfma_f32_16x16x32_bf16 v[244:247], a[20:23], v[52:55], v[244:247]\n"
"ds_read_b64_tr_b16 a[6:7], v46 offset:16640\n"
"v_mfma_f32_16x16x32_bf16 v[248:251], a[8:11], v[48:51], v[248:251]\n"
"ds_read_b64_tr_b16 a[8:9], v45 offset:16896\n"
"v_mfma_f32_16x16x32_bf16 v[248:251], a[24:27], v[52:55], v[248:251]\n"
"s_add_u32 s76, s91, 0x40000\n"
"s_add_u32 m0, s30, 0x7000\n"
"ds_read_b64_tr_b16 a[10:11], v45 offset:17152\n"
"v_mfma_f32_16x16x32_bf16 v[252:255], a[12:15], v[48:51], v[252:255]\n"
"buffer_load_dwordx4 v37, s[40:43], s76 offen offset:0 lds\n"
"ds_read_b128 v[48:51], v41 offset:10240\n"
"v_mfma_f32_16x16x32_bf16 v[252:255], a[28:31], v[52:55], v[252:255]\n"
"s_waitcnt lgkmcnt(9)\n"
"ds_read_b128 v[52:55], v41 offset:10752\n"
"v_mfma_f32_16x16x32_bf16 v[112:115], a[32:35], v[56:59], v[112:115]\n"
"ds_read_b64_tr_b16 a[12:13], v46 offset:16896\n"
"v_mfma_f32_16x16x32_bf16 v[112:115], a[48:51], v[60:63], v[112:115]\n"
"s_add_u32 s89, s72, 11\n"
"buffer_load_dwordx4 v38, s[40:43], s76 offen offset:1024 lds\n"
"ds_read_b64_tr_b16 a[14:15], v46 offset:17152\n"
"v_mfma_f32_16x16x32_bf16 v[116:119], a[36:39], v[56:59], v[116:119]\n"
"s_and_b32 s89, s89, 15\n"
"v_mfma_f32_16x16x32_bf16 v[116:119], a[52:55], v[60:63], v[116:119]\n"
"s_lshl_b32 s89, s89, 7\n"
"v_mfma_f32_16x16x32_bf16 v[120:123], a[40:43], v[56:59], v[120:123]\n"
"v_mfma_f32_16x16x32_bf16 v[120:123], a[56:59], v[60:63], v[120:123]\n"
"buffer_load_dwordx4 v39, s[40:43], s76 offen offset:2048 lds\n"
"v_mfma_f32_16x16x32_bf16 v[124:127], a[44:47], v[56:59], v[124:127]\n"
"ds_read_b128 v[56:59], v42 offset:8192\n"
"v_mfma_f32_16x16x32_bf16 v[124:127], a[60:63], v[60:63], v[124:127]\n"
"ds_read_b128 v[60:63], v42 offset:8704\n"
"v_mfma_f32_16x16x32_bf16 v[128:131], a[32:35], v[64:67], v[128:131]\n"
"s_waitcnt vmcnt(12)\n"
"ds_read_b64_tr_b16 a[16:17], v45 offset:20480\n"
"v_mfma_f32_16x16x32_bf16 v[128:131], a[48:51], v[68:71], v[128:131]\n"
"buffer_load_dwordx4 v40, s[40:43], s76 offen offset:3072 lds\n"
"ds_read_b64_tr_b16 a[18:19], v45 offset:20736\n"
"v_mfma_f32_16x16x32_bf16 v[132:135], a[36:39], v[64:67], v[132:135]\n"
"ds_read_b64_tr_b16 a[20:21], v46 offset:20480\n"
"v_mfma_f32_16x16x32_bf16 v[132:135], a[52:55], v[68:71], v[132:135]\n"
"ds_read_b64_tr_b16 a[22:23], v46 offset:20736\n"
"v_mfma_f32_16x16x32_bf16 v[136:139], a[40:43], v[64:67], v[136:139]\n"
"s_add_u32 m0, s30, 0x3000\n"
"ds_read_b64_tr_b16 a[24:25], v45 offset:20992\n"
"v_mfma_f32_16x16x32_bf16 v[136:139], a[56:59], v[68:71], v[136:139]\n"
"buffer_load_dwordx4 v32, s[36:39], s89 offen offset:0 lds\n"
"ds_read_b64_tr_b16 a[26:27], v45 offset:21248\n"
"v_mfma_f32_16x16x32_bf16 v[140:143], a[44:47], v[64:67], v[140:143]\n"
"ds_read_b128 v[64:67], v42 offset:10240\n"
"v_mfma_f32_16x16x32_bf16 v[140:143], a[60:63], v[68:71], v[140:143]\n"
"s_waitcnt lgkmcnt(9)\n"
"ds_read_b128 v[68:71], v42 offset:10752\n"
"v_mfma_f32_16x16x32_bf16 v[144:147], a[32:35], v[72:75], v[144:147]\n"
"ds_read_b64_tr_b16 a[28:29], v46 offset:20992\n"
"v_mfma_f32_16x16x32_bf16 v[144:147], a[48:51], v[76:79], v[144:147]\n"
"s_add_u32 s74, s72, 12\n"
"buffer_load_dwordx4 v33, s[36:39], s89 offen offset:1024 lds\n"
"ds_read_b64_tr_b16 a[30:31], v46 offset:21248\n"
"v_mfma_f32_16x16x32_bf16 v[148:151], a[36:39], v[72:75], v[148:151]\n"
"s_and_b32 s74, s74, 15\n"
"v_mfma_f32_16x16x32_bf16 v[148:151], a[52:55], v[76:79], v[148:151]\n"
"s_lshl_b32 s74, s74, 19\n"
"v_mfma_f32_16x16x32_bf16 v[152:155], a[40:43], v[72:75], v[152:155]\n"
"v_mfma_f32_16x16x32_bf16 v[152:155], a[56:59], v[76:79], v[152:155]\n"
"buffer_load_dwordx4 v34, s[36:39], s89 offen offset:2048 lds\n"
"v_mfma_f32_16x16x32_bf16 v[156:159], a[44:47], v[72:75], v[156:159]\n"
"ds_read_b128 v[72:75], v43 offset:8192\n"
"v_mfma_f32_16x16x32_bf16 v[156:159], a[60:63], v[76:79], v[156:159]\n"
"ds_read_b128 v[76:79], v43 offset:8704\n"
"v_mfma_f32_16x16x32_bf16 v[160:163], a[32:35], v[48:51], v[160:163]\n"
"v_mfma_f32_16x16x32_bf16 v[160:163], a[48:51], v[52:55], v[160:163]\n"
"buffer_load_dwordx4 v35, s[36:39], s89 offen offset:3072 lds\n"
"v_mfma_f32_16x16x32_bf16 v[164:167], a[36:39], v[48:51], v[164:167]\n"
"v_mfma_f32_16x16x32_bf16 v[164:167], a[52:55], v[52:55], v[164:167]\n"
"v_mfma_f32_16x16x32_bf16 v[168:171], a[40:43], v[48:51], v[168:171]\n"
"s_add_u32 m0, s30, 0x4000\n"
"v_mfma_f32_16x16x32_bf16 v[168:171], a[56:59], v[52:55], v[168:171]\n"
"buffer_load_dwordx4 v37, s[40:43], s74 offen offset:0 lds\n"
"v_mfma_f32_16x16x32_bf16 v[172:175], a[44:47], v[48:51], v[172:175]\n"
"ds_read_b128 v[48:51], v43 offset:10240\n"
"v_mfma_f32_16x16x32_bf16 v[172:175], a[60:63], v[52:55], v[172:175]\n"
"s_waitcnt lgkmcnt(3)\n"
"buffer_load_dwordx4 a[128:131], v19, s[48:51], s77 offen offset:0 nt\n"
"ds_read_b128 v[52:55], v43 offset:10752\n"
"v_mfma_f32_16x16x32_bf16 v[176:179], a[32:35], v[56:59], v[176:179]\n"
"v_mfma_f32_16x16x32_bf16 v[176:179], a[48:51], v[60:63], v[176:179]\n"
"v_mfma_f32_16x16x32_bf16 v[180:183], a[36:39], v[56:59], v[180:183]\n"
"v_mfma_f32_16x16x32_bf16 v[180:183], a[52:55], v[60:63], v[180:183]\n"
"buffer_load_dwordx4 a[132:135], v20, s[48:51], s77 offen offset:1024 nt\n"
"v_mfma_f32_16x16x32_bf16 v[184:187], a[40:43], v[56:59], v[184:187]\n"
"v_mfma_f32_16x16x32_bf16 v[184:187], a[56:59], v[60:63], v[184:187]\n"
"v_mfma_f32_16x16x32_bf16 v[188:191], a[44:47], v[56:59], v[188:191]\n"
"ds_read_b128 v[56:59], v30 offset:0\n"
"v_mfma_f32_16x16x32_bf16 v[188:191], a[60:63], v[60:63], v[188:191]\n"
"buffer_load_dwordx4 a[136:139], v19, s[48:51], s77 offen offset:2048 nt\n"
"ds_read_b128 v[60:63], v30 offset:512\n"
"v_mfma_f32_16x16x32_bf16 v[192:195], a[32:35], v[64:67], v[192:195]\n"
"v_mfma_f32_16x16x32_bf16 v[192:195], a[48:51], v[68:71], v[192:195]\n"
"v_mfma_f32_16x16x32_bf16 v[196:199], a[36:39], v[64:67], v[196:199]\n"
"v_mfma_f32_16x16x32_bf16 v[196:199], a[52:55], v[68:71], v[196:199]\n"
"buffer_load_dwordx4 a[140:143], v20, s[48:51], s77 offen offset:3072 nt\n"
"v_mfma_f32_16x16x32_bf16 v[200:203], a[40:43], v[64:67], v[200:203]\n"
"v_mfma_f32_16x16x32_bf16 v[200:203], a[56:59], v[68:71], v[200:203]\n"
"v_mfma_f32_16x16x32_bf16 v[204:207], a[44:47], v[64:67], v[204:207]\n"
"s_mov_b32 s78, 0xc000\n"
"v_mfma_f32_16x16x32_bf16 v[204:207], a[60:63], v[68:71], v[204:207]\n"
"s_waitcnt lgkmcnt(2)\n"
"buffer_load_dwordx4 a[144:147], v17, s[44:47], s78 offen offset:0 nt\n"
"v_mfma_f32_16x16x32_bf16 v[208:211], a[32:35], v[72:75], v[208:211]\n"
"v_mfma_f32_16x16x32_bf16 v[208:211], a[48:51], v[76:79], v[208:211]\n"
"v_mfma_f32_16x16x32_bf16 v[212:215], a[36:39], v[72:75], v[212:215]\n"
"v_mfma_f32_16x16x32_bf16 v[212:215], a[52:55], v[76:79], v[212:215]\n"
"buffer_load_dwordx4 a[148:151], v17, s[44:47], s78 offen offset:1024 nt\n"
"v_mfma_f32_16x16x32_bf16 v[216:219], a[40:43], v[72:75], v[216:219]\n"
"v_mfma_f32_16x16x32_bf16 v[216:219], a[56:59], v[76:79], v[216:219]\n"
"v_mfma_f32_16x16x32_bf16 v[220:223], a[44:47], v[72:75], v[220:223]\n"
"v_mfma_f32_16x16x32_bf16 v[220:223], a[60:63], v[76:79], v[220:223]\n"
"buffer_load_dwordx4 a[152:155], v18, s[44:47], s78 offen offset:2048 nt\n"
"v_mfma_f32_16x16x32_bf16 v[224:227], a[32:35], v[48:51], v[224:227]\n"
"v_mfma_f32_16x16x32_bf16 v[224:227], a[48:51], v[52:55], v[224:227]\n"
"s_waitcnt vmcnt(20) lgkmcnt(0)\n"
"s_barrier\n"
"v_mfma_f32_16x16x32_bf16 v[228:231], a[36:39], v[48:51], v[228:231]\n"
"ds_read_b128 v[64:67], v27 offset:32768\n"
"ds_read_b128 v[68:71], v27 offset:33280\n"
"v_mfma_f32_16x16x32_bf16 v[228:231], a[52:55], v[52:55], v[228:231]\n"
"buffer_load_dwordx4 a[156:159], v18, s[44:47], s78 offen offset:3072 nt\n"
"ds_read_b128 v[72:75], v27 offset:34816\n"
"v_mfma_f32_16x16x32_bf16 v[232:235], a[40:43], v[48:51], v[232:235]\n"
"ds_read_b128 v[76:79], v27 offset:35328\n"
"v_mfma_f32_16x16x32_bf16 v[232:235], a[56:59], v[52:55], v[232:235]\n"
"v_mfma_f32_16x16x32_bf16 v[236:239], a[44:47], v[48:51], v[236:239]\n"
"ds_read_b128 v[48:51], v41 offset:32768\n"
"v_mfma_f32_16x16x32_bf16 v[236:239], a[60:63], v[52:55], v[236:239]\n"
"ds_read_b128 v[52:55], v41 offset:33280\n"
"v_mfma_f32_16x16x32_bf16 v[240:243], a[32:35], v[56:59], v[240:243]\n"
"s_waitcnt vmcnt(17)\n"
"ds_read_b64_tr_b16 a[32:33], v45 offset:24576\n"
"v_mfma_f32_16x16x32_bf16 v[240:243], a[48:51], v[60:63], v[240:243]\n"
"ds_read_b64_tr_b16 a[34:35], v45 offset:24832\n"
"v_mfma_f32_16x16x32_bf16 v[244:247], a[36:39], v[56:59], v[244:247]\n"
"s_waitcnt vmcnt(13)\n"
"ds_read_b64_tr_b16 a[48:49], v45 offset:28672\n"
"v_mfma_f32_16x16x32_bf16 v[244:247], a[52:55], v[60:63], v[244:247]\n"
"ds_read_b64_tr_b16 a[50:51], v45 offset:28928\n"
"v_mfma_f32_16x16x32_bf16 v[248:251], a[40:43], v[56:59], v[248:251]\n"
"ds_read_b64_tr_b16 a[36:37], v46 offset:24576\n"
"v_mfma_f32_16x16x32_bf16 v[248:251], a[56:59], v[60:63], v[248:251]\n"
"ds_read_b64_tr_b16 a[38:39], v46 offset:24832\n"
"v_mfma_f32_16x16x32_bf16 v[252:255], a[44:47], v[56:59], v[252:255]\n"
"ds_read_b128 v[56:59], v41 offset:34816\n"
"v_mfma_f32_16x16x32_bf16 v[252:255], a[60:63], v[60:63], v[252:255]\n"
"s_waitcnt lgkmcnt(9)\n"
"ds_read_b128 v[60:63], v41 offset:35328\n"
"v_mfma_f32_16x16x32_bf16 v[112:115], a[0:3], v[64:67], v[112:115]\n"
"ds_read_b64_tr_b16 a[52:53], v46 offset:28672\n"
"v_mfma_f32_16x16x32_bf16 v[112:115], a[16:19], v[68:71], v[112:115]\n"
"ds_read_b64_tr_b16 a[54:55], v46 offset:28928\n"
"v_mfma_f32_16x16x32_bf16 v[116:119], a[4:7], v[64:67], v[116:119]\n"
"ds_read_b64_tr_b16 a[40:41], v45 offset:25088\n"
"v_mfma_f32_16x16x32_bf16 v[116:119], a[20:23], v[68:71], v[116:119]\n"
"ds_read_b64_tr_b16 a[42:43], v45 offset:25344\n"
"v_mfma_f32_16x16x32_bf16 v[120:123], a[8:11], v[64:67], v[120:123]\n"
"ds_read_b64_tr_b16 a[56:57], v45 offset:29184\n"
"v_mfma_f32_16x16x32_bf16 v[120:123], a[24:27], v[68:71], v[120:123]\n"
"ds_read_b64_tr_b16 a[58:59], v45 offset:29440\n"
"v_mfma_f32_16x16x32_bf16 v[124:127], a[12:15], v[64:67], v[124:127]\n"
"ds_read_b128 v[64:67], v42 offset:32768\n"
"v_mfma_f32_16x16x32_bf16 v[124:127], a[28:31], v[68:71], v[124:127]\n"
"ds_read_b128 v[68:71], v42 offset:33280\n"
"v_mfma_f32_16x16x32_bf16 v[128:131], a[0:3], v[72:75], v[128:131]\n"
"ds_read_b64_tr_b16 a[44:45], v46 offset:25088\n"
"v_mfma_f32_16x16x32_bf16 v[128:131], a[16:19], v[76:79], v[128:131]\n"
"ds_read_b64_tr_b16 a[46:47], v46 offset:25344\n"
"v_mfma_f32_16x16x32_bf16 v[132:135], a[4:7], v[72:75], v[132:135]\n"
"ds_read_b64_tr_b16 a[60:61], v46 offset:29184\n"
"v_mfma_f32_16x16x32_bf16 v[132:135], a[20:23], v[76:79], v[132:135]\n"
"buffer_load_dwordx4 v38, s[40:43], s74 offen offset:1024 lds\n"
"ds_read_b64_tr_b16 a[62:63], v46 offset:29440\n"
"v_mfma_f32_16x16x32_bf16 v[136:139], a[8:11], v[72:75], v[136:139]\n"
"v_mfma_f32_16x16x32_bf16 v[136:139], a[24:27], v[76:79], v[136:139]\n"
"v_mfma_f32_16x16x32_bf16 v[140:143], a[12:15], v[72:75], v[140:143]\n"
"ds_read_b128 v[72:75], v42 offset:34816\n"
"v_mfma_f32_16x16x32_bf16 v[140:143], a[28:31], v[76:79], v[140:143]\n"
"s_waitcnt lgkmcnt(8)\n"
"buffer_load_dwordx4 v39, s[40:43], s74 offen offset:2048 lds\n"
"ds_read_b128 v[76:79], v42 offset:35328\n"
"v_mfma_f32_16x16x32_bf16 v[144:147], a[0:3], v[48:51], v[144:147]\n"
"v_mfma_f32_16x16x32_bf16 v[144:147], a[16:19], v[52:55], v[144:147]\n"
"v_mfma_f32_16x16x32_bf16 v[148:151], a[4:7], v[48:51], v[148:151]\n"
"v_mfma_f32_16x16x32_bf16 v[148:151], a[20:23], v[52:55], v[148:151]\n"
"buffer_load_dwordx4 v40, s[40:43], s74 offen offset:3072 lds\n"
"v_mfma_f32_16x16x32_bf16 v[152:155], a[8:11], v[48:51], v[152:155]\n"
"v_mfma_f32_16x16x32_bf16 v[152:155], a[24:27], v[52:55], v[152:155]\n"
"v_mfma_f32_16x16x32_bf16 v[156:159], a[12:15], v[48:51], v[156:159]\n"
"s_add_u32 s84, s74, 0x40000\n"
"s_add_u32 m0, s30, 0x5000\n"
"ds_read_b128 v[48:51], v43 offset:32768\n"
"v_mfma_f32_16x16x32_bf16 v[156:159], a[28:31], v[52:55], v[156:159]\n"
"buffer_load_dwordx4 v37, s[40:43], s84 offen offset:0 lds\n"
"ds_read_b128 v[52:55], v43 offset:33280\n"
"v_mfma_f32_16x16x32_bf16 v[160:163], a[0:3], v[56:59], v[160:163]\n"
"v_mfma_f32_16x16x32_bf16 v[160:163], a[16:19], v[60:63], v[160:163]\n"
"v_mfma_f32_16x16x32_bf16 v[164:167], a[4:7], v[56:59], v[164:167]\n"
"v_mfma_f32_16x16x32_bf16 v[164:167], a[20:23], v[60:63], v[164:167]\n"
"s_add_u32 s73, s72, 12\n"
"buffer_load_dwordx4 v38, s[40:43], s84 offen offset:1024 lds\n"
"v_mfma_f32_16x16x32_bf16 v[168:171], a[8:11], v[56:59], v[168:171]\n"
"s_and_b32 s73, s73, 15\n"
"v_mfma_f32_16x16x32_bf16 v[168:171], a[24:27], v[60:63], v[168:171]\n"
"s_lshl_b32 s73, s73, 7\n"
"v_mfma_f32_16x16x32_bf16 v[172:175], a[12:15], v[56:59], v[172:175]\n"
"ds_read_b128 v[56:59], v43 offset:34816\n"
"v_mfma_f32_16x16x32_bf16 v[172:175], a[28:31], v[60:63], v[172:175]\n"
"s_waitcnt lgkmcnt(3)\n"
"buffer_load_dwordx4 v39, s[40:43], s84 offen offset:2048 lds\n"
"ds_read_b128 v[60:63], v43 offset:35328\n"
"v_mfma_f32_16x16x32_bf16 v[176:179], a[0:3], v[64:67], v[176:179]\n"
"v_mfma_f32_16x16x32_bf16 v[176:179], a[16:19], v[68:71], v[176:179]\n"
"v_mfma_f32_16x16x32_bf16 v[180:183], a[4:7], v[64:67], v[180:183]\n"
"v_mfma_f32_16x16x32_bf16 v[180:183], a[20:23], v[68:71], v[180:183]\n"
"buffer_load_dwordx4 v40, s[40:43], s84 offen offset:3072 lds\n"
"v_mfma_f32_16x16x32_bf16 v[184:187], a[8:11], v[64:67], v[184:187]\n"
"v_mfma_f32_16x16x32_bf16 v[184:187], a[24:27], v[68:71], v[184:187]\n"
"v_mfma_f32_16x16x32_bf16 v[188:191], a[12:15], v[64:67], v[188:191]\n"
"s_add_u32 m0, s30, 0x2000\n"
"ds_read_b128 v[64:67], v44 offset:1024\n"
"v_mfma_f32_16x16x32_bf16 v[188:191], a[28:31], v[68:71], v[188:191]\n"
"buffer_load_dwordx4 v32, s[36:39], s73 offen offset:0 lds\n"
"ds_read_b128 v[68:71], v44 offset:1536\n"
"v_mfma_f32_16x16x32_bf16 v[192:195], a[0:3], v[72:75], v[192:195]\n"
"v_mfma_f32_16x16x32_bf16 v[192:195], a[16:19], v[76:79], v[192:195]\n"
"v_mfma_f32_16x16x32_bf16 v[196:199], a[4:7], v[72:75], v[196:199]\n"
"v_mfma_f32_16x16x32_bf16 v[196:199], a[20:23], v[76:79], v[196:199]\n"
"buffer_load_dwordx4 v33, s[36:39], s73 offen offset:1024 lds\n"
"v_mfma_f32_16x16x32_bf16 v[200:203], a[8:11], v[72:75], v[200:203]\n"
"v_mfma_f32_16x16x32_bf16 v[200:203], a[24:27], v[76:79], v[200:203]\n"
"v_mfma_f32_16x16x32_bf16 v[204:207], a[12:15], v[72:75], v[204:207]\n"
"v_mfma_f32_16x16x32_bf16 v[204:207], a[28:31], v[76:79], v[204:207]\n"
"s_waitcnt lgkmcnt(2)\n"
"s_add_u32 s90, s72, 13\n"
"buffer_load_dwordx4 v34, s[36:39], s73 offen offset:2048 lds\n"
"v_mfma_f32_16x16x32_bf16 v[208:211], a[0:3], v[48:51], v[208:211]\n"
"s_and_b32 s90, s90, 15\n"
"v_mfma_f32_16x16x32_bf16 v[208:211], a[16:19], v[52:55], v[208:211]\n"
"s_lshl_b32 s90, s90, 19\n"
"v_mfma_f32_16x16x32_bf16 v[212:215], a[4:7], v[48:51], v[212:215]\n"
"v_mfma_f32_16x16x32_bf16 v[212:215], a[20:23], v[52:55], v[212:215]\n"
"buffer_load_dwordx4 v35, s[36:39], s73 offen offset:3072 lds\n"
"v_mfma_f32_16x16x32_bf16 v[216:219], a[8:11], v[48:51], v[216:219]\n"
"v_mfma_f32_16x16x32_bf16 v[216:219], a[24:27], v[52:55], v[216:219]\n"
"v_mfma_f32_16x16x32_bf16 v[220:223], a[12:15], v[48:51], v[220:223]\n"
"s_add_u32 m0, s30, 0x9000\n"
"v_mfma_f32_16x16x32_bf16 v[220:223], a[28:31], v[52:55], v[220:223]\n"
"buffer_load_dwordx4 v36, s[36:39], s73 offen offset:0 lds\n"
"v_mfma_f32_16x16x32_bf16 v[224:227], a[0:3], v[56:59], v[224:227]\n"
"v_mfma_f32_16x16x32_bf16 v[224:227], a[16:19], v[60:63], v[224:227]\n"
"s_waitcnt vmcnt(21) lgkmcnt(0)\n"
"s_barrier\n"
"v_mfma_f32_16x16x32_bf16 v[228:231], a[4:7], v[56:59], v[228:231]\n"
"s_add_u32 m0, s30, 0x6000\n"
"ds_read_b128 v[72:75], v27 offset:12288\n"
"ds_read_b128 v[76:79], v27 offset:12800\n"
"v_mfma_f32_16x16x32_bf16 v[228:231], a[20:23], v[60:63], v[228:231]\n"
"buffer_load_dwordx4 v37, s[40:43], s90 offen offset:0 lds\n"
"ds_read_b128 v[48:51], v27 offset:14336\n"
"v_mfma_f32_16x16x32_bf16 v[232:235], a[8:11], v[56:59], v[232:235]\n"
"ds_read_b128 v[52:55], v27 offset:14848\n"
"v_mfma_f32_16x16x32_bf16 v[232:235], a[24:27], v[60:63], v[232:235]\n"
"v_mfma_f32_16x16x32_bf16 v[236:239], a[12:15], v[56:59], v[236:239]\n"
"ds_read_b128 v[56:59], v41 offset:12288\n"
"v_mfma_f32_16x16x32_bf16 v[236:239], a[28:31], v[60:63], v[236:239]\n"
"buffer_load_dwordx4 v38, s[40:43], s90 offen offset:1024 lds\n"
"ds_read_b128 v[60:63], v41 offset:12800\n"
"v_mfma_f32_16x16x32_bf16 v[240:243], a[0:3], v[64:67], v[240:243]\n"
"v_mfma_f32_16x16x32_bf16 v[240:243], a[16:19], v[68:71], v[240:243]\n"
"v_mfma_f32_16x16x32_bf16 v[244:247], a[4:7], v[64:67], v[244:247]\n"
"v_mfma_f32_16x16x32_bf16 v[244:247], a[20:23], v[68:71], v[244:247]\n"
"buffer_load_dwordx4 v39, s[40:43], s90 offen offset:2048 lds\n"
"v_mfma_f32_16x16x32_bf16 v[248:251], a[8:11], v[64:67], v[248:251]\n"
"v_mfma_f32_16x16x32_bf16 v[248:251], a[24:27], v[68:71], v[248:251]\n"
"s_waitcnt vmcnt(12)\n"
"ds_read_b64_tr_b16 a[0:1], v45 offset:16384\n"
"v_mfma_f32_16x16x32_bf16 v[252:255], a[12:15], v[64:67], v[252:255]\n"
"ds_read_b128 v[64:67], v41 offset:14336\n"
"v_mfma_f32_16x16x32_bf16 v[252:255], a[28:31], v[68:71], v[252:255]\n"
"s_waitcnt lgkmcnt(4)\n"
"buffer_load_dwordx4 v40, s[40:43], s90 offen offset:3072 lds\n"
"ds_read_b128 v[68:71], v41 offset:14848\n"
"v_mfma_f32_16x16x32_bf16 v[112:115], a[32:35], v[72:75], v[112:115]\n"
"ds_read_b64_tr_b16 a[2:3], v45 offset:16640\n"
"v_mfma_f32_16x16x32_bf16 v[112:115], a[48:51], v[76:79], v[112:115]\n"
"s_add_u32 s96, s90, 0x40000\n"
"s_add_u32 m0, s30, 0x7000\n"
"ds_read_b64_tr_b16 a[4:5], v46 offset:16384\n"
"v_mfma_f32_16x16x32_bf16 v[116:119], a[36:39], v[72:75], v[116:119]\n"
"buffer_load_dwordx4 v37, s[40:43], s96 offen offset:0 lds\n"
"ds_read_b64_tr_b16 a[6:7], v46 offset:16640\n"
"v_mfma_f32_16x16x32_bf16 v[116:119], a[52:55], v[76:79], v[116:119]\n"
"ds_read_b64_tr_b16 a[8:9], v45 offset:16896\n"
"v_mfma_f32_16x16x32_bf16 v[120:123], a[40:43], v[72:75], v[120:123]\n"
"ds_read_b64_tr_b16 a[10:11], v45 offset:17152\n"
"v_mfma_f32_16x16x32_bf16 v[120:123], a[56:59], v[76:79], v[120:123]\n"
"ds_read_b64_tr_b16 a[12:13], v46 offset:16896\n"
"v_mfma_f32_16x16x32_bf16 v[124:127], a[44:47], v[72:75], v[124:127]\n"
"s_add_u32 s88, s72, 13\n"
"buffer_load_dwordx4 v38, s[40:43], s96 offen offset:1024 lds\n"
"ds_read_b128 v[72:75], v42 offset:12288\n"
"v_mfma_f32_16x16x32_bf16 v[124:127], a[60:63], v[76:79], v[124:127]\n"
"s_and_b32 s88, s88, 15\n"
"ds_read_b128 v[76:79], v42 offset:12800\n"
"v_mfma_f32_16x16x32_bf16 v[128:131], a[32:35], v[48:51], v[128:131]\n"
"s_lshl_b32 s88, s88, 7\n"
"ds_read_b64_tr_b16 a[14:15], v46 offset:17152\n"
"v_mfma_f32_16x16x32_bf16 v[128:131], a[48:51], v[52:55], v[128:131]\n"
"v_mfma_f32_16x16x32_bf16 v[132:135], a[36:39], v[48:51], v[132:135]\n"
"buffer_load_dwordx4 v39, s[40:43], s96 offen offset:2048 lds\n"
"v_mfma_f32_16x16x32_bf16 v[132:135], a[52:55], v[52:55], v[132:135]\n"
"v_mfma_f32_16x16x32_bf16 v[136:139], a[40:43], v[48:51], v[136:139]\n"
"v_mfma_f32_16x16x32_bf16 v[136:139], a[56:59], v[52:55], v[136:139]\n"
"s_waitcnt vmcnt(12)\n"
"ds_read_b64_tr_b16 a[16:17], v45 offset:20480\n"
"v_mfma_f32_16x16x32_bf16 v[140:143], a[44:47], v[48:51], v[140:143]\n"
"buffer_load_dwordx4 v40, s[40:43], s96 offen offset:3072 lds\n"
"ds_read_b128 v[48:51], v42 offset:14336\n"
"v_mfma_f32_16x16x32_bf16 v[140:143], a[60:63], v[52:55], v[140:143]\n"
"s_waitcnt lgkmcnt(6)\n"
"ds_read_b128 v[52:55], v42 offset:14848\n"
"v_mfma_f32_16x16x32_bf16 v[144:147], a[32:35], v[56:59], v[144:147]\n"
"ds_read_b64_tr_b16 a[18:19], v45 offset:20736\n"
"v_mfma_f32_16x16x32_bf16 v[144:147], a[48:51], v[60:63], v[144:147]\n"
"s_add_u32 m0, s30, 0x8000\n"
"ds_read_b64_tr_b16 a[20:21], v46 offset:20480\n"
"v_mfma_f32_16x16x32_bf16 v[148:151], a[36:39], v[56:59], v[148:151]\n"
"buffer_load_dwordx4 v32, s[36:39], s88 offen offset:0 lds\n"
"ds_read_b64_tr_b16 a[22:23], v46 offset:20736\n"
"v_mfma_f32_16x16x32_bf16 v[148:151], a[52:55], v[60:63], v[148:151]\n"
"ds_read_b64_tr_b16 a[24:25], v45 offset:20992\n"
"v_mfma_f32_16x16x32_bf16 v[152:155], a[40:43], v[56:59], v[152:155]\n"
"ds_read_b64_tr_b16 a[26:27], v45 offset:21248\n"
"v_mfma_f32_16x16x32_bf16 v[152:155], a[56:59], v[60:63], v[152:155]\n"
"ds_read_b64_tr_b16 a[28:29], v46 offset:20992\n"
"v_mfma_f32_16x16x32_bf16 v[156:159], a[44:47], v[56:59], v[156:159]\n"
"buffer_load_dwordx4 v33, s[36:39], s88 offen offset:1024 lds\n"
"ds_read_b128 v[56:59], v43 offset:12288\n"
"v_mfma_f32_16x16x32_bf16 v[156:159], a[60:63], v[60:63], v[156:159]\n"
"ds_read_b128 v[60:63], v43 offset:12800\n"
"v_mfma_f32_16x16x32_bf16 v[160:163], a[32:35], v[64:67], v[160:163]\n"
"ds_read_b64_tr_b16 a[30:31], v46 offset:21248\n"
"v_mfma_f32_16x16x32_bf16 v[160:163], a[48:51], v[68:71], v[160:163]\n"
"v_mfma_f32_16x16x32_bf16 v[164:167], a[36:39], v[64:67], v[164:167]\n"
"buffer_load_dwordx4 v34, s[36:39], s88 offen offset:2048 lds\n"
"v_mfma_f32_16x16x32_bf16 v[164:167], a[52:55], v[68:71], v[164:167]\n"
"v_mfma_f32_16x16x32_bf16 v[168:171], a[40:43], v[64:67], v[168:171]\n"
"v_mfma_f32_16x16x32_bf16 v[168:171], a[56:59], v[68:71], v[168:171]\n"
"v_mfma_f32_16x16x32_bf16 v[172:175], a[44:47], v[64:67], v[172:175]\n"
"buffer_load_dwordx4 v35, s[36:39], s88 offen offset:3072 lds\n"
"ds_read_b128 v[64:67], v43 offset:14336\n"
"v_mfma_f32_16x16x32_bf16 v[172:175], a[60:63], v[68:71], v[172:175]\n"
"s_waitcnt lgkmcnt(5)\n"
"s_mul_i32 s88, s70, 0x2400\n"
"ds_read_b128 v[68:71], v43 offset:14848\n"
"v_mfma_f32_16x16x32_bf16 v[176:179], a[32:35], v[72:75], v[176:179]\n"
"buffer_load_dword v26, v22, s[52:55], s31 offen offset:0\n"
"v_mfma_f32_16x16x32_bf16 v[176:179], a[48:51], v[76:79], v[176:179]\n"
"v_mfma_f32_16x16x32_bf16 v[180:183], a[36:39], v[72:75], v[180:183]\n"
"v_mfma_f32_16x16x32_bf16 v[180:183], a[52:55], v[76:79], v[180:183]\n"
"v_mfma_f32_16x16x32_bf16 v[184:187], a[40:43], v[72:75], v[184:187]\n"
"buffer_load_dword v26, v22, s[52:55], s31 offen offset:1024\n"
"v_mfma_f32_16x16x32_bf16 v[184:187], a[56:59], v[76:79], v[184:187]\n"
"v_mfma_f32_16x16x32_bf16 v[188:191], a[44:47], v[72:75], v[188:191]\n"
"ds_read_b128 v[72:75], v30 offset:1024\n"
"v_mfma_f32_16x16x32_bf16 v[188:191], a[60:63], v[76:79], v[188:191]\n"
"s_add_u32 s75, s31, 0x200000\n"
"ds_read_b128 v[76:79], v30 offset:1536\n"
"v_mfma_f32_16x16x32_bf16 v[192:195], a[32:35], v[48:51], v[192:195]\n"
"buffer_load_dword v26, v22, s[52:55], s75 offen offset:0\n"
"v_mfma_f32_16x16x32_bf16 v[192:195], a[48:51], v[52:55], v[192:195]\n"
"v_mfma_f32_16x16x32_bf16 v[196:199], a[36:39], v[48:51], v[196:199]\n"
"v_mfma_f32_16x16x32_bf16 v[196:199], a[52:55], v[52:55], v[196:199]\n"
"v_mfma_f32_16x16x32_bf16 v[200:203], a[40:43], v[48:51], v[200:203]\n"
"buffer_load_dword v26, v22, s[52:55], s75 offen offset:1024\n"
"v_mfma_f32_16x16x32_bf16 v[200:203], a[56:59], v[52:55], v[200:203]\n"
"v_mfma_f32_16x16x32_bf16 v[204:207], a[44:47], v[48:51], v[204:207]\n"
"v_mfma_f32_16x16x32_bf16 v[204:207], a[60:63], v[52:55], v[204:207]\n"
"s_waitcnt lgkmcnt(2)\n"
"s_add_u32 s76, s31, 0x40000\n"
"v_mfma_f32_16x16x32_bf16 v[208:211], a[32:35], v[56:59], v[208:211]\n"
"buffer_load_dword v26, v22, s[52:55], s76 offen offset:0\n"
"v_mfma_f32_16x16x32_bf16 v[208:211], a[48:51], v[60:63], v[208:211]\n"
"v_mfma_f32_16x16x32_bf16 v[212:215], a[36:39], v[56:59], v[212:215]\n"
"v_mfma_f32_16x16x32_bf16 v[212:215], a[52:55], v[60:63], v[212:215]\n"
"v_mfma_f32_16x16x32_bf16 v[216:219], a[40:43], v[56:59], v[216:219]\n"
"buffer_load_dword v26, v22, s[52:55], s76 offen offset:1024\n"
"v_mfma_f32_16x16x32_bf16 v[216:219], a[56:59], v[60:63], v[216:219]\n"
"v_mfma_f32_16x16x32_bf16 v[220:223], a[44:47], v[56:59], v[220:223]\n"
"v_mfma_f32_16x16x32_bf16 v[220:223], a[60:63], v[60:63], v[220:223]\n"
"s_add_u32 s77, s31, 0x240000\n"
"v_mfma_f32_16x16x32_bf16 v[224:227], a[32:35], v[64:67], v[224:227]\n"
"buffer_load_dword v26, v22, s[52:55], s77 offen offset:0\n"
"v_mfma_f32_16x16x32_bf16 v[224:227], a[48:51], v[68:71], v[224:227]\n"
"s_waitcnt vmcnt(19) lgkmcnt(0)\n"
"s_barrier\n"
"v_mfma_f32_16x16x32_bf16 v[228:231], a[36:39], v[64:67], v[228:231]\n"
"ds_read_b128 v[48:51], v27 offset:8192\n"
"ds_read_b128 v[52:55], v27 offset:8704\n"
"v_mfma_f32_16x16x32_bf16 v[228:231], a[52:55], v[68:71], v[228:231]\n"
"ds_read_b128 v[56:59], v27 offset:10240\n"
"ds_read_b128 v[60:63], v27 offset:10752\n"
"v_mfma_f32_16x16x32_bf16 v[232:235], a[40:43], v[64:67], v[232:235]\n"
"buffer_load_dword v26, v22, s[52:55], s77 offen offset:1024\n"
"v_mfma_f32_16x16x32_bf16 v[232:235], a[56:59], v[68:71], v[232:235]\n"
"v_mfma_f32_16x16x32_bf16 v[236:239], a[44:47], v[64:67], v[236:239]\n"
"ds_read_b128 v[64:67], v41 offset:8192\n"
"v_mfma_f32_16x16x32_bf16 v[236:239], a[60:63], v[68:71], v[236:239]\n"
"ds_read_b128 v[68:71], v41 offset:8704\n"
"v_mfma_f32_16x16x32_bf16 v[240:243], a[32:35], v[72:75], v[240:243]\n"
"s_waitcnt vmcnt(16)\n"
"ds_read_b64_tr_b16 a[32:33], v45 offset:24576\n"
"v_mfma_f32_16x16x32_bf16 v[240:243], a[48:51], v[76:79], v[240:243]\n"
"s_getpc_b64 s[98:99]\n"
"7:\n"
"s_add_u32 s98, s98, .Lmha_VARJ-7b\n"
"s_addc_u32 s99, s99, (.Lmha_VARJ-7b)>>32\n"
"ds_read_b64_tr_b16 a[34:35], v45 offset:24832\n"
"v_mfma_f32_16x16x32_bf16 v[244:247], a[36:39], v[72:75], v[244:247]\n"
"s_waitcnt vmcnt(12)\n"
"ds_read_b64_tr_b16 a[48:49], v45 offset:28672\n"
"v_mfma_f32_16x16x32_bf16 v[244:247], a[52:55], v[76:79], v[244:247]\n"
"ds_read_b64_tr_b16 a[50:51], v45 offset:28928\n"
"v_mfma_f32_16x16x32_bf16 v[248:251], a[40:43], v[72:75], v[248:251]\n"
"global_load_dword v26, v0, s[98:99] offset:0\n"
"ds_read_b64_tr_b16 a[36:37], v46 offset:24576\n"
"v_mfma_f32_16x16x32_bf16 v[248:251], a[56:59], v[76:79], v[248:251]\n"
"ds_read_b64_tr_b16 a[38:39], v46 offset:24832\n"
"v_mfma_f32_16x16x32_bf16 v[252:255], a[44:47], v[72:75], v[252:255]\n"
"ds_read_b128 v[72:75], v41 offset:10240\n"
"v_mfma_f32_16x16x32_bf16 v[252:255], a[60:63], v[76:79], v[252:255]\n"
"s_waitcnt lgkmcnt(9)\n"
"ds_read_b128 v[76:79], v41 offset:10752\n"
"v_mfma_f32_16x16x32_bf16 v[112:115], a[0:3], v[48:51], v[112:115]\n"
"s_add_u32 s91, s72, 14\n"
"ds_read_b64_tr_b16 a[52:53], v46 offset:28672\n"
"v_mfma_f32_16x16x32_bf16 v[112:115], a[16:19], v[52:55], v[112:115]\n"
"s_and_b32 s91, s91, 15\n"
"ds_read_b64_tr_b16 a[54:55], v46 offset:28928\n"
"v_mfma_f32_16x16x32_bf16 v[116:119], a[4:7], v[48:51], v[116:119]\n"
"s_lshl_b32 s91, s91, 19\n"
"ds_read_b64_tr_b16 a[40:41], v45 offset:25088\n"
"v_mfma_f32_16x16x32_bf16 v[116:119], a[20:23], v[52:55], v[116:119]\n"
"ds_read_b64_tr_b16 a[42:43], v45 offset:25344\n"
"v_mfma_f32_16x16x32_bf16 v[120:123], a[8:11], v[48:51], v[120:123]\n"
"ds_read_b64_tr_b16 a[56:57], v45 offset:29184\n"
"v_mfma_f32_16x16x32_bf16 v[120:123], a[24:27], v[52:55], v[120:123]\n"
"ds_read_b64_tr_b16 a[58:59], v45 offset:29440\n"
"v_mfma_f32_16x16x32_bf16 v[124:127], a[12:15], v[48:51], v[124:127]\n"
"ds_read_b128 v[48:51], v42 offset:8192\n"
"v_mfma_f32_16x16x32_bf16 v[124:127], a[28:31], v[52:55], v[124:127]\n"
"ds_read_b128 v[52:55], v42 offset:8704\n"
"v_mfma_f32_16x16x32_bf16 v[128:131], a[0:3], v[56:59], v[128:131]\n"
"ds_read_b64_tr_b16 a[44:45], v46 offset:25088\n"
"v_mfma_f32_16x16x32_bf16 v[128:131], a[16:19], v[60:63], v[128:131]\n"
"ds_read_b64_tr_b16 a[46:47], v46 offset:25344\n"
"v_mfma_f32_16x16x32_bf16 v[132:135], a[4:7], v[56:59], v[132:135]\n"
"ds_read_b64_tr_b16 a[60:61], v46 offset:29184\n"
"v_mfma_f32_16x16x32_bf16 v[132:135], a[20:23], v[60:63], v[132:135]\n"
"s_add_u32 m0, s30, 0x4000\n"
"ds_read_b64_tr_b16 a[62:63], v46 offset:29440\n"
"v_mfma_f32_16x16x32_bf16 v[136:139], a[8:11], v[56:59], v[136:139]\n"
"buffer_load_dwordx4 v37, s[40:43], s91 offen offset:0 lds\n"
"v_mfma_f32_16x16x32_bf16 v[136:139], a[24:27], v[60:63], v[136:139]\n"
"v_mfma_f32_16x16x32_bf16 v[140:143], a[12:15], v[56:59], v[140:143]\n"
"ds_read_b128 v[56:59], v42 offset:10240\n"
"v_mfma_f32_16x16x32_bf16 v[140:143], a[28:31], v[60:63], v[140:143]\n"
"s_waitcnt lgkmcnt(8)\n"
"ds_read_b128 v[60:63], v42 offset:10752\n"
"v_mfma_f32_16x16x32_bf16 v[144:147], a[0:3], v[64:67], v[144:147]\n"
"buffer_load_dwordx4 v38, s[40:43], s91 offen offset:1024 lds\n"
"v_mfma_f32_16x16x32_bf16 v[144:147], a[16:19], v[68:71], v[144:147]\n"
"v_mfma_f32_16x16x32_bf16 v[148:151], a[4:7], v[64:67], v[148:151]\n"
"v_mfma_f32_16x16x32_bf16 v[148:151], a[20:23], v[68:71], v[148:151]\n"
"v_mfma_f32_16x16x32_bf16 v[152:155], a[8:11], v[64:67], v[152:155]\n"
"buffer_load_dwordx4 v39, s[40:43], s91 offen offset:2048 lds\n"
"v_mfma_f32_16x16x32_bf16 v[152:155], a[24:27], v[68:71], v[152:155]\n"
"v_mfma_f32_16x16x32_bf16 v[156:159], a[12:15], v[64:67], v[156:159]\n"
"ds_read_b128 v[64:67], v43 offset:8192\n"
"v_mfma_f32_16x16x32_bf16 v[156:159], a[28:31], v[68:71], v[156:159]\n"
"ds_read_b128 v[68:71], v43 offset:8704\n"
"v_mfma_f32_16x16x32_bf16 v[160:163], a[0:3], v[72:75], v[160:163]\n"
"buffer_load_dwordx4 v40, s[40:43], s91 offen offset:3072 lds\n"
"v_mfma_f32_16x16x32_bf16 v[160:163], a[16:19], v[76:79], v[160:163]\n"
"v_mfma_f32_16x16x32_bf16 v[164:167], a[4:7], v[72:75], v[164:167]\n"
"v_mfma_f32_16x16x32_bf16 v[164:167], a[20:23], v[76:79], v[164:167]\n"
"s_add_u32 s78, s91, 0x40000\n"
"s_add_u32 m0, s30, 0x5000\n"
"v_mfma_f32_16x16x32_bf16 v[168:171], a[8:11], v[72:75], v[168:171]\n"
"buffer_load_dwordx4 v37, s[40:43], s78 offen offset:0 lds\n"
"v_mfma_f32_16x16x32_bf16 v[168:171], a[24:27], v[76:79], v[168:171]\n"
"v_mfma_f32_16x16x32_bf16 v[172:175], a[12:15], v[72:75], v[172:175]\n"
"ds_read_b128 v[72:75], v43 offset:10240\n"
"v_mfma_f32_16x16x32_bf16 v[172:175], a[28:31], v[76:79], v[172:175]\n"
"s_waitcnt lgkmcnt(3)\n"
"ds_read_b128 v[76:79], v43 offset:10752\n"
"v_mfma_f32_16x16x32_bf16 v[176:179], a[0:3], v[48:51], v[176:179]\n"
"s_add_u32 s74, s72, 15\n"
"buffer_load_dwordx4 v38, s[40:43], s78 offen offset:1024 lds\n"
"v_mfma_f32_16x16x32_bf16 v[176:179], a[16:19], v[52:55], v[176:179]\n"
"s_and_b32 s74, s74, 15\n"
"v_mfma_f32_16x16x32_bf16 v[180:183], a[4:7], v[48:51], v[180:183]\n"
"s_lshl_b32 s74, s74, 19\n"
"v_mfma_f32_16x16x32_bf16 v[180:183], a[20:23], v[52:55], v[180:183]\n"
"v_mfma_f32_16x16x32_bf16 v[184:187], a[8:11], v[48:51], v[184:187]\n"
"buffer_load_dwordx4 v39, s[40:43], s78 offen offset:2048 lds\n"
"v_mfma_f32_16x16x32_bf16 v[184:187], a[24:27], v[52:55], v[184:187]\n"
"v_mfma_f32_16x16x32_bf16 v[188:191], a[12:15], v[48:51], v[188:191]\n"
"ds_read_b128 v[48:51], v44 offset:0\n"
"v_mfma_f32_16x16x32_bf16 v[188:191], a[28:31], v[52:55], v[188:191]\n"
"ds_read_b128 v[52:55], v44 offset:512\n"
"v_mfma_f32_16x16x32_bf16 v[192:195], a[0:3], v[56:59], v[192:195]\n"
"buffer_load_dwordx4 v40, s[40:43], s78 offen offset:3072 lds\n"
"v_mfma_f32_16x16x32_bf16 v[192:195], a[16:19], v[60:63], v[192:195]\n"
"v_mfma_f32_16x16x32_bf16 v[196:199], a[4:7], v[56:59], v[196:199]\n"
"v_mfma_f32_16x16x32_bf16 v[196:199], a[20:23], v[60:63], v[196:199]\n"
"s_add_u32 m0, s30, 0x6000\n"
"v_mfma_f32_16x16x32_bf16 v[200:203], a[8:11], v[56:59], v[200:203]\n"
"buffer_load_dwordx4 v37, s[40:43], s74 offen offset:0 lds\n"
"v_mfma_f32_16x16x32_bf16 v[200:203], a[24:27], v[60:63], v[200:203]\n"
"v_mfma_f32_16x16x32_bf16 v[204:207], a[12:15], v[56:59], v[204:207]\n"
"v_mfma_f32_16x16x32_bf16 v[204:207], a[28:31], v[60:63], v[204:207]\n"
"s_waitcnt lgkmcnt(2)\n"
"v_mfma_f32_16x16x32_bf16 v[208:211], a[0:3], v[64:67], v[208:211]\n"
"buffer_load_dwordx4 v38, s[40:43], s74 offen offset:1024 lds\n"
"v_mfma_f32_16x16x32_bf16 v[208:211], a[16:19], v[68:71], v[208:211]\n"
"v_mfma_f32_16x16x32_bf16 v[212:215], a[4:7], v[64:67], v[212:215]\n"
"v_mfma_f32_16x16x32_bf16 v[212:215], a[20:23], v[68:71], v[212:215]\n"
"v_mfma_f32_16x16x32_bf16 v[216:219], a[8:11], v[64:67], v[216:219]\n"
"buffer_load_dwordx4 v39, s[40:43], s74 offen offset:2048 lds\n"
"v_mfma_f32_16x16x32_bf16 v[216:219], a[24:27], v[68:71], v[216:219]\n"
"v_mfma_f32_16x16x32_bf16 v[220:223], a[12:15], v[64:67], v[220:223]\n"
"v_mfma_f32_16x16x32_bf16 v[220:223], a[28:31], v[68:71], v[220:223]\n"
"v_mfma_f32_16x16x32_bf16 v[224:227], a[0:3], v[72:75], v[224:227]\n"
"buffer_load_dwordx4 v40, s[40:43], s74 offen offset:3072 lds\n"
"v_mfma_f32_16x16x32_bf16 v[224:227], a[16:19], v[76:79], v[224:227]\n"
"s_waitcnt vmcnt(21) lgkmcnt(0)\n"
"s_barrier\n"
"v_mfma_f32_16x16x32_bf16 v[228:231], a[4:7], v[72:75], v[228:231]\n"
"s_add_u32 m0, s30, 0x7000\n"
"ds_read_b128 v[56:59], v27 offset:32768\n"
"ds_read_b128 v[60:63], v27 offset:33280\n"
"v_mfma_f32_16x16x32_bf16 v[228:231], a[20:23], v[76:79], v[228:231]\n"
"s_add_u32 s84, s74, 0x40000\n"
"ds_read_b128 v[64:67], v27 offset:34816\n"
"ds_read_b128 v[68:71], v27 offset:35328\n"
"v_mfma_f32_16x16x32_bf16 v[232:235], a[8:11], v[72:75], v[232:235]\n"
"buffer_load_dwordx4 v37, s[40:43], s84 offen offset:0 lds\n"
"v_mfma_f32_16x16x32_bf16 v[232:235], a[24:27], v[76:79], v[232:235]\n"
"v_mfma_f32_16x16x32_bf16 v[236:239], a[12:15], v[72:75], v[236:239]\n"
"ds_read_b128 v[72:75], v41 offset:32768\n"
"v_mfma_f32_16x16x32_bf16 v[236:239], a[28:31], v[76:79], v[236:239]\n"
"s_add_u32 s89, s72, 14\n"
"ds_read_b128 v[76:79], v41 offset:33280\n"
"v_mfma_f32_16x16x32_bf16 v[240:243], a[0:3], v[48:51], v[240:243]\n"
"s_and_b32 s89, s89, 15\n"
"buffer_load_dwordx4 v38, s[40:43], s84 offen offset:1024 lds\n"
"v_mfma_f32_16x16x32_bf16 v[240:243], a[16:19], v[52:55], v[240:243]\n"
"s_lshl_b32 s89, s89, 7\n"
"v_mfma_f32_16x16x32_bf16 v[244:247], a[4:7], v[48:51], v[244:247]\n"
"v_mfma_f32_16x16x32_bf16 v[244:247], a[20:23], v[52:55], v[244:247]\n"
"v_mfma_f32_16x16x32_bf16 v[248:251], a[8:11], v[48:51], v[248:251]\n"
"buffer_load_dwordx4 v39, s[40:43], s84 offen offset:2048 lds\n"
"v_mfma_f32_16x16x32_bf16 v[248:251], a[24:27], v[52:55], v[248:251]\n"
"v_mfma_f32_16x16x32_bf16 v[252:255], a[12:15], v[48:51], v[252:255]\n"
"ds_read_b128 v[48:51], v41 offset:34816\n"
"v_mfma_f32_16x16x32_bf16 v[252:255], a[28:31], v[52:55], v[252:255]\n"
"s_waitcnt lgkmcnt(3)\n"
"ds_read_b128 v[52:55], v41 offset:35328\n"
"v_mfma_f32_16x16x32_bf16 v[112:115], a[32:35], v[56:59], v[112:115]\n"
"buffer_load_dwordx4 v40, s[40:43], s84 offen offset:3072 lds\n"
"v_mfma_f32_16x16x32_bf16 v[112:115], a[48:51], v[60:63], v[112:115]\n"
"s_mul_i32 s41, s68, 0x2400\n"
"s_add_u32 m0, s30, 0x3000\n"
"v_mfma_f32_16x16x32_bf16 v[116:119], a[36:39], v[56:59], v[116:119]\n"
"s_waitcnt vmcnt(12)\n"
"s_xor_b32 s68, s68, 32\n"
"ds_read_b64_tr_b16 a[0:1], v45 offset:16384\n"
"v_mfma_f32_16x16x32_bf16 v[116:119], a[52:55], v[60:63], v[116:119]\n"
"s_mul_i32 s40, s68, 0x2400\n"
"buffer_load_dwordx4 v32, s[36:39], s89 offen offset:0 lds\n"
"ds_read_b64_tr_b16 a[2:3], v45 offset:16640\n"
"v_mfma_f32_16x16x32_bf16 v[120:123], a[40:43], v[56:59], v[120:123]\n"
"s_lshl_b32 s68, s29, 1\n"
"ds_read_b64_tr_b16 a[4:5], v46 offset:16384\n"
"v_mfma_f32_16x16x32_bf16 v[120:123], a[56:59], v[60:63], v[120:123]\n"
"s_add_u32 s68, s68, s28\n"
"ds_read_b64_tr_b16 a[6:7], v46 offset:16640\n"
"v_mfma_f32_16x16x32_bf16 v[124:127], a[44:47], v[56:59], v[124:127]\n"
"s_lshl_b32 s68, s68, 2\n"
"ds_read_b128 v[56:59], v42 offset:32768\n"
"v_mfma_f32_16x16x32_bf16 v[124:127], a[60:63], v[60:63], v[124:127]\n"
"s_add_u32 s68, s68, s11\n"
"buffer_load_dwordx4 v33, s[36:39], s89 offen offset:1024 lds\n"
"ds_read_b128 v[60:63], v42 offset:33280\n"
"v_mfma_f32_16x16x32_bf16 v[128:131], a[32:35], v[64:67], v[128:131]\n"
"s_lshl_b32 s42, s68, 5\n"
"ds_read_b64_tr_b16 a[8:9], v45 offset:16896\n"
"v_mfma_f32_16x16x32_bf16 v[128:131], a[48:51], v[68:71], v[128:131]\n"
"ds_read_b64_tr_b16 a[10:11], v45 offset:17152\n"
"v_mfma_f32_16x16x32_bf16 v[132:135], a[36:39], v[64:67], v[132:135]\n"
"ds_read_b64_tr_b16 a[12:13], v46 offset:16896\n"
"s_add_u32 s73, s72, 15\n"
"v_mfma_f32_16x16x32_bf16 v[132:135], a[52:55], v[68:71], v[132:135]\n"
"buffer_load_dwordx4 v34, s[36:39], s89 offen offset:2048 lds\n"
"ds_read_b64_tr_b16 a[14:15], v46 offset:17152\n"
"s_and_b32 s73, s73, 15\n"
"v_mfma_f32_16x16x32_bf16 v[136:139], a[40:43], v[64:67], v[136:139]\n"
"s_lshl_b32 s73, s73, 7\n"
"v_mfma_f32_16x16x32_bf16 v[136:139], a[56:59], v[68:71], v[136:139]\n"
"buffer_load_dwordx4 v35, s[36:39], s89 offen offset:3072 lds\n"
"s_add_u32 m0, s30, 0x9400\n"
"s_nop 0\n"
"buffer_load_dwordx4 v36, s[36:39], s89 offen offset:0 lds\n"
"v_mfma_f32_16x16x32_bf16 v[140:143], a[44:47], v[64:67], v[140:143]\n"
"s_lshl_b32 s89, s70, 16\n"
"s_add_u32 m0, s30, 0x2000\n"
"ds_read_b128 v[64:67], v42 offset:34816\n"
"buffer_load_dwordx4 v32, s[36:39], s73 offen offset:0 lds\n"
"buffer_load_dwordx4 v33, s[36:39], s73 offen offset:1024 lds\n"
"v_mfma_f32_16x16x32_bf16 v[140:143], a[60:63], v[68:71], v[140:143]\n"
"s_waitcnt lgkmcnt(8)\n"
"s_and_b32 s70, s27, 7\n"
"ds_read_b128 v[68:71], v42 offset:35328\n"
"buffer_load_dwordx4 v34, s[36:39], s73 offen offset:2048 lds\n"
"buffer_load_dwordx4 v35, s[36:39], s73 offen offset:3072 lds\n"
"v_mfma_f32_16x16x32_bf16 v[144:147], a[32:35], v[72:75], v[144:147]\n"
"s_sub_u32 s70, s70, 7\n"
"s_lshl_b32 s39, s27, 7\n"
"v_mfma_f32_16x16x32_bf16 v[144:147], a[48:51], v[76:79], v[144:147]\n"
"s_waitcnt vmcnt(17)\n"
"s_lshr_b32 s70, s70, 31\n"
"ds_read_b64_tr_b16 a[16:17], v45 offset:20480\n"
"v_mfma_f32_16x16x32_bf16 v[148:151], a[36:39], v[72:75], v[148:151]\n"
"s_add_u32 s70, s70, s71\n"
"s_add_u32 s39, s39, s94\n"
"ds_read_b64_tr_b16 a[18:19], v45 offset:20736\n"
"v_mfma_f32_16x16x32_bf16 v[148:151], a[52:55], v[76:79], v[148:151]\n"
"s_add_u32 s70, s70, 6\n"
"ds_read_b64_tr_b16 a[20:21], v46 offset:20480\n"
"v_mfma_f32_16x16x32_bf16 v[152:155], a[40:43], v[72:75], v[152:155]\n"
"s_lshl_b32 s70, s70, 3\n"
"ds_read_b64_tr_b16 a[22:23], v46 offset:20736\n"
"v_mfma_f32_16x16x32_bf16 v[152:155], a[56:59], v[76:79], v[152:155]\n"
"s_add_u32 s70, s70, s11\n"
"ds_read_b64_tr_b16 a[24:25], v45 offset:20992\n"
"v_mfma_f32_16x16x32_bf16 v[156:159], a[44:47], v[72:75], v[156:159]\n"
"s_mul_i32 s90, s70, 0x2400\n"
"s_lshl_b32 s91, s70, 16\n"
"ds_read_b128 v[72:75], v43 offset:32768\n"
"v_mfma_f32_16x16x32_bf16 v[156:159], a[60:63], v[76:79], v[156:159]\n"
"ds_read_b128 v[76:79], v43 offset:33280\n"
"v_mfma_f32_16x16x32_bf16 v[160:163], a[32:35], v[48:51], v[160:163]\n"
"ds_read_b64_tr_b16 a[26:27], v45 offset:21248\n"
"v_mfma_f32_16x16x32_bf16 v[160:163], a[48:51], v[52:55], v[160:163]\n"
"ds_read_b64_tr_b16 a[28:29], v46 offset:20992\n"
"v_mfma_f32_16x16x32_bf16 v[164:167], a[36:39], v[48:51], v[164:167]\n"
"ds_read_b64_tr_b16 a[30:31], v46 offset:21248\n"
"v_mfma_f32_16x16x32_bf16 v[164:167], a[52:55], v[52:55], v[164:167]\n"
"v_mfma_f32_16x16x32_bf16 v[168:171], a[40:43], v[48:51], v[168:171]\n"
"v_mfma_f32_16x16x32_bf16 v[168:171], a[56:59], v[52:55], v[168:171]\n"
"v_mfma_f32_16x16x32_bf16 v[172:175], a[44:47], v[48:51], v[172:175]\n"
"ds_read_b128 v[48:51], v43 offset:34816\n"
"v_mfma_f32_16x16x32_bf16 v[172:175], a[60:63], v[52:55], v[172:175]\n"
"s_waitcnt lgkmcnt(7)\n"
"ds_read_b128 v[52:55], v43 offset:35328\n"
"v_mfma_f32_16x16x32_bf16 v[176:179], a[32:35], v[56:59], v[176:179]\n"
"v_mfma_f32_16x16x32_bf16 v[176:179], a[48:51], v[60:63], v[176:179]\n"
"s_waitcnt vmcnt(13)\n"
"s_mov_b32 s96, 0x8000\n"
"ds_read_b64_tr_b16 v[80:81], v45 offset:24576\n"
"v_mfma_f32_16x16x32_bf16 v[180:183], a[36:39], v[56:59], v[180:183]\n"
"buffer_load_dwordx4 a[160:163], v19, s[48:51], s96 offen offset:0\n"
"ds_read_b64_tr_b16 v[82:83], v45 offset:24832\n"
"v_mfma_f32_16x16x32_bf16 v[180:183], a[52:55], v[60:63], v[180:183]\n"
"ds_read_b64_tr_b16 v[84:85], v46 offset:24576\n"
"v_mfma_f32_16x16x32_bf16 v[184:187], a[40:43], v[56:59], v[184:187]\n"
"ds_read_b64_tr_b16 v[86:87], v46 offset:24832\n"
"v_mfma_f32_16x16x32_bf16 v[184:187], a[56:59], v[60:63], v[184:187]\n"
"ds_read_b64_tr_b16 v[88:89], v45 offset:25088\n"
"v_mfma_f32_16x16x32_bf16 v[188:191], a[44:47], v[56:59], v[188:191]\n"
"ds_read_b128 v[56:59], v30 offset:0\n"
"v_mfma_f32_16x16x32_bf16 v[188:191], a[60:63], v[60:63], v[188:191]\n"
"ds_read_b128 v[60:63], v30 offset:512\n"
"v_mfma_f32_16x16x32_bf16 v[192:195], a[32:35], v[64:67], v[192:195]\n"
"ds_read_b64_tr_b16 v[90:91], v45 offset:25344\n"
"v_mfma_f32_16x16x32_bf16 v[192:195], a[48:51], v[68:71], v[192:195]\n"
"ds_read_b64_tr_b16 v[92:93], v46 offset:25088\n"
"v_mfma_f32_16x16x32_bf16 v[196:199], a[36:39], v[64:67], v[196:199]\n"
"ds_read_b64_tr_b16 v[94:95], v46 offset:25344\n"
"v_mfma_f32_16x16x32_bf16 v[196:199], a[52:55], v[68:71], v[196:199]\n"
"v_mfma_f32_16x16x32_bf16 v[200:203], a[40:43], v[64:67], v[200:203]\n"
"v_mfma_f32_16x16x32_bf16 v[200:203], a[56:59], v[68:71], v[200:203]\n"
"v_mfma_f32_16x16x32_bf16 v[204:207], a[44:47], v[64:67], v[204:207]\n"
"v_mfma_f32_16x16x32_bf16 v[204:207], a[60:63], v[68:71], v[204:207]\n"
"s_waitcnt lgkmcnt(6)\n"
"buffer_load_dwordx4 a[164:167], v20, s[48:51], s96 offen offset:1024\n"
"v_mfma_f32_16x16x32_bf16 v[208:211], a[32:35], v[72:75], v[208:211]\n"
"v_mfma_f32_16x16x32_bf16 v[208:211], a[48:51], v[76:79], v[208:211]\n"
"s_waitcnt vmcnt(11)\n"
"ds_read_b64_tr_b16 v[96:97], v45 offset:28672\n"
"v_mfma_f32_16x16x32_bf16 v[212:215], a[36:39], v[72:75], v[212:215]\n"
"ds_read_b64_tr_b16 v[98:99], v45 offset:28928\n"
"v_mfma_f32_16x16x32_bf16 v[212:215], a[52:55], v[76:79], v[212:215]\n"
"ds_read_b64_tr_b16 v[100:101], v46 offset:28672\n"
"v_mfma_f32_16x16x32_bf16 v[216:219], a[40:43], v[72:75], v[216:219]\n"
"ds_read_b64_tr_b16 v[102:103], v46 offset:28928\n"
"v_mfma_f32_16x16x32_bf16 v[216:219], a[56:59], v[76:79], v[216:219]\n"
"ds_read_b64_tr_b16 v[104:105], v45 offset:29184\n"
"v_mfma_f32_16x16x32_bf16 v[220:223], a[44:47], v[72:75], v[220:223]\n"
"ds_read_b64_tr_b16 v[106:107], v45 offset:29440\n"
"v_mfma_f32_16x16x32_bf16 v[220:223], a[60:63], v[76:79], v[220:223]\n"
"ds_read_b64_tr_b16 v[108:109], v46 offset:29184\n"
"v_mfma_f32_16x16x32_bf16 v[224:227], a[32:35], v[48:51], v[224:227]\n"
"ds_read_b64_tr_b16 v[110:111], v46 offset:29440\n"
"v_mfma_f32_16x16x32_bf16 v[224:227], a[48:51], v[52:55], v[224:227]\n"
"v_mfma_f32_16x16x32_bf16 v[228:231], a[36:39], v[48:51], v[228:231]\n"
"v_mfma_f32_16x16x32_bf16 v[228:231], a[52:55], v[52:55], v[228:231]\n"
"buffer_load_dwordx4 a[168:171], v19, s[48:51], s96 offen offset:2048\n"
"v_mfma_f32_16x16x32_bf16 v[232:235], a[40:43], v[48:51], v[232:235]\n"
"v_mfma_f32_16x16x32_bf16 v[232:235], a[56:59], v[52:55], v[232:235]\n"
"v_mfma_f32_16x16x32_bf16 v[236:239], a[44:47], v[48:51], v[236:239]\n"
"v_mfma_f32_16x16x32_bf16 v[236:239], a[60:63], v[52:55], v[236:239]\n"
"s_waitcnt lgkmcnt(4)\n"
"v_mfma_f32_16x16x32_bf16 v[240:243], a[32:35], v[56:59], v[240:243]\n"
"v_mfma_f32_16x16x32_bf16 v[240:243], a[48:51], v[60:63], v[240:243]\n"
"v_mfma_f32_16x16x32_bf16 v[244:247], a[36:39], v[56:59], v[244:247]\n"
"s_waitcnt vmcnt(3)\n"
"s_barrier\n"
"ds_read_b128 v[64:67], v44 offset:1024\n"
"ds_read_b128 v[68:71], v44 offset:1536\n"
"v_mfma_f32_16x16x32_bf16 v[244:247], a[52:55], v[60:63], v[244:247]\n"
"ds_read_b128 v[72:75], v30 offset:1024\n"
"ds_read_b128 v[76:79], v30 offset:1536\n"
"v_mfma_f32_16x16x32_bf16 v[248:251], a[40:43], v[56:59], v[248:251]\n"
"ds_read_b128 v[48:51], v27 offset:12288\n"
"ds_read_b128 v[52:55], v27 offset:12800\n"
"v_mfma_f32_16x16x32_bf16 v[248:251], a[56:59], v[60:63], v[248:251]\n"
"ds_read_b128 a[32:35], v21 offset:0\n"
"v_add_u32 v30, 0x8000, v22\n"
"v_mfma_f32_16x16x32_bf16 v[252:255], a[44:47], v[56:59], v[252:255]\n"
"ds_read_b128 v[56:59], v27 offset:8192\n"
"v_mfma_f32_16x16x32_bf16 v[252:255], a[60:63], v[60:63], v[252:255]\n"
"s_mov_b32 s75, 0x8000\n"
"ds_read_b128 v[60:63], v27 offset:8704\n"
"ds_read_b128 a[36:39], v21 offset:256\n"
"ds_read_b128 a[40:43], v21 offset:512\n"
"ds_read_b128 a[44:47], v21 offset:768\n"
"s_waitcnt lgkmcnt(11)\n"
"v_mfma_f32_16x16x32_bf16 v[240:243], a[0:3], v[64:67], v[240:243]\n"
"s_waitcnt lgkmcnt(10)\n"
"buffer_load_dwordx4 a[172:175], v20, s[48:51], s75 offen offset:3072\n"
"ds_read_b128 a[48:51], v1 offset:0\n"
"v_mfma_f32_16x16x32_bf16 v[240:243], a[16:19], v[68:71], v[240:243]\n"
"s_waitcnt lgkmcnt(10)\n"
"ds_read_b128 a[52:55], v1 offset:256\n"
"v_mfma_f32_16x16x32_bf16 v[240:243], v[80:83], v[72:75], v[240:243]\n"
"s_waitcnt lgkmcnt(10)\n"
"ds_read_b128 a[56:59], v1 offset:512\n"
"v_mfma_f32_16x16x32_bf16 v[240:243], v[96:99], v[76:79], v[240:243]\n"
"ds_read_b128 a[60:63], v1 offset:768\n"
"v_mfma_f32_16x16x32_bf16 v[244:247], a[4:7], v[64:67], v[244:247]\n"
"v_mfma_f32_16x16x32_bf16 v[244:247], a[20:23], v[68:71], v[244:247]\n"
"s_nop 0\n"
"s_nop 0\n"
"s_nop 0\n"
"v_mfma_f32_16x16x32_bf16 v[244:247], v[84:87], v[72:75], v[244:247]\n"
"v_mfma_f32_16x16x32_bf16 v[244:247], v[100:103], v[76:79], v[244:247]\n"
"v_mul_f32 v240, s85, v240\n"
"v_mul_f32 v241, s85, v241\n"
"v_mfma_f32_16x16x32_bf16 v[248:251], a[8:11], v[64:67], v[248:251]\n"
"v_mul_f32 v242, s85, v242\n"
"v_mul_f32 v243, s85, v243\n"
"v_mfma_f32_16x16x32_bf16 v[248:251], a[24:27], v[68:71], v[248:251]\n"
"v_cvt_pk_bf16_f32 v240, v240, v241\n"
"v_cvt_pk_bf16_f32 v241, v242, v243\n"
"v_mfma_f32_16x16x32_bf16 v[248:251], v[88:91], v[72:75], v[248:251]\n"
"ds_write_b64 v47, v[240:241] offset:32768\n"
"v_mfma_f32_16x16x32_bf16 v[248:251], v[104:107], v[76:79], v[248:251]\n"
"s_mov_b32 s76, 0x10000\n"
"v_mul_f32 v244, s85, v244\n"
"v_mul_f32 v245, s85, v245\n"
"v_mfma_f32_16x16x32_bf16 v[252:255], a[12:15], v[64:67], v[252:255]\n"
"buffer_load_dwordx4 a[176:179], v17, s[44:47], s76 offen offset:0\n"
"v_mul_f32 v246, s85, v246\n"
"v_mfma_f32_16x16x32_bf16 v[252:255], a[28:31], v[68:71], v[252:255]\n"
"ds_read_b128 v[64:67], v27 offset:14336\n"
"v_mul_f32 v247, s85, v247\n"
"v_mfma_f32_16x16x32_bf16 v[252:255], v[92:95], v[72:75], v[252:255]\n"
"ds_read_b128 v[68:71], v27 offset:14848\n"
"v_cvt_pk_bf16_f32 v244, v244, v245\n"
"v_mfma_f32_16x16x32_bf16 v[252:255], v[108:111], v[76:79], v[252:255]\n"
"s_waitcnt lgkmcnt(3)\n"
"ds_read_b128 v[72:75], v27 offset:10240\n"
"v_cvt_pk_bf16_f32 v245, v246, v247\n"
"v_mfma_f32_16x16x32_bf16 v[112:115], a[0:3], v[48:51], v[112:115]\n"
"ds_write_b64 v47, v[244:245] offset:33280\n"
"v_mfma_f32_16x16x32_bf16 v[112:115], a[16:19], v[52:55], v[112:115]\n"
"v_mul_f32 v248, s85, v248\n"
"v_mul_f32 v249, s85, v249\n"
"v_mul_f32 v250, s85, v250\n"
"v_mfma_f32_16x16x32_bf16 v[112:115], v[80:83], v[56:59], v[112:115]\n"
"ds_read_b128 v[76:79], v27 offset:10752\n"
"v_mul_f32 v251, s85, v251\n"
"v_cvt_pk_bf16_f32 v248, v248, v249\n"
"s_branch .Lmha_VARJ\n"
".Lmha_SLOW0:\n"
"s_waitcnt vmcnt(0) lgkmcnt(0)\n"
"s_nop 15\n"
"s_lshl_b32 s68, s10, 2\n"
"s_add_u32 s68, s68, s11\n"
"s_mul_i32 s68, s68, 0x8000\n"
"s_add_u32 s68, s68, 0x940000\n"
"s_add_u32 s69, s68, 0\n"
"buffer_store_dwordx4 v[152:155], v22, s[64:67], s69 offen offset:0\n"
"s_add_u32 s69, s68, 0x400\n"
"buffer_store_dwordx4 v[156:159], v22, s[64:67], s69 offen offset:0\n"
"s_add_u32 s69, s68, 0x800\n"
"buffer_store_dwordx4 v[160:163], v22, s[64:67], s69 offen offset:0\n"
"s_add_u32 s69, s68, 0xc00\n"
"buffer_store_dwordx4 v[164:167], v22, s[64:67], s69 offen offset:0\n"
"s_add_u32 s69, s68, 0x1000\n"
"buffer_store_dwordx4 v[168:171], v22, s[64:67], s69 offen offset:0\n"
"s_add_u32 s69, s68, 0x1400\n"
"buffer_store_dwordx4 v[172:175], v22, s[64:67], s69 offen offset:0\n"
"s_add_u32 s69, s68, 0x1800\n"
"buffer_store_dwordx4 v[176:179], v22, s[64:67], s69 offen offset:0\n"
"s_add_u32 s69, s68, 0x1c00\n"
"buffer_store_dwordx4 v[180:183], v22, s[64:67], s69 offen offset:0\n"
"s_add_u32 s69, s68, 0x2000\n"
"buffer_store_dwordx4 v[184:187], v22, s[64:67], s69 offen offset:0\n"
"s_add_u32 s69, s68, 0x2400\n"
"buffer_store_dwordx4 v[188:191], v22, s[64:67], s69 offen offset:0\n"
"s_add_u32 s69, s68, 0x2800\n"
"buffer_store_dwordx4 v[192:195], v22, s[64:67], s69 offen offset:0\n"
"s_add_u32 s69, s68, 0x2c00\n"
"buffer_store_dwordx4 v[196:199], v22, s[64:67], s69 offen offset:0\n"
"s_add_u32 s69, s68, 0x3000\n"
"buffer_store_dwordx4 v[200:203], v22, s[64:67], s69 offen offset:0\n"
"s_add_u32 s69, s68, 0x3400\n"
"buffer_store_dwordx4 v[204:207], v22, s[64:67], s69 offen offset:0\n"
"s_add_u32 s69, s68, 0x3800\n"
"buffer_store_dwordx4 v[208:211], v22, s[64:67], s69 offen offset:0\n"
"s_add_u32 s69, s68, 0x3c00\n"
"buffer_store_dwordx4 v[212:215], v22, s[64:67], s69 offen offset:0\n"
"s_add_u32 s69, s68, 0x4000\n"
"buffer_store_dwordx4 v[216:219], v22, s[64:67], s69 offen offset:0\n"
"s_add_u32 s69, s68, 0x4400\n"
"buffer_store_dwordx4 v[220:223], v22, s[64:67], s69 offen offset:0\n"
"s_add_u32 s69, s68, 0x4800\n"
"buffer_store_dwordx4 v[224:227], v22, s[64:67], s69 offen offset:0\n"
"s_add_u32 s69, s68, 0x4c00\n"
"buffer_store_dwordx4 v[228:231], v22, s[64:67], s69 offen offset:0\n"
"s_add_u32 s69, s68, 0x5000\n"
"buffer_store_dwordx4 v[232:235], v22, s[64:67], s69 offen offset:0\n"
"s_add_u32 s69, s68, 0x5400\n"
"buffer_store_dwordx4 v[236:239], v22, s[64:67], s69 offen offset:0\n"
"s_add_u32 s69, s68, 0x5800\n"
"buffer_store_dwordx4 v[240:243], v22, s[64:67], s69 offen offset:0\n"
"s_waitcnt vmcnt(0) lgkmcnt(0)\n"
"s_nop 1\n"
"v_mov_b32 v240, 0x3f803f80\n"
"v_mov_b32 v241, 0x3f803f80\n"
"v_mov_b32 v242, 0x3f803f80\n"
"v_mov_b32 v243, 0x3f803f80\n"
"v_lshrrev_b32 v234, 4, v22\n"
"v_and_b32 v235, 15, v234\n"
"v_lshrrev_b32 v234, 4, v234\n"
"v_lshlrev_b32 v232, 8, v235\n"
"v_lshl_add_u32 v232, v234, 4, v232\n"
"v_lshlrev_b32 v233, 10, v234\n"
"v_lshl_add_u32 v233, v235, 1, v233\n"
"v_mov_b32 v236, 0xff800000\n"
"v_mov_b32 v237, 0xff800000\n"
"v_mov_b32 v238, 0xff800000\n"
"s_mov_b32 s70, 0\n"
".Lmha_SL1_0:\n"
"buffer_load_dwordx4 v[152:155], v232, s[44:47], s70 offen offset:0\n"
"buffer_load_dwordx4 v[156:159], v232, s[44:47], s70 offen offset:64\n"
"buffer_load_dwordx4 v[160:163], v232, s[44:47], s70 offen offset:128\n"
"buffer_load_dwordx4 v[164:167], v232, s[44:47], s70 offen offset:192\n"
"s_waitcnt vmcnt(0) lgkmcnt(0)\n"
"v_mfma_f32_16x16x32_bf16 v[168:171], v[152:155], v[32:35], 0\n"
"v_mfma_f32_16x16x32_bf16 v[168:171], v[156:159], v[36:39], v[168:171]\n"
"v_mfma_f32_16x16x32_bf16 v[168:171], v[160:163], v[40:43], v[168:171]\n"
"v_mfma_f32_16x16x32_bf16 v[168:171], v[164:167], v[44:47], v[168:171]\n"
"v_mfma_f32_16x16x32_bf16 v[176:179], v[152:155], v[48:51], 0\n"
"v_mfma_f32_16x16x32_bf16 v[176:179], v[156:159], v[52:55], v[176:179]\n"
"v_mfma_f32_16x16x32_bf16 v[176:179], v[160:163], v[56:59], v[176:179]\n"
"v_mfma_f32_16x16x32_bf16 v[176:179], v[164:167], v[60:63], v[176:179]\n"
"v_mfma_f32_16x16x32_bf16 v[184:187], v[152:155], v[64:67], 0\n"
"v_mfma_f32_16x16x32_bf16 v[184:187], v[156:159], v[68:71], v[184:187]\n"
"v_mfma_f32_16x16x32_bf16 v[184:187], v[160:163], v[72:75], v[184:187]\n"
"v_mfma_f32_16x16x32_bf16 v[184:187], v[164:167], v[76:79], v[184:187]\n"
"s_nop 15\n"
"v_max3_f32 v236, v236, v168, v169\n"
"v_max3_f32 v236, v236, v170, v171\n"
"v_max3_f32 v237, v237, v176, v177\n"
"v_max3_f32 v237, v237, v178, v179\n"
"v_max3_f32 v238, v238, v184, v185\n"
"v_max3_f32 v238, v238, v186, v187\n"
"s_add_u32 s70, s70, 0x1000\n"
"s_cmp_lg_u32 s70, 0x20000\n"
"s_cbranch_scc1 .Lmha_SL1_0\n"
"v_mov_b32 v235, v236\n"
"s_nop 1\n"
"v_permlane32_swap_b32 v236, v235\n"
"s_nop 1\n"
"v_max_f32 v236, v236, v235\n"
"v_mov_b32 v235, v236\n"
"s_nop 1\n"
"v_permlane16_swap_b32 v236, v235\n"
"s_nop 1\n"
"v_max_f32 v236, v236, v235\n"
"v_mul_f32 v204, -1.0, v236\n"
"v_mul_f32 v205, -1.0, v236\n"
"v_mul_f32 v206, -1.0, v236\n"
"v_mul_f32 v207, -1.0, v236\n"
"v_mov_b32 v235, v237\n"
"s_nop 1\n"
"v_permlane32_swap_b32 v237, v235\n"
"s_nop 1\n"
"v_max_f32 v237, v237, v235\n"
"v_mov_b32 v235, v237\n"
"s_nop 1\n"
"v_permlane16_swap_b32 v237, v235\n"
"s_nop 1\n"
"v_max_f32 v237, v237, v235\n"
"v_mul_f32 v208, -1.0, v237\n"
"v_mul_f32 v209, -1.0, v237\n"
"v_mul_f32 v210, -1.0, v237\n"
"v_mul_f32 v211, -1.0, v237\n"
"v_mov_b32 v235, v238\n"
"s_nop 1\n"
"v_permlane32_swap_b32 v238, v235\n"
"s_nop 1\n"
"v_max_f32 v238, v238, v235\n"
"v_mov_b32 v235, v238\n"
"s_nop 1\n"
"v_permlane16_swap_b32 v238, v235\n"
"s_nop 1\n"
"v_max_f32 v238, v238, v235\n"
"v_mul_f32 v212, -1.0, v238\n"
"v_mul_f32 v213, -1.0, v238\n"
"v_mul_f32 v214, -1.0, v238\n"
"v_mul_f32 v215, -1.0, v238\n"
"v_mov_b32 v220, 0\n"
"v_mov_b32 v221, 0\n"
"v_mov_b32 v222, 0\n"
"v_mov_b32 v223, 0\n"
"v_mov_b32 v224, 0\n"
"v_mov_b32 v225, 0\n"
"v_mov_b32 v226, 0\n"
"v_mov_b32 v227, 0\n"
"v_mov_b32 v228, 0\n"
"v_mov_b32 v229, 0\n"
"v_mov_b32 v230, 0\n"
"v_mov_b32 v231, 0\n"
"v_mov_b32 v80, 0\n"
"v_mov_b32 v81, 0\n"
"v_mov_b32 v82, 0\n"
"v_mov_b32 v83, 0\n"
"v_mov_b32 v84, 0\n"
"v_mov_b32 v85, 0\n"
"v_mov_b32 v86, 0\n"
"v_mov_b32 v87, 0\n"
"v_mov_b32 v88, 0\n"
"v_mov_b32 v89, 0\n"
"v_mov_b32 v90, 0\n"
"v_mov_b32 v91, 0\n"
"v_mov_b32 v92, 0\n"
"v_mov_b32 v93, 0\n"
"v_mov_b32 v94, 0\n"
"v_mov_b32 v95, 0\n"
"v_mov_b32 v96, 0\n"
"v_mov_b32 v97, 0\n"
"v_mov_b32 v98, 0\n"
"v_mov_b32 v99, 0\n"
"v_mov_b32 v100, 0\n"
"v_mov_b32 v101, 0\n"
"v_mov_b32 v102, 0\n"
"v_mov_b32 v103, 0\n"
"v_mov_b32 v104, 0\n"
"v_mov_b32 v105, 0\n"
"v_mov_b32 v106, 0\n"
"v_mov_b32 v107, 0\n"
"v_mov_b32 v108, 0\n"
"v_mov_b32 v109, 0\n"
"v_mov_b32 v110, 0\n"
"v_mov_b32 v111, 0\n"
"v_mov_b32 v112, 0\n"
"v_mov_b32 v113, 0\n"
"v_mov_b32 v114, 0\n"
"v_mov_b32 v115, 0\n"
"v_mov_b32 v116, 0\n"
"v_mov_b32 v117, 0\n"
"v_mov_b32 v118, 0\n"
"v_mov_b32 v119, 0\n"
"v_mov_b32 v120, 0\n"
"v_mov_b32 v121, 0\n"
"v_mov_b32 v122, 0\n"
"v_mov_b32 v123, 0\n"
"v_mov_b32 v124, 0\n"
"v_mov_b32 v125, 0\n"
"v_mov_b32 v126, 0\n"
"v_mov_b32 v127, 0\n"
"v_mov_b32 v128, 0\n"
"v_mov_b32 v129, 0\n"
"v_mov_b32 v130, 0\n"
"v_mov_b32 v131, 0\n"
"v_mov_b32 v132, 0\n"
"v_mov_b32 v133, 0\n"
"v_mov_b32 v134, 0\n"
"v_mov_b32 v135, 0\n"
"v_mov_b32 v136, 0\n"
"v_mov_b32 v137, 0\n"
"v_mov_b32 v138, 0\n"
"v_mov_b32 v139, 0\n"
"v_mov_b32 v140, 0\n"
"v_mov_b32 v141, 0\n"
"v_mov_b32 v142, 0\n"
"v_mov_b32 v143, 0\n"
"v_mov_b32 v144, 0\n"
"v_mov_b32 v145, 0\n"
"v_mov_b32 v146, 0\n"
"v_mov_b32 v147, 0\n"
"v_mov_b32 v148, 0\n"
"v_mov_b32 v149, 0\n"
"v_mov_b32 v150, 0\n"
"v_mov_b32 v151, 0\n"
"s_nop 3\n"
"s_mov_b32 s70, 0\n"
".Lmha_SL2_0:\n"
"s_mov_b32 s71, s70\n"
"buffer_load_dwordx4 v[152:155], v232, s[44:47], s70 offen offset:0\n"
"buffer_load_dwordx4 v[156:159], v232, s[44:47], s70 offen offset:64\n"
"buffer_load_dwordx4 v[160:163], v232, s[44:47], s70 offen offset:128\n"
"buffer_load_dwordx4 v[164:167], v232, s[44:47], s70 offen offset:192\n"
"s_waitcnt vmcnt(0) lgkmcnt(0)\n"
"v_mfma_f32_16x16x32_bf16 v[168:171], v[152:155], v[32:35], v[204:207]\n"
"v_mfma_f32_16x16x32_bf16 v[168:171], v[156:159], v[36:39], v[168:171]\n"
"v_mfma_f32_16x16x32_bf16 v[168:171], v[160:163], v[40:43], v[168:171]\n"
"v_mfma_f32_16x16x32_bf16 v[168:171], v[164:167], v[44:47], v[168:171]\n"
"v_mfma_f32_16x16x32_bf16 v[176:179], v[152:155], v[48:51], v[208:211]\n"
"v_mfma_f32_16x16x32_bf16 v[176:179], v[156:159], v[52:55], v[176:179]\n"
"v_mfma_f32_16x16x32_bf16 v[176:179], v[160:163], v[56:59], v[176:179]\n"
"v_mfma_f32_16x16x32_bf16 v[176:179], v[164:167], v[60:63], v[176:179]\n"
"v_mfma_f32_16x16x32_bf16 v[184:187], v[152:155], v[64:67], v[212:215]\n"
"v_mfma_f32_16x16x32_bf16 v[184:187], v[156:159], v[68:71], v[184:187]\n"
"v_mfma_f32_16x16x32_bf16 v[184:187], v[160:163], v[72:75], v[184:187]\n"
"v_mfma_f32_16x16x32_bf16 v[184:187], v[164:167], v[76:79], v[184:187]\n"
"s_nop 15\n"
"s_add_u32 s70, s70, 0x1000\n"
"buffer_load_dwordx4 v[152:155], v232, s[44:47], s70 offen offset:0\n"
"buffer_load_dwordx4 v[156:159], v232, s[44:47], s70 offen offset:64\n"
"buffer_load_dwordx4 v[160:163], v232, s[44:47], s70 offen offset:128\n"
"buffer_load_dwordx4 v[164:167], v232, s[44:47], s70 offen offset:192\n"
"s_waitcnt vmcnt(0) lgkmcnt(0)\n"
"v_mfma_f32_16x16x32_bf16 v[172:175], v[152:155], v[32:35], v[204:207]\n"
"v_mfma_f32_16x16x32_bf16 v[172:175], v[156:159], v[36:39], v[172:175]\n"
"v_mfma_f32_16x16x32_bf16 v[172:175], v[160:163], v[40:43], v[172:175]\n"
"v_mfma_f32_16x16x32_bf16 v[172:175], v[164:167], v[44:47], v[172:175]\n"
"v_mfma_f32_16x16x32_bf16 v[180:183], v[152:155], v[48:51], v[208:211]\n"
"v_mfma_f32_16x16x32_bf16 v[180:183], v[156:159], v[52:55], v[180:183]\n"
"v_mfma_f32_16x16x32_bf16 v[180:183], v[160:163], v[56:59], v[180:183]\n"
"v_mfma_f32_16x16x32_bf16 v[180:183], v[164:167], v[60:63], v[180:183]\n"
"v_mfma_f32_16x16x32_bf16 v[188:191], v[152:155], v[64:67], v[212:215]\n"
"v_mfma_f32_16x16x32_bf16 v[188:191], v[156:159], v[68:71], v[188:191]\n"
"v_mfma_f32_16x16x32_bf16 v[188:191], v[160:163], v[72:75], v[188:191]\n"
"v_mfma_f32_16x16x32_bf16 v[188:191], v[164:167], v[76:79], v[188:191]\n"
"s_nop 15\n"
"s_add_u32 s70, s70, 0x1000\n"
"v_exp_f32 v168, v168\n"
"v_exp_f32 v169, v169\n"
"v_exp_f32 v170, v170\n"
"v_exp_f32 v171, v171\n"
"v_exp_f32 v172, v172\n"
"v_exp_f32 v173, v173\n"
"v_exp_f32 v174, v174\n"
"v_exp_f32 v175, v175\n"
"v_exp_f32 v176, v176\n"
"v_exp_f32 v177, v177\n"
"v_exp_f32 v178, v178\n"
"v_exp_f32 v179, v179\n"
"v_exp_f32 v180, v180\n"
"v_exp_f32 v181, v181\n"
"v_exp_f32 v182, v182\n"
"v_exp_f32 v183, v183\n"
"v_exp_f32 v184, v184\n"
"v_exp_f32 v185, v185\n"
"v_exp_f32 v186, v186\n"
"v_exp_f32 v187, v187\n"
"v_exp_f32 v188, v188\n"
"v_exp_f32 v189, v189\n"
"v_exp_f32 v190, v190\n"
"v_exp_f32 v191, v191\n"
"s_nop 1\n"
"v_cvt_pk_bf16_f32 v192, v168, v169\n"
"v_cvt_pk_bf16_f32 v193, v170, v171\n"
"v_cvt_pk_bf16_f32 v194, v172, v173\n"
"v_cvt_pk_bf16_f32 v195, v174, v175\n"
"v_cvt_pk_bf16_f32 v196, v176, v177\n"
"v_cvt_pk_bf16_f32 v197, v178, v179\n"
"v_cvt_pk_bf16_f32 v198, v180, v181\n"
"v_cvt_pk_bf16_f32 v199, v182, v183\n"
"v_cvt_pk_bf16_f32 v200, v184, v185\n"
"v_cvt_pk_bf16_f32 v201, v186, v187\n"
"v_cvt_pk_bf16_f32 v202, v188, v189\n"
"v_cvt_pk_bf16_f32 v203, v190, v191\n"
"s_nop 3\n"
"v_mfma_f32_16x16x32_bf16 v[220:223], v[240:243], v[192:195], v[220:223]\n"
"v_mfma_f32_16x16x32_bf16 v[224:227], v[240:243], v[196:199], v[224:227]\n"
"v_mfma_f32_16x16x32_bf16 v[228:231], v[240:243], v[200:203], v[228:231]\n"
"s_xor_b32 s69, s11, 0\n"
"s_lshl_b32 s69, s69, 6\n"
"s_add_u32 s69, s69, s71\n"
"buffer_load_ushort v152, v233, s[48:51], s69 offen offset:0\n"
"buffer_load_ushort v153, v233, s[48:51], s69 offen offset:256\n"
"buffer_load_ushort v154, v233, s[48:51], s69 offen offset:512\n"
"buffer_load_ushort v155, v233, s[48:51], s69 offen offset:768\n"
"s_waitcnt vmcnt(0) lgkmcnt(0)\n"
"v_lshl_or_b32 v216, v153, 16, v152\n"
"v_lshl_or_b32 v217, v155, 16, v154\n"
"s_xor_b32 s69, s69, 0x1000\n"
"buffer_load_ushort v152, v233, s[48:51], s69 offen offset:0\n"
"buffer_load_ushort v153, v233, s[48:51], s69 offen offset:256\n"
"buffer_load_ushort v154, v233, s[48:51], s69 offen offset:512\n"
"buffer_load_ushort v155, v233, s[48:51], s69 offen offset:768\n"
"s_waitcnt vmcnt(0) lgkmcnt(0)\n"
"v_lshl_or_b32 v218, v153, 16, v152\n"
"v_lshl_or_b32 v219, v155, 16, v154\n"
"s_xor_b32 s69, s69, 0x1000\n"
"s_nop 3\n"
"v_mfma_f32_16x16x32_bf16 v[80:83], v[216:219], v[192:195], v[80:83]\n"
"v_mfma_f32_16x16x32_bf16 v[112:115], v[216:219], v[196:199], v[112:115]\n"
"v_mfma_f32_16x16x32_bf16 v[144:147], v[216:219], v[200:203], v[144:147]\n"
"s_nop 15\n"
"buffer_load_ushort v152, v233, s[48:51], s69 offen offset:32\n"
"buffer_load_ushort v153, v233, s[48:51], s69 offen offset:288\n"
"buffer_load_ushort v154, v233, s[48:51], s69 offen offset:544\n"
"buffer_load_ushort v155, v233, s[48:51], s69 offen offset:800\n"
"s_waitcnt vmcnt(0) lgkmcnt(0)\n"
"v_lshl_or_b32 v216, v153, 16, v152\n"
"v_lshl_or_b32 v217, v155, 16, v154\n"
"s_xor_b32 s69, s69, 0x1000\n"
"buffer_load_ushort v152, v233, s[48:51], s69 offen offset:32\n"
"buffer_load_ushort v153, v233, s[48:51], s69 offen offset:288\n"
"buffer_load_ushort v154, v233, s[48:51], s69 offen offset:544\n"
"buffer_load_ushort v155, v233, s[48:51], s69 offen offset:800\n"
"s_waitcnt vmcnt(0) lgkmcnt(0)\n"
"v_lshl_or_b32 v218, v153, 16, v152\n"
"v_lshl_or_b32 v219, v155, 16, v154\n"
"s_xor_b32 s69, s69, 0x1000\n"
"s_nop 3\n"
"v_mfma_f32_16x16x32_bf16 v[84:87], v[216:219], v[192:195], v[84:87]\n"
"v_mfma_f32_16x16x32_bf16 v[116:119], v[216:219], v[196:199], v[116:119]\n"
"v_mfma_f32_16x16x32_bf16 v[148:151], v[216:219], v[200:203], v[148:151]\n"
"s_nop 15\n"
"s_xor_b32 s69, s11, 1\n"
"s_lshl_b32 s69, s69, 6\n"
"s_add_u32 s69, s69, s71\n"
"buffer_load_ushort v152, v233, s[48:51], s69 offen offset:0\n"
"buffer_load_ushort v153, v233, s[48:51], s69 offen offset:256\n"
"buffer_load_ushort v154, v233, s[48:51], s69 offen offset:512\n"
"buffer_load_ushort v155, v233, s[48:51], s69 offen offset:768\n"
"s_waitcnt vmcnt(0) lgkmcnt(0)\n"
"v_lshl_or_b32 v216, v153, 16, v152\n"
"v_lshl_or_b32 v217, v155, 16, v154\n"
"s_xor_b32 s69, s69, 0x1000\n"
"buffer_load_ushort v152, v233, s[48:51], s69 offen offset:0\n"
"buffer_load_ushort v153, v233, s[48:51], s69 offen offset:256\n"
"buffer_load_ushort v154, v233, s[48:51], s69 offen offset:512\n"
"buffer_load_ushort v155, v233, s[48:51], s69 offen offset:768\n"
"s_waitcnt vmcnt(0) lgkmcnt(0)\n"
"v_lshl_or_b32 v218, v153, 16, v152\n"
"v_lshl_or_b32 v219, v155, 16, v154\n"
"s_xor_b32 s69, s69, 0x1000\n"
"s_nop 3\n"
"v_mfma_f32_16x16x32_bf16 v[88:91], v[216:219], v[192:195], v[88:91]\n"
"v_mfma_f32_16x16x32_bf16 v[120:123], v[216:219], v[196:199], v[120:123]\n"
"s_nop 15\n"
"buffer_load_ushort v152, v233, s[48:51], s69 offen offset:32\n"
"buffer_load_ushort v153, v233, s[48:51], s69 offen offset:288\n"
"buffer_load_ushort v154, v233, s[48:51], s69 offen offset:544\n"
"buffer_load_ushort v155, v233, s[48:51], s69 offen offset:800\n"
"s_waitcnt vmcnt(0) lgkmcnt(0)\n"
"v_lshl_or_b32 v216, v153, 16, v152\n"
"v_lshl_or_b32 v217, v155, 16, v154\n"
"s_xor_b32 s69, s69, 0x1000\n"
"buffer_load_ushort v152, v233, s[48:51], s69 offen offset:32\n"
"buffer_load_ushort v153, v233, s[48:51], s69 offen offset:288\n"
"buffer_load_ushort v154, v233, s[48:51], s69 offen offset:544\n"
"buffer_load_ushort v155, v233, s[48:51], s69 offen offset:800\n"
"s_waitcnt vmcnt(0) lgkmcnt(0)\n"
"v_lshl_or_b32 v218, v153, 16, v152\n"
"v_lshl_or_b32 v219, v155, 16, v154\n"
"s_xor_b32 s69, s69, 0x1000\n"
"s_nop 3\n"
"v_mfma_f32_16x16x32_bf16 v[92:95], v[216:219], v[192:195], v[92:95]\n"
"v_mfma_f32_16x16x32_bf16 v[124:127], v[216:219], v[196:199], v[124:127]\n"
"s_nop 15\n"
"s_xor_b32 s69, s11, 2\n"
"s_lshl_b32 s69, s69, 6\n"
"s_add_u32 s69, s69, s71\n"
"buffer_load_ushort v152, v233, s[48:51], s69 offen offset:0\n"
"buffer_load_ushort v153, v233, s[48:51], s69 offen offset:256\n"
"buffer_load_ushort v154, v233, s[48:51], s69 offen offset:512\n"
"buffer_load_ushort v155, v233, s[48:51], s69 offen offset:768\n"
"s_waitcnt vmcnt(0) lgkmcnt(0)\n"
"v_lshl_or_b32 v216, v153, 16, v152\n"
"v_lshl_or_b32 v217, v155, 16, v154\n"
"s_xor_b32 s69, s69, 0x1000\n"
"buffer_load_ushort v152, v233, s[48:51], s69 offen offset:0\n"
"buffer_load_ushort v153, v233, s[48:51], s69 offen offset:256\n"
"buffer_load_ushort v154, v233, s[48:51], s69 offen offset:512\n"
"buffer_load_ushort v155, v233, s[48:51], s69 offen offset:768\n"
"s_waitcnt vmcnt(0) lgkmcnt(0)\n"
"v_lshl_or_b32 v218, v153, 16, v152\n"
"v_lshl_or_b32 v219, v155, 16, v154\n"
"s_xor_b32 s69, s69, 0x1000\n"
"s_nop 3\n"
"v_mfma_f32_16x16x32_bf16 v[96:99], v[216:219], v[192:195], v[96:99]\n"
"v_mfma_f32_16x16x32_bf16 v[128:131], v[216:219], v[196:199], v[128:131]\n"
"s_nop 15\n"
"buffer_load_ushort v152, v233, s[48:51], s69 offen offset:32\n"
"buffer_load_ushort v153, v233, s[48:51], s69 offen offset:288\n"
"buffer_load_ushort v154, v233, s[48:51], s69 offen offset:544\n"
"buffer_load_ushort v155, v233, s[48:51], s69 offen offset:800\n"
"s_waitcnt vmcnt(0) lgkmcnt(0)\n"
"v_lshl_or_b32 v216, v153, 16, v152\n"
"v_lshl_or_b32 v217, v155, 16, v154\n"
"s_xor_b32 s69, s69, 0x1000\n"
"buffer_load_ushort v152, v233, s[48:51], s69 offen offset:32\n"
"buffer_load_ushort v153, v233, s[48:51], s69 offen offset:288\n"
"buffer_load_ushort v154, v233, s[48:51], s69 offen offset:544\n"
"buffer_load_ushort v155, v233, s[48:51], s69 offen offset:800\n"
"s_waitcnt vmcnt(0) lgkmcnt(0)\n"
"v_lshl_or_b32 v218, v153, 16, v152\n"
"v_lshl_or_b32 v219, v155, 16, v154\n"
"s_xor_b32 s69, s69, 0x1000\n"
"s_nop 3\n"
"v_mfma_f32_16x16x32_bf16 v[100:103], v[216:219], v[192:195], v[100:103]\n"
"v_mfma_f32_16x16x32_bf16 v[132:135], v[216:219], v[196:199], v[132:135]\n"
"s_nop 15\n"
"s_xor_b32 s69, s11, 3\n"
"s_lshl_b32 s69, s69, 6\n"
"s_add_u32 s69, s69, s71\n"
"buffer_load_ushort v152, v233, s[48:51], s69 offen offset:0\n"
"buffer_load_ushort v153, v233, s[48:51], s69 offen offset:256\n"
"buffer_load_ushort v154, v233, s[48:51], s69 offen offset:512\n"
"buffer_load_ushort v155, v233, s[48:51], s69 offen offset:768\n"
"s_waitcnt vmcnt(0) lgkmcnt(0)\n"
"v_lshl_or_b32 v216, v153, 16, v152\n"
"v_lshl_or_b32 v217, v155, 16, v154\n"
"s_xor_b32 s69, s69, 0x1000\n"
"buffer_load_ushort v152, v233, s[48:51], s69 offen offset:0\n"
"buffer_load_ushort v153, v233, s[48:51], s69 offen offset:256\n"
"buffer_load_ushort v154, v233, s[48:51], s69 offen offset:512\n"
"buffer_load_ushort v155, v233, s[48:51], s69 offen offset:768\n"
"s_waitcnt vmcnt(0) lgkmcnt(0)\n"
"v_lshl_or_b32 v218, v153, 16, v152\n"
"v_lshl_or_b32 v219, v155, 16, v154\n"
"s_xor_b32 s69, s69, 0x1000\n"
"s_nop 3\n"
"v_mfma_f32_16x16x32_bf16 v[104:107], v[216:219], v[192:195], v[104:107]\n"
"v_mfma_f32_16x16x32_bf16 v[136:139], v[216:219], v[196:199], v[136:139]\n"
"s_nop 15\n"
"buffer_load_ushort v152, v233, s[48:51], s69 offen offset:32\n"
"buffer_load_ushort v153, v233, s[48:51], s69 offen offset:288\n"
"buffer_load_ushort v154, v233, s[48:51], s69 offen offset:544\n"
"buffer_load_ushort v155, v233, s[48:51], s69 offen offset:800\n"
"s_waitcnt vmcnt(0) lgkmcnt(0)\n"
"v_lshl_or_b32 v216, v153, 16, v152\n"
"v_lshl_or_b32 v217, v155, 16, v154\n"
"s_xor_b32 s69, s69, 0x1000\n"
"buffer_load_ushort v152, v233, s[48:51], s69 offen offset:32\n"
"buffer_load_ushort v153, v233, s[48:51], s69 offen offset:288\n"
"buffer_load_ushort v154, v233, s[48:51], s69 offen offset:544\n"
"buffer_load_ushort v155, v233, s[48:51], s69 offen offset:800\n"
"s_waitcnt vmcnt(0) lgkmcnt(0)\n"
"v_lshl_or_b32 v218, v153, 16, v152\n"
"v_lshl_or_b32 v219, v155, 16, v154\n"
"s_xor_b32 s69, s69, 0x1000\n"
"s_nop 3\n"
"v_mfma_f32_16x16x32_bf16 v[108:111], v[216:219], v[192:195], v[108:111]\n"
"v_mfma_f32_16x16x32_bf16 v[140:143], v[216:219], v[196:199], v[140:143]\n"
"s_nop 15\n"
"s_cmp_lg_u32 s70, 0x20000\n"
"s_cbranch_scc1 .Lmha_SL2_0\n"
"v_mov_b32 v252, v220\n"
"v_mov_b32 v253, v224\n"
"v_mov_b32 v255, v228\n"
"s_nop 1\n"
"s_add_u32 s69, s68, 0\n"
"buffer_load_dwordx4 v[152:155], v22, s[64:67], s69 offen offset:0 sc1\n"
"s_add_u32 s69, s68, 0x400\n"
"buffer_load_dwordx4 v[156:159], v22, s[64:67], s69 offen offset:0 sc1\n"
"s_add_u32 s69, s68, 0x800\n"
"buffer_load_dwordx4 v[160:163], v22, s[64:67], s69 offen offset:0 sc1\n"
"s_add_u32 s69, s68, 0xc00\n"
"buffer_load_dwordx4 v[164:167], v22, s[64:67], s69 offen offset:0 sc1\n"
"s_add_u32 s69, s68, 0x1000\n"
"buffer_load_dwordx4 v[168:171], v22, s[64:67], s69 offen offset:0 sc1\n"
"s_add_u32 s69, s68, 0x1400\n"
"buffer_load_dwordx4 v[172:175], v22, s[64:67], s69 offen offset:0 sc1\n"
"s_add_u32 s69, s68, 0x1800\n"
"buffer_load_dwordx4 v[176:179], v22, s[64:67], s69 offen offset:0 sc1\n"
"s_add_u32 s69, s68, 0x1c00\n"
"buffer_load_dwordx4 v[180:183], v22, s[64:67], s69 offen offset:0 sc1\n"
"s_add_u32 s69, s68, 0x2000\n"
"buffer_load_dwordx4 v[184:187], v22, s[64:67], s69 offen offset:0 sc1\n"
"s_add_u32 s69, s68, 0x2400\n"
"buffer_load_dwordx4 v[188:191], v22, s[64:67], s69 offen offset:0 sc1\n"
"s_add_u32 s69, s68, 0x2800\n"
"buffer_load_dwordx4 v[192:195], v22, s[64:67], s69 offen offset:0 sc1\n"
"s_add_u32 s69, s68, 0x2c00\n"
"buffer_load_dwordx4 v[196:199], v22, s[64:67], s69 offen offset:0 sc1\n"
"s_add_u32 s69, s68, 0x3000\n"
"buffer_load_dwordx4 v[200:203], v22, s[64:67], s69 offen offset:0 sc1\n"
"s_add_u32 s69, s68, 0x3400\n"
"buffer_load_dwordx4 v[204:207], v22, s[64:67], s69 offen offset:0 sc1\n"
"s_add_u32 s69, s68, 0x3800\n"
"buffer_load_dwordx4 v[208:211], v22, s[64:67], s69 offen offset:0 sc1\n"
"s_add_u32 s69, s68, 0x3c00\n"
"buffer_load_dwordx4 v[212:215], v22, s[64:67], s69 offen offset:0 sc1\n"
"s_add_u32 s69, s68, 0x4000\n"
"buffer_load_dwordx4 v[216:219], v22, s[64:67], s69 offen offset:0 sc1\n"
"s_add_u32 s69, s68, 0x4400\n"
"buffer_load_dwordx4 v[220:223], v22, s[64:67], s69 offen offset:0 sc1\n"
"s_add_u32 s69, s68, 0x4800\n"
"buffer_load_dwordx4 v[224:227], v22, s[64:67], s69 offen offset:0 sc1\n"
"s_add_u32 s69, s68, 0x4c00\n"
"buffer_load_dwordx4 v[228:231], v22, s[64:67], s69 offen offset:0 sc1\n"
"s_add_u32 s69, s68, 0x5000\n"
"buffer_load_dwordx4 v[232:235], v22, s[64:67], s69 offen offset:0 sc1\n"
"s_add_u32 s69, s68, 0x5400\n"
"buffer_load_dwordx4 v[236:239], v22, s[64:67], s69 offen offset:0 sc1\n"
"s_add_u32 s69, s68, 0x5800\n"
"buffer_load_dwordx4 v[240:243], v22, s[64:67], s69 offen offset:0 sc1\n"
"s_waitcnt vmcnt(0) lgkmcnt(0)\n"
"s_nop 7\n"
"s_branch .Lmha_SLOWRET0\n"
".Lmha_SLOW1:\n"
"s_waitcnt vmcnt(0) lgkmcnt(0)\n"
"s_nop 15\n"
"s_lshl_b32 s68, s10, 2\n"
"s_add_u32 s68, s68, s11\n"
"s_mul_i32 s68, s68, 0x8000\n"
"s_add_u32 s68, s68, 0x940000\n"
"s_add_u32 s69, s68, 0\n"
"buffer_store_dwordx4 v[152:155], v22, s[64:67], s69 offen offset:0\n"
"s_add_u32 s69, s68, 0x400\n"
"buffer_store_dwordx4 v[156:159], v22, s[64:67], s69 offen offset:0\n"
"s_add_u32 s69, s68, 0x800\n"
"buffer_store_dwordx4 v[160:163], v22, s[64:67], s69 offen offset:0\n"
"s_add_u32 s69, s68, 0xc00\n"
"buffer_store_dwordx4 v[164:167], v22, s[64:67], s69 offen offset:0\n"
"s_add_u32 s69, s68, 0x1000\n"
"buffer_store_dwordx4 v[168:171], v22, s[64:67], s69 offen offset:0\n"
"s_add_u32 s69, s68, 0x1400\n"
"buffer_store_dwordx4 v[172:175], v22, s[64:67], s69 offen offset:0\n"
"s_add_u32 s69, s68, 0x1800\n"
"buffer_store_dwordx4 v[176:179], v22, s[64:67], s69 offen offset:0\n"
"s_add_u32 s69, s68, 0x1c00\n"
"buffer_store_dwordx4 v[180:183], v22, s[64:67], s69 offen offset:0\n"
"s_add_u32 s69, s68, 0x2000\n"
"buffer_store_dwordx4 v[184:187], v22, s[64:67], s69 offen offset:0\n"
"s_add_u32 s69, s68, 0x2400\n"
"buffer_store_dwordx4 v[188:191], v22, s[64:67], s69 offen offset:0\n"
"s_add_u32 s69, s68, 0x2800\n"
"buffer_store_dwordx4 v[192:195], v22, s[64:67], s69 offen offset:0\n"
"s_add_u32 s69, s68, 0x2c00\n"
"buffer_store_dwordx4 v[196:199], v22, s[64:67], s69 offen offset:0\n"
"s_add_u32 s69, s68, 0x3000\n"
"buffer_store_dwordx4 v[200:203], v22, s[64:67], s69 offen offset:0\n"
"s_add_u32 s69, s68, 0x3400\n"
"buffer_store_dwordx4 v[204:207], v22, s[64:67], s69 offen offset:0\n"
"s_add_u32 s69, s68, 0x3800\n"
"buffer_store_dwordx4 v[208:211], v22, s[64:67], s69 offen offset:0\n"
"s_add_u32 s69, s68, 0x3c00\n"
"buffer_store_dwordx4 v[212:215], v22, s[64:67], s69 offen offset:0\n"
"s_add_u32 s69, s68, 0x4000\n"
"buffer_store_dwordx4 v[216:219], v22, s[64:67], s69 offen offset:0\n"
"s_add_u32 s69, s68, 0x4400\n"
"buffer_store_dwordx4 v[220:223], v22, s[64:67], s69 offen offset:0\n"
"s_add_u32 s69, s68, 0x4800\n"
"buffer_store_dwordx4 v[224:227], v22, s[64:67], s69 offen offset:0\n"
"s_add_u32 s69, s68, 0x4c00\n"
"buffer_store_dwordx4 v[228:231], v22, s[64:67], s69 offen offset:0\n"
"s_add_u32 s69, s68, 0x5000\n"
"buffer_store_dwordx4 v[232:235], v22, s[64:67], s69 offen offset:0\n"
"s_add_u32 s69, s68, 0x5400\n"
"buffer_store_dwordx4 v[236:239], v22, s[64:67], s69 offen offset:0\n"
"s_add_u32 s69, s68, 0x5800\n"
"buffer_store_dwordx4 v[240:243], v22, s[64:67], s69 offen offset:0\n"
"s_waitcnt vmcnt(0) lgkmcnt(0)\n"
"s_nop 1\n"
"v_mov_b32 v240, 0x3f803f80\n"
"v_mov_b32 v241, 0x3f803f80\n"
"v_mov_b32 v242, 0x3f803f80\n"
"v_mov_b32 v243, 0x3f803f80\n"
"v_lshrrev_b32 v234, 4, v22\n"
"v_and_b32 v235, 15, v234\n"
"v_lshrrev_b32 v234, 4, v234\n"
"v_lshlrev_b32 v232, 8, v235\n"
"v_lshl_add_u32 v232, v234, 4, v232\n"
"v_lshlrev_b32 v233, 10, v234\n"
"v_lshl_add_u32 v233, v235, 1, v233\n"
"v_mov_b32 v236, 0xff800000\n"
"v_mov_b32 v237, 0xff800000\n"
"v_mov_b32 v238, 0xff800000\n"
"s_mov_b32 s70, 0x20000\n"
".Lmha_SL1_1:\n"
"buffer_load_dwordx4 v[152:155], v232, s[44:47], s70 offen offset:0\n"
"buffer_load_dwordx4 v[156:159], v232, s[44:47], s70 offen offset:64\n"
"buffer_load_dwordx4 v[160:163], v232, s[44:47], s70 offen offset:128\n"
"buffer_load_dwordx4 v[164:167], v232, s[44:47], s70 offen offset:192\n"
"s_waitcnt vmcnt(0) lgkmcnt(0)\n"
"v_mfma_f32_16x16x32_bf16 v[168:171], v[152:155], a[64:67], 0\n"
"v_mfma_f32_16x16x32_bf16 v[168:171], v[156:159], a[68:71], v[168:171]\n"
"v_mfma_f32_16x16x32_bf16 v[168:171], v[160:163], a[72:75], v[168:171]\n"
"v_mfma_f32_16x16x32_bf16 v[168:171], v[164:167], a[76:79], v[168:171]\n"
"v_mfma_f32_16x16x32_bf16 v[176:179], v[152:155], a[80:83], 0\n"
"v_mfma_f32_16x16x32_bf16 v[176:179], v[156:159], a[84:87], v[176:179]\n"
"v_mfma_f32_16x16x32_bf16 v[176:179], v[160:163], a[88:91], v[176:179]\n"
"v_mfma_f32_16x16x32_bf16 v[176:179], v[164:167], a[92:95], v[176:179]\n"
"v_mfma_f32_16x16x32_bf16 v[184:187], v[152:155], a[96:99], 0\n"
"v_mfma_f32_16x16x32_bf16 v[184:187], v[156:159], a[100:103], v[184:187]\n"
"v_mfma_f32_16x16x32_bf16 v[184:187], v[160:163], a[104:107], v[184:187]\n"
"v_mfma_f32_16x16x32_bf16 v[184:187], v[164:167], a[108:111], v[184:187]\n"
"s_nop 15\n"
"v_max3_f32 v236, v236, v168, v169\n"
"v_max3_f32 v236, v236, v170, v171\n"
"v_max3_f32 v237, v237, v176, v177\n"
"v_max3_f32 v237, v237, v178, v179\n"
"v_max3_f32 v238, v238, v184, v185\n"
"v_max3_f32 v238, v238, v186, v187\n"
"s_add_u32 s70, s70, 0x1000\n"
"s_cmp_lg_u32 s70, 0x40000\n"
"s_cbranch_scc1 .Lmha_SL1_1\n"
"v_mov_b32 v235, v236\n"
"s_nop 1\n"
"v_permlane32_swap_b32 v236, v235\n"
"s_nop 1\n"
"v_max_f32 v236, v236, v235\n"
"v_mov_b32 v235, v236\n"
"s_nop 1\n"
"v_permlane16_swap_b32 v236, v235\n"
"s_nop 1\n"
"v_max_f32 v236, v236, v235\n"
"v_mul_f32 v204, -1.0, v236\n"
"v_mul_f32 v205, -1.0, v236\n"
"v_mul_f32 v206, -1.0, v236\n"
"v_mul_f32 v207, -1.0, v236\n"
"v_mov_b32 v235, v237\n"
"s_nop 1\n"
"v_permlane32_swap_b32 v237, v235\n"
"s_nop 1\n"
"v_max_f32 v237, v237, v235\n"
"v_mov_b32 v235, v237\n"
"s_nop 1\n"
"v_permlane16_swap_b32 v237, v235\n"
"s_nop 1\n"
"v_max_f32 v237, v237, v235\n"
"v_mul_f32 v208, -1.0, v237\n"
"v_mul_f32 v209, -1.0, v237\n"
"v_mul_f32 v210, -1.0, v237\n"
"v_mul_f32 v211, -1.0, v237\n"
"v_mov_b32 v235, v238\n"
"s_nop 1\n"
"v_permlane32_swap_b32 v238, v235\n"
"s_nop 1\n"
"v_max_f32 v238, v238, v235\n"
"v_mov_b32 v235, v238\n"
"s_nop 1\n"
"v_permlane16_swap_b32 v238, v235\n"
"s_nop 1\n"
"v_max_f32 v238, v238, v235\n"
"v_mul_f32 v212, -1.0, v238\n"
"v_mul_f32 v213, -1.0, v238\n"
"v_mul_f32 v214, -1.0, v238\n"
"v_mul_f32 v215, -1.0, v238\n"
"v_mov_b32 v220, 0\n"
"v_mov_b32 v221, 0\n"
"v_mov_b32 v222, 0\n"
"v_mov_b32 v223, 0\n"
"v_mov_b32 v224, 0\n"
"v_mov_b32 v225, 0\n"
"v_mov_b32 v226, 0\n"
"v_mov_b32 v227, 0\n"
"v_mov_b32 v228, 0\n"
"v_mov_b32 v229, 0\n"
"v_mov_b32 v230, 0\n"
"v_mov_b32 v231, 0\n"
"v_accvgpr_write_b32 a128, 0\n"
"v_accvgpr_write_b32 a129, 0\n"
"v_accvgpr_write_b32 a130, 0\n"
"v_accvgpr_write_b32 a131, 0\n"
"v_accvgpr_write_b32 a132, 0\n"
"v_accvgpr_write_b32 a133, 0\n"
"v_accvgpr_write_b32 a134, 0\n"
"v_accvgpr_write_b32 a135, 0\n"
"v_accvgpr_write_b32 a136, 0\n"
"v_accvgpr_write_b32 a137, 0\n"
"v_accvgpr_write_b32 a138, 0\n"
"v_accvgpr_write_b32 a139, 0\n"
"v_accvgpr_write_b32 a140, 0\n"
"v_accvgpr_write_b32 a141, 0\n"
"v_accvgpr_write_b32 a142, 0\n"
"v_accvgpr_write_b32 a143, 0\n"
"v_accvgpr_write_b32 a144, 0\n"
"v_accvgpr_write_b32 a145, 0\n"
"v_accvgpr_write_b32 a146, 0\n"
"v_accvgpr_write_b32 a147, 0\n"
"v_accvgpr_write_b32 a148, 0\n"
"v_accvgpr_write_b32 a149, 0\n"
"v_accvgpr_write_b32 a150, 0\n"
"v_accvgpr_write_b32 a151, 0\n"
"v_accvgpr_write_b32 a152, 0\n"
"v_accvgpr_write_b32 a153, 0\n"
"v_accvgpr_write_b32 a154, 0\n"
"v_accvgpr_write_b32 a155, 0\n"
"v_accvgpr_write_b32 a156, 0\n"
"v_accvgpr_write_b32 a157, 0\n"
"v_accvgpr_write_b32 a158, 0\n"
"v_accvgpr_write_b32 a159, 0\n"
"v_accvgpr_write_b32 a160, 0\n"
"v_accvgpr_write_b32 a161, 0\n"
"v_accvgpr_write_b32 a162, 0\n"
"v_accvgpr_write_b32 a163, 0\n"
"v_accvgpr_write_b32 a164, 0\n"
"v_accvgpr_write_b32 a165, 0\n"
"v_accvgpr_write_b32 a166, 0\n"
"v_accvgpr_write_b32 a167, 0\n"
"v_accvgpr_write_b32 a168, 0\n"
"v_accvgpr_write_b32 a169, 0\n"
"v_accvgpr_write_b32 a170, 0\n"
"v_accvgpr_write_b32 a171, 0\n"
"v_accvgpr_write_b32 a172, 0\n"
"v_accvgpr_write_b32 a173, 0\n"
"v_accvgpr_write_b32 a174, 0\n"
"v_accvgpr_write_b32 a175, 0\n"
"v_accvgpr_write_b32 a176, 0\n"
"v_accvgpr_write_b32 a177, 0\n"
"v_accvgpr_write_b32 a178, 0\n"
"v_accvgpr_write_b32 a179, 0\n"
"v_accvgpr_write_b32 a180, 0\n"
"v_accvgpr_write_b32 a181, 0\n"
"v_accvgpr_write_b32 a182, 0\n"
"v_accvgpr_write_b32 a183, 0\n"
"v_accvgpr_write_b32 a184, 0\n"
"v_accvgpr_write_b32 a185, 0\n"
"v_accvgpr_write_b32 a186, 0\n"
"v_accvgpr_write_b32 a187, 0\n"
"v_accvgpr_write_b32 a188, 0\n"
"v_accvgpr_write_b32 a189, 0\n"
"v_accvgpr_write_b32 a190, 0\n"
"v_accvgpr_write_b32 a191, 0\n"
"v_accvgpr_write_b32 a192, 0\n"
"v_accvgpr_write_b32 a193, 0\n"
"v_accvgpr_write_b32 a194, 0\n"
"v_accvgpr_write_b32 a195, 0\n"
"v_accvgpr_write_b32 a196, 0\n"
"v_accvgpr_write_b32 a197, 0\n"
"v_accvgpr_write_b32 a198, 0\n"
"v_accvgpr_write_b32 a199, 0\n"
"s_nop 3\n"
"s_mov_b32 s70, 0x20000\n"
".Lmha_SL2_1:\n"
"s_mov_b32 s71, s70\n"
"buffer_load_dwordx4 v[152:155], v232, s[44:47], s70 offen offset:0\n"
"buffer_load_dwordx4 v[156:159], v232, s[44:47], s70 offen offset:64\n"
"buffer_load_dwordx4 v[160:163], v232, s[44:47], s70 offen offset:128\n"
"buffer_load_dwordx4 v[164:167], v232, s[44:47], s70 offen offset:192\n"
"s_waitcnt vmcnt(0) lgkmcnt(0)\n"
"v_mfma_f32_16x16x32_bf16 v[168:171], v[152:155], a[64:67], v[204:207]\n"
"v_mfma_f32_16x16x32_bf16 v[168:171], v[156:159], a[68:71], v[168:171]\n"
"v_mfma_f32_16x16x32_bf16 v[168:171], v[160:163], a[72:75], v[168:171]\n"
"v_mfma_f32_16x16x32_bf16 v[168:171], v[164:167], a[76:79], v[168:171]\n"
"v_mfma_f32_16x16x32_bf16 v[176:179], v[152:155], a[80:83], v[208:211]\n"
"v_mfma_f32_16x16x32_bf16 v[176:179], v[156:159], a[84:87], v[176:179]\n"
"v_mfma_f32_16x16x32_bf16 v[176:179], v[160:163], a[88:91], v[176:179]\n"
"v_mfma_f32_16x16x32_bf16 v[176:179], v[164:167], a[92:95], v[176:179]\n"
"v_mfma_f32_16x16x32_bf16 v[184:187], v[152:155], a[96:99], v[212:215]\n"
"v_mfma_f32_16x16x32_bf16 v[184:187], v[156:159], a[100:103], v[184:187]\n"
"v_mfma_f32_16x16x32_bf16 v[184:187], v[160:163], a[104:107], v[184:187]\n"
"v_mfma_f32_16x16x32_bf16 v[184:187], v[164:167], a[108:111], v[184:187]\n"
"s_nop 15\n"
"s_add_u32 s70, s70, 0x1000\n"
"buffer_load_dwordx4 v[152:155], v232, s[44:47], s70 offen offset:0\n"
"buffer_load_dwordx4 v[156:159], v232, s[44:47], s70 offen offset:64\n"
"buffer_load_dwordx4 v[160:163], v232, s[44:47], s70 offen offset:128\n"
"buffer_load_dwordx4 v[164:167], v232, s[44:47], s70 offen offset:192\n"
"s_waitcnt vmcnt(0) lgkmcnt(0)\n"
"v_mfma_f32_16x16x32_bf16 v[172:175], v[152:155], a[64:67], v[204:207]\n"
"v_mfma_f32_16x16x32_bf16 v[172:175], v[156:159], a[68:71], v[172:175]\n"
"v_mfma_f32_16x16x32_bf16 v[172:175], v[160:163], a[72:75], v[172:175]\n"
"v_mfma_f32_16x16x32_bf16 v[172:175], v[164:167], a[76:79], v[172:175]\n"
"v_mfma_f32_16x16x32_bf16 v[180:183], v[152:155], a[80:83], v[208:211]\n"
"v_mfma_f32_16x16x32_bf16 v[180:183], v[156:159], a[84:87], v[180:183]\n"
"v_mfma_f32_16x16x32_bf16 v[180:183], v[160:163], a[88:91], v[180:183]\n"
"v_mfma_f32_16x16x32_bf16 v[180:183], v[164:167], a[92:95], v[180:183]\n"
"v_mfma_f32_16x16x32_bf16 v[188:191], v[152:155], a[96:99], v[212:215]\n"
"v_mfma_f32_16x16x32_bf16 v[188:191], v[156:159], a[100:103], v[188:191]\n"
"v_mfma_f32_16x16x32_bf16 v[188:191], v[160:163], a[104:107], v[188:191]\n"
"v_mfma_f32_16x16x32_bf16 v[188:191], v[164:167], a[108:111], v[188:191]\n"
"s_nop 15\n"
"s_add_u32 s70, s70, 0x1000\n"
"v_exp_f32 v168, v168\n"
"v_exp_f32 v169, v169\n"
"v_exp_f32 v170, v170\n"
"v_exp_f32 v171, v171\n"
"v_exp_f32 v172, v172\n"
"v_exp_f32 v173, v173\n"
"v_exp_f32 v174, v174\n"
"v_exp_f32 v175, v175\n"
"v_exp_f32 v176, v176\n"
"v_exp_f32 v177, v177\n"
"v_exp_f32 v178, v178\n"
"v_exp_f32 v179, v179\n"
"v_exp_f32 v180, v180\n"
"v_exp_f32 v181, v181\n"
"v_exp_f32 v182, v182\n"
"v_exp_f32 v183, v183\n"
"v_exp_f32 v184, v184\n"
"v_exp_f32 v185, v185\n"
"v_exp_f32 v186, v186\n"
"v_exp_f32 v187, v187\n"
"v_exp_f32 v188, v188\n"
"v_exp_f32 v189, v189\n"
"v_exp_f32 v190, v190\n"
"v_exp_f32 v191, v191\n"
"s_nop 1\n"
"v_cvt_pk_bf16_f32 v192, v168, v169\n"
"v_cvt_pk_bf16_f32 v193, v170, v171\n"
"v_cvt_pk_bf16_f32 v194, v172, v173\n"
"v_cvt_pk_bf16_f32 v195, v174, v175\n"
"v_cvt_pk_bf16_f32 v196, v176, v177\n"
"v_cvt_pk_bf16_f32 v197, v178, v179\n"
"v_cvt_pk_bf16_f32 v198, v180, v181\n"
"v_cvt_pk_bf16_f32 v199, v182, v183\n"
"v_cvt_pk_bf16_f32 v200, v184, v185\n"
"v_cvt_pk_bf16_f32 v201, v186, v187\n"
"v_cvt_pk_bf16_f32 v202, v188, v189\n"
"v_cvt_pk_bf16_f32 v203, v190, v191\n"
"s_nop 3\n"
"v_mfma_f32_16x16x32_bf16 v[220:223], v[240:243], v[192:195], v[220:223]\n"
"v_mfma_f32_16x16x32_bf16 v[224:227], v[240:243], v[196:199], v[224:227]\n"
"v_mfma_f32_16x16x32_bf16 v[228:231], v[240:243], v[200:203], v[228:231]\n"
"s_xor_b32 s69, s11, 0\n"
"s_lshl_b32 s69, s69, 6\n"
"s_add_u32 s69, s69, s71\n"
"buffer_load_ushort v152, v233, s[48:51], s69 offen offset:0\n"
"buffer_load_ushort v153, v233, s[48:51], s69 offen offset:256\n"
"buffer_load_ushort v154, v233, s[48:51], s69 offen offset:512\n"
"buffer_load_ushort v155, v233, s[48:51], s69 offen offset:768\n"
"s_waitcnt vmcnt(0) lgkmcnt(0)\n"
"v_lshl_or_b32 v216, v153, 16, v152\n"
"v_lshl_or_b32 v217, v155, 16, v154\n"
"s_xor_b32 s69, s69, 0x1000\n"
"buffer_load_ushort v152, v233, s[48:51], s69 offen offset:0\n"
"buffer_load_ushort v153, v233, s[48:51], s69 offen offset:256\n"
"buffer_load_ushort v154, v233, s[48:51], s69 offen offset:512\n"
"buffer_load_ushort v155, v233, s[48:51], s69 offen offset:768\n"
"s_waitcnt vmcnt(0) lgkmcnt(0)\n"
"v_lshl_or_b32 v218, v153, 16, v152\n"
"v_lshl_or_b32 v219, v155, 16, v154\n"
"s_xor_b32 s69, s69, 0x1000\n"
"s_nop 3\n"
"v_mfma_f32_16x16x32_bf16 a[128:131], v[216:219], v[192:195], a[128:131]\n"
"v_mfma_f32_16x16x32_bf16 a[160:163], v[216:219], v[196:199], a[160:163]\n"
"v_mfma_f32_16x16x32_bf16 a[192:195], v[216:219], v[200:203], a[192:195]\n"
"s_nop 15\n"
"buffer_load_ushort v152, v233, s[48:51], s69 offen offset:32\n"
"buffer_load_ushort v153, v233, s[48:51], s69 offen offset:288\n"
"buffer_load_ushort v154, v233, s[48:51], s69 offen offset:544\n"
"buffer_load_ushort v155, v233, s[48:51], s69 offen offset:800\n"
"s_waitcnt vmcnt(0) lgkmcnt(0)\n"
"v_lshl_or_b32 v216, v153, 16, v152\n"
"v_lshl_or_b32 v217, v155, 16, v154\n"
"s_xor_b32 s69, s69, 0x1000\n"
"buffer_load_ushort v152, v233, s[48:51], s69 offen offset:32\n"
"buffer_load_ushort v153, v233, s[48:51], s69 offen offset:288\n"
"buffer_load_ushort v154, v233, s[48:51], s69 offen offset:544\n"
"buffer_load_ushort v155, v233, s[48:51], s69 offen offset:800\n"
"s_waitcnt vmcnt(0) lgkmcnt(0)\n"
"v_lshl_or_b32 v218, v153, 16, v152\n"
"v_lshl_or_b32 v219, v155, 16, v154\n"
"s_xor_b32 s69, s69, 0x1000\n"
"s_nop 3\n"
"v_mfma_f32_16x16x32_bf16 a[132:135], v[216:219], v[192:195], a[132:135]\n"
"v_mfma_f32_16x16x32_bf16 a[164:167], v[216:219], v[196:199], a[164:167]\n"
"v_mfma_f32_16x16x32_bf16 a[196:199], v[216:219], v[200:203], a[196:199]\n"
"s_nop 15\n"
"s_xor_b32 s69, s11, 1\n"
"s_lshl_b32 s69, s69, 6\n"
"s_add_u32 s69, s69, s71\n"
"buffer_load_ushort v152, v233, s[48:51], s69 offen offset:0\n"
"buffer_load_ushort v153, v233, s[48:51], s69 offen offset:256\n"
"buffer_load_ushort v154, v233, s[48:51], s69 offen offset:512\n"
"buffer_load_ushort v155, v233, s[48:51], s69 offen offset:768\n"
"s_waitcnt vmcnt(0) lgkmcnt(0)\n"
"v_lshl_or_b32 v216, v153, 16, v152\n"
"v_lshl_or_b32 v217, v155, 16, v154\n"
"s_xor_b32 s69, s69, 0x1000\n"
"buffer_load_ushort v152, v233, s[48:51], s69 offen offset:0\n"
"buffer_load_ushort v153, v233, s[48:51], s69 offen offset:256\n"
"buffer_load_ushort v154, v233, s[48:51], s69 offen offset:512\n"
"buffer_load_ushort v155, v233, s[48:51], s69 offen offset:768\n"
"s_waitcnt vmcnt(0) lgkmcnt(0)\n"
"v_lshl_or_b32 v218, v153, 16, v152\n"
"v_lshl_or_b32 v219, v155, 16, v154\n"
"s_xor_b32 s69, s69, 0x1000\n"
"s_nop 3\n"
"v_mfma_f32_16x16x32_bf16 a[136:139], v[216:219], v[192:195], a[136:139]\n"
"v_mfma_f32_16x16x32_bf16 a[168:171], v[216:219], v[196:199], a[168:171]\n"
"s_nop 15\n"
"buffer_load_ushort v152, v233, s[48:51], s69 offen offset:32\n"
"buffer_load_ushort v153, v233, s[48:51], s69 offen offset:288\n"
"buffer_load_ushort v154, v233, s[48:51], s69 offen offset:544\n"
"buffer_load_ushort v155, v233, s[48:51], s69 offen offset:800\n"
"s_waitcnt vmcnt(0) lgkmcnt(0)\n"
"v_lshl_or_b32 v216, v153, 16, v152\n"
"v_lshl_or_b32 v217, v155, 16, v154\n"
"s_xor_b32 s69, s69, 0x1000\n"
"buffer_load_ushort v152, v233, s[48:51], s69 offen offset:32\n"
"buffer_load_ushort v153, v233, s[48:51], s69 offen offset:288\n"
"buffer_load_ushort v154, v233, s[48:51], s69 offen offset:544\n"
"buffer_load_ushort v155, v233, s[48:51], s69 offen offset:800\n"
"s_waitcnt vmcnt(0) lgkmcnt(0)\n"
"v_lshl_or_b32 v218, v153, 16, v152\n"
"v_lshl_or_b32 v219, v155, 16, v154\n"
"s_xor_b32 s69, s69, 0x1000\n"
"s_nop 3\n"
"v_mfma_f32_16x16x32_bf16 a[140:143], v[216:219], v[192:195], a[140:143]\n"
"v_mfma_f32_16x16x32_bf16 a[172:175], v[216:219], v[196:199], a[172:175]\n"
"s_nop 15\n"
"s_xor_b32 s69, s11, 2\n"
"s_lshl_b32 s69, s69, 6\n"
"s_add_u32 s69, s69, s71\n"
"buffer_load_ushort v152, v233, s[48:51], s69 offen offset:0\n"
"buffer_load_ushort v153, v233, s[48:51], s69 offen offset:256\n"
"buffer_load_ushort v154, v233, s[48:51], s69 offen offset:512\n"
"buffer_load_ushort v155, v233, s[48:51], s69 offen offset:768\n"
"s_waitcnt vmcnt(0) lgkmcnt(0)\n"
"v_lshl_or_b32 v216, v153, 16, v152\n"
"v_lshl_or_b32 v217, v155, 16, v154\n"
"s_xor_b32 s69, s69, 0x1000\n"
"buffer_load_ushort v152, v233, s[48:51], s69 offen offset:0\n"
"buffer_load_ushort v153, v233, s[48:51], s69 offen offset:256\n"
"buffer_load_ushort v154, v233, s[48:51], s69 offen offset:512\n"
"buffer_load_ushort v155, v233, s[48:51], s69 offen offset:768\n"
"s_waitcnt vmcnt(0) lgkmcnt(0)\n"
"v_lshl_or_b32 v218, v153, 16, v152\n"
"v_lshl_or_b32 v219, v155, 16, v154\n"
"s_xor_b32 s69, s69, 0x1000\n"
"s_nop 3\n"
"v_mfma_f32_16x16x32_bf16 a[144:147], v[216:219], v[192:195], a[144:147]\n"
"v_mfma_f32_16x16x32_bf16 a[176:179], v[216:219], v[196:199], a[176:179]\n"
"s_nop 15\n"
"buffer_load_ushort v152, v233, s[48:51], s69 offen offset:32\n"
"buffer_load_ushort v153, v233, s[48:51], s69 offen offset:288\n"
"buffer_load_ushort v154, v233, s[48:51], s69 offen offset:544\n"
"buffer_load_ushort v155, v233, s[48:51], s69 offen offset:800\n"
"s_waitcnt vmcnt(0) lgkmcnt(0)\n"
"v_lshl_or_b32 v216, v153, 16, v152\n"
"v_lshl_or_b32 v217, v155, 16, v154\n"
"s_xor_b32 s69, s69, 0x1000\n"
"buffer_load_ushort v152, v233, s[48:51], s69 offen offset:32\n"
"buffer_load_ushort v153, v233, s[48:51], s69 offen offset:288\n"
"buffer_load_ushort v154, v233, s[48:51], s69 offen offset:544\n"
"buffer_load_ushort v155, v233, s[48:51], s69 offen offset:800\n"
"s_waitcnt vmcnt(0) lgkmcnt(0)\n"
"v_lshl_or_b32 v218, v153, 16, v152\n"
"v_lshl_or_b32 v219, v155, 16, v154\n"
"s_xor_b32 s69, s69, 0x1000\n"
"s_nop 3\n"
"v_mfma_f32_16x16x32_bf16 a[148:151], v[216:219], v[192:195], a[148:151]\n"
"v_mfma_f32_16x16x32_bf16 a[180:183], v[216:219], v[196:199], a[180:183]\n"
"s_nop 15\n"
"s_xor_b32 s69, s11, 3\n"
"s_lshl_b32 s69, s69, 6\n"
"s_add_u32 s69, s69, s71\n"
"buffer_load_ushort v152, v233, s[48:51], s69 offen offset:0\n"
"buffer_load_ushort v153, v233, s[48:51], s69 offen offset:256\n"
"buffer_load_ushort v154, v233, s[48:51], s69 offen offset:512\n"
"buffer_load_ushort v155, v233, s[48:51], s69 offen offset:768\n"
"s_waitcnt vmcnt(0) lgkmcnt(0)\n"
"v_lshl_or_b32 v216, v153, 16, v152\n"
"v_lshl_or_b32 v217, v155, 16, v154\n"
"s_xor_b32 s69, s69, 0x1000\n"
"buffer_load_ushort v152, v233, s[48:51], s69 offen offset:0\n"
"buffer_load_ushort v153, v233, s[48:51], s69 offen offset:256\n"
"buffer_load_ushort v154, v233, s[48:51], s69 offen offset:512\n"
"buffer_load_ushort v155, v233, s[48:51], s69 offen offset:768\n"
"s_waitcnt vmcnt(0) lgkmcnt(0)\n"
"v_lshl_or_b32 v218, v153, 16, v152\n"
"v_lshl_or_b32 v219, v155, 16, v154\n"
"s_xor_b32 s69, s69, 0x1000\n"
"s_nop 3\n"
"v_mfma_f32_16x16x32_bf16 a[152:155], v[216:219], v[192:195], a[152:155]\n"
"v_mfma_f32_16x16x32_bf16 a[184:187], v[216:219], v[196:199], a[184:187]\n"
"s_nop 15\n"
"buffer_load_ushort v152, v233, s[48:51], s69 offen offset:32\n"
"buffer_load_ushort v153, v233, s[48:51], s69 offen offset:288\n"
"buffer_load_ushort v154, v233, s[48:51], s69 offen offset:544\n"
"buffer_load_ushort v155, v233, s[48:51], s69 offen offset:800\n"
"s_waitcnt vmcnt(0) lgkmcnt(0)\n"
"v_lshl_or_b32 v216, v153, 16, v152\n"
"v_lshl_or_b32 v217, v155, 16, v154\n"
"s_xor_b32 s69, s69, 0x1000\n"
"buffer_load_ushort v152, v233, s[48:51], s69 offen offset:32\n"
"buffer_load_ushort v153, v233, s[48:51], s69 offen offset:288\n"
"buffer_load_ushort v154, v233, s[48:51], s69 offen offset:544\n"
"buffer_load_ushort v155, v233, s[48:51], s69 offen offset:800\n"
"s_waitcnt vmcnt(0) lgkmcnt(0)\n"
"v_lshl_or_b32 v218, v153, 16, v152\n"
"v_lshl_or_b32 v219, v155, 16, v154\n"
"s_xor_b32 s69, s69, 0x1000\n"
"s_nop 3\n"
"v_mfma_f32_16x16x32_bf16 a[156:159], v[216:219], v[192:195], a[156:159]\n"
"v_mfma_f32_16x16x32_bf16 a[188:191], v[216:219], v[196:199], a[188:191]\n"
"s_nop 15\n"
"s_cmp_lg_u32 s70, 0x40000\n"
"s_cbranch_scc1 .Lmha_SL2_1\n"
"v_mov_b32 v252, v220\n"
"v_mov_b32 v253, v224\n"
"v_mov_b32 v255, v228\n"
"s_nop 1\n"
"s_add_u32 s69, s68, 0\n"
"buffer_load_dwordx4 v[152:155], v22, s[64:67], s69 offen offset:0 sc1\n"
"s_add_u32 s69, s68, 0x400\n"
"buffer_load_dwordx4 v[156:159], v22, s[64:67], s69 offen offset:0 sc1\n"
"s_add_u32 s69, s68, 0x800\n"
"buffer_load_dwordx4 v[160:163], v22, s[64:67], s69 offen offset:0 sc1\n"
"s_add_u32 s69, s68, 0xc00\n"
"buffer_load_dwordx4 v[164:167], v22, s[64:67], s69 offen offset:0 sc1\n"
"s_add_u32 s69, s68, 0x1000\n"
"buffer_load_dwordx4 v[168:171], v22, s[64:67], s69 offen offset:0 sc1\n"
"s_add_u32 s69, s68, 0x1400\n"
"buffer_load_dwordx4 v[172:175], v22, s[64:67], s69 offen offset:0 sc1\n"
"s_add_u32 s69, s68, 0x1800\n"
"buffer_load_dwordx4 v[176:179], v22, s[64:67], s69 offen offset:0 sc1\n"
"s_add_u32 s69, s68, 0x1c00\n"
"buffer_load_dwordx4 v[180:183], v22, s[64:67], s69 offen offset:0 sc1\n"
"s_add_u32 s69, s68, 0x2000\n"
"buffer_load_dwordx4 v[184:187], v22, s[64:67], s69 offen offset:0 sc1\n"
"s_add_u32 s69, s68, 0x2400\n"
"buffer_load_dwordx4 v[188:191], v22, s[64:67], s69 offen offset:0 sc1\n"
"s_add_u32 s69, s68, 0x2800\n"
"buffer_load_dwordx4 v[192:195], v22, s[64:67], s69 offen offset:0 sc1\n"
"s_add_u32 s69, s68, 0x2c00\n"
"buffer_load_dwordx4 v[196:199], v22, s[64:67], s69 offen offset:0 sc1\n"
"s_add_u32 s69, s68, 0x3000\n"
"buffer_load_dwordx4 v[200:203], v22, s[64:67], s69 offen offset:0 sc1\n"
"s_add_u32 s69, s68, 0x3400\n"
"buffer_load_dwordx4 v[204:207], v22, s[64:67], s69 offen offset:0 sc1\n"
"s_add_u32 s69, s68, 0x3800\n"
"buffer_load_dwordx4 v[208:211], v22, s[64:67], s69 offen offset:0 sc1\n"
"s_add_u32 s69, s68, 0x3c00\n"
"buffer_load_dwordx4 v[212:215], v22, s[64:67], s69 offen offset:0 sc1\n"
"s_add_u32 s69, s68, 0x4000\n"
"buffer_load_dwordx4 v[216:219], v22, s[64:67], s69 offen offset:0 sc1\n"
"s_add_u32 s69, s68, 0x4400\n"
"buffer_load_dwordx4 v[220:223], v22, s[64:67], s69 offen offset:0 sc1\n"
"s_add_u32 s69, s68, 0x4800\n"
"buffer_load_dwordx4 v[224:227], v22, s[64:67], s69 offen offset:0 sc1\n"
"s_add_u32 s69, s68, 0x4c00\n"
"buffer_load_dwordx4 v[228:231], v22, s[64:67], s69 offen offset:0 sc1\n"
"s_add_u32 s69, s68, 0x5000\n"
"buffer_load_dwordx4 v[232:235], v22, s[64:67], s69 offen offset:0 sc1\n"
"s_add_u32 s69, s68, 0x5400\n"
"buffer_load_dwordx4 v[236:239], v22, s[64:67], s69 offen offset:0 sc1\n"
"s_add_u32 s69, s68, 0x5800\n"
"buffer_load_dwordx4 v[240:243], v22, s[64:67], s69 offen offset:0 sc1\n"
"s_waitcnt vmcnt(0) lgkmcnt(0)\n"
"s_nop 7\n"
"s_branch .Lmha_SLOWRET1\n"
".Lmha_LIM2:\n"
"s_memrealtime s[24:25]\n"
"s_waitcnt lgkmcnt(0)\n"
"s_sub_u32 s25, s5, s24\n"
"s_sub_u32 s25, s25, 1\n"
"s_cmp_lt_u32 s25, 400\n"
"s_cbranch_scc1 .Lmha_LIM2\n"
"s_mov_b32 s24, s5\n"
"s_branch .Lmha_LIM2R\n"
".Lmha_LIM3:\n"
"s_memrealtime s[24:25]\n"
"s_waitcnt lgkmcnt(0)\n"
"s_sub_u32 s25, s5, s24\n"
"s_sub_u32 s25, s25, 1\n"
"s_cmp_lt_u32 s25, 400\n"
"s_cbranch_scc1 .Lmha_LIM3\n"
"s_mov_b32 s24, s5\n"
"s_branch .Lmha_LIM3R\n"
".Lmha_LIM4:\n"
"s_memrealtime s[24:25]\n"
"s_waitcnt lgkmcnt(0)\n"
"s_sub_u32 s25, s5, s24\n"
"s_sub_u32 s25, s25, 1\n"
"s_cmp_lt_u32 s25, 400\n"
"s_cbranch_scc1 .Lmha_LIM4\n"
"s_mov_b32 s24, s5\n"
"s_branch .Lmha_LIM4R\n"
".Lmha_LIM5:\n"
"s_memrealtime s[24:25]\n"
"s_waitcnt lgkmcnt(0)\n"
"s_sub_u32 s25, s5, s24\n"
"s_sub_u32 s25, s25, 1\n"
"s_cmp_lt_u32 s25, 400\n"
"s_cbranch_scc1 .Lmha_LIM5\n"
"s_mov_b32 s24, s5\n"
"s_branch .Lmha_LIM5R\n"
".Lmha_LIM6:\n"
"s_memrealtime s[24:25]\n"
"s_waitcnt lgkmcnt(0)\n"
"s_sub_u32 s25, s5, s24\n"
"s_sub_u32 s25, s25, 1\n"
"s_cmp_lt_u32 s25, 400\n"
"s_cbranch_scc1 .Lmha_LIM6\n"
"s_mov_b32 s24, s5\n"
"s_branch .Lmha_LIM6R\n"
".Lmha_LIM7:\n"
"s_memrealtime s[24:25]\n"
"s_waitcnt lgkmcnt(0)\n"
"s_sub_u32 s25, s5, s24\n"
"s_sub_u32 s25, s25, 1\n"
"s_cmp_lt_u32 s25, 400\n"
"s_cbranch_scc1 .Lmha_LIM7\n"
"s_mov_b32 s24, s5\n"
"s_branch .Lmha_LIM7R\n"
".Lmha_LIM8:\n"
"s_memrealtime s[24:25]\n"
"s_waitcnt lgkmcnt(0)\n"
"s_sub_u32 s25, s5, s24\n"
"s_sub_u32 s25, s25, 1\n"
"s_cmp_lt_u32 s25, 400\n"
"s_cbranch_scc1 .Lmha_LIM8\n"
"s_mov_b32 s24, s5\n"
"s_branch .Lmha_LIM8R\n"
".Lmha_LIM9:\n"
"s_memrealtime s[24:25]\n"
"s_waitcnt lgkmcnt(0)\n"
"s_sub_u32 s25, s5, s24\n"
"s_sub_u32 s25, s25, 1\n"
"s_cmp_lt_u32 s25, 400\n"
"s_cbranch_scc1 .Lmha_LIM9\n"
"s_mov_b32 s24, s5\n"
"s_branch .Lmha_LIM9R\n"
".Lmha_LIM10:\n"
"s_memrealtime s[24:25]\n"
"s_waitcnt lgkmcnt(0)\n"
"s_sub_u32 s25, s5, s24\n"
"s_sub_u32 s25, s25, 1\n"
"s_cmp_lt_u32 s25, 400\n"
"s_cbranch_scc1 .Lmha_LIM10\n"
"s_mov_b32 s24, s5\n"
"s_branch .Lmha_LIM10R\n"
".Lmha_LIM11:\n"
"s_memrealtime s[24:25]\n"
"s_waitcnt lgkmcnt(0)\n"
"s_sub_u32 s25, s5, s24\n"
"s_sub_u32 s25, s25, 1\n"
"s_cmp_lt_u32 s25, 400\n"
"s_cbranch_scc1 .Lmha_LIM11\n"
"s_mov_b32 s24, s5\n"
"s_branch .Lmha_LIM11R\n"
".Lmha_LIM12:\n"
"s_memrealtime s[24:25]\n"
"s_waitcnt lgkmcnt(0)\n"
"s_sub_u32 s25, s5, s24\n"
"s_sub_u32 s25, s25, 1\n"
"s_cmp_lt_u32 s25, 400\n"
"s_cbranch_scc1 .Lmha_LIM12\n"
"s_mov_b32 s24, s5\n"
"s_branch .Lmha_LIM12R\n"
".Lmha_LIM13:\n"
"s_memrealtime s[24:25]\n"
"s_waitcnt lgkmcnt(0)\n"
"s_sub_u32 s25, s5, s24\n"
"s_sub_u32 s25, s25, 1\n"
"s_cmp_lt_u32 s25, 400\n"
"s_cbranch_scc1 .Lmha_LIM13\n"
"s_mov_b32 s24, s5\n"
"s_branch .Lmha_LIM13R\n"
".Lmha_LIM14:\n"
"s_memrealtime s[24:25]\n"
"s_waitcnt lgkmcnt(0)\n"
"s_sub_u32 s25, s5, s24\n"
"s_sub_u32 s25, s25, 1\n"
"s_cmp_lt_u32 s25, 400\n"
"s_cbranch_scc1 .Lmha_LIM14\n"
"s_mov_b32 s24, s5\n"
"s_branch .Lmha_LIM14R\n"
".Lmha_LIM15:\n"
"s_memrealtime s[24:25]\n"
"s_waitcnt lgkmcnt(0)\n"
"s_sub_u32 s25, s5, s24\n"
"s_sub_u32 s25, s25, 1\n"
"s_cmp_lt_u32 s25, 400\n"
"s_cbranch_scc1 .Lmha_LIM15\n"
"s_mov_b32 s24, s5\n"
"s_branch .Lmha_LIM15R\n"
".Lmha_SPA:\n"
"s_add_u32 s95, s95, 1\n"
"s_cmp_gt_u32 s95, 0x20000\n"
"s_cbranch_scc1 .Lmha_SPAR\n"
"s_sleep 1\n"
"buffer_load_dwordx2 v[28:29], v30, s[64:67], s38 offen offset:0 sc1\n"
"s_waitcnt vmcnt(0)\n"
"v_cmp_ne_u64 s[36:37], s[86:87], v[28:29]\n"
"s_nop 3\n"
"s_cmp_lg_u64 s[36:37], 0\n"
"s_cbranch_scc1 .Lmha_SPA\n"
"s_branch .Lmha_SPAR\n"
".Lmha_SPB:\n"
"s_add_u32 s95, s95, 1\n"
"s_cmp_gt_u32 s95, 0x20000\n"
"s_cbranch_scc1 .Lmha_SPBR\n"
"s_sleep 1\n"
"buffer_load_dwordx2 v[28:29], v30, s[64:67], s38 offen offset:0 sc1\n"
"s_waitcnt vmcnt(0)\n"
"v_cmp_ne_u64 s[36:37], s[86:87], v[28:29]\n"
"s_nop 3\n"
"s_cmp_lg_u64 s[36:37], 0\n"
"s_cbranch_scc1 .Lmha_SPB\n"
"s_branch .Lmha_SPBR\n"
".Lmha_SPP0:\n"
"s_add_u32 s95, s95, 1\n"
"s_cmp_gt_u32 s95, 0x20000\n"
"s_cbranch_scc1 .Lmha_SPP0R\n"
"s_sleep 6\n"
"s_add_u32 s38, s41, 0\n"
"buffer_load_dwordx4 v[188:191], v21, s[64:67], s38 offen offset:0 sc1\n"
"s_add_u32 s38, s41, 0\n"
"buffer_load_dwordx4 v[192:195], v21, s[64:67], s38 offen offset:1024 sc1\n"
"s_add_u32 s38, s41, 0\n"
"buffer_load_dwordx4 v[196:199], v21, s[64:67], s38 offen offset:2048 sc1\n"
"s_add_u32 s38, s41, 0\n"
"buffer_load_dwordx4 v[200:203], v21, s[64:67], s38 offen offset:3072 sc1\n"
"s_waitcnt vmcnt(0)\n"
"v_min3_u32 v28, v26, v189, v191\n"
"v_max3_u32 v29, v26, v189, v191\n"
"v_min3_u32 v28, v28, v193, v195\n"
"v_max3_u32 v29, v29, v193, v195\n"
"v_min3_u32 v28, v28, v197, v199\n"
"v_max3_u32 v29, v29, v197, v199\n"
"v_min3_u32 v28, v28, v201, v203\n"
"v_max3_u32 v29, v29, v201, v203\n"
"v_cmp_ne_u32 s[36:37], v28, v29\n"
"s_nop 3\n"
"s_cmp_lg_u64 s[36:37], 0\n"
"s_cbranch_scc1 .Lmha_SPP0\n"
"s_branch .Lmha_SPP0R\n"
".Lmha_SPP1:\n"
"s_add_u32 s95, s95, 1\n"
"s_cmp_gt_u32 s95, 0x20000\n"
"s_cbranch_scc1 .Lmha_SPP1R\n"
"s_sleep 6\n"
"s_add_u32 s38, s41, 0x1000\n"
"buffer_load_dwordx4 v[204:207], v21, s[64:67], s38 offen offset:0 sc1\n"
"s_add_u32 s38, s41, 0x1000\n"
"buffer_load_dwordx4 v[208:211], v21, s[64:67], s38 offen offset:1024 sc1\n"
"s_add_u32 s38, s41, 0x1000\n"
"buffer_load_dwordx4 v[212:215], v21, s[64:67], s38 offen offset:2048 sc1\n"
"s_add_u32 s38, s41, 0x1000\n"
"buffer_load_dwordx4 v[216:219], v21, s[64:67], s38 offen offset:3072 sc1\n"
"s_add_u32 s38, s41, 0x2000\n"
"buffer_load_dwordx4 v[220:223], v21, s[64:67], s38 offen offset:0 sc1\n"
"s_waitcnt vmcnt(0)\n"
"v_min3_u32 v28, v26, v205, v207\n"
"v_max3_u32 v29, v26, v205, v207\n"
"v_min3_u32 v28, v28, v209, v211\n"
"v_max3_u32 v29, v29, v209, v211\n"
"v_min3_u32 v28, v28, v213, v215\n"
"v_max3_u32 v29, v29, v213, v215\n"
"v_min3_u32 v28, v28, v217, v219\n"
"v_max3_u32 v29, v29, v217, v219\n"
"v_min3_u32 v28, v28, v221, v223\n"
"v_max3_u32 v29, v29, v221, v223\n"
"v_cmp_ne_u32 s[36:37], v28, v29\n"
"s_nop 3\n"
"s_cmp_lg_u64 s[36:37], 0\n"
"s_cbranch_scc1 .Lmha_SPP1\n"
"s_branch .Lmha_SPP1R\n"
".fill 2560, 4, 0xbf800000\n"
".Lmha_END:\n"
  : "+v"(tid) : "s"(__builtin_amdgcn_kernarg_segment_ptr()), "s"(blockIdx.x)
  : "v1","v2","v3","v4","v5","v6","v7","v8","v9","v10","v11","v12","v13","v14","v15","v16","v17","v18","v19","v20","v21","v22","v23","v24","v25","v26","v27","v28","v29","v30","v31","v32","v33","v34","v35","v36","v37","v38","v39","v40","v41","v42","v43","v44","v45","v46","v47","v48","v49","v50","v51","v52","v53","v54","v55","v56","v57","v58","v59","v60","v61","v62","v63","v64","v65","v66","v67","v68","v69","v70","v71","v72","v73","v74","v75","v76","v77","v78","v79","v80","v81","v82","v83","v84","v85","v86","v87","v88","v89","v90","v91","v92","v93","v94","v95","v96","v97","v98","v99","v100","v101","v102","v103","v104","v105","v106","v107","v108","v109","v110","v111","v112","v113","v114","v115","v116","v117","v118","v119","v120","v121","v122","v123","v124","v125","v126","v127","v128","v129","v130","v131","v132","v133","v134","v135","v136","v137","v138","v139","v140","v141","v142","v143","v144","v145","v146","v147","v148","v149","v150","v151","v152","v153","v154","v155","v156","v157","v158","v159","v160","v161","v162","v163","v164","v165","v166","v167","v168","v169","v170","v171","v172","v173","v174","v175","v176","v177","v178","v179","v180","v181","v182","v183","v184","v185","v186","v187","v188","v189","v190","v191","v192","v193","v194","v195","v196","v197","v198","v199","v200","v201","v202","v203","v204","v205","v206","v207","v208","v209","v210","v211","v212","v213","v214","v215","v216","v217","v218","v219","v220","v221","v222","v223","v224","v225","v226","v227","v228","v229","v230","v231","v232","v233","v234","v235","v236","v237","v238","v239","v240","v241","v242","v243","v244","v245","v246","v247","v248","v249","v250","v251","v252","v253","v254","v255","a0","a1","a2","a3","a4","a5","a6","a7","a8","a9","a10","a11","a12","a13","a14","a15","a16","a17","a18","a19","a20","a21","a22","a23","a24","a25","a26","a27","a28","a29","a30","a31","a32","a33","a34","a35","a36","a37","a38","a39","a40","a41","a42","a43","a44","a45","a46","a47","a48","a49","a50","a51","a52","a53","a54","a55","a56","a57","a58","a59","a60","a61","a62","a63","a64","a65","a66","a67","a68","a69","a70","a71","a72","a73","a74","a75","a76","a77","a78","a79","a80","a81","a82","a83","a84","a85","a86","a87","a88","a89","a90","a91","a92","a93","a94","a95","a96","a97","a98","a99","a100","a101","a102","a103","a104","a105","a106","a107","a108","a109","a110","a111","a112","a113","a114","a115","a116","a117","a118","a119","a120","a121","a122","a123","a124","a125","a126","a127","a128","a129","a130","a131","a132","a133","a134","a135","a136","a137","a138","a139","a140","a141","a142","a143","a144","a145","a146","a147","a148","a149","a150","a151","a152","a153","a154","a155","a156","a157","a158","a159","a160","a161","a162","a163","a164","a165","a166","a167","a168","a169","a170","a171","a172","a173","a174","a175","a176","a177","a178","a179","a180","a181","a182","a183","a184","a185","a186","a187","a188","a189","a190","a191","a192","a193","a194","a195","a196","a197","a198","a199","a200","a201","a202","a203","a204","a205","a206","a207","a208","a209","a210","a211","a212","a213","a214","a215","a216","a217","a218","a219","a220","a221","a222","a223","a224","a225","a226","a227","a228","a229","a230","a231","a232","a233","a234","a235","a236","a237","a238","a239","a240","a241","a242","a243","a244","a245","a246","a247","a248","a249","a250","a251","a252","a253","a254","a255","s3","s4","s5","s6","s7","s8","s9","s10","s11","s12","s13","s14","s15","s16","s17","s18","s19","s20","s21","s22","s23","s24","s25","s26","s27","s28","s29","s30","s31","s34","s35","s36","s37","s38","s39","s40","s41","s42","s43","s44","s45","s46","s47","s48","s49","s50","s51","s52","s53","s54","s55","s56","s57","s58","s59","s60","s61","s62","s63","s64","s65","s66","s67","s68","s69","s70","s71","s72","s73","s74","s75","s76","s77","s78","s79","s80","s81","s82","s83","s84","s85","s86","s87","s88","s89","s90","s91","s92","s93","s94","s95","s96","s97","s98","s99","vcc","scc","memory");
}

extern "C" void kernel_launch(void* const* d_in, const int* in_sizes, int n_in, void* d_out, int out_size, void* d_ws, size_t ws_size, hipStream_t stream) {
  if (n_in != 5 || out_size != 16 * 144 * 1024 || ws_size < (size_t)112 * 1024 * 1024) return;
  (void)hipFuncSetAttribute((const void*)mha_fused, hipFuncAttributeMaxDynamicSharedMemorySize, 163840);
  mha_fused<<<256, 256, 163840, stream>>>(d_in[0], d_in[1], d_in[2], d_in[3], d_in[4], d_out, d_ws);
}
```
